```python
import torch

def _softmax(x, axis=-1):
    return torch.softmax(x, dim=axis)

def _gelu(x):
    return torch.nn.functional.gelu(x, approximate='tanh')

def _silu(x):
    return torch.nn.functional.silu(x)

def _sigmoid(x):
    return torch.sigmoid(x)

def _layernorm(x, axis=-1, eps=1e-05):
    m = x.mean(dim=axis, keepdim=True)
    v = x.var(dim=axis, keepdim=True, unbiased=False)
    return (x - m) / torch.sqrt(v + eps)

def _scatter_add(tgt, idx, src, axis=0):
    return tgt.clone().index_add_(axis, idx, src)

def _segment_sum(src, seg, n):
    out = torch.zeros((n,) + src.shape[1:], dtype=src.dtype)
    return out.index_add_(0, seg, src)

def _pad(x, pw):
    flat = tuple((v for pair in reversed(pw) for v in pair))
    return torch.nn.functional.pad(x, flat)
kernel_name = 'parallel_sum__ssm_conv_h3__mixer_gmlp__prompt'

def setup_inputs():

    _w = lambda a: (lambda t: t.to(torch.bfloat16) if t.is_floating_point() else t)(torch.as_tensor(a))

    def _A_setup():
        B, S, D = (1, 2048, 576)
        ks = 4
        return {'q': _w((lambda *s: torch.randn(*s, dtype=torch.bfloat16) * 0.05)(B, S, D)), 'k': _w((lambda *s: torch.randn(*s, dtype=torch.bfloat16) * 1.6)(B, S, D)), 'v': _w((lambda *s: torch.randn(*s, dtype=torch.bfloat16) * 0.4)(B, S, D)), 'shift_k': _w((lambda *s: torch.randn(*s, dtype=torch.bfloat16) * 1.6)(D, ks) * 0.5), 'ssm_k': _w((lambda *s: torch.randn(*s, dtype=torch.bfloat16) * 3.2)(D, S) * (1.0 / S ** 0.5)), 'ssm_D': _w((lambda *s: torch.randn(*s, dtype=torch.bfloat16) * 0.8)(D))}
    _A_in = _A_setup()

    def _B_setup():
        B, S, D, F = (1, 2048, 576, 4608)
        Fh = F // 2
        return {'x': _w((lambda *s: torch.randn(*s, dtype=torch.bfloat16) * 0.05)(B, S, D)), 'w_in': _w((lambda *s: torch.randn(*s, dtype=torch.bfloat16) * 0.4)(D, 2 * Fh)), 'w_sp': _w(0.01 * (lambda *s: torch.randn(*s, dtype=torch.bfloat16) * 0.4)(S, S)), 'b_sp': _w(1.0 + 0.01 * (lambda *s: torch.randn(*s, dtype=torch.bfloat16) * 0.05)(S)), 'w_out': _w((lambda *s: torch.randn(*s, dtype=torch.bfloat16) * 0.05)(Fh, D))}
    _B_in = _B_setup()
    return {'A_q': _A_in['q'], 'A_k': _A_in['k'], 'A_v': _A_in['v'], 'A_shift_k': _A_in['shift_k'], 'A_ssm_k': _A_in['ssm_k'], 'A_ssm_D': _A_in['ssm_D'], 'B_x': _B_in['x'], 'B_w_in': _B_in['w_in'], 'B_w_sp': _B_in['w_sp'], 'B_b_sp': _B_in['b_sp'], 'B_w_out': _B_in['w_out']}

def reference(A_q, A_k, A_v, A_shift_k, A_ssm_k, A_ssm_D, B_x, B_w_in, B_w_sp, B_b_sp, B_w_out):

    def _A_ref(q, k, v, shift_k, ssm_k, ssm_D):
        B, S, D = q.shape
        ks = shift_k.shape[-1]
        k_pad = _pad(k, ((0, 0), (ks - 1, 0), (0, 0)))
        k_shift = torch.zeros_like(k)
        for t in range(ks):
            k_shift = k_shift + k_pad[:, t:t + S, :] * shift_k[:, ks - 1 - t][None, None, :]
        kv = k_shift * v
        L = 2 * S
        kv_pad = _pad(kv, ((0, 0), (0, S), (0, 0)))
        kf_pad = _pad(ssm_k, ((0, 0), (0, S)))
        kv_f = (lambda x, axes=(-2, -1): torch.fft.rfft2(x.float(), dim=axes))(kv_pad[:, None, :, :], axes=(1, 2))
        kf_f = (lambda x, axes=(-2, -1): torch.fft.rfft2(x.float(), dim=axes))(kf_pad[:, None, :], axes=(1, 2))
        kf_f = (lambda x, *d: x.permute(*d))(kf_f, 1, 2, 0)
        y_f = kv_f * kf_f[None, :, :, :]
        y = (lambda x, s, axes=(-2, -1): torch.fft.irfft2(x, s=s, dim=axes).to(torch.bfloat16))(y_f, (1, L), axes=(1, 2))[:, 0, :S, :]
        y = y + kv * ssm_D[None, None, :]
        return q * y

    def _B_ref(x, w_in, w_sp, b_sp, w_out):
        B, S, D = x.shape
        F = w_in.shape[-1]
        Fh = F // 2
        z = _gelu(torch.einsum('bsd,df->bsf', x, w_in))
        z1 = z[..., :Fh].clone()
        z2 = _layernorm(z[..., Fh:], axis=-1)
        g = torch.einsum('ts,bsf->btf', w_sp, z2) + b_sp[None, :, None]
        return torch.einsum('bsf,fd->bsd', z1 * g, w_out)
    x = A_q
    xn = _layernorm(x, axis=-1)
    return x + _A_ref(xn, A_k, A_v, A_shift_k, A_ssm_k, A_ssm_D) + _B_ref(xn, B_w_in, B_w_sp, B_b_sp, B_w_out)
```

```cpp
#include <hip/hip_runtime.h>
#include <hip/hip_bf16.h>
typedef unsigned short u16;
typedef short bf16x8 __attribute__((ext_vector_type(8)));
typedef float f32x4 __attribute__((ext_vector_type(4)));
constexpr int S = 2048, D = 576, F = 4608, FH = 2304;
__device__ __forceinline__ float b2f(u16 h) { return __uint_as_float((unsigned)h << 16); }
__device__ __forceinline__ u16 f2b(float f) { unsigned u = __float_as_uint(f); u += 0x7fff + ((u >> 16) & 1); return u >> 16; }

__global__ void k_ln(const u16* in, int ld, int n, u16* out) {
  const int row = blockIdx.x, tid = threadIdx.x; __shared__ float red[2][4];
  float s = 0; for (int i = tid; i < n; i += 256) s += b2f(in[(size_t)row * ld + i]);
  for (int o = 32; o; o >>= 1) s += __shfl_xor(s, o);
  if ((tid & 63) == 0) red[0][tid >> 6] = s; __syncthreads();
  const float m = (red[0][0] + red[0][1] + red[0][2] + red[0][3]) / n;
  float q = 0; for (int i = tid; i < n; i += 256) { float d = b2f(in[(size_t)row * ld + i]) - m; q += d * d; }
  for (int o = 32; o; o >>= 1) q += __shfl_xor(q, o);
  if ((tid & 63) == 0) red[1][tid >> 6] = q; __syncthreads();
  const float r = rsqrtf((red[1][0] + red[1][1] + red[1][2] + red[1][3]) / n + 1e-5f);
  for (int i = tid; i < n; i += 256) out[(size_t)row * n + i] = f2b((b2f(in[(size_t)row * ld + i]) - m) * r);
}
__global__ void k_kv(const u16* k, const u16* v, const u16* sh, u16* kvT) {
  const int i = blockIdx.x * 256 + threadIdx.x; const int t = i / D, d = i % D;
  float a = 0;
  for (int j = 0; j < 4; ++j) if (t - j >= 0) a += b2f(k[(size_t)(t - j) * D + d]) * b2f(sh[d * 4 + j]);
  kvT[(size_t)d * S + t] = f2b(b2f(f2b(a)) * b2f(v[i]));
}
__global__ void k_conv(const u16* kvT, const u16* h, const u16* Dv, float* yT) {
  __shared__ float su[S], shh[S]; const int d = blockIdx.x;
  for (int i = threadIdx.x; i < S; i += 256) { su[i] = b2f(kvT[(size_t)d * S + i]); shh[i] = b2f(h[(size_t)d * S + i]); }
  __syncthreads();
  for (int t = threadIdx.x; t < S; t += 256) { float a = 0; for (int s = 0; s <= t; ++s) a += shh[t - s] * su[s]; yT[(size_t)d * S + t] = a + su[t] * b2f(Dv[d]); }
}
template<int EPI>
__global__ __launch_bounds__(256) void k_gemm(const u16* A, const u16* B, int N, int K, u16* C, const u16* e0, const u16* e1, const float* e2) {
  __shared__ __attribute__((aligned(16))) u16 sA[64][40], sB[64][40];
  const int tid = threadIdx.x, lane = tid & 63, w = tid >> 6, wm = w >> 1, wn = w & 1;
  const int m0 = blockIdx.y * 64, n0 = blockIdx.x * 64;
  f32x4 acc[2][2] = {};
  for (int k0 = 0; k0 < K; k0 += 32) {
    *(uint4*)&sA[tid >> 2][(tid & 3) * 8] = *(const uint4*)&A[(size_t)(m0 + (tid >> 2)) * K + k0 + (tid & 3) * 8];
    { uint4 v = *(const uint4*)&B[(size_t)(k0 + (tid >> 3)) * N + n0 + (tid & 7) * 8]; const u16* p = (const u16*)&v;
      for (int j = 0; j < 8; ++j) sB[(tid & 7) * 8 + j][tid >> 3] = p[j]; }
    __syncthreads();
    bf16x8 a[2], b[2];
    for (int i = 0; i < 2; ++i) { a[i] = *(bf16x8*)&sA[wm * 32 + i * 16 + (lane & 15)][(lane >> 4) * 8]; b[i] = *(bf16x8*)&sB[wn * 32 + i * 16 + (lane & 15)][(lane >> 4) * 8]; }
    for (int i = 0; i < 2; ++i) for (int j = 0; j < 2; ++j) acc[i][j] = __builtin_amdgcn_mfma_f32_16x16x32_bf16(a[i], b[j], acc[i][j], 0, 0, 0);
    __syncthreads();
  }
  for (int i = 0; i < 2; ++i) for (int j = 0; j < 2; ++j) for (int e = 0; e < 4; ++e) {
    const int m = m0 + wm * 32 + i * 16 + (lane >> 4) * 4 + e, n = n0 + wn * 32 + j * 16 + (lane & 15); float x = acc[i][j][e];
    if (EPI == 0) { float u = 0.7978845608f * (x + 0.044715f * x * x * x); x = x / (1.f + __expf(-2.f * u)); }
    if (EPI == 1) x = b2f(e0[(size_t)m * F + n]) * (x + b2f(e1[m]));
    if (EPI == 2) x = b2f(e0[(size_t)m * D + n]) + b2f(e1[(size_t)m * D + n]) * e2[(size_t)n * S + m] + x;
    C[(size_t)m * N + n] = f2b(x);
  }
}
extern "C" void kernel_launch(void* const* d_in, const int* in_sizes, int n_in, void* d_out, int out_size, void* d_ws, size_t ws_size, hipStream_t stream) {
  const u16 *q = (const u16*)d_in[0], *k = (const u16*)d_in[1], *v = (const u16*)d_in[2], *sh = (const u16*)d_in[3], *ssmk = (const u16*)d_in[4], *ssmD = (const u16*)d_in[5];
  const u16 *win = (const u16*)d_in[7], *wsp = (const u16*)d_in[8], *bsp = (const u16*)d_in[9], *wout = (const u16*)d_in[10];
  char* ws = (char*)d_ws;
  u16* xn = (u16*)ws; u16* kvT = (u16*)(ws + (4 << 20)); float* yT = (float*)(ws + (8 << 20)); u16* z = (u16*)(ws + (16 << 20)); u16* z2n = (u16*)(ws + (40 << 20)); u16* h = (u16*)(ws + (52 << 20));
  k_ln<<<S, 256, 0, stream>>>(q, D, D, xn);
  k_kv<<<S * D / 256, 256, 0, stream>>>(k, v, sh, kvT);
  k_conv<<<D, 256, 0, stream>>>(kvT, ssmk, ssmD, yT);
  k_gemm<0><<<dim3(F / 64, S / 64), 256, 0, stream>>>(xn, win, F, D, z, nullptr, nullptr, nullptr);
  k_ln<<<S, 256, 0, stream>>>(z + FH, F, FH, z2n);
  k_gemm<1><<<dim3(FH / 64, S / 64), 256, 0, stream>>>(wsp, z2n, FH, S, h, z, bsp, nullptr);
  k_gemm<2><<<dim3(D / 64, S / 64), 256, 0, stream>>>(h, wout, D, FH, (u16*)d_out, q, xn, yT);
}
```

```cpp
#include <hip/hip_runtime.h>
#include <hip/hip_bf16.h>
typedef unsigned short u16;
typedef short bf16x8 __attribute__((ext_vector_type(8)));
typedef float f32x4 __attribute__((ext_vector_type(4)));
constexpr int S = 2048, D = 576, F = 4608, FH = 2304;
__device__ __forceinline__ float b2f(u16 h) { return __uint_as_float((unsigned)h << 16); }
__device__ __forceinline__ u16 f2b(float f) { unsigned u = __float_as_uint(f); u += 0x7fff + ((u >> 16) & 1); return u >> 16; }

__global__ void k_ln(const u16* in, int ld, int n, u16* out) {
  const int row = blockIdx.x, tid = threadIdx.x; __shared__ float red[2][4];
  float s = 0; for (int i = tid; i < n; i += 256) s += b2f(in[(size_t)row * ld + i]);
  for (int o = 32; o; o >>= 1) s += __shfl_xor(s, o);
  if ((tid & 63) == 0) red[0][tid >> 6] = s; __syncthreads();
  const float m = (red[0][0] + red[0][1] + red[0][2] + red[0][3]) / n;
  float q = 0; for (int i = tid; i < n; i += 256) { float d = b2f(in[(size_t)row * ld + i]) - m; q += d * d; }
  for (int o = 32; o; o >>= 1) q += __shfl_xor(q, o);
  if ((tid & 63) == 0) red[1][tid >> 6] = q; __syncthreads();
  const float r = rsqrtf((red[1][0] + red[1][1] + red[1][2] + red[1][3]) / n + 1e-5f);
  for (int i = tid; i < n; i += 256) out[(size_t)row * n + i] = f2b((b2f(in[(size_t)row * ld + i]) - m) * r);
}
__global__ void k_kv(const u16* k, const u16* v, const u16* sh, u16* kvT) {
  const int i = blockIdx.x * 256 + threadIdx.x; const int t = i / D, d = i % D;
  float a = 0;
  for (int j = 0; j < 4; ++j) if (t - j >= 0) a += b2f(k[(size_t)(t - j) * D + d]) * b2f(sh[d * 4 + j]);
  kvT[(size_t)d * S + t] = f2b(b2f(f2b(a)) * b2f(v[i]));
}
__global__ void k_conv(const u16* kvT, const u16* h, const u16* Dv, float* yT) {
  __shared__ float su[S], shh[S]; const int d = blockIdx.x;
  for (int i = threadIdx.x; i < S; i += 256) { su[i] = b2f(kvT[(size_t)d * S + i]); shh[i] = b2f(h[(size_t)d * S + i]); }
  __syncthreads();
  for (int t = threadIdx.x; t < S; t += 256) { float a = 0; for (int s = 0; s <= t; ++s) a += shh[t - s] * su[s]; yT[(size_t)d * S + t] = a + su[t] * b2f(Dv[d]); }
}
template<int EPI>
__global__ __launch_bounds__(256) void k_gemm(const u16* A, const u16* B, int N, int K, u16* C, const u16* e0, const u16* e1, const float* e2) {
  __shared__ __attribute__((aligned(16))) u16 sA[64][40], sB[64][40];
  const int tid = threadIdx.x, lane = tid & 63, w = tid >> 6, wm = w >> 1, wn = w & 1;
  const int m0 = blockIdx.y * 64, n0 = blockIdx.x * 64;
  f32x4 acc[2][2] = {};
  for (int k0 = 0; k0 < K; k0 += 32) {
    *(uint4*)&sA[tid >> 2][(tid & 3) * 8] = *(const uint4*)&A[(size_t)(m0 + (tid >> 2)) * K + k0 + (tid & 3) * 8];
    { uint4 v = *(const uint4*)&B[(size_t)(k0 + (tid >> 3)) * N + n0 + (tid & 7) * 8]; const u16* p = (const u16*)&v;
      for (int j = 0; j < 8; ++j) sB[(tid & 7) * 8 + j][tid >> 3] = p[j]; }
    __syncthreads();
    bf16x8 a[2], b[2];
    for (int i = 0; i < 2; ++i) { a[i] = *(bf16x8*)&sA[wm * 32 + i * 16 + (lane & 15)][(lane >> 4) * 8]; b[i] = *(bf16x8*)&sB[wn * 32 + i * 16 + (lane & 15)][(lane >> 4) * 8]; }
    for (int i = 0; i < 2; ++i) for (int j = 0; j < 2; ++j) acc[i][j] = __builtin_amdgcn_mfma_f32_16x16x32_bf16(a[i], b[j], acc[i][j], 0, 0, 0);
    __syncthreads();
  }
  for (int i = 0; i < 2; ++i) for (int j = 0; j < 2; ++j) for (int e = 0; e < 4; ++e) {
    const int m = m0 + wm * 32 + i * 16 + (lane >> 4) * 4 + e, n = n0 + wn * 32 + j * 16 + (lane & 15); float x = acc[i][j][e];
    if (EPI == 0) { float u = 0.7978845608f * (x + 0.044715f * x * x * x); x = x / (1.f + __expf(-2.f * u)); }
    if (EPI == 1) x = b2f(e0[(size_t)m * F + n]) * (x + b2f(e1[m]));
    if (EPI == 2) x = b2f(e0[(size_t)m * D + n]) + b2f(e1[(size_t)m * D + n]) * e2[(size_t)n * S + m] + x;
    C[(size_t)m * N + n] = f2b(x);
  }
}

typedef unsigned u32x4 __attribute__((ext_vector_type(4)));
struct PA { const char* p[8]; int n[8]; };
__global__ __launch_bounds__(512) void k_p1(PA a, int cnt, unsigned long long* st) {
  unsigned long long t0 = __builtin_amdgcn_s_memrealtime();
  u32x4 acc = {0,0,0,0};
  #pragma unroll
  for (int j = 0; j < 8; ++j) if (j < cnt) {
    const int per = a.n[j] / 256; const char* b = a.p[j] + (size_t)blockIdx.x * per;
    #pragma unroll 4
    for (int o = threadIdx.x * 16; o < per; o += 8192) acc ^= *(const u32x4*)(b + o);
  }
  asm volatile("" :: "v"(acc));
  __syncthreads();
  unsigned long long t1 = __builtin_amdgcn_s_memrealtime();
  if (threadIdx.x == 0) { st[blockIdx.x * 2] = t0; st[blockIdx.x * 2 + 1] = t1; }
}
__global__ void k_p2(const unsigned long long* st) {
  unsigned long long lo = ~0ull, hi = 0;
  for (int i = 0; i < 256; ++i) { lo = st[2*i] < lo ? st[2*i] : lo; hi = st[2*i+1] > hi ? st[2*i+1] : hi; }
  unsigned long long d = (hi - lo) * 200; if (d > 500000) d = 500000;
  unsigned long long t0 = __builtin_amdgcn_s_memrealtime();
  while (__builtin_amdgcn_s_memrealtime() - t0 < d) __builtin_amdgcn_s_sleep(4);
}
extern "C" void kernel_launch(void* const* d_in, const int* in_sizes, int n_in, void* d_out, int out_size, void* d_ws, size_t ws_size, hipStream_t stream) {
  const u16 *q = (const u16*)d_in[0], *k = (const u16*)d_in[1], *v = (const u16*)d_in[2], *sh = (const u16*)d_in[3], *ssmk = (const u16*)d_in[4], *ssmD = (const u16*)d_in[5];
  const u16 *win = (const u16*)d_in[7], *wsp = (const u16*)d_in[8], *bsp = (const u16*)d_in[9], *wout = (const u16*)d_in[10];
  char* ws = (char*)d_ws;
  u16* xn = (u16*)ws; u16* kvT = (u16*)(ws + (4 << 20)); float* yT = (float*)(ws + (8 << 20)); u16* z = (u16*)(ws + (16 << 20)); u16* z2n = (u16*)(ws + (40 << 20)); u16* h = (u16*)(ws + (52 << 20));
  PA pa = {{(const char*)wsp, (const char*)win, (const char*)k, (const char*)v, (const char*)wsp, (const char*)ssmk, (const char*)wout, (const char*)q}, {S*S*2, D*F*2, S*D*2, S*D*2, S*S*2, S*D*2, FH*D*2, 0}};
  k_p1<<<256, 512, 0, stream>>>(pa, 1, (unsigned long long*)(ws + (100 << 20)));
  k_p2<<<1, 1, 0, stream>>>((unsigned long long*)(ws + (100 << 20)));
  k_ln<<<S, 256, 0, stream>>>(q, D, D, xn);
  k_kv<<<S * D / 256, 256, 0, stream>>>(k, v, sh, kvT);
  k_conv<<<D, 256, 0, stream>>>(kvT, ssmk, ssmD, yT);
  k_gemm<0><<<dim3(F / 64, S / 64), 256, 0, stream>>>(xn, win, F, D, z, nullptr, nullptr, nullptr);
  k_ln<<<S, 256, 0, stream>>>(z + FH, F, FH, z2n);
  k_gemm<1><<<dim3(FH / 64, S / 64), 256, 0, stream>>>(wsp, z2n, FH, S, h, z, bsp, nullptr);
  k_gemm<2><<<dim3(D / 64, S / 64), 256, 0, stream>>>(h, wout, D, FH, (u16*)d_out, q, xn, yT);
}
```

```cpp
#include <hip/hip_runtime.h>
#include <hip/hip_bf16.h>
typedef unsigned short u16;
typedef short bf16x8 __attribute__((ext_vector_type(8)));
typedef float f32x4 __attribute__((ext_vector_type(4)));
constexpr int S = 2048, D = 576, F = 4608, FH = 2304;
__device__ __forceinline__ float b2f(u16 h) { return __uint_as_float((unsigned)h << 16); }
__device__ __forceinline__ u16 f2b(float f) { unsigned u = __float_as_uint(f); u += 0x7fff + ((u >> 16) & 1); return u >> 16; }

__global__ void k_ln(const u16* in, int ld, int n, u16* out) {
  const int row = blockIdx.x, tid = threadIdx.x; __shared__ float red[2][4];
  float s = 0; for (int i = tid; i < n; i += 256) s += b2f(in[(size_t)row * ld + i]);
  for (int o = 32; o; o >>= 1) s += __shfl_xor(s, o);
  if ((tid & 63) == 0) red[0][tid >> 6] = s; __syncthreads();
  const float m = (red[0][0] + red[0][1] + red[0][2] + red[0][3]) / n;
  float q = 0; for (int i = tid; i < n; i += 256) { float d = b2f(in[(size_t)row * ld + i]) - m; q += d * d; }
  for (int o = 32; o; o >>= 1) q += __shfl_xor(q, o);
  if ((tid & 63) == 0) red[1][tid >> 6] = q; __syncthreads();
  const float r = rsqrtf((red[1][0] + red[1][1] + red[1][2] + red[1][3]) / n + 1e-5f);
  for (int i = tid; i < n; i += 256) out[(size_t)row * n + i] = f2b((b2f(in[(size_t)row * ld + i]) - m) * r);
}
__global__ void k_kv(const u16* k, const u16* v, const u16* sh, u16* kvT) {
  const int i = blockIdx.x * 256 + threadIdx.x; const int t = i / D, d = i % D;
  float a = 0;
  for (int j = 0; j < 4; ++j) if (t - j >= 0) a += b2f(k[(size_t)(t - j) * D + d]) * b2f(sh[d * 4 + j]);
  kvT[(size_t)d * S + t] = f2b(b2f(f2b(a)) * b2f(v[i]));
}
__global__ void k_conv(const u16* kvT, const u16* h, const u16* Dv, float* yT) {
  __shared__ float su[S], shh[S]; const int d = blockIdx.x;
  for (int i = threadIdx.x; i < S; i += 256) { su[i] = b2f(kvT[(size_t)d * S + i]); shh[i] = b2f(h[(size_t)d * S + i]); }
  __syncthreads();
  for (int t = threadIdx.x; t < S; t += 256) { float a = 0; for (int s = 0; s <= t; ++s) a += shh[t - s] * su[s]; yT[(size_t)d * S + t] = a + su[t] * b2f(Dv[d]); }
}
template<int EPI>
__global__ __launch_bounds__(256) void k_gemm(const u16* A, const u16* B, int N, int K, u16* C, const u16* e0, const u16* e1, const float* e2) {
  __shared__ __attribute__((aligned(16))) u16 sA[64][40], sB[64][40];
  const int tid = threadIdx.x, lane = tid & 63, w = tid >> 6, wm = w >> 1, wn = w & 1;
  const int m0 = blockIdx.y * 64, n0 = blockIdx.x * 64;
  f32x4 acc[2][2] = {};
  for (int k0 = 0; k0 < K; k0 += 32) {
    *(uint4*)&sA[tid >> 2][(tid & 3) * 8] = *(const uint4*)&A[(size_t)(m0 + (tid >> 2)) * K + k0 + (tid & 3) * 8];
    { uint4 v = *(const uint4*)&B[(size_t)(k0 + (tid >> 3)) * N + n0 + (tid & 7) * 8]; const u16* p = (const u16*)&v;
      for (int j = 0; j < 8; ++j) sB[(tid & 7) * 8 + j][tid >> 3] = p[j]; }
    __syncthreads();
    bf16x8 a[2], b[2];
    for (int i = 0; i < 2; ++i) { a[i] = *(bf16x8*)&sA[wm * 32 + i * 16 + (lane & 15)][(lane >> 4) * 8]; b[i] = *(bf16x8*)&sB[wn * 32 + i * 16 + (lane & 15)][(lane >> 4) * 8]; }
    for (int i = 0; i < 2; ++i) for (int j = 0; j < 2; ++j) acc[i][j] = __builtin_amdgcn_mfma_f32_16x16x32_bf16(a[i], b[j], acc[i][j], 0, 0, 0);
    __syncthreads();
  }
  for (int i = 0; i < 2; ++i) for (int j = 0; j < 2; ++j) for (int e = 0; e < 4; ++e) {
    const int m = m0 + wm * 32 + i * 16 + (lane >> 4) * 4 + e, n = n0 + wn * 32 + j * 16 + (lane & 15); float x = acc[i][j][e];
    if (EPI == 0) { float u = 0.7978845608f * (x + 0.044715f * x * x * x); x = x / (1.f + __expf(-2.f * u)); }
    if (EPI == 1) x = b2f(e0[(size_t)m * F + n]) * (x + b2f(e1[m]));
    if (EPI == 2) x = b2f(e0[(size_t)m * D + n]) + b2f(e1[(size_t)m * D + n]) * e2[(size_t)n * S + m] + x;
    C[(size_t)m * N + n] = f2b(x);
  }
}

#define F8 "v_fma_f32 %0, %0, %4, %4\n v_fma_f32 %1, %1, %4, %4\n v_fma_f32 %2, %2, %4, %4\n v_fma_f32 %3, %3, %4, %4\n v_fma_f32 %0, %0, %4, %4\n v_fma_f32 %1, %1, %4, %4\n v_fma_f32 %2, %2, %4, %4\n v_fma_f32 %3, %3, %4, %4\n"
#define F64 F8 F8 F8 F8 F8 F8 F8 F8
#define F512 F64 F64 F64 F64 F64 F64 F64 F64
__global__ __launch_bounds__(256) void k_p1(float* out, unsigned long long* st, int passes) {
  float a = threadIdx.x, b = a + 1, c = a + 2, d = a + 3, e = 0.5f;
  for (int p = 0; p < passes; ++p) {
    unsigned long long t0, t1;
    asm volatile("s_memtime %0\n s_waitcnt lgkmcnt(0)" : "=s"(t0) :: "memory");
    asm volatile(F512 F512 F512 F512 : "+v"(a), "+v"(b), "+v"(c), "+v"(d) : "v"(e));
    asm volatile("s_memtime %0\n s_waitcnt lgkmcnt(0)" : "=s"(t1) :: "memory");
    if (threadIdx.x == 0) st[blockIdx.x * 4 + p] = t1 - t0;
  }
  out[blockIdx.x * 256 + threadIdx.x] = a + b + c + d;
}
__global__ void k_p2(const unsigned long long* st, int mode) {
  unsigned long long s = 0, mx = 0;
  for (int i = 0; i < 256; ++i) { long long d = (long long)st[4*i] - (long long)st[4*i+1]; if (d < 0) d = 0; s += d; mx = (unsigned long long)d > mx ? d : mx; }
  unsigned long long d = (mode ? mx : s / 256) * 10; if (d > 500000) d = 500000;
  unsigned long long t0 = __builtin_amdgcn_s_memrealtime();
  while (__builtin_amdgcn_s_memrealtime() - t0 < d) __builtin_amdgcn_s_sleep(4);
}
extern "C" void kernel_launch(void* const* d_in, const int* in_sizes, int n_in, void* d_out, int out_size, void* d_ws, size_t ws_size, hipStream_t stream) {
  const u16 *q = (const u16*)d_in[0], *k = (const u16*)d_in[1], *v = (const u16*)d_in[2], *sh = (const u16*)d_in[3], *ssmk = (const u16*)d_in[4], *ssmD = (const u16*)d_in[5];
  const u16 *win = (const u16*)d_in[7], *wsp = (const u16*)d_in[8], *bsp = (const u16*)d_in[9], *wout = (const u16*)d_in[10];
  char* ws = (char*)d_ws;
  u16* xn = (u16*)ws; u16* kvT = (u16*)(ws + (4 << 20)); float* yT = (float*)(ws + (8 << 20)); u16* z = (u16*)(ws + (16 << 20)); u16* z2n = (u16*)(ws + (40 << 20)); u16* h = (u16*)(ws + (52 << 20));
  k_p1<<<256, 256, 0, stream>>>((float*)(ws + (101 << 20)), (unsigned long long*)(ws + (100 << 20)), 2);
  k_p2<<<1, 1, 0, stream>>>((unsigned long long*)(ws + (100 << 20)), 0);
  k_ln<<<S, 256, 0, stream>>>(q, D, D, xn);
  k_kv<<<S * D / 256, 256, 0, stream>>>(k, v, sh, kvT);
  k_conv<<<D, 256, 0, stream>>>(kvT, ssmk, ssmD, yT);
  k_gemm<0><<<dim3(F / 64, S / 64), 256, 0, stream>>>(xn, win, F, D, z, nullptr, nullptr, nullptr);
  k_ln<<<S, 256, 0, stream>>>(z + FH, F, FH, z2n);
  k_gemm<1><<<dim3(FH / 64, S / 64), 256, 0, stream>>>(wsp, z2n, FH, S, h, z, bsp, nullptr);
  k_gemm<2><<<dim3(D / 64, S / 64), 256, 0, stream>>>(h, wout, D, FH, (u16*)d_out, q, xn, yT);
}
```

```cpp
#include <hip/hip_runtime.h>
#include <hip/hip_bf16.h>
typedef unsigned short u16;
typedef short bf16x8 __attribute__((ext_vector_type(8)));
typedef float f32x4 __attribute__((ext_vector_type(4)));
constexpr int S = 2048, D = 576, F = 4608, FH = 2304;
__device__ __forceinline__ float b2f(u16 h) { return __uint_as_float((unsigned)h << 16); }
__device__ __forceinline__ u16 f2b(float f) { unsigned u = __float_as_uint(f); u += 0x7fff + ((u >> 16) & 1); return u >> 16; }

__global__ void k_ln(const u16* in, int ld, int n, u16* out) {
  const int row = blockIdx.x, tid = threadIdx.x; __shared__ float red[2][4];
  float s = 0; for (int i = tid; i < n; i += 256) s += b2f(in[(size_t)row * ld + i]);
  for (int o = 32; o; o >>= 1) s += __shfl_xor(s, o);
  if ((tid & 63) == 0) red[0][tid >> 6] = s; __syncthreads();
  const float m = (red[0][0] + red[0][1] + red[0][2] + red[0][3]) / n;
  float q = 0; for (int i = tid; i < n; i += 256) { float d = b2f(in[(size_t)row * ld + i]) - m; q += d * d; }
  for (int o = 32; o; o >>= 1) q += __shfl_xor(q, o);
  if ((tid & 63) == 0) red[1][tid >> 6] = q; __syncthreads();
  const float r = rsqrtf((red[1][0] + red[1][1] + red[1][2] + red[1][3]) / n + 1e-5f);
  for (int i = tid; i < n; i += 256) out[(size_t)row * n + i] = f2b((b2f(in[(size_t)row * ld + i]) - m) * r);
}
__global__ void k_kv(const u16* k, const u16* v, const u16* sh, u16* kvT) {
  const int i = blockIdx.x * 256 + threadIdx.x; const int t = i / D, d = i % D;
  float a = 0;
  for (int j = 0; j < 4; ++j) if (t - j >= 0) a += b2f(k[(size_t)(t - j) * D + d]) * b2f(sh[d * 4 + j]);
  kvT[(size_t)d * S + t] = f2b(b2f(f2b(a)) * b2f(v[i]));
}
__global__ void k_conv(const u16* kvT, const u16* h, const u16* Dv, float* yT) {
  __shared__ float su[S], shh[S]; const int d = blockIdx.x;
  for (int i = threadIdx.x; i < S; i += 256) { su[i] = b2f(kvT[(size_t)d * S + i]); shh[i] = b2f(h[(size_t)d * S + i]); }
  __syncthreads();
  for (int t = threadIdx.x; t < S; t += 256) { float a = 0; for (int s = 0; s <= t; ++s) a += shh[t - s] * su[s]; yT[(size_t)d * S + t] = a + su[t] * b2f(Dv[d]); }
}
template<int EPI>
__global__ __launch_bounds__(256) void k_gemm(const u16* A, const u16* B, int N, int K, u16* C, const u16* e0, const u16* e1, const float* e2) {
  __shared__ __attribute__((aligned(16))) u16 sA[64][40], sB[64][40];
  const int tid = threadIdx.x, lane = tid & 63, w = tid >> 6, wm = w >> 1, wn = w & 1;
  const int m0 = blockIdx.y * 64, n0 = blockIdx.x * 64;
  f32x4 acc[2][2] = {};
  for (int k0 = 0; k0 < K; k0 += 32) {
    *(uint4*)&sA[tid >> 2][(tid & 3) * 8] = *(const uint4*)&A[(size_t)(m0 + (tid >> 2)) * K + k0 + (tid & 3) * 8];
    { uint4 v = *(const uint4*)&B[(size_t)(k0 + (tid >> 3)) * N + n0 + (tid & 7) * 8]; const u16* p = (const u16*)&v;
      for (int j = 0; j < 8; ++j) sB[(tid & 7) * 8 + j][tid >> 3] = p[j]; }
    __syncthreads();
    bf16x8 a[2], b[2];
    for (int i = 0; i < 2; ++i) { a[i] = *(bf16x8*)&sA[wm * 32 + i * 16 + (lane & 15)][(lane >> 4) * 8]; b[i] = *(bf16x8*)&sB[wn * 32 + i * 16 + (lane & 15)][(lane >> 4) * 8]; }
    for (int i = 0; i < 2; ++i) for (int j = 0; j < 2; ++j) acc[i][j] = __builtin_amdgcn_mfma_f32_16x16x32_bf16(a[i], b[j], acc[i][j], 0, 0, 0);
    __syncthreads();
  }
  for (int i = 0; i < 2; ++i) for (int j = 0; j < 2; ++j) for (int e = 0; e < 4; ++e) {
    const int m = m0 + wm * 32 + i * 16 + (lane >> 4) * 4 + e, n = n0 + wn * 32 + j * 16 + (lane & 15); float x = acc[i][j][e];
    if (EPI == 0) { float u = 0.7978845608f * (x + 0.044715f * x * x * x); x = x / (1.f + __expf(-2.f * u)); }
    if (EPI == 1) x = b2f(e0[(size_t)m * F + n]) * (x + b2f(e1[m]));
    if (EPI == 2) x = b2f(e0[(size_t)m * D + n]) + b2f(e1[(size_t)m * D + n]) * e2[(size_t)n * S + m] + x;
    C[(size_t)m * N + n] = f2b(x);
  }
}

#define F8 "v_fma_f32 %0, %0, %4, %4\n v_fma_f32 %1, %1, %4, %4\n v_fma_f32 %2, %2, %4, %4\n v_fma_f32 %3, %3, %4, %4\n v_fma_f32 %0, %0, %4, %4\n v_fma_f32 %1, %1, %4, %4\n v_fma_f32 %2, %2, %4, %4\n v_fma_f32 %3, %3, %4, %4\n"
#define F64 F8 F8 F8 F8 F8 F8 F8 F8
#define F512 F64 F64 F64 F64 F64 F64 F64 F64
__global__ __launch_bounds__(256) void k_p1(float* out, unsigned long long* st, int passes) {
  float a = threadIdx.x, b = a + 1, c = a + 2, d = a + 3, e = 0.5f;
  {
    unsigned long long pc; asm volatile("s_getpc_b64 %0" : "=s"(pc));
    const char* cp = (const char*)(pc & ~127ull) + threadIdx.x * 128;
    if (threadIdx.x < 136) { unsigned v; asm volatile("global_load_dword %0, %1, off\n s_waitcnt vmcnt(0)" : "=&v"(v) : "v"(cp) : "memory"); }
    unsigned long long r0 = __builtin_amdgcn_s_memrealtime();
    while (__builtin_amdgcn_s_memrealtime() - r0 < 0) __builtin_amdgcn_s_sleep(1);
  }
  for (int p = 0; p < passes; ++p) {
    unsigned long long t0, t1;
    asm volatile("s_memtime %0\n s_waitcnt lgkmcnt(0)" : "=s"(t0) :: "memory");
    asm volatile(F512 F512 F512 F512 : "+v"(a), "+v"(b), "+v"(c), "+v"(d) : "v"(e));
    asm volatile("s_memtime %0\n s_waitcnt lgkmcnt(0)" : "=s"(t1) :: "memory");
    if (threadIdx.x == 0) st[blockIdx.x * 4 + p] = t1 - t0;
  }
  out[blockIdx.x * 256 + threadIdx.x] = a + b + c + d;
}
__global__ void k_p2(const unsigned long long* st, int mode) {
  unsigned long long s = 0, mx = 0;
  for (int i = 0; i < 256; ++i) { long long d = (long long)st[4*i] - (long long)st[4*i+1]; if (d < 0) d = 0; s += d; mx = (unsigned long long)d > mx ? d : mx; }
  unsigned long long d = (mode ? mx : s / 256) * 10; if (d > 500000) d = 500000;
  unsigned long long t0 = __builtin_amdgcn_s_memrealtime();
  while (__builtin_amdgcn_s_memrealtime() - t0 < d) __builtin_amdgcn_s_sleep(4);
}
extern "C" void kernel_launch(void* const* d_in, const int* in_sizes, int n_in, void* d_out, int out_size, void* d_ws, size_t ws_size, hipStream_t stream) {
  const u16 *q = (const u16*)d_in[0], *k = (const u16*)d_in[1], *v = (const u16*)d_in[2], *sh = (const u16*)d_in[3], *ssmk = (const u16*)d_in[4], *ssmD = (const u16*)d_in[5];
  const u16 *win = (const u16*)d_in[7], *wsp = (const u16*)d_in[8], *bsp = (const u16*)d_in[9], *wout = (const u16*)d_in[10];
  char* ws = (char*)d_ws;
  u16* xn = (u16*)ws; u16* kvT = (u16*)(ws + (4 << 20)); float* yT = (float*)(ws + (8 << 20)); u16* z = (u16*)(ws + (16 << 20)); u16* z2n = (u16*)(ws + (40 << 20)); u16* h = (u16*)(ws + (52 << 20));
  k_p1<<<256, 256, 0, stream>>>((float*)(ws + (101 << 20)), (unsigned long long*)(ws + (100 << 20)), 2);
  k_p2<<<1, 1, 0, stream>>>((unsigned long long*)(ws + (100 << 20)), 0);
  k_ln<<<S, 256, 0, stream>>>(q, D, D, xn);
  k_kv<<<S * D / 256, 256, 0, stream>>>(k, v, sh, kvT);
  k_conv<<<D, 256, 0, stream>>>(kvT, ssmk, ssmD, yT);
  k_gemm<0><<<dim3(F / 64, S / 64), 256, 0, stream>>>(xn, win, F, D, z, nullptr, nullptr, nullptr);
  k_ln<<<S, 256, 0, stream>>>(z + FH, F, FH, z2n);
  k_gemm<1><<<dim3(FH / 64, S / 64), 256, 0, stream>>>(wsp, z2n, FH, S, h, z, bsp, nullptr);
  k_gemm<2><<<dim3(D / 64, S / 64), 256, 0, stream>>>(h, wout, D, FH, (u16*)d_out, q, xn, yT);
}
```

```cpp
#include <hip/hip_runtime.h>
#include <hip/hip_bf16.h>
#include <type_traits>

namespace {
typedef unsigned short u16;
typedef unsigned long long u64;
typedef short bf16x8 __attribute__((ext_vector_type(8)));
typedef short bf16x4 __attribute__((ext_vector_type(4)));
typedef __bf16 bf2 __attribute__((ext_vector_type(2)));
typedef float f32x2 __attribute__((ext_vector_type(2)));
typedef float f32x4 __attribute__((ext_vector_type(4)));
typedef unsigned u32x2 __attribute__((ext_vector_type(2)));
typedef unsigned u32x4 __attribute__((ext_vector_type(4)));
typedef __amdgpu_buffer_rsrc_t rsrc_t;
#define LAS __attribute__((address_space(3)))
#define DEV __device__ __forceinline__

constexpr int S = 2048, D = 576, F = 4608, FH = 2304;
constexpr int ASLOT = 16384, BSLOT = 18432, BBASE = 4 * ASLOT;
constexpr int STG = 139264, STG_W = 9984;
constexpr int T_M1 = 159232, T_R1 = T_M1 + 512, T_CS2 = T_R1 + 512, T_CS1 = T_CS2 + 576, T_N2 = T_CS1 + 576, T_R2 = T_N2 + 512, T_TAG = T_R2 + 512;
constexpr int LDS_BYTES = 163840;
constexpr int G3SLOT = 20480;
constexpr int CV = 102400, CV_CH = 13056, CV_O = 4160, CV_U = 8320;
constexpr int CV_PS = 141568, CV_PM23 = 149760;
constexpr int YTILE = 122880;
constexpr int W_EPOCH = 0, W_F2 = 4096, W_F3 = 8192, W_F4 = 12288, W_F5 = 16384;
constexpr int W_G0X = 0x8000, W_G0C = 0x10000, W_G1S = 0x60000;
constexpr int W_KVT = 1 << 20, W_YT = 4 << 20, W_WOF = 7 << 20, W_ZF = 10 << 20, W_HF = 20 << 20, W_END = 30 << 20;
constexpr int SC1 = 16;

#define BAR() do { asm volatile("" ::: "memory"); __builtin_amdgcn_s_barrier(); asm volatile("" ::: "memory"); } while (0)
#define SB() __builtin_amdgcn_sched_barrier(0)
#define SG(m, n) __builtin_amdgcn_sched_group_barrier(m, n, 0)
#define WAITV(n) asm volatile("s_waitcnt vmcnt(%0)" ::"i"(n) : "memory")
#define WAITL(n) asm volatile("s_waitcnt lgkmcnt(%0)" ::"i"(n) : "memory")
#define FENCE() asm volatile("" ::: "memory")
#define MFMA(a, b, c) __builtin_amdgcn_mfma_f32_16x16x32_bf16(a, b, c, 0, 0, 0)
#define STAMP(i) do {} while (0)
#define STAMP_ARG
#define STAMP_PASS

struct Params {
  const u16 *x, *k, *v, *shk, *ssmk, *ssmd, *win, *wsp, *bsp, *wout;
  u16* out; char* ws;
};

DEV rsrc_t mkrs(const void* p, unsigned bytes) { return __builtin_amdgcn_make_buffer_rsrc((void*)p, 0, bytes, 0x00020000); }
template <int AUX> DEV void dma(rsrc_t rs, LAS char* l, unsigned voff, unsigned soff) { __builtin_amdgcn_raw_ptr_buffer_load_lds(rs, (LAS void*)l, 16, voff, soff, 0, AUX); }
template <int AUX> DEV u32x4 ld16(rsrc_t rs, unsigned voff, unsigned soff) { return __builtin_amdgcn_raw_buffer_load_b128(rs, voff, soff, AUX); }
template <int AUX> DEV void st16(u32x4 v, rsrc_t rs, unsigned voff, unsigned soff) { __builtin_amdgcn_raw_buffer_store_b128(v, rs, voff, soff, AUX); }
template <int AUX> DEV void st8(u32x2 v, rsrc_t rs, unsigned voff, unsigned soff) { __builtin_amdgcn_raw_buffer_store_b64(v, rs, voff, soff, AUX); }
template <class T> DEV T lds(LAS char* p) { return *(LAS T*)p; }
DEV bf16x4 ldtr(LAS char* p) { return __builtin_amdgcn_ds_read_tr16_b64_v4i16((LAS bf16x4*)p); }
DEV bf16x8 cat(bf16x4 a, bf16x4 b) { return __builtin_shufflevector(a, b, 0, 1, 2, 3, 4, 5, 6, 7); }
DEV float blo(unsigned u) { return __uint_as_float(u << 16); }
DEV float bhi(unsigned u) { return __uint_as_float(u & 0xffff0000u); }
DEV unsigned pk(float a, float b) { return __builtin_bit_cast(unsigned, __builtin_convertvector((f32x2){a, b}, bf2)); }
DEV float dot2(unsigned a, unsigned b, float c) { return __builtin_amdgcn_fdot2_f32_bf16(__builtin_bit_cast(bf2, a), __builtin_bit_cast(bf2, b), c, false); }
template <int CTRL> DEV float dpp(float x) { return __builtin_bit_cast(float, __builtin_amdgcn_mov_dpp(__builtin_bit_cast(int, x), CTRL, 0xf, 0xf, true)); }
constexpr int XOR1 = 0xB1, XOR2 = 0x4E, XOR7 = 0x141, XOR8 = 0x128;
DEV f32x2 gelu2(f32x2 x) {
  const f32x2 a = x * (x * x * -0.10294324f + -2.30220819f);
  f32x2 e; e.x = __builtin_amdgcn_exp2f(a.x); e.y = __builtin_amdgcn_exp2f(a.y);
  e = e + 1.0f;
  f32x2 r; r.x = __builtin_amdgcn_rcpf(e.x); r.y = __builtin_amdgcn_rcpf(e.y);
  return x * r;
}
DEV f32x4 gelu4(f32x4 x) { const f32x2 a = gelu2(x.xy), b = gelu2(x.zw); return (f32x4){a.x, a.y, b.x, b.y}; }
DEV u32x4 seal(float a, float b, u32x2 tag) { const unsigned ua = __float_as_uint(a), ub = __float_as_uint(b); return (u32x4){ua, ub, ua ^ tag.x, ub ^ tag.y}; }
DEV bool sealed(u32x4 g, u32x2 tag) { return (g.x ^ g.z) == tag.x && (g.y ^ g.w) == tag.y; }
DEV bool expired(u64 t0) { return __builtin_amdgcn_s_memrealtime() - t0 > 2000000ull; }
template <int NL> DEV void wait_flags(rsrc_t ws, unsigned voff, u32x2 tag) {
  const u64 t0 = __builtin_amdgcn_s_memrealtime();
  for (;;) {
    bool ok = true;
#pragma unroll
    for (int i = 0; i < NL; ++i) { const u32x4 f = ld16<SC1>(ws, voff, i * 1024); ok &= f.x == tag.x && f.y == tag.y && f.z == tag.x && f.w == tag.y; }
    if (__all(ok) || expired(t0)) break;
    FENCE();
  }
  FENCE();
}
DEV void set_flag(rsrc_t ws, unsigned off, u32x2 tag, int lane) { if (lane == 0) st8<SC1>(tag, ws, off, 0); }

struct Ids { int b, tp, ft, lane; };

DEV void role_L(const Params& P, LAS char* sm, const Ids id, const int lw STAMP_ARG) {
  const int lane = id.lane, rr = lane >> 3, sl = lane & 7;
  const rsrc_t rx = mkrs(P.x, S * D * 2), rwin = mkrs(P.win, D * F * 2), rwsp = mkrs(P.wsp, S * S * 2), rws = mkrs(P.ws, W_END);
  const unsigned swz = (sl ^ (4 * lw + (rr >> 1))) * 16;
  const unsigned vxa = (16 * lw + 8 * (rr >> 2) + (rr & 3)) * (D * 2) + swz;
  unsigned vb[9];
#pragma unroll
  for (int i = 0; i < 9; ++i) { const int ci = 64 * (2 * i + lw) + lane, rho = ci / 18, ch = ci - rho * 18;
    const int kr = (rho & ~12) | ((rho & 4) << 1) | ((rho & 8) >> 1); vb[i] = kr * (F * 2) + ch * 16; }
  auto issue1 = [&](int n, int ks, bool passA, unsigned vx) {
    LAS char* a = sm + (n & 3) * ASLOT + lw * 1024; LAS char* bb = sm + BBASE + (n & 3) * BSLOT + lw * 1024;
    const unsigned sa = id.tp * 128 * (D * 2) + ks * 128, sb = ks * 64 * (F * 2) + ((passA ? FH : 0) + id.ft * 144) * 2;
#pragma unroll
    for (int i = 0; i < 9; ++i) {
      if (i < 8) dma<0>(rx, a + i * 2048, vx, sa + (passA ? 32 * (i >> 1) + 4 * (i & 1) : 16 * i) * (D * 2));
      dma<0>(rwin, bb + i * 2048, vb[i], sb);
    }
  };
  for (int n = 0; n < 3; ++n) issue1(n, n, true, vxa);
  STAMP(0);
  const unsigned vxb = (8 * lw + rr) * (D * 2) + swz, vw = (8 * lw + rr) * (S * 2) + swz, vl = lane * 16 + lw * 1024;
  auto issue2a = [&](int n) { LAS char* a = sm + (n & 3) * ASLOT + lw * 1024; const unsigned sa = id.tp * 128 * (S * 2) + (n - 18) * 128;
#pragma unroll
    for (int i = 0; i < 8; ++i) dma<0>(rwsp, a + i * 2048, vw, sa + 16 * i * (S * 2)); };
  auto issue2b = [&](int n) { LAS char* bb = sm + BBASE + (n & 3) * BSLOT + lw * 1024; const unsigned sb = W_ZF + (id.ft * 576 + (n - 18) * 18) * 1024;
#pragma unroll
    for (int i = 0; i < 9; ++i) dma<SC1>(rws, bb + i * 2048, vl, sb + i * 2048); };
#pragma unroll 1
  for (int n = 0; n < 6; ++n) { WAITV(34); BAR(); issue1(n + 3, n + 3, true, vxa); }
#pragma unroll 1
  for (int n = 6; n < 15; ++n) { WAITV(34); BAR(); issue1(n + 3, n - 6, false, vxb); }
  WAITV(34); BAR(); issue2a(18);
  WAITV(25); BAR(); issue2a(19);
  WAITV(16); BAR(); issue2a(20);
  { const u32x2 tag = lds<u32x2>(sm + T_TAG); wait_flags<1>(rws, W_F2 + id.ft * 128 + (lane & 7) * 16, tag); }
  STAMP(1);
  issue2b(18); issue2b(19); issue2b(20);
  WAITV(18); BAR(); issue2a(21); issue2b(21);
  WAITV(26); BAR(); issue2a(22); issue2b(22);
#pragma unroll 1
  for (int n = 20; n < 47; ++n) { WAITV(34); BAR(); issue2a(n + 3); issue2b(n + 3); }
  WAITV(34); BAR(); WAITV(17); BAR(); WAITV(0); BAR();
  BAR();
  const int g = id.ft & 7, u = g >> 1;
  auto issue3 = [&](int m, int slot) { LAS char* a = sm + slot * G3SLOT + lw * 1024; const unsigned sb = W_WOF + (u * 72 + 4 * m) * 9 * 1024;
#pragma unroll
    for (int i = 0; i < 10; ++i) { const int pc = 2 * i + lw, kk = pc / 5, tt = pc - kk * 5, tile = (tt == 4) ? 4 : tt + (g & 1) * 5;
      dma<SC1>(rws, a + i * 2048, lane * 16, sb + (kk * 9 + tile) * 1024); } };
  for (int m = 0; m < 5; ++m) issue3(m, m);
  BAR(); BAR();
  WAITV(40); BAR();
  WAITV(30); BAR(); issue3(5, 5); issue3(6, 0);
  int slot = 1;
#pragma unroll 1
  for (int m = 2; m < 13; ++m) { WAITV(40); BAR(); issue3(m + 5, slot); slot = slot == 5 ? 0 : slot + 1; }
  WAITV(40); BAR(); WAITV(30); BAR(); WAITV(20); BAR(); WAITV(10); BAR(); WAITV(0); BAR();
}

DEV void role_X(const Params& P, LAS char* sm, const Ids id, const int xw STAMP_ARG) {
  const int lane = id.lane;
  const rsrc_t rws = mkrs(P.ws, W_END);
  u32x2 tag; u64 epoch;
  { const u32x2 e = __builtin_amdgcn_raw_buffer_load_b64(rws, W_EPOCH, 0, SC1);
    epoch = ((u64)__builtin_amdgcn_readfirstlane(e.y) << 32) | __builtin_amdgcn_readfirstlane(e.x);
    const u64 t = ((epoch + 1) * 0x9E3779B97F4A7C15ull) | 1ull; tag.x = (unsigned)t; tag.y = (unsigned)(t >> 32); }
  if (xw == 0) {
    const rsrc_t rx = mkrs(P.x, S * D * 2);
    const int row = id.tp * 128 + id.ft * 8 + (lane >> 3);
    u32x4 v[9];
#pragma unroll
    for (int i = 0; i < 9; ++i) v[i] = ld16<0>(rx, row * (D * 2) + (lane & 7) * 16, i * 128);
    if (lane == 0) *(LAS u32x2*)(sm + T_TAG) = tag;
    WAITL(0); BAR();
    float s = 0.f, q = 0.f; unsigned ones = 0x3f803f80u; asm volatile("" : "+v"(ones));
#pragma unroll
    for (int i = 0; i < 9; ++i)
#pragma unroll
      for (int e = 0; e < 4; ++e) { s = dot2(v[i][e], ones, s); q = dot2(v[i][e], v[i][e], q); }
    s += dpp<XOR1>(s); q += dpp<XOR1>(q); s += dpp<XOR2>(s); q += dpp<XOR2>(q); s += dpp<XOR7>(s); q += dpp<XOR7>(q);
    const float m = s * (1.0f / D), var = fmaxf(q * (1.0f / D) - m * m, 0.f), r = __builtin_amdgcn_rsqf(var + 1e-5f);
    if ((lane & 7) == 0) st16<SC1>(seal(m, r, tag), rws, W_G0X + row * 16, 0);
    BAR();
  } else {
    const rsrc_t rwin = mkrs(P.win, D * F * 2);
    const int rg = lane / 18, ch = lane - rg * 18;
    const unsigned vo = lane < 54 ? ((id.tp >> 1) * 72 + rg) * (F * 2) + (((id.tp & 1) ? 0 : FH) + id.ft * 144) * 2 + ch * 16 : 0xF0000000u;
    u32x4 v[24];
#pragma unroll
    for (int i = 0; i < 24; ++i) v[i] = ld16<0>(rwin, vo, i * 3 * (F * 2));
    BAR();
    float a[8]; unsigned selo = 0x00003f80u, sehi = 0x3f800000u; asm volatile("" : "+v"(selo), "+v"(sehi));
#pragma unroll
    for (int e = 0; e < 8; ++e) a[e] = 0.f;
#pragma unroll
    for (int i = 0; i < 24; ++i)
#pragma unroll
      for (int e = 0; e < 4; ++e) { a[2 * e] = dot2(v[i][e], selo, a[2 * e]); a[2 * e + 1] = dot2(v[i][e], sehi, a[2 * e + 1]); }
#pragma unroll
    for (int e = 0; e < 8; ++e) a[e] += __shfl(a[e], lane + 18) + __shfl(a[e], lane + 36);
    if (lane < 18) {
#pragma unroll
      for (int e = 0; e < 4; ++e) st16<SC1>(seal(a[2 * e], a[2 * e + 1], tag), rws, W_G0C + ((id.ft * 16 + id.tp) * 72 + ch * 4 + e) * 16, 0);
    }
    BAR();
  }
  STAMP(0);
  BAR(); BAR(); BAR();
  if (xw == 0) {
    const u64 t0 = __builtin_amdgcn_s_memrealtime();
    int round = 0;
    for (;;) {
      u32x4 gx[2], gc[18];
#pragma unroll
      for (int i = 0; i < 2; ++i) gx[i] = ld16<SC1>(rws, W_G0X + (id.tp * 128 + lane) * 16, i * 1024);
#pragma unroll
      for (int i = 0; i < 18; ++i) gc[i] = ld16<SC1>(rws, W_G0C + (id.ft * 1152 + lane) * 16, i * 1024);
      if (round == 0) { FENCE(); BAR(); BAR(); FENCE(); }
      bool ok = sealed(gx[0], tag) && sealed(gx[1], tag);
#pragma unroll
      for (int i = 0; i < 18; ++i) ok &= sealed(gc[i], tag);
      ++round;
      if (__all(ok) || expired(t0)) {
#pragma unroll
        for (int i = 0; i < 2; ++i) { *(LAS unsigned*)(sm + T_M1 + (64 * i + lane) * 4) = gx[i].x; *(LAS unsigned*)(sm + T_R1 + (64 * i + lane) * 4) = gx[i].y; }
#pragma unroll
        for (int i = 0; i < 18; ++i) *(LAS u32x2*)(sm + STG + 4096 + (64 * i + lane) * 8) = gc[i].xy;
        break;
      }
      FENCE();
    }
    WAITL(0);
#pragma unroll
    for (int hz = 0; hz < 2; ++hz)
#pragma unroll
      for (int pass = 0; pass < 2; ++pass) { const int cp = pass * 64 + lane;
        if (cp < 72) { f32x2 a = {0.f, 0.f};
#pragma unroll
          for (int e = 0; e < 8; ++e) a += lds<f32x2>(sm + STG + 4096 + ((2 * e + hz) * 72 + cp) * 8);
          *(LAS f32x2*)(sm + (hz ? T_CS1 : T_CS2) + cp * 8) = a; } }
    WAITL(0); BAR();
  } else { BAR(); BAR(); BAR(); }
  STAMP(1);
  BAR();
  unsigned sink = 0;
  if (xw == 1) {
    auto touch = [&](const void* p, unsigned per, unsigned lines) { const rsrc_t r = mkrs(p, per * 256);
      for (unsigned l0 = 0; l0 < lines; l0 += 64) { const unsigned o = l0 + lane < lines ? id.b * per + (l0 + lane) * 128 : 0xF0000000u;
        asm volatile("buffer_load_dword %0, %1, %2, 0 offen" : "+v"(sink) : "v"(o), "s"(r) : "memory"); } };
    touch(P.wsp, 32768, 256); touch(P.k, 9216, 72); touch(P.v, 9216, 72); touch(P.wout, 10368, 81); touch(P.ssmk, 9216, 72);
  }
  BAR();
  { const int row = 64 * xw + lane;
    const f32x2 a = lds<f32x2>(sm + STG + row * 8) + lds<f32x2>(sm + STG + 1024 + row * 8);
    st16<SC1>(seal(a.x, a.y, tag), rws, W_G1S + ((id.tp * 16 + id.ft) * 128 + row) * 16, 0);
    BAR(); BAR();
    const u64 t0 = __builtin_amdgcn_s_memrealtime();
    int round = 0;
    for (;;) {
      u32x4 gs[16];
#pragma unroll
      for (int i = 0; i < 16; ++i) gs[i] = ld16<SC1>(rws, W_G1S + (id.tp * 16 * 128 + row) * 16, i * 2048);
      if (round == 0) { FENCE(); BAR(); FENCE(); }
      bool ok = true; float s = 0.f, q = 0.f;
#pragma unroll
      for (int i = 0; i < 16; ++i) { ok &= sealed(gs[i], tag); s += __uint_as_float(gs[i].x); q += __uint_as_float(gs[i].y); }
      ++round;
      if (__all(ok) || expired(t0)) {
        const float m = s * (1.0f / FH), var = fmaxf(q * (1.0f / FH) - m * m, 0.f), r = __builtin_amdgcn_rsqf(var + 1e-5f);
        *(LAS float*)(sm + T_R2 + row * 4) = r; *(LAS float*)(sm + T_N2 + row * 4) = -m * r;
        break;
      }
      FENCE();
    }
    asm volatile("s_waitcnt vmcnt(0) lgkmcnt(0)" : "+v"(sink) :: "memory"); BAR();
  }
  STAMP(2);
  BAR(); BAR(); BAR();
  if (xw == 0) set_flag(rws, W_F2 + (id.ft * 16 + id.tp) * 8, tag, lane);
  BAR(); BAR(); BAR();
  LAS char* stg = sm + STG + xw * STG_W;
  { const int tile = xw ? id.b : 256 + id.b - 32; const bool on = xw || (id.b >= 32 && id.b < 64);
    const int tb = tile / 9, db = tile - tb * 9, cc = lane & 7, rg = lane >> 3;
    u32x4 kk[11], vv[8], sh[4];
    if (on) {
      const rsrc_t rk = mkrs(P.k, S * D * 2), rv = mkrs(P.v, S * D * 2), rs = mkrs(P.shk, D * 4 * 2);
      const int t0 = 64 * tb + 8 * rg; const unsigned col = (64 * db + 8 * cc) * 2;
#pragma unroll
      for (int i = 0; i < 11; ++i) kk[i] = ld16<0>(rk, t0 + i - 3 >= 0 ? (unsigned)(t0 + i - 3) * (D * 2) + col : 0xF0000000u, 0);
#pragma unroll
      for (int i = 0; i < 8; ++i) vv[i] = ld16<0>(rv, t0 * (D * 2) + col, i * (D * 2));
#pragma unroll
      for (int i = 0; i < 4; ++i) sh[i] = ld16<0>(rs, col * 4, i * 16);
    }
    FENCE(); BAR(); BAR(); FENCE();
#pragma unroll
    for (int e = 0; e < 8; ++e) {
      unsigned o[4];
#pragma unroll
      for (int hf = 0; hf < 2; ++hf) {
        if (on) {
          float c[4], kf[7], y[4];
#pragma unroll
          for (int j = 0; j < 4; ++j) { const unsigned w = sh[e >> 1][(e & 1) * 2 + (j >> 1)]; c[j] = (j & 1) ? bhi(w) : blo(w); }
#pragma unroll
          for (int i = 0; i < 7; ++i) { const unsigned w = kk[4 * hf + i][e >> 1]; kf[i] = (e & 1) ? bhi(w) : blo(w); }
#pragma unroll
          for (int i = 0; i < 4; ++i) { const unsigned w = vv[4 * hf + i][e >> 1];
            float a = c[3] * kf[i]; asm volatile("" : "+v"(a)); a = fmaf(c[2], kf[i + 1], a); a = fmaf(c[1], kf[i + 2], a); a = fmaf(c[0], kf[i + 3], a);
            y[i] = a * ((e & 1) ? bhi(w) : blo(w)); asm volatile("" : "+v"(y[i])); }
          o[3 - 2 * hf] = pk(y[1], y[0]); o[2 - 2 * hf] = pk(y[3], y[2]);
          if (hf == 1) *(LAS u32x4*)(stg + (8 * cc + e) * 128 + (((7 - rg) ^ cc) * 16)) = (u32x4){o[0], o[1], o[2], o[3]};
        }
        FENCE(); BAR(); FENCE();
      }
    }
    if (on) {
#pragma unroll
      for (int i = 0; i < 8; ++i) { const u32x4 t = lds<u32x4>(stg + (8 * i + rg) * 128 + ((cc ^ i) * 16));
        st16<SC1>(t, rws, W_KVT + (64 * db + rg) * (S * 2) + (S - 64 * (tb + 1)) * 2 + cc * 16, 8 * i * (S * 2)); }
    }
    WAITL(0); BAR();
  }
  { const int unit = xw ? 256 + id.b : id.b; const bool on = !xw || id.b < 32;
    const int ks = unit >> 2, u = unit & 3;
    if (on) {
      const rsrc_t rwo = mkrs(P.wout, FH * D * 2);
#pragma unroll
      for (int i = 0; i < 9; ++i) { const int ci = 64 * i + lane, rho = ci / 18, ch = ci - rho * 18;
        const int f = ks < 64 ? 144 * (ks >> 2) + 32 * (ks & 3) + rho : 144 * (2 * (ks - 64) + (rho >> 4)) + 128 + (rho & 15);
        dma<0>(rwo, stg + i * 1024, f * (D * 2) + u * 288 + ch * 16, 0); }
    }
    FENCE(); BAR(); BAR();
    WAITV(0);
    if (on) {
      const int q = lane >> 4, qq = (lane & 15) >> 2, p = lane & 3;
      LAS char* base = stg + (4 * q + qq) * 288;
#pragma unroll
      for (int T = 0; T < 9; ++T) { const int cb = T < 4 ? 32 * (T >> 1) + 4 * (T & 1) : T == 4 ? 64 : 80 + 32 * ((T - 5) >> 1) + 4 * ((T - 5) & 1);
        LAS char* a = base + cb * 2 + p * (T == 4 ? 8 : 16);
        const bf16x8 fr = cat(ldtr(a), ldtr(a + 16 * 288));
        st16<SC1>(__builtin_bit_cast(u32x4, fr), rws, W_WOF + lane * 16, ((u * 72 + ks) * 9 + T) * 1024); }
    }
    FENCE(); BAR(); BAR();
    WAITV(0); BAR();
    if (xw == 0) set_flag(rws, W_F5 + id.b * 8, tag, lane);
  }
  STAMP(3);
  BAR(); BAR(); BAR(); BAR(); BAR();
  if (xw == 0) wait_flags<2>(rws, W_F5 + lane * 16, tag);
  BAR(); BAR();
  STAMP(4);
  { const rsrc_t rh = mkrs(P.ssmk, D * S * 2);
    const int nch = xw ? 1 : 2;
    for (int c = 0; c < nch; ++c) { const int ci = xw ? 1 : 2 * c, chn = ci == 2 ? 512 + (id.b >> 2) : 2 * id.b + ci;
      LAS char* cb = sm + CV + ci * CV_CH;
#pragma unroll
      for (int i = 0; i < 4; ++i) { dma<0>(rh, cb + 32 + i * 1024, lane * 16, chn * (S * 2) + i * 1024);
        dma<0>(rh, cb + CV_O + 32 + i * 1024, lane * 16 + 2, chn * (S * 2) + i * 1024);
        dma<SC1>(rws, cb + CV_U + i * 1024, lane * 16, W_KVT + chn * (S * 2) + i * 1024); }
      const unsigned h0 = P.ssmk[chn * S];
      if (lane < 8) { *(LAS unsigned*)(cb + lane * 4) = 0u; *(LAS unsigned*)(cb + CV_O + lane * 4) = lane == 7 ? h0 << 16 : 0u; }
      if (lane < 34) *(LAS u32x4*)(cb + CV_U + 4096 + lane * 16) = (u32x4){0u, 0u, 0u, 0u};
    }
    WAITV(0); WAITL(0); BAR();
  }
  BAR();
  if (xw == 0) { set_flag(rws, W_F3 + (id.tp * 16 + id.ft) * 8, tag, lane); wait_flags<1>(rws, W_F3 + id.tp * 128 + (lane & 7) * 16, tag); }
  STAMP(5);
  BAR();
  BAR(); BAR(); BAR();
  if (xw == 0) set_flag(rws, W_F4 + id.b * 8, tag, lane);
  BAR(); BAR(); BAR(); BAR(); BAR(); BAR();
  if (xw == 0) {
    wait_flags<2>(rws, W_F4 + lane * 16, tag);
    const int g = id.ft & 7, d0 = 144 * (g >> 1) + 64 * (g & 1);
#pragma unroll
    for (int i = 0; i < 10; ++i) dma<SC1>(rws, sm + YTILE + i * 1024, W_YT + (d0 + (lane >> 3)) * (S * 2) + (id.tp * 128 + 64 * (id.ft >> 3)) * 2 + (lane & 7) * 16, i * 8 * (S * 2));
    if (id.b == 0 && lane == 0) st8<SC1>((u32x2){(unsigned)(epoch + 1), (unsigned)((epoch + 1) >> 32)}, rws, W_EPOCH, 0);
  }
  STAMP(6);
  BAR(); BAR(); BAR(); BAR(); BAR(); BAR(); BAR();
  WAITV(0); BAR();
}

DEV void role_C(const Params& P, LAS char* sm, const Ids id, const int wid STAMP_ARG) {
  const int lane = id.lane, wt = wid >> 1, wf = wid & 1, c = lane & 15, q = lane >> 4;
  const rsrc_t rws = mkrs(P.ws, W_END);
  __builtin_amdgcn_s_setprio(1);
  f32x4 acc[4][4], accx[2];
  bf16x8 tf[2][4], ff[2][5];
  LAS char *ta[2][2], *fb[2], *fx[2];
#pragma unroll
  for (int s = 0; s < 2; ++s)
#pragma unroll
    for (int h = 0; h < 2; ++h) ta[s][h] = sm + (64 * wt + 32 * (h ^ wf) + c) * 128 + (((q + 4 * s) ^ ((c >> 1) & 7)) * 16);
  { const int o = (16 * (q >> 1) + 4 * (q & 1) + (c >> 2)) * 288 + 8 * (c & 3);
#pragma unroll
    for (int h = 0; h < 2; ++h) { fb[h] = sm + BBASE + h * 2 * BSLOT + o + wf * 128; fx[h] = sm + BBASE + h * 2 * BSLOT + o + 256; } }
  auto rd = [&](auto mode, int b, int slot, int s) {
    constexpr int MODE = decltype(mode)::value;
    if (MODE < 2) {
      const int o = (slot & 1) * BSLOT + 32 * s * 288;
#pragma unroll
      for (int j = 0; j < 4; ++j) ff[b][j] = cat(ldtr(fb[slot >> 1] + o + j * 32), ldtr(fb[slot >> 1] + o + j * 32 + 8 * 288));
    } else {
#pragma unroll
      for (int j = 0; j < 4; ++j) ff[b][j] = lds<bf16x8>(fb[slot >> 1] + (slot & 1) * BSLOT + (s * 9 + j) * 1024);
    }
#pragma unroll
    for (int i = 0; i < 4; ++i) tf[b][i] = lds<bf16x8>(ta[s][i >> 1] + slot * ASLOT + (i & 1) * 2048);
    if (MODE < 2) { const int o = (slot & 1) * BSLOT + 32 * s * 288; ff[b][4] = cat(ldtr(fx[slot >> 1] + o), ldtr(fx[slot >> 1] + o + 8 * 288)); }
    else ff[b][4] = lds<bf16x8>(fx[slot >> 1] + (slot & 1) * BSLOT + s * 9 * 1024);
  };
  auto mm = [&](auto mode, int b, int lo, int hi) {
    constexpr int MODE = decltype(mode)::value;
#pragma unroll
    for (int n = 0; n < 18; ++n) if (n >= lo && n < hi) {
      if (n < 16) { const int i = n >> 2, j = (i & 1) ? 3 - (n & 3) : (n & 3);
        acc[i][j] = MODE == 0 ? MFMA(tf[b][i], ff[b][j], acc[i][j]) : MFMA(ff[b][j], tf[b][i], acc[i][j]); }
      else accx[n - 16] = MODE == 0 ? MFMA(tf[b][n - 16], ff[b][4], accx[n - 16]) : MFMA(ff[b][4], tf[b][n - 16], accx[n - 16]);
    }
  };
  auto stage = [&](auto mode, int slot, bool drain) {
    constexpr int MODE = decltype(mode)::value;
    mm(mode, 1, 0, 4); SB();
    if (drain) WAITV(0);
    WAITL(0); BAR(); SB();
    rd(mode, 0, slot, 0); mm(mode, 1, 4, 18);
    if (MODE < 2) { for (int i = 0; i < 14; ++i) { SG(0x100, 1); SG(0x8, 1); } }
    else { for (int i = 0; i < 4; ++i) { SG(0x100, 1); SG(0x8, 1); SG(0x100, 1); SG(0x8, 2); } SG(0x100, 1); SG(0x8, 2); }
    SB();
    rd(mode, 1, slot, 1); mm(mode, 0, 0, 18);
    if (MODE < 2) { for (int i = 0; i < 4; ++i) { SG(0x100, 1); SG(0x8, 1); SG(0x100, 1); SG(0x8, 1); SG(0x100, 1); SG(0x8, 2); } SG(0x100, 1); SG(0x8, 1); SG(0x100, 1); SG(0x8, 1); }
    else { for (int i = 0; i < 9; ++i) { SG(0x100, 1); SG(0x8, 2); } }
    SB();
  };
  auto first = [&](auto mode, int slot) { WAITL(0); BAR(); SB(); rd(mode, 0, slot, 0); rd(mode, 1, slot, 1); mm(mode, 0, 0, 18); SB(); };
  auto zero = [&]() {
#pragma unroll
    for (int i = 0; i < 4; ++i)
#pragma unroll
      for (int j = 0; j < 4; ++j) acc[i][j] = (f32x4){0.f, 0.f, 0.f, 0.f};
    accx[0] = accx[1] = (f32x4){0.f, 0.f, 0.f, 0.f}; };
  const std::integral_constant<int, 0> M0; const std::integral_constant<int, 1> M1; const std::integral_constant<int, 2> M2;

  zero();
  first(M0, 0);
  STAMP(0);
#pragma unroll 1
  for (int it = 0; it < 2; ++it) { stage(M0, 1, false); stage(M0, 2, false); stage(M0, 3, false); stage(M0, 0, false); }
  mm(M0, 1, 0, 18); SB();
  STAMP(1);
  u32x4 z2p[2][4], z2x;
  { float cs[5];
#pragma unroll
    for (int j = 0; j < 5; ++j) cs[j] = lds<float>(sm + T_CS2 + ((j < 4 ? 4 * wf + j : 8) * 16 + c) * 4);
    float red[2][16];
#pragma unroll
    for (int i = 0; i < 4; ++i) {
      const int ro = (64 * wt + 32 * ((i >> 1) ^ wf) + 8 * q + 4 * (i & 1)) * 4;
      const f32x4 m = lds<f32x4>(sm + T_M1 + ro), r = lds<f32x4>(sm + T_R1 + ro);
      f32x4 su = {0.f, 0.f, 0.f, 0.f}, sq = {0.f, 0.f, 0.f, 0.f};
#pragma unroll
      for (int j = 0; j < (i < 2 ? 5 : 4); ++j) {
        const f32x4 z = gelu4(((j < 4 ? acc[i][j] : accx[i]) - m * cs[j]) * r);
        su += z; sq += z * z;
        u32x4& dst = j < 4 ? z2p[i >> 1][j] : z2x;
        dst[2 * (i & 1)] = pk(z[0], z[1]); dst[2 * (i & 1) + 1] = pk(z[2], z[3]);
      }
#pragma unroll
      for (int e = 0; e < 4; ++e) { red[0][4 * i + e] = su[e]; red[1][4 * i + e] = sq[e]; }
    }
    float r8[2][8], r4[2][4];
#pragma unroll
    for (int st = 0; st < 2; ++st) {
#pragma unroll
      for (int e = 0; e < 8; ++e) { const float a = red[st][e] + dpp<XOR8>(red[st][e]), b = red[st][8 + e] + dpp<XOR8>(red[st][8 + e]); r8[st][e] = (c & 8) ? b : a; }
#pragma unroll
      for (int e = 0; e < 4; ++e) { const float a = r8[st][e] + dpp<XOR7>(r8[st][e]), b = r8[st][4 + e] + dpp<XOR7>(r8[st][4 + e]); float t = (c & 4) ? b : a;
        t += dpp<XOR1>(t); t += dpp<XOR2>(t); r4[st][e] = t; }
    }
    if ((c & 3) == 0) { LAS char* pp = sm + STG + wf * 1024 + (64 * wt + 32 * ((c >> 3) ^ wf) + 8 * q + 4 * ((c >> 2) & 1)) * 8;
      *(LAS f32x4*)pp = (f32x4){r4[0][0], r4[1][0], r4[0][1], r4[1][1]}; *(LAS f32x4*)(pp + 16) = (f32x4){r4[0][2], r4[1][2], r4[0][3], r4[1][3]}; }
#pragma unroll
    for (int i = 0; i < 2; ++i) asm volatile("" : "+v"(z2p[i][0]), "+v"(z2p[i][1]), "+v"(z2p[i][2]), "+v"(z2p[i][3]));
    asm volatile("" : "+v"(z2x));
  }
  STAMP(2);
  zero();
  first(M1, 1);
  stage(M1, 2, false); stage(M1, 3, false); stage(M1, 0, false); stage(M1, 1, false);
  STAMP(3);
  {
#pragma unroll
    for (int i = 0; i < 2; ++i) asm volatile("" : "+v"(z2p[i][0]), "+v"(z2p[i][1]), "+v"(z2p[i][2]), "+v"(z2p[i][3]));
    asm volatile("" : "+v"(z2x));
#pragma unroll
    for (int ip = 0; ip < 2; ++ip) {
      const int ro = (64 * wt + 32 * (ip ^ wf) + 8 * q) * 4;
      const f32x4 r0 = lds<f32x4>(sm + T_R2 + ro), r1 = lds<f32x4>(sm + T_R2 + ro + 16), n0 = lds<f32x4>(sm + T_N2 + ro), n1 = lds<f32x4>(sm + T_N2 + ro + 16);
      const unsigned so = W_ZF + ((id.ft * 64 + 4 * id.tp + 2 * wt + (ip ^ wf)) * 9) * 1024;
#pragma unroll
      for (int j = 0; j < (ip == 0 ? 5 : 4); ++j) { const u32x4 z = j < 4 ? z2p[ip][j] : z2x;
        const f32x4 a = (f32x4){blo(z[0]), bhi(z[0]), blo(z[1]), bhi(z[1])} * r0 + n0, b = (f32x4){blo(z[2]), bhi(z[2]), blo(z[3]), bhi(z[3])} * r1 + n1;
        st16<SC1>((u32x4){pk(a[0], a[1]), pk(a[2], a[3]), pk(b[0], b[1]), pk(b[2], b[3])}, rws, lane * 16, so + (j < 4 ? 4 * wf + j : 8) * 1024); }
    }
    SB();
  }
  unsigned braw[4];
  { const rsrc_t rb = mkrs(P.bsp, S * 2);
#pragma unroll
    for (int i = 0; i < 4; ++i) braw[i] = __builtin_amdgcn_raw_buffer_load_b16(rb, (id.tp * 128 + 64 * wt + 16 * (i ^ (2 * wf)) + c) * 2, 0, 0); }
  STAMP(4);
  stage(M1, 2, false); stage(M1, 3, false); stage(M1, 0, true); stage(M1, 1, false);
  mm(M1, 1, 0, 18); SB();
  STAMP(5);
  u32x2 z1p[4][4], z1x[2];
  { float bias[4];
#pragma unroll
    for (int i = 0; i < 4; ++i) bias[i] = blo(braw[i]);
    f32x4 cs[5];
#pragma unroll
    for (int j = 0; j < 5; ++j) cs[j] = lds<f32x4>(sm + T_CS1 + ((j < 4 ? 4 * wf + j : 8) * 16 + 4 * q) * 4);
#pragma unroll
    for (int i = 0; i < 4; ++i) { const int ro = (64 * wt + 16 * (i ^ (2 * wf)) + c) * 4;
      const float m = lds<float>(sm + T_M1 + ro), r = lds<float>(sm + T_R1 + ro);
#pragma unroll
      for (int j = 0; j < (i < 2 ? 5 : 4); ++j) {
        f32x4& a = j < 4 ? acc[i][j] : accx[i];
        const f32x4 z = gelu4((a - m * cs[j]) * r);
        (j < 4 ? z1p[i][j] : z1x[i]) = (u32x2){pk(z[0], z[1]), pk(z[2], z[3])};
        a = (f32x4){bias[i], bias[i], bias[i], bias[i]};
      } }
#pragma unroll
    for (int h = 0; h < 2; ++h) { fb[h] = sm + BBASE + h * 2 * BSLOT + lane * 16 + wf * 4096; fx[h] = sm + BBASE + h * 2 * BSLOT + lane * 16 + 8192; }
#pragma unroll
    for (int i = 0; i < 4; ++i) asm volatile("" : "+v"(z1p[i][0]), "+v"(z1p[i][1]), "+v"(z1p[i][2]), "+v"(z1p[i][3]), "+v"(acc[i][0]), "+v"(acc[i][1]), "+v"(acc[i][2]), "+v"(acc[i][3]));
    asm volatile("" : "+v"(z1x[0]), "+v"(z1x[1]), "+v"(accx[0]), "+v"(accx[1]));
  }
  STAMP(6);
  first(M2, 2);
  STAMP(7);
#pragma unroll 1
  for (int it = 0; it < 7; ++it) { stage(M2, 3, false); stage(M2, 0, false); stage(M2, 1, false); stage(M2, 2, false); }
  stage(M2, 3, false); stage(M2, 0, false); stage(M2, 1, false);
  mm(M2, 1, 0, 4); SB(); WAITL(0); BAR(); SB();
  mm(M2, 1, 4, 18); SB();
  STAMP(8);
#pragma unroll
  for (int i = 0; i < 4; ++i) asm volatile("" : "+v"(z1p[i][0]), "+v"(z1p[i][1]), "+v"(z1p[i][2]), "+v"(z1p[i][3]));
  asm volatile("" : "+v"(z1x[0]), "+v"(z1x[1]));
#pragma unroll
  for (int i = 0; i < 4; ++i) {
    const unsigned so = W_HF + (id.tp * 8 + 4 * wt + (i ^ (2 * wf))) * 72 * 1024;
    auto gate = [&](u32x2 z, f32x4 a) { const f32x4 h = (f32x4){blo(z.x), bhi(z.x), blo(z.y), bhi(z.y)} * a; return (u32x2){pk(h[0], h[1]), pk(h[2], h[3])}; };
#pragma unroll
    for (int pp = 0; pp < 2; ++pp) { const u32x2 lo = gate(z1p[i][2 * pp], acc[i][2 * pp]), hi = gate(z1p[i][2 * pp + 1], acc[i][2 * pp + 1]);
      st16<SC1>((u32x4){lo.x, lo.y, hi.x, hi.y}, rws, lane * 16, so + (4 * id.ft + 2 * wf + pp) * 1024); }
    if (i < 2) st8<SC1>(gate(z1x[i], accx[i]), rws, lane * 16 + (id.ft & 1) * 8, so + (64 + (id.ft >> 1)) * 1024);
  }
  SB(); BAR(); SB();
  STAMP(9);
  f32x4 cm[8];
  { const unsigned dm = ((const unsigned*)P.ssmd)[id.b], dsh = ((const unsigned*)P.ssmd)[256 + (id.b >> 3)];
    const float dvm = wt ? bhi(dm) : blo(dm), dvs = (id.b & 4) ? bhi(dsh) : blo(dsh);
    const int aoff = ((c & 1) ? 0 : CV_O) + (4 * q + ((c + 1) >> 1)) * 4, boff = CV_U + (2032 - 16 * c + 8 * q) * 2;
    auto afrag = [&](LAS char* p) { const unsigned d0 = lds<unsigned>(p), d1 = lds<unsigned>(p + 4), d2 = lds<unsigned>(p + 8), d3 = lds<unsigned>(p + 12);
      return __builtin_bit_cast(bf16x8, (u32x4){d0, d1, d2, d3}); };
    { const int jq = id.b & 3; LAS char* cb = sm + CV + 2 * CV_CH;
      f32x4 s0 = {0.f, 0.f, 0.f, 0.f}, s1 = s0;
#pragma unroll
      for (int kk = 0; kk < 2; ++kk) { const int k0 = 2 * wid + kk;
#pragma unroll
        for (int e = 0; e < 8; ++e) { const bf16x8 a = afrag(cb + aoff + (k0 + 8 * e) * 64);
          const int o1 = 7 - jq - e >= 0 ? 64 * k0 - 512 * (7 - jq - e) : 512;
          s1 = MFMA(a, lds<bf16x8>(cb + boff + o1), s1);
          if (e < 4) { const int o0 = jq - e >= 0 ? 64 * k0 - 512 * (jq - e) : 512; s0 = MFMA(a, lds<bf16x8>(cb + boff + o0), s0); } } }
      *(LAS f32x4*)(sm + CV_PS + (wid * 2) * 1024 + lane * 16) = s0; *(LAS f32x4*)(sm + CV_PS + (wid * 2 + 1) * 1024 + lane * 16) = s1; }
    LAS char* cb = sm + CV + wt * CV_CH;
#pragma unroll
    for (int j = 0; j < 8; ++j) cm[j] = (f32x4){0.f, 0.f, 0.f, 0.f};
    auto block = [&](int k0) { bf16x8 a[8], bb[8];
#pragma unroll
      for (int e = 0; e < 8; ++e) { bb[e] = lds<bf16x8>(cb + boff + 64 * k0 - 512 * e); a[e] = afrag(cb + aoff + (k0 + 8 * e) * 64); }
#pragma unroll
      for (int e = 0; e < 8; ++e)
#pragma unroll
        for (int j = e; j < 8; ++j) cm[j] = MFMA(a[e], bb[j - e], cm[j]); };
    block(4 * wf);
    SB(); WAITV(0); WAITL(0); BAR(); SB();
    STAMP(10);
    f32x4 ys = {0.f, 0.f, 0.f, 0.f};
    if (wid < 2) {
#pragma unroll
      for (int w = 0; w < 4; ++w) ys += lds<f32x4>(sm + CV_PS + (w * 2 + wid) * 1024 + lane * 16);
    }
#pragma unroll 1
    for (int kk = 1; kk < 4; ++kk) block(4 * wf + kk);
    LAS char* pm = sm + (wt ? CV_PM23 : CV + 2 * CV_CH);
#pragma unroll
    for (int j = 0; j < 4; ++j) { f32x4 lo = cm[j], hi = cm[4 + j]; asm volatile("" : "+v"(lo), "+v"(hi));
      *(LAS f32x4*)(pm + wf * 4096 + j * 1024 + lane * 16) = wf ? lo : hi; cm[j] = wf ? hi : lo; }
    SB(); WAITL(0); BAR(); SB();
    STAMP(11);
    const int half = id.ft >> 3;
    const unsigned hso = W_HF + (id.tp * 8 + 4 * half + wid) * 72 * 1024;
    u32x4 hb[12];
#pragma unroll
    for (int i = 0; i < 12; ++i) hb[i] = ld16<SC1>(rws, lane * 16, hso + i * 1024);
    auto yout = [&](f32x4 a, int chn, LAS char* ub, int j, float dv) {
      const u32x2 u = lds<u32x2>(ub + CV_U + (2044 - 256 * j - 16 * c - 4 * q) * 2);
      const f32x4 y = a + (f32x4){bhi(u.y), blo(u.y), bhi(u.x), blo(u.x)} * dv;
      st8<SC1>((u32x2){pk(y[0], y[1]), pk(y[2], y[3])}, rws, (16 * c + 4 * q) * 2, W_YT + chn * (S * 2) + j * 512); };
#pragma unroll
    for (int j = 0; j < 4; ++j) yout(cm[j] + lds<f32x4>(pm + (wf ^ 1) * 4096 + j * 1024 + lane * 16), 2 * id.b + wt, cb, 4 * wf + j, dvm);
    if (wid < 2) yout(ys, 512 + (id.b >> 2), sm + CV + 2 * CV_CH, wid ? 7 - (id.b & 3) : (id.b & 3), dvs);
    const int g = id.ft & 7, dp = 144 * (g >> 1) + 80 * (g & 1), ds = 144 * (g >> 1) + 64, trow = 64 * half + 16 * wid + c;
    const rsrc_t rx = mkrs(P.x, S * D * 2);
    const unsigned xo = (id.tp * 128 + trow) * (D * 2);
    const u32x4 x0 = ld16<0>(rx, xo + q * 16, dp * 2), x1 = ld16<0>(rx, xo + q * 16, dp * 2 + 64);
    const u32x2 x2 = __builtin_amdgcn_raw_buffer_load_b64(rx, xo + q * 8, ds * 2, 0);
    SB();
    f32x4 o[5];
#pragma unroll
    for (int t = 0; t < 5; ++t) o[t] = (f32x4){0.f, 0.f, 0.f, 0.f};
    LAS char* wb[2] = {sm + lane * 16, sm + 3 * G3SLOT + lane * 16};
    bf16x8 wq[2][5];
    auto rdw = [&](int b, int slot, int kk) {
#pragma unroll
      for (int t = 0; t < 5; ++t) wq[b][t] = lds<bf16x8>(wb[slot / 3] + (slot % 3) * G3SLOT + (kk * 5 + t) * 1024); };
    rdw(0, 0, 0);
#pragma unroll 1
    for (int it = 0; it < 3; ++it) {
#pragma unroll
      for (int sl = 0; sl < 6; ++sl)
#pragma unroll
        for (int kk = 0; kk < 4; ++kk) { const int n = sl * 4 + kk;
          if (kk == 3) { if (sl == 5 && it == 2) {} else { SB(); if (sl == 2 && it == 0) WAITV(12); WAITL(0); BAR(); SB(); rdw((n + 1) & 1, sl == 5 ? 0 : sl + 1, 0); } }
          else rdw((n + 1) & 1, sl, kk + 1);
          const bf16x8 hf = __builtin_bit_cast(bf16x8, hb[n % 12]);
#pragma unroll
          for (int t = 0; t < 5; ++t) o[t] = MFMA(wq[n & 1][t], hf, o[t]);
          hb[n % 12] = ld16<SC1>(rws, lane * 16, it * 24 + n + 12 < 72 ? hso + (it * 24 + n + 12) * 1024 : 0xF0000000u);
          SB();
        }
    }
    STAMP(12);
    const float m = lds<float>(sm + T_M1 + trow * 4), r = lds<float>(sm + T_R1 + trow * 4);
    LAS char* yb = sm + YTILE + (c >> 2) * 128 + (16 * wid + 4 * (c & 3)) * 2;
    const rsrc_t ro = mkrs(P.out, S * D * 2);
    auto fin = [&](u32x2 xv, bf16x4 yv, f32x4 a) { const u32x2 yu = __builtin_bit_cast(u32x2, yv);
      const f32x4 xf = {blo(xv.x), bhi(xv.x), blo(xv.y), bhi(xv.y)}, yf = {blo(yu.x), bhi(yu.x), blo(yu.y), bhi(yu.y)};
      const f32x4 t = xf + (xf - m) * r * yf + a; return (u32x2){pk(t[0], t[1]), pk(t[2], t[3])}; };
    const int yp = 16 * (g & 1), ysh = 64 * ((g & 1) ^ 1);
#pragma unroll
    for (int pp = 0; pp < 2; ++pp) { const u32x4 xv = pp ? x1 : x0;
      const u32x2 lo = fin(xv.xy, ldtr(yb + (yp + 32 * pp + 8 * q) * 128), o[2 * pp]), hi = fin(xv.zw, ldtr(yb + (yp + 32 * pp + 8 * q + 4) * 128), o[2 * pp + 1]);
      st16<0>((u32x4){lo.x, lo.y, hi.x, hi.y}, ro, xo + q * 16, dp * 2 + 64 * pp); }
    const u32x2 ls = fin(x2, ldtr(yb + (ysh + 4 * q) * 128), o[4]);
    if (wt == (g & 1)) st8<0>(ls, ro, xo + q * 8, ds * 2);
    STAMP(13);
  }
}
}

extern "C" __global__ __launch_bounds__(512, 2) void h3_gmlp_fused(const Params P) {
  extern __shared__ __attribute__((aligned(16))) char smem_[];
  LAS char* sm = (LAS char*)smem_;
  const int wid = __builtin_amdgcn_readfirstlane(threadIdx.x >> 6);
  Ids id; id.b = blockIdx.x; id.lane = threadIdx.x & 63;
  const int xcd = id.b & 7, loc = id.b >> 3;
  id.tp = (xcd >> 1) * 4 + (loc >> 3); id.ft = (xcd & 1) * 8 + (loc & 7);
  if (wid == 0) {
    unsigned lo, hi, bytes, sink;
    asm volatile("s_getpc_b64 vcc\n1:\n\ts_sub_u32 %0, vcc_lo, 1b-h3_gmlp_fused\n\ts_subb_u32 %1, vcc_hi, 0\n\ts_mov_b32 %2, h3_gmlp_end-h3_gmlp_fused" : "=s"(lo), "=s"(hi), "=s"(bytes) :: "vcc", "scc");
    const rsrc_t rc = mkrs((const void*)(((u64)hi << 32) | lo), bytes);
    asm volatile("buffer_load_dword %0, %1, %2, 0 offen\n\ts_waitcnt vmcnt(0)" : "=&v"(sink) : "v"((id.lane < 16 ? id.lane * 32 + loc : 0x100000) * 128), "s"(rc) : "memory");
  }
  if (wid == 4 || wid == 5) role_L(P, sm, id, wid - 4 STAMP_PASS);
  else if (wid >= 6) role_X(P, sm, id, wid - 6 STAMP_PASS);
  else role_C(P, sm, id, wid STAMP_PASS);
}
extern "C" __global__ void h3_gmlp_end() {}

extern "C" void kernel_launch(void* const* d_in, const int* in_sizes, int n_in, void* d_out, int out_size, void* d_ws, size_t ws_size, hipStream_t stream) {
  Params p;
  p.x = (const u16*)d_in[0]; p.k = (const u16*)d_in[1]; p.v = (const u16*)d_in[2]; p.shk = (const u16*)d_in[3]; p.ssmk = (const u16*)d_in[4]; p.ssmd = (const u16*)d_in[5];
  p.win = (const u16*)d_in[7]; p.wsp = (const u16*)d_in[8]; p.bsp = (const u16*)d_in[9]; p.wout = (const u16*)d_in[10];
  p.out = (u16*)d_out; p.ws = (char*)d_ws;
  (void)hipFuncSetAttribute((const void*)h3_gmlp_fused, hipFuncAttributeMaxDynamicSharedMemorySize, LDS_BYTES);
  h3_gmlp_fused<<<256, 512, LDS_BYTES, stream>>>(p);
}
```

```cpp
#define MK_STAMP
#define MK_AMP 0
#include <hip/hip_runtime.h>
#include <hip/hip_bf16.h>
#include <type_traits>

namespace {
typedef unsigned short u16;
typedef unsigned long long u64;
typedef short bf16x8 __attribute__((ext_vector_type(8)));
typedef short bf16x4 __attribute__((ext_vector_type(4)));
typedef __bf16 bf2 __attribute__((ext_vector_type(2)));
typedef float f32x2 __attribute__((ext_vector_type(2)));
typedef float f32x4 __attribute__((ext_vector_type(4)));
typedef unsigned u32x2 __attribute__((ext_vector_type(2)));
typedef unsigned u32x4 __attribute__((ext_vector_type(4)));
typedef __amdgpu_buffer_rsrc_t rsrc_t;
#define LAS __attribute__((address_space(3)))
#define DEV __device__ __forceinline__

constexpr int S = 2048, D = 576, F = 4608, FH = 2304;
constexpr int ASLOT = 16384, BSLOT = 18432, BBASE = 4 * ASLOT;
constexpr int STG = 139264, STG_W = 9984;
constexpr int T_M1 = 159232, T_R1 = T_M1 + 512, T_CS2 = T_R1 + 512, T_CS1 = T_CS2 + 576, T_N2 = T_CS1 + 576, T_R2 = T_N2 + 512, T_TAG = T_R2 + 512;
constexpr int LDS_BYTES = 163840;
constexpr int G3SLOT = 20480;
constexpr int CV = 102400, CV_CH = 13056, CV_O = 4160, CV_U = 8320;
constexpr int CV_PS = 141568, CV_PM23 = 149760;
constexpr int YTILE = 122880;
constexpr int W_EPOCH = 0, W_F2 = 4096, W_F3 = 8192, W_F4 = 12288, W_F5 = 16384;
constexpr int W_G0X = 0x8000, W_G0C = 0x10000, W_G1S = 0x60000;
constexpr int W_KVT = 1 << 20, W_YT = 4 << 20, W_WOF = 7 << 20, W_ZF = 10 << 20, W_HF = 20 << 20, W_END = 30 << 20;
constexpr int SC1 = 16;

#define BAR() do { asm volatile("" ::: "memory"); __builtin_amdgcn_s_barrier(); asm volatile("" ::: "memory"); } while (0)
#define SB() __builtin_amdgcn_sched_barrier(0)
#define SG(m, n) __builtin_amdgcn_sched_group_barrier(m, n, 0)
#define WAITV(n) asm volatile("s_waitcnt vmcnt(%0)" ::"i"(n) : "memory")
#define WAITL(n) asm volatile("s_waitcnt lgkmcnt(%0)" ::"i"(n) : "memory")
#define FENCE() asm volatile("" ::: "memory")
#define MFMA(a, b, c) __builtin_amdgcn_mfma_f32_16x16x32_bf16(a, b, c, 0, 0, 0)
#define STAMP(i) do { const unsigned t_ = (unsigned)__builtin_amdgcn_s_memrealtime(); stampv = lane == (i) ? t_ : stampv; asm volatile("" : "+v"(stampv)); } while (0)
#define LBAR() do { BAR(); const int bi_ = bcount - (lw ? 0 : 40); ++bcount; if (bi_ >= 0 && bi_ < 60) STAMP(bi_); } while (0)
#define STAMP_ARG , unsigned& stampv
#define STAMP_PASS , stampv

struct Params {
  const u16 *x, *k, *v, *shk, *ssmk, *ssmd, *win, *wsp, *bsp, *wout;
  u16* out; char* ws;
};

DEV rsrc_t mkrs(const void* p, unsigned bytes) { return __builtin_amdgcn_make_buffer_rsrc((void*)p, 0, bytes, 0x00020000); }
template <int AUX> DEV void dma(rsrc_t rs, LAS char* l, unsigned voff, unsigned soff) { __builtin_amdgcn_raw_ptr_buffer_load_lds(rs, (LAS void*)l, 16, voff, soff, 0, AUX); }
template <int AUX> DEV u32x4 ld16(rsrc_t rs, unsigned voff, unsigned soff) { return __builtin_amdgcn_raw_buffer_load_b128(rs, voff, soff, AUX); }
template <int AUX> DEV void st16(u32x4 v, rsrc_t rs, unsigned voff, unsigned soff) { __builtin_amdgcn_raw_buffer_store_b128(v, rs, voff, soff, AUX); }
template <int AUX> DEV void st8(u32x2 v, rsrc_t rs, unsigned voff, unsigned soff) { __builtin_amdgcn_raw_buffer_store_b64(v, rs, voff, soff, AUX); }
template <class T> DEV T lds(LAS char* p) { return *(LAS T*)p; }
DEV bf16x4 ldtr(LAS char* p) { return __builtin_amdgcn_ds_read_tr16_b64_v4i16((LAS bf16x4*)p); }
DEV bf16x8 cat(bf16x4 a, bf16x4 b) { return __builtin_shufflevector(a, b, 0, 1, 2, 3, 4, 5, 6, 7); }
DEV float blo(unsigned u) { return __uint_as_float(u << 16); }
DEV float bhi(unsigned u) { return __uint_as_float(u & 0xffff0000u); }
DEV unsigned pk(float a, float b) { return __builtin_bit_cast(unsigned, __builtin_convertvector((f32x2){a, b}, bf2)); }
DEV float dot2(unsigned a, unsigned b, float c) { return __builtin_amdgcn_fdot2_f32_bf16(__builtin_bit_cast(bf2, a), __builtin_bit_cast(bf2, b), c, false); }
template <int CTRL> DEV float dpp(float x) { return __builtin_bit_cast(float, __builtin_amdgcn_mov_dpp(__builtin_bit_cast(int, x), CTRL, 0xf, 0xf, true)); }
constexpr int XOR1 = 0xB1, XOR2 = 0x4E, XOR7 = 0x141, XOR8 = 0x128;
DEV f32x2 gelu2(f32x2 x) {
  const f32x2 a = x * (x * x * -0.10294324f + -2.30220819f);
  f32x2 e; e.x = __builtin_amdgcn_exp2f(a.x); e.y = __builtin_amdgcn_exp2f(a.y);
  e = e + 1.0f;
  f32x2 r; r.x = __builtin_amdgcn_rcpf(e.x); r.y = __builtin_amdgcn_rcpf(e.y);
  return x * r;
}
DEV f32x4 gelu4(f32x4 x) { const f32x2 a = gelu2(x.xy), b = gelu2(x.zw); return (f32x4){a.x, a.y, b.x, b.y}; }
DEV u32x4 seal(float a, float b, u32x2 tag) { const unsigned ua = __float_as_uint(a), ub = __float_as_uint(b); return (u32x4){ua, ub, ua ^ tag.x, ub ^ tag.y}; }
DEV bool sealed(u32x4 g, u32x2 tag) { return (g.x ^ g.z) == tag.x && (g.y ^ g.w) == tag.y; }
DEV bool expired(u64 t0) { return __builtin_amdgcn_s_memrealtime() - t0 > 2000000ull; }
template <int NL> DEV void wait_flags(rsrc_t ws, unsigned voff, u32x2 tag) {
  const u64 t0 = __builtin_amdgcn_s_memrealtime();
  for (;;) {
    bool ok = true;
#pragma unroll
    for (int i = 0; i < NL; ++i) { const u32x4 f = ld16<SC1>(ws, voff, i * 1024); ok &= f.x == tag.x && f.y == tag.y && f.z == tag.x && f.w == tag.y; }
    if (__all(ok) || expired(t0)) break;
    FENCE();
  }
  FENCE();
}
DEV void set_flag(rsrc_t ws, unsigned off, u32x2 tag, int lane) { if (lane == 0) st8<SC1>(tag, ws, off, 0); }

struct Ids { int b, tp, ft, lane; };

DEV void role_L(const Params& P, LAS char* sm, const Ids id, const int lw STAMP_ARG) {
  int bcount = 0; (void)bcount;
  const int lane = id.lane, rr = lane >> 3, sl = lane & 7;
  const rsrc_t rx = mkrs(P.x, S * D * 2), rwin = mkrs(P.win, D * F * 2), rwsp = mkrs(P.wsp, S * S * 2), rws = mkrs(P.ws, W_END);
  const unsigned swz = (sl ^ (4 * lw + (rr >> 1))) * 16;
  const unsigned vxa = (16 * lw + 8 * (rr >> 2) + (rr & 3)) * (D * 2) + swz;
  unsigned vb[9];
#pragma unroll
  for (int i = 0; i < 9; ++i) { const int ci = 64 * (2 * i + lw) + lane, rho = ci / 18, ch = ci - rho * 18;
    const int kr = (rho & ~12) | ((rho & 4) << 1) | ((rho & 8) >> 1); vb[i] = kr * (F * 2) + ch * 16; }
  auto issue1 = [&](int n, int ks, bool passA, unsigned vx) {
    LAS char* a = sm + (n & 3) * ASLOT + lw * 1024; LAS char* bb = sm + BBASE + (n & 3) * BSLOT + lw * 1024;
    const unsigned sa = id.tp * 128 * (D * 2) + ks * 128, sb = ks * 64 * (F * 2) + ((passA ? FH : 0) + id.ft * 144) * 2;
#pragma unroll
    for (int i = 0; i < 9; ++i) {
      if (i < 8) dma<0>(rx, a + i * 2048, vx, sa + (passA ? 32 * (i >> 1) + 4 * (i & 1) : 16 * i) * (D * 2));
      dma<0>(rwin, bb + i * 2048, vb[i], sb);
    }
  };
  for (int n = 0; n < 3; ++n) issue1(n, n, true, vxa);
  STAMP(60);
  const unsigned vxb = (8 * lw + rr) * (D * 2) + swz, vw = (8 * lw + rr) * (S * 2) + swz, vl = lane * 16 + lw * 1024;
  auto issue2a = [&](int n) { LAS char* a = sm + (n & 3) * ASLOT + lw * 1024; const unsigned sa = id.tp * 128 * (S * 2) + (n - 18) * 128;
#pragma unroll
    for (int i = 0; i < 8; ++i) dma<0>(rwsp, a + i * 2048, vw, sa + 16 * i * (S * 2)); };
  auto issue2b = [&](int n) { LAS char* bb = sm + BBASE + (n & 3) * BSLOT + lw * 1024; const unsigned sb = W_ZF + (id.ft * 576 + (n - 18) * 18) * 1024;
#pragma unroll
    for (int i = 0; i < 9; ++i) dma<SC1>(rws, bb + i * 2048, vl, sb + i * 2048); };
#pragma unroll 1
  for (int n = 0; n < 6; ++n) { WAITV(34); LBAR(); issue1(n + 3, n + 3, true, vxa); }
#pragma unroll 1
  for (int n = 6; n < 15; ++n) { WAITV(34); LBAR(); issue1(n + 3, n - 6, false, vxb); }
  WAITV(34); LBAR(); issue2a(18);
  WAITV(25); LBAR(); issue2a(19);
  WAITV(16); LBAR(); issue2a(20);
  { const u32x2 tag = lds<u32x2>(sm + T_TAG); wait_flags<1>(rws, W_F2 + id.ft * 128 + (lane & 7) * 16, tag); }
  STAMP(61);
  issue2b(18); issue2b(19); issue2b(20);
  WAITV(18); LBAR(); issue2a(21); issue2b(21);
  WAITV(26); LBAR(); issue2a(22); issue2b(22);
#pragma unroll 1
  for (int n = 20; n < 47; ++n) { WAITV(34); LBAR(); issue2a(n + 3); issue2b(n + 3); }
  WAITV(34); LBAR(); WAITV(17); LBAR(); WAITV(0); LBAR();
  LBAR();
  const int g = id.ft & 7, u = g >> 1;
  auto issue3 = [&](int m, int slot) { LAS char* a = sm + slot * G3SLOT + lw * 1024; const unsigned sb = W_WOF + (u * 72 + 4 * m) * 9 * 1024;
#pragma unroll
    for (int i = 0; i < 10; ++i) { const int pc = 2 * i + lw, kk = pc / 5, tt = pc - kk * 5, tile = (tt == 4) ? 4 : tt + (g & 1) * 5;
      dma<SC1>(rws, a + i * 2048, lane * 16, sb + (kk * 9 + tile) * 1024); } };
  for (int m = 0; m < 5; ++m) issue3(m, m);
  LBAR(); LBAR();
  WAITV(40); LBAR();
  WAITV(30); LBAR(); issue3(5, 5); issue3(6, 0);
  int slot = 1;
#pragma unroll 1
  for (int m = 2; m < 13; ++m) { WAITV(40); LBAR(); issue3(m + 5, slot); slot = slot == 5 ? 0 : slot + 1; }
  WAITV(40); LBAR(); WAITV(30); LBAR(); WAITV(20); LBAR(); WAITV(10); LBAR(); WAITV(0); LBAR();
}

DEV void role_X(const Params& P, LAS char* sm, const Ids id, const int xw STAMP_ARG) {
  const int lane = id.lane;
  const rsrc_t rws = mkrs(P.ws, W_END);
  u32x2 tag; u64 epoch;
  { const u32x2 e = __builtin_amdgcn_raw_buffer_load_b64(rws, W_EPOCH, 0, SC1);
    epoch = ((u64)__builtin_amdgcn_readfirstlane(e.y) << 32) | __builtin_amdgcn_readfirstlane(e.x);
    const u64 t = ((epoch + 1) * 0x9E3779B97F4A7C15ull) | 1ull; tag.x = (unsigned)t; tag.y = (unsigned)(t >> 32); }
  if (xw == 0) {
    const rsrc_t rx = mkrs(P.x, S * D * 2);
    const int row = id.tp * 128 + id.ft * 8 + (lane >> 3);
    u32x4 v[9];
#pragma unroll
    for (int i = 0; i < 9; ++i) v[i] = ld16<0>(rx, row * (D * 2) + (lane & 7) * 16, i * 128);
    if (lane == 0) *(LAS u32x2*)(sm + T_TAG) = tag;
    WAITL(0); BAR();
    float s = 0.f, q = 0.f; unsigned ones = 0x3f803f80u; asm volatile("" : "+v"(ones));
#pragma unroll
    for (int i = 0; i < 9; ++i)
#pragma unroll
      for (int e = 0; e < 4; ++e) { s = dot2(v[i][e], ones, s); q = dot2(v[i][e], v[i][e], q); }
    s += dpp<XOR1>(s); q += dpp<XOR1>(q); s += dpp<XOR2>(s); q += dpp<XOR2>(q); s += dpp<XOR7>(s); q += dpp<XOR7>(q);
    const float m = s * (1.0f / D), var = fmaxf(q * (1.0f / D) - m * m, 0.f), r = __builtin_amdgcn_rsqf(var + 1e-5f);
    if ((lane & 7) == 0) st16<SC1>(seal(m, r, tag), rws, W_G0X + row * 16, 0);
    BAR();
  } else {
    const rsrc_t rwin = mkrs(P.win, D * F * 2);
    const int rg = lane / 18, ch = lane - rg * 18;
    const unsigned vo = lane < 54 ? ((id.tp >> 1) * 72 + rg) * (F * 2) + (((id.tp & 1) ? 0 : FH) + id.ft * 144) * 2 + ch * 16 : 0xF0000000u;
    u32x4 v[24];
#pragma unroll
    for (int i = 0; i < 24; ++i) v[i] = ld16<0>(rwin, vo, i * 3 * (F * 2));
    BAR();
    float a[8]; unsigned selo = 0x00003f80u, sehi = 0x3f800000u; asm volatile("" : "+v"(selo), "+v"(sehi));
#pragma unroll
    for (int e = 0; e < 8; ++e) a[e] = 0.f;
#pragma unroll
    for (int i = 0; i < 24; ++i)
#pragma unroll
      for (int e = 0; e < 4; ++e) { a[2 * e] = dot2(v[i][e], selo, a[2 * e]); a[2 * e + 1] = dot2(v[i][e], sehi, a[2 * e + 1]); }
#pragma unroll
    for (int e = 0; e < 8; ++e) a[e] += __shfl(a[e], lane + 18) + __shfl(a[e], lane + 36);
    if (lane < 18) {
#pragma unroll
      for (int e = 0; e < 4; ++e) st16<SC1>(seal(a[2 * e], a[2 * e + 1], tag), rws, W_G0C + ((id.ft * 16 + id.tp) * 72 + ch * 4 + e) * 16, 0);
    }
    BAR();
  }
  STAMP(0);
  BAR(); BAR(); BAR();
  if (xw == 0) {
    const u64 t0 = __builtin_amdgcn_s_memrealtime();
    int round = 0;
    for (;;) {
      u32x4 gx[2], gc[18];
#pragma unroll
      for (int i = 0; i < 2; ++i) gx[i] = ld16<SC1>(rws, W_G0X + (id.tp * 128 + lane) * 16, i * 1024);
#pragma unroll
      for (int i = 0; i < 18; ++i) gc[i] = ld16<SC1>(rws, W_G0C + (id.ft * 1152 + lane) * 16, i * 1024);
      if (round == 0) { FENCE(); BAR(); BAR(); FENCE(); }
      bool ok = sealed(gx[0], tag) && sealed(gx[1], tag);
#pragma unroll
      for (int i = 0; i < 18; ++i) ok &= sealed(gc[i], tag);
      ++round;
      if (__all(ok) || expired(t0)) {
#pragma unroll
        for (int i = 0; i < 2; ++i) { *(LAS unsigned*)(sm + T_M1 + (64 * i + lane) * 4) = gx[i].x; *(LAS unsigned*)(sm + T_R1 + (64 * i + lane) * 4) = gx[i].y; }
#pragma unroll
        for (int i = 0; i < 18; ++i) *(LAS u32x2*)(sm + STG + 4096 + (64 * i + lane) * 8) = gc[i].xy;
        break;
      }
      FENCE();
    }
    WAITL(0);
#pragma unroll
    for (int hz = 0; hz < 2; ++hz)
#pragma unroll
      for (int pass = 0; pass < 2; ++pass) { const int cp = pass * 64 + lane;
        if (cp < 72) { f32x2 a = {0.f, 0.f};
#pragma unroll
          for (int e = 0; e < 8; ++e) a += lds<f32x2>(sm + STG + 4096 + ((2 * e + hz) * 72 + cp) * 8);
          *(LAS f32x2*)(sm + (hz ? T_CS1 : T_CS2) + cp * 8) = a; } }
    WAITL(0); BAR();
  } else { BAR(); BAR(); BAR(); }
  STAMP(1);
  BAR();
  unsigned sink = 0;
  if (xw == 1) {
    auto touch = [&](const void* p, unsigned per, unsigned lines) { const rsrc_t r = mkrs(p, per * 256);
      for (unsigned l0 = 0; l0 < lines; l0 += 64) { const unsigned o = l0 + lane < lines ? id.b * per + (l0 + lane) * 128 : 0xF0000000u;
        asm volatile("buffer_load_dword %0, %1, %2, 0 offen" : "+v"(sink) : "v"(o), "s"(r) : "memory"); } };
    touch(P.wsp, 32768, 256); touch(P.k, 9216, 72); touch(P.v, 9216, 72); touch(P.wout, 10368, 81); touch(P.ssmk, 9216, 72);
  }
  BAR();
  { const int row = 64 * xw + lane;
    const f32x2 a = lds<f32x2>(sm + STG + row * 8) + lds<f32x2>(sm + STG + 1024 + row * 8);
    st16<SC1>(seal(a.x, a.y, tag), rws, W_G1S + ((id.tp * 16 + id.ft) * 128 + row) * 16, 0);
    BAR(); BAR();
    const u64 t0 = __builtin_amdgcn_s_memrealtime();
    int round = 0;
    for (;;) {
      u32x4 gs[16];
#pragma unroll
      for (int i = 0; i < 16; ++i) gs[i] = ld16<SC1>(rws, W_G1S + (id.tp * 16 * 128 + row) * 16, i * 2048);
      if (round == 0) { FENCE(); BAR(); FENCE(); }
      bool ok = true; float s = 0.f, q = 0.f;
#pragma unroll
      for (int i = 0; i < 16; ++i) { ok &= sealed(gs[i], tag); s += __uint_as_float(gs[i].x); q += __uint_as_float(gs[i].y); }
      ++round;
      if (__all(ok) || expired(t0)) {
        const float m = s * (1.0f / FH), var = fmaxf(q * (1.0f / FH) - m * m, 0.f), r = __builtin_amdgcn_rsqf(var + 1e-5f);
        *(LAS float*)(sm + T_R2 + row * 4) = r; *(LAS float*)(sm + T_N2 + row * 4) = -m * r;
        break;
      }
      FENCE();
    }
    asm volatile("s_waitcnt vmcnt(0) lgkmcnt(0)" : "+v"(sink) :: "memory"); BAR();
  }
  STAMP(2);
  BAR(); BAR(); BAR();
  if (xw == 0) set_flag(rws, W_F2 + (id.ft * 16 + id.tp) * 8, tag, lane);
  BAR(); BAR(); BAR();
  LAS char* stg = sm + STG + xw * STG_W;
  { const int tile = xw ? id.b : 256 + id.b - 32; const bool on = xw || (id.b >= 32 && id.b < 64);
    const int tb = tile / 9, db = tile - tb * 9, cc = lane & 7, rg = lane >> 3;
    u32x4 kk[11], vv[8], sh[4];
    if (on) {
      const rsrc_t rk = mkrs(P.k, S * D * 2), rv = mkrs(P.v, S * D * 2), rs = mkrs(P.shk, D * 4 * 2);
      const int t0 = 64 * tb + 8 * rg; const unsigned col = (64 * db + 8 * cc) * 2;
#pragma unroll
      for (int i = 0; i < 11; ++i) kk[i] = ld16<0>(rk, t0 + i - 3 >= 0 ? (unsigned)(t0 + i - 3) * (D * 2) + col : 0xF0000000u, 0);
#pragma unroll
      for (int i = 0; i < 8; ++i) vv[i] = ld16<0>(rv, t0 * (D * 2) + col, i * (D * 2));
#pragma unroll
      for (int i = 0; i < 4; ++i) sh[i] = ld16<0>(rs, col * 4, i * 16);
    }
    FENCE(); BAR(); BAR(); FENCE();
#pragma unroll
    for (int e = 0; e < 8; ++e) {
      unsigned o[4];
#pragma unroll
      for (int hf = 0; hf < 2; ++hf) {
        if (on) {
          float c[4], kf[7], y[4];
#pragma unroll
          for (int j = 0; j < 4; ++j) { const unsigned w = sh[e >> 1][(e & 1) * 2 + (j >> 1)]; c[j] = (j & 1) ? bhi(w) : blo(w); }
#pragma unroll
          for (int i = 0; i < 7; ++i) { const unsigned w = kk[4 * hf + i][e >> 1]; kf[i] = (e & 1) ? bhi(w) : blo(w); }
#pragma unroll
          for (int i = 0; i < 4; ++i) { const unsigned w = vv[4 * hf + i][e >> 1];
            float a = c[3] * kf[i]; asm volatile("" : "+v"(a)); a = fmaf(c[2], kf[i + 1], a); a = fmaf(c[1], kf[i + 2], a); a = fmaf(c[0], kf[i + 3], a);
            y[i] = a * ((e & 1) ? bhi(w) : blo(w)); asm volatile("" : "+v"(y[i])); }
          o[3 - 2 * hf] = pk(y[1], y[0]); o[2 - 2 * hf] = pk(y[3], y[2]);
          if (hf == 1) *(LAS u32x4*)(stg + (8 * cc + e) * 128 + (((7 - rg) ^ cc) * 16)) = (u32x4){o[0], o[1], o[2], o[3]};
        }
        FENCE(); BAR(); FENCE();
      }
    }
    if (on) {
#pragma unroll
      for (int i = 0; i < 8; ++i) { const u32x4 t = lds<u32x4>(stg + (8 * i + rg) * 128 + ((cc ^ i) * 16));
        st16<SC1>(t, rws, W_KVT + (64 * db + rg) * (S * 2) + (S - 64 * (tb + 1)) * 2 + cc * 16, 8 * i * (S * 2)); }
    }
    WAITL(0); BAR();
  }
  { const int unit = xw ? 256 + id.b : id.b; const bool on = !xw || id.b < 32;
    const int ks = unit >> 2, u = unit & 3;
    if (on) {
      const rsrc_t rwo = mkrs(P.wout, FH * D * 2);
#pragma unroll
      for (int i = 0; i < 9; ++i) { const int ci = 64 * i + lane, rho = ci / 18, ch = ci - rho * 18;
        const int f = ks < 64 ? 144 * (ks >> 2) + 32 * (ks & 3) + rho : 144 * (2 * (ks - 64) + (rho >> 4)) + 128 + (rho & 15);
        dma<0>(rwo, stg + i * 1024, f * (D * 2) + u * 288 + ch * 16, 0); }
    }
    FENCE(); BAR(); BAR();
    WAITV(0);
    if (on) {
      const int q = lane >> 4, qq = (lane & 15) >> 2, p = lane & 3;
      LAS char* base = stg + (4 * q + qq) * 288;
#pragma unroll
      for (int T = 0; T < 9; ++T) { const int cb = T < 4 ? 32 * (T >> 1) + 4 * (T & 1) : T == 4 ? 64 : 80 + 32 * ((T - 5) >> 1) + 4 * ((T - 5) & 1);
        LAS char* a = base + cb * 2 + p * (T == 4 ? 8 : 16);
        const bf16x8 fr = cat(ldtr(a), ldtr(a + 16 * 288));
        st16<SC1>(__builtin_bit_cast(u32x4, fr), rws, W_WOF + lane * 16, ((u * 72 + ks) * 9 + T) * 1024); }
    }
    FENCE(); BAR(); BAR();
    WAITV(0); BAR();
    if (xw == 0) set_flag(rws, W_F5 + id.b * 8, tag, lane);
  }
  STAMP(3);
  BAR(); BAR(); BAR(); BAR(); BAR();
  if (xw == 0) wait_flags<2>(rws, W_F5 + lane * 16, tag);
  BAR(); BAR();
  STAMP(4);
  { const rsrc_t rh = mkrs(P.ssmk, D * S * 2);
    const int nch = xw ? 1 : 2;
    for (int c = 0; c < nch; ++c) { const int ci = xw ? 1 : 2 * c, chn = ci == 2 ? 512 + (id.b >> 2) : 2 * id.b + ci;
      LAS char* cb = sm + CV + ci * CV_CH;
#pragma unroll
      for (int i = 0; i < 4; ++i) { dma<0>(rh, cb + 32 + i * 1024, lane * 16, chn * (S * 2) + i * 1024);
        dma<0>(rh, cb + CV_O + 32 + i * 1024, lane * 16 + 2, chn * (S * 2) + i * 1024);
        dma<SC1>(rws, cb + CV_U + i * 1024, lane * 16, W_KVT + chn * (S * 2) + i * 1024); }
      const unsigned h0 = P.ssmk[chn * S];
      if (lane < 8) { *(LAS unsigned*)(cb + lane * 4) = 0u; *(LAS unsigned*)(cb + CV_O + lane * 4) = lane == 7 ? h0 << 16 : 0u; }
      if (lane < 34) *(LAS u32x4*)(cb + CV_U + 4096 + lane * 16) = (u32x4){0u, 0u, 0u, 0u};
    }
    WAITV(0); WAITL(0); BAR();
  }
  BAR();
  if (xw == 0) { set_flag(rws, W_F3 + (id.tp * 16 + id.ft) * 8, tag, lane); wait_flags<1>(rws, W_F3 + id.tp * 128 + (lane & 7) * 16, tag); }
  STAMP(5);
  BAR();
  BAR(); BAR(); BAR();
  if (xw == 0) set_flag(rws, W_F4 + id.b * 8, tag, lane);
  BAR(); BAR(); BAR(); BAR(); BAR(); BAR();
  if (xw == 0) {
    wait_flags<2>(rws, W_F4 + lane * 16, tag);
    const int g = id.ft & 7, d0 = 144 * (g >> 1) + 64 * (g & 1);
#pragma unroll
    for (int i = 0; i < 10; ++i) dma<SC1>(rws, sm + YTILE + i * 1024, W_YT + (d0 + (lane >> 3)) * (S * 2) + (id.tp * 128 + 64 * (id.ft >> 3)) * 2 + (lane & 7) * 16, i * 8 * (S * 2));
    if (id.b == 0 && lane == 0) st8<SC1>((u32x2){(unsigned)(epoch + 1), (unsigned)((epoch + 1) >> 32)}, rws, W_EPOCH, 0);
  }
  STAMP(6);
  BAR(); BAR(); BAR(); BAR(); BAR(); BAR(); BAR();
  WAITV(0); BAR();
}

DEV void role_C(const Params& P, LAS char* sm, const Ids id, const int wid STAMP_ARG) {
  const int lane = id.lane, wt = wid >> 1, wf = wid & 1, c = lane & 15, q = lane >> 4;
  const rsrc_t rws = mkrs(P.ws, W_END);
  __builtin_amdgcn_s_setprio(1);
  f32x4 acc[4][4], accx[2];
  bf16x8 tf[2][4], ff[2][5];
  LAS char *ta[2][2], *fb[2], *fx[2];
#pragma unroll
  for (int s = 0; s < 2; ++s)
#pragma unroll
    for (int h = 0; h < 2; ++h) ta[s][h] = sm + (64 * wt + 32 * (h ^ wf) + c) * 128 + (((q + 4 * s) ^ ((c >> 1) & 7)) * 16);
  { const int o = (16 * (q >> 1) + 4 * (q & 1) + (c >> 2)) * 288 + 8 * (c & 3);
#pragma unroll
    for (int h = 0; h < 2; ++h) { fb[h] = sm + BBASE + h * 2 * BSLOT + o + wf * 128; fx[h] = sm + BBASE + h * 2 * BSLOT + o + 256; } }
  auto rd = [&](auto mode, int b, int slot, int s) {
    constexpr int MODE = decltype(mode)::value;
    if (MODE < 2) {
      const int o = (slot & 1) * BSLOT + 32 * s * 288;
#pragma unroll
      for (int j = 0; j < 4; ++j) ff[b][j] = cat(ldtr(fb[slot >> 1] + o + j * 32), ldtr(fb[slot >> 1] + o + j * 32 + 8 * 288));
    } else {
#pragma unroll
      for (int j = 0; j < 4; ++j) ff[b][j] = lds<bf16x8>(fb[slot >> 1] + (slot & 1) * BSLOT + (s * 9 + j) * 1024);
    }
#pragma unroll
    for (int i = 0; i < 4; ++i) tf[b][i] = lds<bf16x8>(ta[s][i >> 1] + slot * ASLOT + (i & 1) * 2048);
    if (MODE < 2) { const int o = (slot & 1) * BSLOT + 32 * s * 288; ff[b][4] = cat(ldtr(fx[slot >> 1] + o), ldtr(fx[slot >> 1] + o + 8 * 288)); }
    else ff[b][4] = lds<bf16x8>(fx[slot >> 1] + (slot & 1) * BSLOT + s * 9 * 1024);
  };
  auto mm = [&](auto mode, int b, int lo, int hi) {
    constexpr int MODE = decltype(mode)::value;
#pragma unroll
    for (int n = 0; n < 18; ++n) if (n >= lo && n < hi) {
      if (n < 16) { const int i = n >> 2, j = (i & 1) ? 3 - (n & 3) : (n & 3);
        acc[i][j] = MODE == 0 ? MFMA(tf[b][i], ff[b][j], acc[i][j]) : MFMA(ff[b][j], tf[b][i], acc[i][j]); }
      else accx[n - 16] = MODE == 0 ? MFMA(tf[b][n - 16], ff[b][4], accx[n - 16]) : MFMA(ff[b][4], tf[b][n - 16], accx[n - 16]);
    }
  };
  auto stage = [&](auto mode, int slot, bool drain) {
    constexpr int MODE = decltype(mode)::value;
    mm(mode, 1, 0, 4); SB();
    if (drain) WAITV(0);
    WAITL(0); BAR(); SB();
    rd(mode, 0, slot, 0); mm(mode, 1, 4, 18);
    if (MODE < 2) { for (int i = 0; i < 14; ++i) { SG(0x100, 1); SG(0x8, 1); } }
    else { for (int i = 0; i < 4; ++i) { SG(0x100, 1); SG(0x8, 1); SG(0x100, 1); SG(0x8, 2); } SG(0x100, 1); SG(0x8, 2); }
    SB();
    rd(mode, 1, slot, 1); mm(mode, 0, 0, 18);
    if (MODE < 2) { for (int i = 0; i < 4; ++i) { SG(0x100, 1); SG(0x8, 1); SG(0x100, 1); SG(0x8, 1); SG(0x100, 1); SG(0x8, 2); } SG(0x100, 1); SG(0x8, 1); SG(0x100, 1); SG(0x8, 1); }
    else { for (int i = 0; i < 9; ++i) { SG(0x100, 1); SG(0x8, 2); } }
    SB();
  };
  auto first = [&](auto mode, int slot) { WAITL(0); BAR(); SB(); rd(mode, 0, slot, 0); rd(mode, 1, slot, 1); mm(mode, 0, 0, 18); SB(); };
  auto zero = [&]() {
#pragma unroll
    for (int i = 0; i < 4; ++i)
#pragma unroll
      for (int j = 0; j < 4; ++j) acc[i][j] = (f32x4){0.f, 0.f, 0.f, 0.f};
    accx[0] = accx[1] = (f32x4){0.f, 0.f, 0.f, 0.f}; };
  const std::integral_constant<int, 0> M0; const std::integral_constant<int, 1> M1; const std::integral_constant<int, 2> M2;

  zero();
  first(M0, 0);
  STAMP(0);
#pragma unroll 1
  for (int it = 0; it < 2; ++it) { stage(M0, 1, false); stage(M0, 2, false); stage(M0, 3, false); stage(M0, 0, false); }
  mm(M0, 1, 0, 18); SB();
  STAMP(1);
  u32x4 z2p[2][4], z2x;
  { float cs[5];
#pragma unroll
    for (int j = 0; j < 5; ++j) cs[j] = lds<float>(sm + T_CS2 + ((j < 4 ? 4 * wf + j : 8) * 16 + c) * 4);
    float red[2][16];
#pragma unroll
    for (int i = 0; i < 4; ++i) {
      const int ro = (64 * wt + 32 * ((i >> 1) ^ wf) + 8 * q + 4 * (i & 1)) * 4;
      const f32x4 m = lds<f32x4>(sm + T_M1 + ro), r = lds<f32x4>(sm + T_R1 + ro);
      f32x4 su = {0.f, 0.f, 0.f, 0.f}, sq = {0.f, 0.f, 0.f, 0.f};
#pragma unroll
      for (int j = 0; j < (i < 2 ? 5 : 4); ++j) {
        const f32x4 z = gelu4(((j < 4 ? acc[i][j] : accx[i]) - m * cs[j]) * r);
        su += z; sq += z * z;
        u32x4& dst = j < 4 ? z2p[i >> 1][j] : z2x;
        dst[2 * (i & 1)] = pk(z[0], z[1]); dst[2 * (i & 1) + 1] = pk(z[2], z[3]);
      }
#pragma unroll
      for (int e = 0; e < 4; ++e) { red[0][4 * i + e] = su[e]; red[1][4 * i + e] = sq[e]; }
    }
    float r8[2][8], r4[2][4];
#pragma unroll
    for (int st = 0; st < 2; ++st) {
#pragma unroll
      for (int e = 0; e < 8; ++e) { const float a = red[st][e] + dpp<XOR8>(red[st][e]), b = red[st][8 + e] + dpp<XOR8>(red[st][8 + e]); r8[st][e] = (c & 8) ? b : a; }
#pragma unroll
      for (int e = 0; e < 4; ++e) { const float a = r8[st][e] + dpp<XOR7>(r8[st][e]), b = r8[st][4 + e] + dpp<XOR7>(r8[st][4 + e]); float t = (c & 4) ? b : a;
        t += dpp<XOR1>(t); t += dpp<XOR2>(t); r4[st][e] = t; }
    }
    if ((c & 3) == 0) { LAS char* pp = sm + STG + wf * 1024 + (64 * wt + 32 * ((c >> 3) ^ wf) + 8 * q + 4 * ((c >> 2) & 1)) * 8;
      *(LAS f32x4*)pp = (f32x4){r4[0][0], r4[1][0], r4[0][1], r4[1][1]}; *(LAS f32x4*)(pp + 16) = (f32x4){r4[0][2], r4[1][2], r4[0][3], r4[1][3]}; }
#pragma unroll
    for (int i = 0; i < 2; ++i) asm volatile("" : "+v"(z2p[i][0]), "+v"(z2p[i][1]), "+v"(z2p[i][2]), "+v"(z2p[i][3]));
    asm volatile("" : "+v"(z2x));
  }
  STAMP(2);
  zero();
  first(M1, 1);
  stage(M1, 2, false); stage(M1, 3, false); stage(M1, 0, false); stage(M1, 1, false);
  STAMP(3);
  {
#pragma unroll
    for (int i = 0; i < 2; ++i) asm volatile("" : "+v"(z2p[i][0]), "+v"(z2p[i][1]), "+v"(z2p[i][2]), "+v"(z2p[i][3]));
    asm volatile("" : "+v"(z2x));
#pragma unroll
    for (int ip = 0; ip < 2; ++ip) {
      const int ro = (64 * wt + 32 * (ip ^ wf) + 8 * q) * 4;
      const f32x4 r0 = lds<f32x4>(sm + T_R2 + ro), r1 = lds<f32x4>(sm + T_R2 + ro + 16), n0 = lds<f32x4>(sm + T_N2 + ro), n1 = lds<f32x4>(sm + T_N2 + ro + 16);
      const unsigned so = W_ZF + ((id.ft * 64 + 4 * id.tp + 2 * wt + (ip ^ wf)) * 9) * 1024;
#pragma unroll
      for (int j = 0; j < (ip == 0 ? 5 : 4); ++j) { const u32x4 z = j < 4 ? z2p[ip][j] : z2x;
        const f32x4 a = (f32x4){blo(z[0]), bhi(z[0]), blo(z[1]), bhi(z[1])} * r0 + n0, b = (f32x4){blo(z[2]), bhi(z[2]), blo(z[3]), bhi(z[3])} * r1 + n1;
        st16<SC1>((u32x4){pk(a[0], a[1]), pk(a[2], a[3]), pk(b[0], b[1]), pk(b[2], b[3])}, rws, lane * 16, so + (j < 4 ? 4 * wf + j : 8) * 1024); }
    }
    SB();
  }
  unsigned braw[4];
  { const rsrc_t rb = mkrs(P.bsp, S * 2);
#pragma unroll
    for (int i = 0; i < 4; ++i) braw[i] = __builtin_amdgcn_raw_buffer_load_b16(rb, (id.tp * 128 + 64 * wt + 16 * (i ^ (2 * wf)) + c) * 2, 0, 0); }
  STAMP(4);
  stage(M1, 2, false); stage(M1, 3, false); stage(M1, 0, true); stage(M1, 1, false);
  mm(M1, 1, 0, 18); SB();
  STAMP(5);
  u32x2 z1p[4][4], z1x[2];
  { float bias[4];
#pragma unroll
    for (int i = 0; i < 4; ++i) bias[i] = blo(braw[i]);
    f32x4 cs[5];
#pragma unroll
    for (int j = 0; j < 5; ++j) cs[j] = lds<f32x4>(sm + T_CS1 + ((j < 4 ? 4 * wf + j : 8) * 16 + 4 * q) * 4);
#pragma unroll
    for (int i = 0; i < 4; ++i) { const int ro = (64 * wt + 16 * (i ^ (2 * wf)) + c) * 4;
      const float m = lds<float>(sm + T_M1 + ro), r = lds<float>(sm + T_R1 + ro);
#pragma unroll
      for (int j = 0; j < (i < 2 ? 5 : 4); ++j) {
        f32x4& a = j < 4 ? acc[i][j] : accx[i];
        const f32x4 z = gelu4((a - m * cs[j]) * r);
        (j < 4 ? z1p[i][j] : z1x[i]) = (u32x2){pk(z[0], z[1]), pk(z[2], z[3])};
        a = (f32x4){bias[i], bias[i], bias[i], bias[i]};
      } }
#pragma unroll
    for (int h = 0; h < 2; ++h) { fb[h] = sm + BBASE + h * 2 * BSLOT + lane * 16 + wf * 4096; fx[h] = sm + BBASE + h * 2 * BSLOT + lane * 16 + 8192; }
#pragma unroll
    for (int i = 0; i < 4; ++i) asm volatile("" : "+v"(z1p[i][0]), "+v"(z1p[i][1]), "+v"(z1p[i][2]), "+v"(z1p[i][3]), "+v"(acc[i][0]), "+v"(acc[i][1]), "+v"(acc[i][2]), "+v"(acc[i][3]));
    asm volatile("" : "+v"(z1x[0]), "+v"(z1x[1]), "+v"(accx[0]), "+v"(accx[1]));
  }
  STAMP(6);
  first(M2, 2);
  STAMP(7);
#pragma unroll 1
  for (int it = 0; it < 7; ++it) { stage(M2, 3, false); stage(M2, 0, false); stage(M2, 1, false); stage(M2, 2, false); }
  stage(M2, 3, false); stage(M2, 0, false); stage(M2, 1, false);
  mm(M2, 1, 0, 4); SB(); WAITL(0); BAR(); SB();
  mm(M2, 1, 4, 18); SB();
  STAMP(8);
#pragma unroll
  for (int i = 0; i < 4; ++i) asm volatile("" : "+v"(z1p[i][0]), "+v"(z1p[i][1]), "+v"(z1p[i][2]), "+v"(z1p[i][3]));
  asm volatile("" : "+v"(z1x[0]), "+v"(z1x[1]));
#pragma unroll
  for (int i = 0; i < 4; ++i) {
    const unsigned so = W_HF + (id.tp * 8 + 4 * wt + (i ^ (2 * wf))) * 72 * 1024;
    auto gate = [&](u32x2 z, f32x4 a) { const f32x4 h = (f32x4){blo(z.x), bhi(z.x), blo(z.y), bhi(z.y)} * a; return (u32x2){pk(h[0], h[1]), pk(h[2], h[3])}; };
#pragma unroll
    for (int pp = 0; pp < 2; ++pp) { const u32x2 lo = gate(z1p[i][2 * pp], acc[i][2 * pp]), hi = gate(z1p[i][2 * pp + 1], acc[i][2 * pp + 1]);
      st16<SC1>((u32x4){lo.x, lo.y, hi.x, hi.y}, rws, lane * 16, so + (4 * id.ft + 2 * wf + pp) * 1024); }
    if (i < 2) st8<SC1>(gate(z1x[i], accx[i]), rws, lane * 16 + (id.ft & 1) * 8, so + (64 + (id.ft >> 1)) * 1024);
  }
  SB(); BAR(); SB();
  STAMP(9);
  f32x4 cm[8];
  { const unsigned dm = ((const unsigned*)P.ssmd)[id.b], dsh = ((const unsigned*)P.ssmd)[256 + (id.b >> 3)];
    const float dvm = wt ? bhi(dm) : blo(dm), dvs = (id.b & 4) ? bhi(dsh) : blo(dsh);
    const int aoff = ((c & 1) ? 0 : CV_O) + (4 * q + ((c + 1) >> 1)) * 4, boff = CV_U + (2032 - 16 * c + 8 * q) * 2;
    auto afrag = [&](LAS char* p) { const unsigned d0 = lds<unsigned>(p), d1 = lds<unsigned>(p + 4), d2 = lds<unsigned>(p + 8), d3 = lds<unsigned>(p + 12);
      return __builtin_bit_cast(bf16x8, (u32x4){d0, d1, d2, d3}); };
    { const int jq = id.b & 3; LAS char* cb = sm + CV + 2 * CV_CH;
      f32x4 s0 = {0.f, 0.f, 0.f, 0.f}, s1 = s0;
#pragma unroll
      for (int kk = 0; kk < 2; ++kk) { const int k0 = 2 * wid + kk;
#pragma unroll
        for (int e = 0; e < 8; ++e) { const bf16x8 a = afrag(cb + aoff + (k0 + 8 * e) * 64);
          const int o1 = 7 - jq - e >= 0 ? 64 * k0 - 512 * (7 - jq - e) : 512;
          s1 = MFMA(a, lds<bf16x8>(cb + boff + o1), s1);
          if (e < 4) { const int o0 = jq - e >= 0 ? 64 * k0 - 512 * (jq - e) : 512; s0 = MFMA(a, lds<bf16x8>(cb + boff + o0), s0); } } }
      *(LAS f32x4*)(sm + CV_PS + (wid * 2) * 1024 + lane * 16) = s0; *(LAS f32x4*)(sm + CV_PS + (wid * 2 + 1) * 1024 + lane * 16) = s1; }
    LAS char* cb = sm + CV + wt * CV_CH;
#pragma unroll
    for (int j = 0; j < 8; ++j) cm[j] = (f32x4){0.f, 0.f, 0.f, 0.f};
    auto block = [&](int k0) { bf16x8 a[8], bb[8];
#pragma unroll
      for (int e = 0; e < 8; ++e) { bb[e] = lds<bf16x8>(cb + boff + 64 * k0 - 512 * e); a[e] = afrag(cb + aoff + (k0 + 8 * e) * 64); }
#pragma unroll
      for (int e = 0; e < 8; ++e)
#pragma unroll
        for (int j = e; j < 8; ++j) cm[j] = MFMA(a[e], bb[j - e], cm[j]); };
    block(4 * wf);
    SB(); WAITV(0); WAITL(0); BAR(); SB();
    STAMP(10);
    f32x4 ys = {0.f, 0.f, 0.f, 0.f};
    if (wid < 2) {
#pragma unroll
      for (int w = 0; w < 4; ++w) ys += lds<f32x4>(sm + CV_PS + (w * 2 + wid) * 1024 + lane * 16);
    }
#pragma unroll 1
    for (int kk = 1; kk < 4; ++kk) block(4 * wf + kk);
    LAS char* pm = sm + (wt ? CV_PM23 : CV + 2 * CV_CH);
#pragma unroll
    for (int j = 0; j < 4; ++j) { f32x4 lo = cm[j], hi = cm[4 + j]; asm volatile("" : "+v"(lo), "+v"(hi));
      *(LAS f32x4*)(pm + wf * 4096 + j * 1024 + lane * 16) = wf ? lo : hi; cm[j] = wf ? hi : lo; }
    SB(); WAITL(0); BAR(); SB();
    STAMP(11);
    const int half = id.ft >> 3;
    const unsigned hso = W_HF + (id.tp * 8 + 4 * half + wid) * 72 * 1024;
    u32x4 hb[12];
#pragma unroll
    for (int i = 0; i < 12; ++i) hb[i] = ld16<SC1>(rws, lane * 16, hso + i * 1024);
    auto yout = [&](f32x4 a, int chn, LAS char* ub, int j, float dv) {
      const u32x2 u = lds<u32x2>(ub + CV_U + (2044 - 256 * j - 16 * c - 4 * q) * 2);
      const f32x4 y = a + (f32x4){bhi(u.y), blo(u.y), bhi(u.x), blo(u.x)} * dv;
      st8<SC1>((u32x2){pk(y[0], y[1]), pk(y[2], y[3])}, rws, (16 * c + 4 * q) * 2, W_YT + chn * (S * 2) + j * 512); };
#pragma unroll
    for (int j = 0; j < 4; ++j) yout(cm[j] + lds<f32x4>(pm + (wf ^ 1) * 4096 + j * 1024 + lane * 16), 2 * id.b + wt, cb, 4 * wf + j, dvm);
    if (wid < 2) yout(ys, 512 + (id.b >> 2), sm + CV + 2 * CV_CH, wid ? 7 - (id.b & 3) : (id.b & 3), dvs);
    const int g = id.ft & 7, dp = 144 * (g >> 1) + 80 * (g & 1), ds = 144 * (g >> 1) + 64, trow = 64 * half + 16 * wid + c;
    const rsrc_t rx = mkrs(P.x, S * D * 2);
    const unsigned xo = (id.tp * 128 + trow) * (D * 2);
    const u32x4 x0 = ld16<0>(rx, xo + q * 16, dp * 2), x1 = ld16<0>(rx, xo + q * 16, dp * 2 + 64);
    const u32x2 x2 = __builtin_amdgcn_raw_buffer_load_b64(rx, xo + q * 8, ds * 2, 0);
    SB();
    f32x4 o[5];
#pragma unroll
    for (int t = 0; t < 5; ++t) o[t] = (f32x4){0.f, 0.f, 0.f, 0.f};
    LAS char* wb[2] = {sm + lane * 16, sm + 3 * G3SLOT + lane * 16};
    bf16x8 wq[2][5];
    auto rdw = [&](int b, int slot, int kk) {
#pragma unroll
      for (int t = 0; t < 5; ++t) wq[b][t] = lds<bf16x8>(wb[slot / 3] + (slot % 3) * G3SLOT + (kk * 5 + t) * 1024); };
    rdw(0, 0, 0);
#pragma unroll 1
    for (int it = 0; it < 3; ++it) {
#pragma unroll
      for (int sl = 0; sl < 6; ++sl)
#pragma unroll
        for (int kk = 0; kk < 4; ++kk) { const int n = sl * 4 + kk;
          if (kk == 3) { if (sl == 5 && it == 2) {} else { SB(); if (sl == 2 && it == 0) WAITV(12); WAITL(0); BAR(); SB(); rdw((n + 1) & 1, sl == 5 ? 0 : sl + 1, 0); } }
          else rdw((n + 1) & 1, sl, kk + 1);
          const bf16x8 hf = __builtin_bit_cast(bf16x8, hb[n % 12]);
#pragma unroll
          for (int t = 0; t < 5; ++t) o[t] = MFMA(wq[n & 1][t], hf, o[t]);
          hb[n % 12] = ld16<SC1>(rws, lane * 16, it * 24 + n + 12 < 72 ? hso + (it * 24 + n + 12) * 1024 : 0xF0000000u);
          SB();
        }
    }
    STAMP(12);
    const float m = lds<float>(sm + T_M1 + trow * 4), r = lds<float>(sm + T_R1 + trow * 4);
    LAS char* yb = sm + YTILE + (c >> 2) * 128 + (16 * wid + 4 * (c & 3)) * 2;
    const rsrc_t ro = mkrs(P.out, S * D * 2);
    auto fin = [&](u32x2 xv, bf16x4 yv, f32x4 a) { const u32x2 yu = __builtin_bit_cast(u32x2, yv);
      const f32x4 xf = {blo(xv.x), bhi(xv.x), blo(xv.y), bhi(xv.y)}, yf = {blo(yu.x), bhi(yu.x), blo(yu.y), bhi(yu.y)};
      const f32x4 t = xf + (xf - m) * r * yf + a; return (u32x2){pk(t[0], t[1]), pk(t[2], t[3])}; };
    const int yp = 16 * (g & 1), ysh = 64 * ((g & 1) ^ 1);
#pragma unroll
    for (int pp = 0; pp < 2; ++pp) { const u32x4 xv = pp ? x1 : x0;
      const u32x2 lo = fin(xv.xy, ldtr(yb + (yp + 32 * pp + 8 * q) * 128), o[2 * pp]), hi = fin(xv.zw, ldtr(yb + (yp + 32 * pp + 8 * q + 4) * 128), o[2 * pp + 1]);
      st16<0>((u32x4){lo.x, lo.y, hi.x, hi.y}, ro, xo + q * 16, dp * 2 + 64 * pp); }
    const u32x2 ls = fin(x2, ldtr(yb + (ysh + 4 * q) * 128), o[4]);
    if (wt == (g & 1)) st8<0>(ls, ro, xo + q * 8, ds * 2);
    STAMP(13);
  }
}
}

extern "C" __global__ __launch_bounds__(512, 2) void h3_gmlp_fused(const Params P) {
  extern __shared__ __attribute__((aligned(16))) char smem_[];
  LAS char* sm = (LAS char*)smem_;
  const int wid = __builtin_amdgcn_readfirstlane(threadIdx.x >> 6);
  Ids id; id.b = blockIdx.x; id.lane = threadIdx.x & 63;
  const int xcd = id.b & 7, loc = id.b >> 3;
  id.tp = (xcd >> 1) * 4 + (loc >> 3); id.ft = (xcd & 1) * 8 + (loc & 7);
  unsigned stampv = 0; { const int lane = id.lane; STAMP(63); }
  if (wid == 0) {
    unsigned lo, hi, bytes, sink;
    asm volatile("s_getpc_b64 vcc\n1:\n\ts_sub_u32 %0, vcc_lo, 1b-h3_gmlp_fused\n\ts_subb_u32 %1, vcc_hi, 0\n\ts_mov_b32 %2, h3_gmlp_end-h3_gmlp_fused" : "=s"(lo), "=s"(hi), "=s"(bytes) :: "vcc", "scc");
    const rsrc_t rc = mkrs((const void*)(((u64)hi << 32) | lo), bytes);
    asm volatile("buffer_load_dword %0, %1, %2, 0 offen\n\ts_waitcnt vmcnt(0)" : "=&v"(sink) : "v"((id.lane < 16 ? id.lane * 32 + loc : 0x100000) * 128), "s"(rc) : "memory");
  }
  if (wid == 4 || wid == 5) role_L(P, sm, id, wid - 4 STAMP_PASS);
  else if (wid >= 6) role_X(P, sm, id, wid - 6 STAMP_PASS);
  else role_C(P, sm, id, wid STAMP_PASS);
  { const int lane = id.lane; STAMP(62); *(unsigned*)(P.ws + (40 << 20) + ((blockIdx.x * 8 + wid) * 64 + lane) * 4) = stampv; }
  if (wid == 5 && blockIdx.x == 0) { const unsigned t_a = __builtin_amdgcn_readlane(stampv, MK_AMP), t_s = __builtin_amdgcn_readlane(stampv, 63);
    const unsigned long long d_ = (unsigned long long)(t_a - t_s) * 20, t0_ = __builtin_amdgcn_s_memrealtime();
    while (__builtin_amdgcn_s_memrealtime() - t0_ < (d_ > 200000 ? 200000 : d_)) __builtin_amdgcn_s_sleep(2); }
}
extern "C" __global__ void h3_gmlp_end() {}

extern "C" void kernel_launch(void* const* d_in, const int* in_sizes, int n_in, void* d_out, int out_size, void* d_ws, size_t ws_size, hipStream_t stream) {
  Params p;
  p.x = (const u16*)d_in[0]; p.k = (const u16*)d_in[1]; p.v = (const u16*)d_in[2]; p.shk = (const u16*)d_in[3]; p.ssmk = (const u16*)d_in[4]; p.ssmd = (const u16*)d_in[5];
  p.win = (const u16*)d_in[7]; p.wsp = (const u16*)d_in[8]; p.bsp = (const u16*)d_in[9]; p.wout = (const u16*)d_in[10];
  p.out = (u16*)d_out; p.ws = (char*)d_ws;
  (void)hipFuncSetAttribute((const void*)h3_gmlp_fused, hipFuncAttributeMaxDynamicSharedMemorySize, LDS_BYTES);
  h3_gmlp_fused<<<256, 512, LDS_BYTES, stream>>>(p);
}
```

```cpp
#include <hip/hip_runtime.h>
#include <hip/hip_bf16.h>
#include <type_traits>

namespace {
typedef unsigned short u16;
typedef unsigned long long u64;
typedef short bf16x8 __attribute__((ext_vector_type(8)));
typedef short bf16x4 __attribute__((ext_vector_type(4)));
typedef __bf16 bf2 __attribute__((ext_vector_type(2)));
typedef float f32x2 __attribute__((ext_vector_type(2)));
typedef float f32x4 __attribute__((ext_vector_type(4)));
typedef unsigned u32x2 __attribute__((ext_vector_type(2)));
typedef unsigned u32x4 __attribute__((ext_vector_type(4)));
typedef __amdgpu_buffer_rsrc_t rsrc_t;
#define LAS __attribute__((address_space(3)))
#define DEV __device__ __forceinline__

constexpr int S = 2048, D = 576, F = 4608, FH = 2304;
constexpr int ASLOT = 16384, BSLOT = 18432, BBASE = 4 * ASLOT;
constexpr int STG = 139264, STG_W = 9984;
constexpr int T_M1 = 159232, T_R1 = T_M1 + 512, T_CS2 = T_R1 + 512, T_CS1 = T_CS2 + 576, T_N2 = T_CS1 + 576, T_R2 = T_N2 + 512, T_TAG = T_R2 + 512;
constexpr int LDS_BYTES = 163840;
constexpr int G3SLOT = 20480;
constexpr int CV = 102400, CV_CH = 13056, CV_O = 4160, CV_U = 8320;
constexpr int CV_PS = 141568, CV_PM23 = 149760;
constexpr int YTILE = 122880;
constexpr int W_EPOCH = 0, W_F2 = 4096, W_F3 = 8192, W_F4 = 12288, W_F5 = 16384;
constexpr int W_G0X = 0x8000, W_G0C = 0x10000, W_G1S = 0x60000;
constexpr int W_KVT = 1 << 20, W_YT = 4 << 20, W_WOF = 7 << 20, W_ZF = 10 << 20, W_HF = 20 << 20, W_END = 30 << 20;
constexpr int SC1 = 16;

#define BAR() do { asm volatile("" ::: "memory"); __builtin_amdgcn_s_barrier(); asm volatile("" ::: "memory"); } while (0)
#define SB() __builtin_amdgcn_sched_barrier(0)
#define SG(m, n) __builtin_amdgcn_sched_group_barrier(m, n, 0)
#define WAITV(n) asm volatile("s_waitcnt vmcnt(%0)" ::"i"(n) : "memory")
#define WAITL(n) asm volatile("s_waitcnt lgkmcnt(%0)" ::"i"(n) : "memory")
#define FENCE() asm volatile("" ::: "memory")
#define MFMA(a, b, c) __builtin_amdgcn_mfma_f32_16x16x32_bf16(a, b, c, 0, 0, 0)
#define STAMP(i) do {} while (0)
#define LBAR() BAR()
#define STAMP_ARG
#define STAMP_PASS

struct Params {
  const u16 *x, *k, *v, *shk, *ssmk, *ssmd, *win, *wsp, *bsp, *wout;
  u16* out; char* ws;
};

DEV rsrc_t mkrs(const void* p, unsigned bytes) { return __builtin_amdgcn_make_buffer_rsrc((void*)p, 0, bytes, 0x00020000); }
template <int AUX> DEV void dma(rsrc_t rs, LAS char* l, unsigned voff, unsigned soff) { __builtin_amdgcn_raw_ptr_buffer_load_lds(rs, (LAS void*)l, 16, voff, soff, 0, AUX); }
template <int AUX> DEV u32x4 ld16(rsrc_t rs, unsigned voff, unsigned soff) { return __builtin_amdgcn_raw_buffer_load_b128(rs, voff, soff, AUX); }
template <int AUX> DEV void st16(u32x4 v, rsrc_t rs, unsigned voff, unsigned soff) { __builtin_amdgcn_raw_buffer_store_b128(v, rs, voff, soff, AUX); }
template <int AUX> DEV void st8(u32x2 v, rsrc_t rs, unsigned voff, unsigned soff) { __builtin_amdgcn_raw_buffer_store_b64(v, rs, voff, soff, AUX); }
template <class T> DEV T lds(LAS char* p) { return *(LAS T*)p; }
DEV bf16x4 ldtr(LAS char* p) { return __builtin_amdgcn_ds_read_tr16_b64_v4i16((LAS bf16x4*)p); }
DEV bf16x8 cat(bf16x4 a, bf16x4 b) { return __builtin_shufflevector(a, b, 0, 1, 2, 3, 4, 5, 6, 7); }
DEV float blo(unsigned u) { return __uint_as_float(u << 16); }
DEV float bhi(unsigned u) { return __uint_as_float(u & 0xffff0000u); }
DEV unsigned pk(float a, float b) { return __builtin_bit_cast(unsigned, __builtin_convertvector((f32x2){a, b}, bf2)); }
DEV float dot2(unsigned a, unsigned b, float c) { return __builtin_amdgcn_fdot2_f32_bf16(__builtin_bit_cast(bf2, a), __builtin_bit_cast(bf2, b), c, false); }
template <int CTRL> DEV float dpp(float x) { return __builtin_bit_cast(float, __builtin_amdgcn_mov_dpp(__builtin_bit_cast(int, x), CTRL, 0xf, 0xf, true)); }
constexpr int XOR1 = 0xB1, XOR2 = 0x4E, XOR7 = 0x141, XOR8 = 0x128;
DEV f32x2 gelu2(f32x2 x) {
  const f32x2 a = x * (x * x * -0.10294324f + -2.30220819f);
  f32x2 e; e.x = __builtin_amdgcn_exp2f(a.x); e.y = __builtin_amdgcn_exp2f(a.y);
  e = e + 1.0f;
  f32x2 r; r.x = __builtin_amdgcn_rcpf(e.x); r.y = __builtin_amdgcn_rcpf(e.y);
  return x * r;
}
DEV f32x4 gelu4(f32x4 x) { const f32x2 a = gelu2(x.xy), b = gelu2(x.zw); return (f32x4){a.x, a.y, b.x, b.y}; }
DEV u32x4 seal(float a, float b, u32x2 tag) { const unsigned ua = __float_as_uint(a), ub = __float_as_uint(b); return (u32x4){ua, ub, ua ^ tag.x, ub ^ tag.y}; }
DEV bool sealed(u32x4 g, u32x2 tag) { return (g.x ^ g.z) == tag.x && (g.y ^ g.w) == tag.y; }
DEV bool expired(u64 t0) { return __builtin_amdgcn_s_memrealtime() - t0 > 2000000ull; }
template <int NL> DEV void wait_flags(rsrc_t ws, unsigned voff, u32x2 tag) {
  const u64 t0 = __builtin_amdgcn_s_memrealtime();
  for (;;) {
    bool ok = true;
#pragma unroll
    for (int i = 0; i < NL; ++i) { const u32x4 f = ld16<SC1>(ws, voff, i * 1024); ok &= f.x == tag.x && f.y == tag.y && f.z == tag.x && f.w == tag.y; }
    if (__all(ok) || expired(t0)) break;
    FENCE();
  }
  FENCE();
}
DEV void set_flag(rsrc_t ws, unsigned off, u32x2 tag, int lane) { if (lane == 0) st8<SC1>(tag, ws, off, 0); }

struct Ids { int b, tp, ft, lane; };

DEV void role_L(const Params& P, LAS char* sm, const Ids id, const int lw STAMP_ARG) {
  int bcount = 0; (void)bcount;
  const int lane = id.lane, rr = lane >> 3, sl = lane & 7;
  const rsrc_t rx = mkrs(P.x, S * D * 2), rwin = mkrs(P.win, D * F * 2), rwsp = mkrs(P.wsp, S * S * 2), rws = mkrs(P.ws, W_END);
  const unsigned swz = (sl ^ (4 * lw + (rr >> 1))) * 16;
  const unsigned vxa = (16 * lw + 8 * (rr >> 2) + (rr & 3)) * (D * 2) + swz;
  unsigned vb[9];
#pragma unroll
  for (int i = 0; i < 9; ++i) { const int ci = 64 * (2 * i + lw) + lane, rho = ci / 18, ch = ci - rho * 18;
    const int kr = (rho & ~12) | ((rho & 4) << 1) | ((rho & 8) >> 1); vb[i] = kr * (F * 2) + ch * 16; }
  auto issue1 = [&](int n, int ks, bool passA, unsigned vx) {
    LAS char* a = sm + (n & 3) * ASLOT + lw * 1024; LAS char* bb = sm + BBASE + (n & 3) * BSLOT + lw * 1024;
    const unsigned sa = id.tp * 128 * (D * 2) + ks * 128, sb = ks * 64 * (F * 2) + ((passA ? FH : 0) + id.ft * 144) * 2;
#pragma unroll
    for (int i = 0; i < 9; ++i) {
      if (i < 8) dma<0>(rx, a + i * 2048, vx, sa + (passA ? 32 * (i >> 1) + 4 * (i & 1) : 16 * i) * (D * 2));
      dma<0>(rwin, bb + i * 2048, vb[i], sb);
    }
  };
  for (int n = 0; n < 3; ++n) issue1(n, n, true, vxa);
  STAMP(60);
  const unsigned vxb = (8 * lw + rr) * (D * 2) + swz, vw = (8 * lw + rr) * (S * 2) + swz, vl = lane * 16 + lw * 1024;
  auto issue2a = [&](int n) { LAS char* a = sm + (n & 3) * ASLOT + lw * 1024; const unsigned sa = id.tp * 128 * (S * 2) + (n - 18) * 128;
#pragma unroll
    for (int i = 0; i < 8; ++i) dma<0>(rwsp, a + i * 2048, vw, sa + 16 * i * (S * 2)); };
  auto issue2b = [&](int n) { LAS char* bb = sm + BBASE + (n & 3) * BSLOT + lw * 1024; const unsigned sb = W_ZF + (id.ft * 576 + (n - 18) * 18) * 1024;
#pragma unroll
    for (int i = 0; i < 9; ++i) dma<SC1>(rws, bb + i * 2048, vl, sb + i * 2048); };
#pragma unroll 1
  for (int n = 0; n < 6; ++n) { WAITV(34); LBAR(); issue1(n + 3, n + 3, true, vxa); }
#pragma unroll 1
  for (int n = 6; n < 15; ++n) { WAITV(34); LBAR(); issue1(n + 3, n - 6, false, vxb); }
  WAITV(34); LBAR(); issue2a(18);
  WAITV(25); LBAR(); issue2a(19);
  WAITV(16); LBAR(); issue2a(20);
  { const u32x2 tag = lds<u32x2>(sm + T_TAG); wait_flags<1>(rws, W_F2 + id.ft * 128 + (lane & 7) * 16, tag); }
  STAMP(61);
  issue2b(18); issue2b(19); issue2b(20);
  WAITV(18); LBAR(); issue2a(21); issue2b(21);
  WAITV(26); LBAR(); issue2a(22); issue2b(22);
#pragma unroll 1
  for (int n = 20; n < 47; ++n) { WAITV(34); LBAR(); issue2a(n + 3); issue2b(n + 3); }
  WAITV(34); LBAR(); WAITV(17); LBAR(); WAITV(0); LBAR();
  LBAR();
  const int g = id.ft & 7, u = g >> 1;
  auto issue3 = [&](int m, int slot) { LAS char* a = sm + slot * G3SLOT + lw * 1024; const unsigned sb = W_WOF + (u * 72 + 4 * m) * 9 * 1024;
#pragma unroll
    for (int i = 0; i < 10; ++i) { const int pc = 2 * i + lw, kk = pc / 5, tt = pc - kk * 5, tile = (tt == 4) ? 4 : tt + (g & 1) * 5;
      dma<SC1>(rws, a + i * 2048, lane * 16, sb + (kk * 9 + tile) * 1024); } };
  for (int m = 0; m < 5; ++m) issue3(m, m);
  LBAR(); LBAR();
  WAITV(40); LBAR();
  WAITV(30); LBAR(); issue3(5, 5); issue3(6, 0);
  int slot = 1;
#pragma unroll 1
  for (int m = 2; m < 13; ++m) { WAITV(40); LBAR(); issue3(m + 5, slot); slot = slot == 5 ? 0 : slot + 1; }
  WAITV(40); LBAR(); WAITV(30); LBAR(); WAITV(20); LBAR(); WAITV(10); LBAR(); WAITV(0); LBAR();
}

DEV void role_X(const Params& P, LAS char* sm, const Ids id, const int xw STAMP_ARG) {
  const int lane = id.lane;
  const rsrc_t rws = mkrs(P.ws, W_END);
  u32x2 tag; u64 epoch;
  { const u32x2 e = __builtin_amdgcn_raw_buffer_load_b64(rws, W_EPOCH, 0, SC1);
    epoch = ((u64)__builtin_amdgcn_readfirstlane(e.y) << 32) | __builtin_amdgcn_readfirstlane(e.x);
    const u64 t = ((epoch + 1) * 0x9E3779B97F4A7C15ull) | 1ull; tag.x = (unsigned)t; tag.y = (unsigned)(t >> 32); }
  if (xw == 0) {
    const rsrc_t rx = mkrs(P.x, S * D * 2);
    const int row = id.tp * 128 + id.ft * 8 + (lane >> 3);
    u32x4 v[9];
#pragma unroll
    for (int i = 0; i < 9; ++i) v[i] = ld16<0>(rx, row * (D * 2) + (lane & 7) * 16, i * 128);
    if (lane == 0) *(LAS u32x2*)(sm + T_TAG) = tag;
    WAITL(0); BAR();
    float s = 0.f, q = 0.f; unsigned ones = 0x3f803f80u; asm volatile("" : "+v"(ones));
#pragma unroll
    for (int i = 0; i < 9; ++i)
#pragma unroll
      for (int e = 0; e < 4; ++e) { s = dot2(v[i][e], ones, s); q = dot2(v[i][e], v[i][e], q); }
    s += dpp<XOR1>(s); q += dpp<XOR1>(q); s += dpp<XOR2>(s); q += dpp<XOR2>(q); s += dpp<XOR7>(s); q += dpp<XOR7>(q);
    const float m = s * (1.0f / D), var = fmaxf(q * (1.0f / D) - m * m, 0.f), r = __builtin_amdgcn_rsqf(var + 1e-5f);
    if ((lane & 7) == 0) st16<SC1>(seal(m, r, tag), rws, W_G0X + row * 16, 0);
    BAR();
  } else {
    const rsrc_t rwin = mkrs(P.win, D * F * 2);
    const int rg = lane / 18, ch = lane - rg * 18;
    const unsigned vo = lane < 54 ? ((id.tp >> 1) * 72 + rg) * (F * 2) + (((id.tp & 1) ? 0 : FH) + id.ft * 144) * 2 + ch * 16 : 0xF0000000u;
    u32x4 v[24];
#pragma unroll
    for (int i = 0; i < 24; ++i) v[i] = ld16<0>(rwin, vo, i * 3 * (F * 2));
    BAR();
    float a[8]; unsigned selo = 0x00003f80u, sehi = 0x3f800000u; asm volatile("" : "+v"(selo), "+v"(sehi));
#pragma unroll
    for (int e = 0; e < 8; ++e) a[e] = 0.f;
#pragma unroll
    for (int i = 0; i < 24; ++i)
#pragma unroll
      for (int e = 0; e < 4; ++e) { a[2 * e] = dot2(v[i][e], selo, a[2 * e]); a[2 * e + 1] = dot2(v[i][e], sehi, a[2 * e + 1]); }
#pragma unroll
    for (int e = 0; e < 8; ++e) a[e] += __shfl(a[e], lane + 18) + __shfl(a[e], lane + 36);
    if (lane < 18) {
#pragma unroll
      for (int e = 0; e < 4; ++e) st16<SC1>(seal(a[2 * e], a[2 * e + 1], tag), rws, W_G0C + ((id.ft * 16 + id.tp) * 72 + ch * 4 + e) * 16, 0);
    }
    BAR();
  }
  STAMP(0);
  BAR(); BAR(); BAR();
  if (xw == 0) {
    const u64 t0 = __builtin_amdgcn_s_memrealtime();
    int round = 0;
    for (;;) {
      u32x4 gx[2], gc[18];
#pragma unroll
      for (int i = 0; i < 2; ++i) gx[i] = ld16<SC1>(rws, W_G0X + (id.tp * 128 + lane) * 16, i * 1024);
#pragma unroll
      for (int i = 0; i < 18; ++i) gc[i] = ld16<SC1>(rws, W_G0C + (id.ft * 1152 + lane) * 16, i * 1024);
      if (round == 0) { FENCE(); BAR(); BAR(); FENCE(); }
      bool ok = sealed(gx[0], tag) && sealed(gx[1], tag);
#pragma unroll
      for (int i = 0; i < 18; ++i) ok &= sealed(gc[i], tag);
      ++round;
      if (__all(ok) || expired(t0)) {
#pragma unroll
        for (int i = 0; i < 2; ++i) { *(LAS unsigned*)(sm + T_M1 + (64 * i + lane) * 4) = gx[i].x; *(LAS unsigned*)(sm + T_R1 + (64 * i + lane) * 4) = gx[i].y; }
#pragma unroll
        for (int i = 0; i < 18; ++i) *(LAS u32x2*)(sm + STG + 4096 + (64 * i + lane) * 8) = gc[i].xy;
        break;
      }
      FENCE();
    }
    WAITL(0);
#pragma unroll
    for (int hz = 0; hz < 2; ++hz)
#pragma unroll
      for (int pass = 0; pass < 2; ++pass) { const int cp = pass * 64 + lane;
        if (cp < 72) { f32x2 a = {0.f, 0.f};
#pragma unroll
          for (int e = 0; e < 8; ++e) a += lds<f32x2>(sm + STG + 4096 + ((2 * e + hz) * 72 + cp) * 8);
          *(LAS f32x2*)(sm + (hz ? T_CS1 : T_CS2) + cp * 8) = a; } }
    WAITL(0); BAR();
  } else { BAR(); BAR(); BAR(); }
  STAMP(1);
  BAR();
  unsigned sink = 0;
  if (xw == 1) {
    auto touch = [&](const void* p, unsigned per, unsigned lines) { const rsrc_t r = mkrs(p, per * 256);
      for (unsigned l0 = 0; l0 < lines; l0 += 64) { const unsigned o = l0 + lane < lines ? id.b * per + (l0 + lane) * 128 : 0xF0000000u;
        asm volatile("buffer_load_dword %0, %1, %2, 0 offen" : "+v"(sink) : "v"(o), "s"(r) : "memory"); } };
    touch(P.wsp, 32768, 256); touch(P.k, 9216, 72); touch(P.v, 9216, 72); touch(P.wout, 10368, 81); touch(P.ssmk, 9216, 72);
  }
  BAR();
  { const int row = 64 * xw + lane;
    const f32x2 a = lds<f32x2>(sm + STG + row * 8) + lds<f32x2>(sm + STG + 1024 + row * 8);
    st16<SC1>(seal(a.x, a.y, tag), rws, W_G1S + ((id.tp * 16 + id.ft) * 128 + row) * 16, 0);
    BAR(); BAR();
    const u64 t0 = __builtin_amdgcn_s_memrealtime();
    int round = 0;
    for (;;) {
      u32x4 gs[16];
#pragma unroll
      for (int i = 0; i < 16; ++i) gs[i] = ld16<SC1>(rws, W_G1S + (id.tp * 16 * 128 + row) * 16, i * 2048);
      if (round == 0) { FENCE(); BAR(); FENCE(); }
      bool ok = true; float s = 0.f, q = 0.f;
#pragma unroll
      for (int i = 0; i < 16; ++i) { ok &= sealed(gs[i], tag); s += __uint_as_float(gs[i].x); q += __uint_as_float(gs[i].y); }
      ++round;
      if (__all(ok) || expired(t0)) {
        const float m = s * (1.0f / FH), var = fmaxf(q * (1.0f / FH) - m * m, 0.f), r = __builtin_amdgcn_rsqf(var + 1e-5f);
        *(LAS float*)(sm + T_R2 + row * 4) = r; *(LAS float*)(sm + T_N2 + row * 4) = -m * r;
        break;
      }
      FENCE();
    }
    asm volatile("s_waitcnt vmcnt(0) lgkmcnt(0)" : "+v"(sink) :: "memory"); BAR();
  }
  STAMP(2);
  BAR(); BAR(); BAR();
  if (xw == 0) set_flag(rws, W_F2 + (id.ft * 16 + id.tp) * 8, tag, lane);
  BAR(); BAR(); BAR();
  LAS char* stg = sm + STG + xw * STG_W;
  { const int tile = xw ? id.b : 256 + id.b - 32; const bool on = xw || (id.b >= 32 && id.b < 64);
    const int tb = tile / 9, db = tile - tb * 9, cc = lane & 7, rg = lane >> 3;
    u32x4 kk[11], vv[8], sh[4];
    if (on) {
      const rsrc_t rk = mkrs(P.k, S * D * 2), rv = mkrs(P.v, S * D * 2), rs = mkrs(P.shk, D * 4 * 2);
      const int t0 = 64 * tb + 8 * rg; const unsigned col = (64 * db + 8 * cc) * 2;
#pragma unroll
      for (int i = 0; i < 11; ++i) kk[i] = ld16<0>(rk, t0 + i - 3 >= 0 ? (unsigned)(t0 + i - 3) * (D * 2) + col : 0xF0000000u, 0);
#pragma unroll
      for (int i = 0; i < 8; ++i) vv[i] = ld16<0>(rv, t0 * (D * 2) + col, i * (D * 2));
#pragma unroll
      for (int i = 0; i < 4; ++i) sh[i] = ld16<0>(rs, col * 4, i * 16);
    }
    FENCE(); BAR(); BAR(); FENCE();
#pragma unroll
    for (int e = 0; e < 8; ++e) {
      unsigned o[4];
#pragma unroll
      for (int hf = 0; hf < 2; ++hf) {
        if (on) {
          float c[4], kf[7], y[4];
#pragma unroll
          for (int j = 0; j < 4; ++j) { const unsigned w = sh[e >> 1][(e & 1) * 2 + (j >> 1)]; c[j] = (j & 1) ? bhi(w) : blo(w); }
#pragma unroll
          for (int i = 0; i < 7; ++i) { const unsigned w = kk[4 * hf + i][e >> 1]; kf[i] = (e & 1) ? bhi(w) : blo(w); }
#pragma unroll
          for (int i = 0; i < 4; ++i) { const unsigned w = vv[4 * hf + i][e >> 1];
            float a = c[3] * kf[i]; asm volatile("" : "+v"(a)); a = fmaf(c[2], kf[i + 1], a); a = fmaf(c[1], kf[i + 2], a); a = fmaf(c[0], kf[i + 3], a);
            y[i] = a * ((e & 1) ? bhi(w) : blo(w)); asm volatile("" : "+v"(y[i])); }
          o[3 - 2 * hf] = pk(y[1], y[0]); o[2 - 2 * hf] = pk(y[3], y[2]);
          if (hf == 1) *(LAS u32x4*)(stg + (8 * cc + e) * 128 + (((7 - rg) ^ cc) * 16)) = (u32x4){o[0], o[1], o[2], o[3]};
        }
        FENCE(); BAR(); FENCE();
      }
    }
    if (on) {
#pragma unroll
      for (int i = 0; i < 8; ++i) { const u32x4 t = lds<u32x4>(stg + (8 * i + rg) * 128 + ((cc ^ i) * 16));
        st16<SC1>(t, rws, W_KVT + (64 * db + rg) * (S * 2) + (S - 64 * (tb + 1)) * 2 + cc * 16, 8 * i * (S * 2)); }
    }
    WAITL(0); BAR();
  }
  { const int unit = xw ? 256 + id.b : id.b; const bool on = !xw || id.b < 32;
    const int ks = unit >> 2, u = unit & 3;
    if (on) {
      const rsrc_t rwo = mkrs(P.wout, FH * D * 2);
#pragma unroll
      for (int i = 0; i < 9; ++i) { const int ci = 64 * i + lane, rho = ci / 18, ch = ci - rho * 18;
        const int f = ks < 64 ? 144 * (ks >> 2) + 32 * (ks & 3) + rho : 144 * (2 * (ks - 64) + (rho >> 4)) + 128 + (rho & 15);
        dma<0>(rwo, stg + i * 1024, f * (D * 2) + u * 288 + ch * 16, 0); }
    }
    FENCE(); BAR(); BAR();
    WAITV(0);
    if (on) {
      const int q = lane >> 4, qq = (lane & 15) >> 2, p = lane & 3;
      LAS char* base = stg + (4 * q + qq) * 288;
#pragma unroll
      for (int T = 0; T < 9; ++T) { const int cb = T < 4 ? 32 * (T >> 1) + 4 * (T & 1) : T == 4 ? 64 : 80 + 32 * ((T - 5) >> 1) + 4 * ((T - 5) & 1);
        LAS char* a = base + cb * 2 + p * (T == 4 ? 8 : 16);
        const bf16x8 fr = cat(ldtr(a), ldtr(a + 16 * 288));
        st16<SC1>(__builtin_bit_cast(u32x4, fr), rws, W_WOF + lane * 16, ((u * 72 + ks) * 9 + T) * 1024); }
    }
    FENCE(); BAR(); BAR();
    WAITV(0); BAR();
    if (xw == 0) set_flag(rws, W_F5 + id.b * 8, tag, lane);
  }
  STAMP(3);
  BAR(); BAR(); BAR(); BAR(); BAR();
  if (xw == 0) wait_flags<2>(rws, W_F5 + lane * 16, tag);
  BAR(); BAR();
  STAMP(4);
  { const rsrc_t rh = mkrs(P.ssmk, D * S * 2);
    const int nch = xw ? 1 : 2;
    for (int c = 0; c < nch; ++c) { const int ci = xw ? 1 : 2 * c, chn = ci == 2 ? 512 + (id.b >> 2) : 2 * id.b + ci;
      LAS char* cb = sm + CV + ci * CV_CH;
#pragma unroll
      for (int i = 0; i < 4; ++i) { dma<0>(rh, cb + 32 + i * 1024, lane * 16, chn * (S * 2) + i * 1024);
        dma<0>(rh, cb + CV_O + 32 + i * 1024, lane * 16 + 2, chn * (S * 2) + i * 1024);
        dma<SC1>(rws, cb + CV_U + i * 1024, lane * 16, W_KVT + chn * (S * 2) + i * 1024); }
      const unsigned h0 = P.ssmk[chn * S];
      if (lane < 8) { *(LAS unsigned*)(cb + lane * 4) = 0u; *(LAS unsigned*)(cb + CV_O + lane * 4) = lane == 7 ? h0 << 16 : 0u; }
      if (lane < 34) *(LAS u32x4*)(cb + CV_U + 4096 + lane * 16) = (u32x4){0u, 0u, 0u, 0u};
    }
    WAITV(0); WAITL(0); BAR();
  }
  BAR();
  if (xw == 0) { set_flag(rws, W_F3 + (id.tp * 16 + id.ft) * 8, tag, lane); wait_flags<1>(rws, W_F3 + id.tp * 128 + (lane & 7) * 16, tag); }
  STAMP(5);
  BAR();
  BAR(); BAR(); BAR();
  if (xw == 0) set_flag(rws, W_F4 + id.b * 8, tag, lane);
  BAR(); BAR(); BAR(); BAR(); BAR(); BAR();
  if (xw == 0) {
    wait_flags<2>(rws, W_F4 + lane * 16, tag);
    const int g = id.ft & 7, d0 = 144 * (g >> 1) + 64 * (g & 1);
#pragma unroll
    for (int i = 0; i < 10; ++i) dma<SC1>(rws, sm + YTILE + i * 1024, W_YT + (d0 + (lane >> 3)) * (S * 2) + (id.tp * 128 + 64 * (id.ft >> 3)) * 2 + (lane & 7) * 16, i * 8 * (S * 2));
    if (id.b == 0 && lane == 0) st8<SC1>((u32x2){(unsigned)(epoch + 1), (unsigned)((epoch + 1) >> 32)}, rws, W_EPOCH, 0);
  }
  STAMP(6);
  BAR(); BAR(); BAR(); BAR(); BAR(); BAR(); BAR();
  WAITV(0); BAR();
}

DEV void role_C(const Params& P, LAS char* sm, const Ids id, const int wid STAMP_ARG) {
  const int lane = id.lane, wt = wid >> 1, wf = wid & 1, c = lane & 15, q = lane >> 4;
  const rsrc_t rws = mkrs(P.ws, W_END);
  __builtin_amdgcn_s_setprio(1);
  f32x4 acc[4][4], accx[2];
  bf16x8 tf[2][4], ff[2][5];
  LAS char *ta[2][2], *fb[2], *fx[2];
#pragma unroll
  for (int s = 0; s < 2; ++s)
#pragma unroll
    for (int h = 0; h < 2; ++h) ta[s][h] = sm + (64 * wt + 32 * (h ^ wf) + c) * 128 + (((q + 4 * s) ^ ((c >> 1) & 7)) * 16);
  { const int o = (16 * (q >> 1) + 4 * (q & 1) + (c >> 2)) * 288 + 8 * (c & 3);
#pragma unroll
    for (int h = 0; h < 2; ++h) { fb[h] = sm + BBASE + h * 2 * BSLOT + o + wf * 128; fx[h] = sm + BBASE + h * 2 * BSLOT + o + 256; } }
  auto rd = [&](auto mode, int b, int slot, int s) {
    constexpr int MODE = decltype(mode)::value;
    if (MODE < 2) {
      const int o = (slot & 1) * BSLOT + 32 * s * 288;
#pragma unroll
      for (int j = 0; j < 4; ++j) ff[b][j] = cat(ldtr(fb[slot >> 1] + o + j * 32), ldtr(fb[slot >> 1] + o + j * 32 + 8 * 288));
    } else {
#pragma unroll
      for (int j = 0; j < 4; ++j) ff[b][j] = lds<bf16x8>(fb[slot >> 1] + (slot & 1) * BSLOT + (s * 9 + j) * 1024);
    }
#pragma unroll
    for (int i = 0; i < 4; ++i) tf[b][i] = lds<bf16x8>(ta[s][i >> 1] + slot * ASLOT + (i & 1) * 2048);
    if (MODE < 2) { const int o = (slot & 1) * BSLOT + 32 * s * 288; ff[b][4] = cat(ldtr(fx[slot >> 1] + o), ldtr(fx[slot >> 1] + o + 8 * 288)); }
    else ff[b][4] = lds<bf16x8>(fx[slot >> 1] + (slot & 1) * BSLOT + s * 9 * 1024);
  };
  auto mm = [&](auto mode, int b, int lo, int hi) {
    constexpr int MODE = decltype(mode)::value;
#pragma unroll
    for (int n = 0; n < 18; ++n) if (n >= lo && n < hi) {
      if (n < 16) { const int i = n >> 2, j = (i & 1) ? 3 - (n & 3) : (n & 3);
        acc[i][j] = MODE == 0 ? MFMA(tf[b][i], ff[b][j], acc[i][j]) : MFMA(ff[b][j], tf[b][i], acc[i][j]); }
      else accx[n - 16] = MODE == 0 ? MFMA(tf[b][n - 16], ff[b][4], accx[n - 16]) : MFMA(ff[b][4], tf[b][n - 16], accx[n - 16]);
    }
  };
  auto stage = [&](auto mode, int slot, bool drain) {
    constexpr int MODE = decltype(mode)::value;
    mm(mode, 1, 0, 4); SB();
    if (drain) WAITV(0);
    WAITL(0); BAR(); SB();
    rd(mode, 0, slot, 0); mm(mode, 1, 4, 18);
    if (MODE < 2) { for (int i = 0; i < 14; ++i) { SG(0x100, 1); SG(0x8, 1); } }
    else { for (int i = 0; i < 4; ++i) { SG(0x100, 1); SG(0x8, 1); SG(0x100, 1); SG(0x8, 2); } SG(0x100, 1); SG(0x8, 2); }
    SB();
    rd(mode, 1, slot, 1); mm(mode, 0, 0, 18);
    if (MODE < 2) { for (int i = 0; i < 4; ++i) { SG(0x100, 1); SG(0x8, 1); SG(0x100, 1); SG(0x8, 1); SG(0x100, 1); SG(0x8, 2); } SG(0x100, 1); SG(0x8, 1); SG(0x100, 1); SG(0x8, 1); }
    else { for (int i = 0; i < 9; ++i) { SG(0x100, 1); SG(0x8, 2); } }
    SB();
  };
  auto first = [&](auto mode, int slot) { WAITL(0); BAR(); SB(); rd(mode, 0, slot, 0); rd(mode, 1, slot, 1); mm(mode, 0, 0, 18); SB(); };
  auto zero = [&]() {
#pragma unroll
    for (int i = 0; i < 4; ++i)
#pragma unroll
      for (int j = 0; j < 4; ++j) acc[i][j] = (f32x4){0.f, 0.f, 0.f, 0.f};
    accx[0] = accx[1] = (f32x4){0.f, 0.f, 0.f, 0.f}; };
  const std::integral_constant<int, 0> M0; const std::integral_constant<int, 1> M1; const std::integral_constant<int, 2> M2;

  zero();
  first(M0, 0);
  STAMP(0);
#pragma unroll 1
  for (int it = 0; it < 2; ++it) { stage(M0, 1, false); stage(M0, 2, false); stage(M0, 3, false); stage(M0, 0, false); }
  mm(M0, 1, 0, 18); SB();
  STAMP(1);
  u32x4 z2p[2][4], z2x;
  { float cs[5];
#pragma unroll
    for (int j = 0; j < 5; ++j) cs[j] = lds<float>(sm + T_CS2 + ((j < 4 ? 4 * wf + j : 8) * 16 + c) * 4);
    float red[2][16];
#pragma unroll
    for (int i = 0; i < 4; ++i) {
      const int ro = (64 * wt + 32 * ((i >> 1) ^ wf) + 8 * q + 4 * (i & 1)) * 4;
      const f32x4 m = lds<f32x4>(sm + T_M1 + ro), r = lds<f32x4>(sm + T_R1 + ro);
      f32x4 su = {0.f, 0.f, 0.f, 0.f}, sq = {0.f, 0.f, 0.f, 0.f};
#pragma unroll
      for (int j = 0; j < (i < 2 ? 5 : 4); ++j) {
        const f32x4 z = gelu4(((j < 4 ? acc[i][j] : accx[i]) - m * cs[j]) * r);
        su += z; sq += z * z;
        u32x4& dst = j < 4 ? z2p[i >> 1][j] : z2x;
        dst[2 * (i & 1)] = pk(z[0], z[1]); dst[2 * (i & 1) + 1] = pk(z[2], z[3]);
      }
#pragma unroll
      for (int e = 0; e < 4; ++e) { red[0][4 * i + e] = su[e]; red[1][4 * i + e] = sq[e]; }
    }
    float r8[2][8], r4[2][4];
#pragma unroll
    for (int st = 0; st < 2; ++st) {
#pragma unroll
      for (int e = 0; e < 8; ++e) { const float a = red[st][e] + dpp<XOR8>(red[st][e]), b = red[st][8 + e] + dpp<XOR8>(red[st][8 + e]); r8[st][e] = (c & 8) ? b : a; }
#pragma unroll
      for (int e = 0; e < 4; ++e) { const float a = r8[st][e] + dpp<XOR7>(r8[st][e]), b = r8[st][4 + e] + dpp<XOR7>(r8[st][4 + e]); float t = (c & 4) ? b : a;
        t += dpp<XOR1>(t); t += dpp<XOR2>(t); r4[st][e] = t; }
    }
    if ((c & 3) == 0) { LAS char* pp = sm + STG + wf * 1024 + (64 * wt + 32 * ((c >> 3) ^ wf) + 8 * q + 4 * ((c >> 2) & 1)) * 8;
      *(LAS f32x4*)pp = (f32x4){r4[0][0], r4[1][0], r4[0][1], r4[1][1]}; *(LAS f32x4*)(pp + 16) = (f32x4){r4[0][2], r4[1][2], r4[0][3], r4[1][3]}; }
#pragma unroll
    for (int i = 0; i < 2; ++i) asm volatile("" : "+v"(z2p[i][0]), "+v"(z2p[i][1]), "+v"(z2p[i][2]), "+v"(z2p[i][3]));
    asm volatile("" : "+v"(z2x));
  }
  STAMP(2);
  zero();
  first(M1, 1);
  stage(M1, 2, false); stage(M1, 3, false); stage(M1, 0, false); stage(M1, 1, false);
  STAMP(3);
  {
#pragma unroll
    for (int i = 0; i < 2; ++i) asm volatile("" : "+v"(z2p[i][0]), "+v"(z2p[i][1]), "+v"(z2p[i][2]), "+v"(z2p[i][3]));
    asm volatile("" : "+v"(z2x));
#pragma unroll
    for (int ip = 0; ip < 2; ++ip) {
      const int ro = (64 * wt + 32 * (ip ^ wf) + 8 * q) * 4;
      const f32x4 r0 = lds<f32x4>(sm + T_R2 + ro), r1 = lds<f32x4>(sm + T_R2 + ro + 16), n0 = lds<f32x4>(sm + T_N2 + ro), n1 = lds<f32x4>(sm + T_N2 + ro + 16);
      const unsigned so = W_ZF + ((id.ft * 64 + 4 * id.tp + 2 * wt + (ip ^ wf)) * 9) * 1024;
#pragma unroll
      for (int j = 0; j < (ip == 0 ? 5 : 4); ++j) { const u32x4 z = j < 4 ? z2p[ip][j] : z2x;
        const f32x4 a = (f32x4){blo(z[0]), bhi(z[0]), blo(z[1]), bhi(z[1])} * r0 + n0, b = (f32x4){blo(z[2]), bhi(z[2]), blo(z[3]), bhi(z[3])} * r1 + n1;
        st16<SC1>((u32x4){pk(a[0], a[1]), pk(a[2], a[3]), pk(b[0], b[1]), pk(b[2], b[3])}, rws, lane * 16, so + (j < 4 ? 4 * wf + j : 8) * 1024); }
    }
    SB();
  }
  unsigned braw[4];
  { const rsrc_t rb = mkrs(P.bsp, S * 2);
#pragma unroll
    for (int i = 0; i < 4; ++i) braw[i] = __builtin_amdgcn_raw_buffer_load_b16(rb, (id.tp * 128 + 64 * wt + 16 * (i ^ (2 * wf)) + c) * 2, 0, 0); }
  STAMP(4);
  stage(M1, 2, false); stage(M1, 3, false); stage(M1, 0, true); stage(M1, 1, false);
  mm(M1, 1, 0, 18); SB();
  STAMP(5);
  u32x2 z1p[4][4], z1x[2];
  { float bias[4];
#pragma unroll
    for (int i = 0; i < 4; ++i) bias[i] = blo(braw[i]);
    f32x4 cs[5];
#pragma unroll
    for (int j = 0; j < 5; ++j) cs[j] = lds<f32x4>(sm + T_CS1 + ((j < 4 ? 4 * wf + j : 8) * 16 + 4 * q) * 4);
#pragma unroll
    for (int i = 0; i < 4; ++i) { const int ro = (64 * wt + 16 * (i ^ (2 * wf)) + c) * 4;
      const float m = lds<float>(sm + T_M1 + ro), r = lds<float>(sm + T_R1 + ro);
#pragma unroll
      for (int j = 0; j < (i < 2 ? 5 : 4); ++j) {
        f32x4& a = j < 4 ? acc[i][j] : accx[i];
        const f32x4 z = gelu4((a - m * cs[j]) * r);
        (j < 4 ? z1p[i][j] : z1x[i]) = (u32x2){pk(z[0], z[1]), pk(z[2], z[3])};
        a = (f32x4){bias[i], bias[i], bias[i], bias[i]};
      } }
#pragma unroll
    for (int h = 0; h < 2; ++h) { fb[h] = sm + BBASE + h * 2 * BSLOT + lane * 16 + wf * 4096; fx[h] = sm + BBASE + h * 2 * BSLOT + lane * 16 + 8192; }
#pragma unroll
    for (int i = 0; i < 4; ++i) asm volatile("" : "+v"(z1p[i][0]), "+v"(z1p[i][1]), "+v"(z1p[i][2]), "+v"(z1p[i][3]), "+v"(acc[i][0]), "+v"(acc[i][1]), "+v"(acc[i][2]), "+v"(acc[i][3]));
    asm volatile("" : "+v"(z1x[0]), "+v"(z1x[1]), "+v"(accx[0]), "+v"(accx[1]));
  }
  STAMP(6);
  first(M2, 2);
  STAMP(7);
#pragma unroll 1
  for (int it = 0; it < 7; ++it) { stage(M2, 3, false); stage(M2, 0, false); stage(M2, 1, false); stage(M2, 2, false); }
  stage(M2, 3, false); stage(M2, 0, false); stage(M2, 1, false);
  mm(M2, 1, 0, 4); SB(); WAITL(0); BAR(); SB();
  mm(M2, 1, 4, 18); SB();
  STAMP(8);
#pragma unroll
  for (int i = 0; i < 4; ++i) asm volatile("" : "+v"(z1p[i][0]), "+v"(z1p[i][1]), "+v"(z1p[i][2]), "+v"(z1p[i][3]));
  asm volatile("" : "+v"(z1x[0]), "+v"(z1x[1]));
#pragma unroll
  for (int i = 0; i < 4; ++i) {
    const unsigned so = W_HF + (id.tp * 8 + 4 * wt + (i ^ (2 * wf))) * 72 * 1024;
    auto gate = [&](u32x2 z, f32x4 a) { const f32x4 h = (f32x4){blo(z.x), bhi(z.x), blo(z.y), bhi(z.y)} * a; return (u32x2){pk(h[0], h[1]), pk(h[2], h[3])}; };
#pragma unroll
    for (int pp = 0; pp < 2; ++pp) { const u32x2 lo = gate(z1p[i][2 * pp], acc[i][2 * pp]), hi = gate(z1p[i][2 * pp + 1], acc[i][2 * pp + 1]);
      st16<SC1>((u32x4){lo.x, lo.y, hi.x, hi.y}, rws, lane * 16, so + (4 * id.ft + 2 * wf + pp) * 1024); }
    if (i < 2) st8<SC1>(gate(z1x[i], accx[i]), rws, lane * 16 + (id.ft & 1) * 8, so + (64 + (id.ft >> 1)) * 1024);
  }
  SB(); BAR(); SB();
  STAMP(9);
  f32x4 cm[8];
  { const unsigned dm = ((const unsigned*)P.ssmd)[id.b], dsh = ((const unsigned*)P.ssmd)[256 + (id.b >> 3)];
    const float dvm = wt ? bhi(dm) : blo(dm), dvs = (id.b & 4) ? bhi(dsh) : blo(dsh);
    const int aoff = ((c & 1) ? 0 : CV_O) + (4 * q + ((c + 1) >> 1)) * 4, boff = CV_U + (2032 - 16 * c + 8 * q) * 2;
    auto afrag = [&](LAS char* p) { const unsigned d0 = lds<unsigned>(p), d1 = lds<unsigned>(p + 4), d2 = lds<unsigned>(p + 8), d3 = lds<unsigned>(p + 12);
      return __builtin_bit_cast(bf16x8, (u32x4){d0, d1, d2, d3}); };
    { const int jq = id.b & 3; LAS char* cb = sm + CV + 2 * CV_CH;
      f32x4 s0 = {0.f, 0.f, 0.f, 0.f}, s1 = s0;
#pragma unroll
      for (int kk = 0; kk < 2; ++kk) { const int k0 = 2 * wid + kk;
#pragma unroll
        for (int e = 0; e < 8; ++e) { const bf16x8 a = afrag(cb + aoff + (k0 + 8 * e) * 64);
          const int o1 = 7 - jq - e >= 0 ? 64 * k0 - 512 * (7 - jq - e) : 512;
          s1 = MFMA(a, lds<bf16x8>(cb + boff + o1), s1);
          if (e < 4) { const int o0 = jq - e >= 0 ? 64 * k0 - 512 * (jq - e) : 512; s0 = MFMA(a, lds<bf16x8>(cb + boff + o0), s0); } } }
      *(LAS f32x4*)(sm + CV_PS + (wid * 2) * 1024 + lane * 16) = s0; *(LAS f32x4*)(sm + CV_PS + (wid * 2 + 1) * 1024 + lane * 16) = s1; }
    LAS char* cb = sm + CV + wt * CV_CH;
#pragma unroll
    for (int j = 0; j < 8; ++j) cm[j] = (f32x4){0.f, 0.f, 0.f, 0.f};
    auto block = [&](int k0) { bf16x8 a[8], bb[8];
#pragma unroll
      for (int e = 0; e < 8; ++e) { bb[e] = lds<bf16x8>(cb + boff + 64 * k0 - 512 * e); a[e] = afrag(cb + aoff + (k0 + 8 * e) * 64); }
#pragma unroll
      for (int e = 0; e < 8; ++e)
#pragma unroll
        for (int j = e; j < 8; ++j) cm[j] = MFMA(a[e], bb[j - e], cm[j]); };
    block(4 * wf);
    SB(); WAITV(0); WAITL(0); BAR(); SB();
    STAMP(10);
    f32x4 ys = {0.f, 0.f, 0.f, 0.f};
    if (wid < 2) {
#pragma unroll
      for (int w = 0; w < 4; ++w) ys += lds<f32x4>(sm + CV_PS + (w * 2 + wid) * 1024 + lane * 16);
    }
#pragma unroll 1
    for (int kk = 1; kk < 4; ++kk) block(4 * wf + kk);
    LAS char* pm = sm + (wt ? CV_PM23 : CV + 2 * CV_CH);
#pragma unroll
    for (int j = 0; j < 4; ++j) { f32x4 lo = cm[j], hi = cm[4 + j]; asm volatile("" : "+v"(lo), "+v"(hi));
      *(LAS f32x4*)(pm + wf * 4096 + j * 1024 + lane * 16) = wf ? lo : hi; cm[j] = wf ? hi : lo; }
    SB(); WAITL(0); BAR(); SB();
    STAMP(11);
    const int half = id.ft >> 3;
    const unsigned hso = W_HF + (id.tp * 8 + 4 * half + wid) * 72 * 1024;
    u32x4 hb[12];
#pragma unroll
    for (int i = 0; i < 12; ++i) hb[i] = ld16<SC1>(rws, lane * 16, hso + i * 1024);
    auto yout = [&](f32x4 a, int chn, LAS char* ub, int j, float dv) {
      const u32x2 u = lds<u32x2>(ub + CV_U + (2044 - 256 * j - 16 * c - 4 * q) * 2);
      const f32x4 y = a + (f32x4){bhi(u.y), blo(u.y), bhi(u.x), blo(u.x)} * dv;
      st8<SC1>((u32x2){pk(y[0], y[1]), pk(y[2], y[3])}, rws, (16 * c + 4 * q) * 2, W_YT + chn * (S * 2) + j * 512); };
#pragma unroll
    for (int j = 0; j < 4; ++j) yout(cm[j] + lds<f32x4>(pm + (wf ^ 1) * 4096 + j * 1024 + lane * 16), 2 * id.b + wt, cb, 4 * wf + j, dvm);
    if (wid < 2) yout(ys, 512 + (id.b >> 2), sm + CV + 2 * CV_CH, wid ? 7 - (id.b & 3) : (id.b & 3), dvs);
    const int g = id.ft & 7, dp = 144 * (g >> 1) + 80 * (g & 1), ds = 144 * (g >> 1) + 64, trow = 64 * half + 16 * wid + c;
    const rsrc_t rx = mkrs(P.x, S * D * 2);
    const unsigned xo = (id.tp * 128 + trow) * (D * 2);
    const u32x4 x0 = ld16<0>(rx, xo + q * 16, dp * 2), x1 = ld16<0>(rx, xo + q * 16, dp * 2 + 64);
    const u32x2 x2 = __builtin_amdgcn_raw_buffer_load_b64(rx, xo + q * 8, ds * 2, 0);
    SB();
    f32x4 o[5];
#pragma unroll
    for (int t = 0; t < 5; ++t) o[t] = (f32x4){0.f, 0.f, 0.f, 0.f};
    LAS char* wb[2] = {sm + lane * 16, sm + 3 * G3SLOT + lane * 16};
    bf16x8 wq[2][5];
    auto rdw = [&](int b, int slot, int kk) {
#pragma unroll
      for (int t = 0; t < 5; ++t) wq[b][t] = lds<bf16x8>(wb[slot / 3] + (slot % 3) * G3SLOT + (kk * 5 + t) * 1024); };
    rdw(0, 0, 0);
#pragma unroll 1
    for (int it = 0; it < 3; ++it) {
#pragma unroll
      for (int sl = 0; sl < 6; ++sl)
#pragma unroll
        for (int kk = 0; kk < 4; ++kk) { const int n = sl * 4 + kk;
          if (kk == 3) { if (sl == 5 && it == 2) {} else { SB(); if (sl == 2 && it == 0) WAITV(12); WAITL(0); BAR(); SB(); rdw((n + 1) & 1, sl == 5 ? 0 : sl + 1, 0); } }
          else rdw((n + 1) & 1, sl, kk + 1);
          const bf16x8 hf = __builtin_bit_cast(bf16x8, hb[n % 12]);
#pragma unroll
          for (int t = 0; t < 5; ++t) o[t] = MFMA(wq[n & 1][t], hf, o[t]);
          hb[n % 12] = ld16<SC1>(rws, lane * 16, it * 24 + n + 12 < 72 ? hso + (it * 24 + n + 12) * 1024 : 0xF0000000u);
          SB();
        }
    }
    STAMP(12);
    const float m = lds<float>(sm + T_M1 + trow * 4), r = lds<float>(sm + T_R1 + trow * 4);
    LAS char* yb = sm + YTILE + (c >> 2) * 128 + (16 * wid + 4 * (c & 3)) * 2;
    const rsrc_t ro = mkrs(P.out, S * D * 2);
    auto fin = [&](u32x2 xv, bf16x4 yv, f32x4 a) { const u32x2 yu = __builtin_bit_cast(u32x2, yv);
      const f32x4 xf = {blo(xv.x), bhi(xv.x), blo(xv.y), bhi(xv.y)}, yf = {blo(yu.x), bhi(yu.x), blo(yu.y), bhi(yu.y)};
      const f32x4 t = xf + (xf - m) * r * yf + a; return (u32x2){pk(t[0], t[1]), pk(t[2], t[3])}; };
    const int yp = 16 * (g & 1), ysh = 64 * ((g & 1) ^ 1);
#pragma unroll
    for (int pp = 0; pp < 2; ++pp) { const u32x4 xv = pp ? x1 : x0;
      const u32x2 lo = fin(xv.xy, ldtr(yb + (yp + 32 * pp + 8 * q) * 128), o[2 * pp]), hi = fin(xv.zw, ldtr(yb + (yp + 32 * pp + 8 * q + 4) * 128), o[2 * pp + 1]);
      st16<0>((u32x4){lo.x, lo.y, hi.x, hi.y}, ro, xo + q * 16, dp * 2 + 64 * pp); }
    const u32x2 ls = fin(x2, ldtr(yb + (ysh + 4 * q) * 128), o[4]);
    if (wt == (g & 1)) st8<0>(ls, ro, xo + q * 8, ds * 2);
    STAMP(13);
  }
}
}

extern "C" __global__ __launch_bounds__(512, 2) void h3_gmlp_fused(const Params P) {
  extern __shared__ __attribute__((aligned(16))) char smem_[];
  LAS char* sm = (LAS char*)smem_;
  const int wid = __builtin_amdgcn_readfirstlane(threadIdx.x >> 6);
  Ids id; id.b = blockIdx.x; id.lane = threadIdx.x & 63;
  const int xcd = id.b & 7, loc = id.b >> 3;
  id.tp = (xcd >> 1) * 4 + (loc >> 3); id.ft = (xcd & 1) * 8 + (loc & 7);
  if (wid == 0) {
    unsigned lo, hi, bytes, sink;
    asm volatile("s_getpc_b64 vcc\n1:\n\ts_sub_u32 %0, vcc_lo, 1b-h3_gmlp_fused\n\ts_subb_u32 %1, vcc_hi, 0\n\ts_mov_b32 %2, h3_gmlp_end-h3_gmlp_fused" : "=s"(lo), "=s"(hi), "=s"(bytes) :: "vcc", "scc");
    const rsrc_t rc = mkrs((const void*)(((u64)hi << 32) | lo), bytes);
    asm volatile("buffer_load_dword %0, %1, %2, 0 offen\n\ts_waitcnt vmcnt(0)" : "=&v"(sink) : "v"((id.lane < 16 ? id.lane * 32 + loc : 0x100000) * 128), "s"(rc) : "memory");
  }
  if (wid == 4 || wid == 5) role_L(P, sm, id, wid - 4 STAMP_PASS);
  else if (wid >= 6) role_X(P, sm, id, wid - 6 STAMP_PASS);
  else role_C(P, sm, id, wid STAMP_PASS);
}
extern "C" __global__ void h3_gmlp_end() {}

extern "C" void kernel_launch(void* const* d_in, const int* in_sizes, int n_in, void* d_out, int out_size, void* d_ws, size_t ws_size, hipStream_t stream) {
  Params p;
  p.x = (const u16*)d_in[0]; p.k = (const u16*)d_in[1]; p.v = (const u16*)d_in[2]; p.shk = (const u16*)d_in[3]; p.ssmk = (const u16*)d_in[4]; p.ssmd = (const u16*)d_in[5];
  p.win = (const u16*)d_in[7]; p.wsp = (const u16*)d_in[8]; p.bsp = (const u16*)d_in[9]; p.wout = (const u16*)d_in[10];
  p.out = (u16*)d_out; p.ws = (char*)d_ws;
  (void)hipFuncSetAttribute((const void*)h3_gmlp_fused, hipFuncAttributeMaxDynamicSharedMemorySize, LDS_BYTES);
  h3_gmlp_fused<<<256, 512, LDS_BYTES, stream>>>(p);
}
```

```cpp
#define MK_STAMP
#define MK_AMP 0
#define MK_K 1
#include <hip/hip_runtime.h>
#include <hip/hip_bf16.h>
#include <type_traits>

namespace {
typedef unsigned short u16;
typedef unsigned long long u64;
typedef short bf16x8 __attribute__((ext_vector_type(8)));
typedef short bf16x4 __attribute__((ext_vector_type(4)));
typedef __bf16 bf2 __attribute__((ext_vector_type(2)));
typedef float f32x2 __attribute__((ext_vector_type(2)));
typedef float f32x4 __attribute__((ext_vector_type(4)));
typedef unsigned u32x2 __attribute__((ext_vector_type(2)));
typedef unsigned u32x4 __attribute__((ext_vector_type(4)));
typedef __amdgpu_buffer_rsrc_t rsrc_t;
#define LAS __attribute__((address_space(3)))
#define DEV __device__ __forceinline__

constexpr int S = 2048, D = 576, F = 4608, FH = 2304;
constexpr int ASLOT = 16384, BSLOT = 18432, BBASE = 4 * ASLOT;
constexpr int STG = 139264, STG_W = 9984;
constexpr int T_M1 = 159232, T_R1 = T_M1 + 512, T_CS2 = T_R1 + 512, T_CS1 = T_CS2 + 576, T_N2 = T_CS1 + 576, T_R2 = T_N2 + 512, T_TAG = T_R2 + 512;
constexpr int LDS_BYTES = 163840;
constexpr int G3SLOT = 20480;
constexpr int CV = 102400, CV_CH = 13056, CV_O = 4160, CV_U = 8320;
constexpr int CV_PS = 141568, CV_PM23 = 149760;
constexpr int YTILE = 122880;
constexpr int W_EPOCH = 0, W_F2 = 4096, W_F3 = 8192, W_F4 = 12288, W_F5 = 16384;
constexpr int W_G0X = 0x8000, W_G0C = 0x10000, W_G1S = 0x60000;
constexpr int W_KVT = 1 << 20, W_YT = 4 << 20, W_WOF = 7 << 20, W_ZF = 10 << 20, W_HF = 20 << 20, W_END = 30 << 20;
constexpr int SC1 = 16;

#define BAR() do { asm volatile("" ::: "memory"); __builtin_amdgcn_s_barrier(); asm volatile("" ::: "memory"); } while (0)
#define SB() __builtin_amdgcn_sched_barrier(0)
#define SG(m, n) __builtin_amdgcn_sched_group_barrier(m, n, 0)
#define WAITV(n) asm volatile("s_waitcnt vmcnt(%0)" ::"i"(n) : "memory")
#define WAITL(n) asm volatile("s_waitcnt lgkmcnt(%0)" ::"i"(n) : "memory")
#define FENCE() asm volatile("" ::: "memory")
#define MFMA(a, b, c) __builtin_amdgcn_mfma_f32_16x16x32_bf16(a, b, c, 0, 0, 0)
#define STAMP(i) do { const unsigned t_ = (unsigned)__builtin_amdgcn_s_memrealtime(); stampv = lane == (i) ? t_ : stampv; asm volatile("" : "+v"(stampv)); } while (0)
#define LBAR() do { BAR(); const int bi_ = bcount - (lw ? 0 : 40); ++bcount; if (bi_ >= 0 && bi_ < 60) STAMP(bi_); } while (0)
#define STAMP_ARG , unsigned& stampv
#define STAMP_PASS , stampv

struct Params {
  const u16 *x, *k, *v, *shk, *ssmk, *ssmd, *win, *wsp, *bsp, *wout;
  u16* out; char* ws;
};

DEV rsrc_t mkrs(const void* p, unsigned bytes) { return __builtin_amdgcn_make_buffer_rsrc((void*)p, 0, bytes, 0x00020000); }
template <int AUX> DEV void dma(rsrc_t rs, LAS char* l, unsigned voff, unsigned soff) { __builtin_amdgcn_raw_ptr_buffer_load_lds(rs, (LAS void*)l, 16, voff, soff, 0, AUX); }
template <int AUX> DEV u32x4 ld16(rsrc_t rs, unsigned voff, unsigned soff) { return __builtin_amdgcn_raw_buffer_load_b128(rs, voff, soff, AUX); }
template <int AUX> DEV void st16(u32x4 v, rsrc_t rs, unsigned voff, unsigned soff) { __builtin_amdgcn_raw_buffer_store_b128(v, rs, voff, soff, AUX); }
template <int AUX> DEV void st8(u32x2 v, rsrc_t rs, unsigned voff, unsigned soff) { __builtin_amdgcn_raw_buffer_store_b64(v, rs, voff, soff, AUX); }
template <class T> DEV T lds(LAS char* p) { return *(LAS T*)p; }
DEV bf16x4 ldtr(LAS char* p) { return __builtin_amdgcn_ds_read_tr16_b64_v4i16((LAS bf16x4*)p); }
DEV bf16x8 cat(bf16x4 a, bf16x4 b) { return __builtin_shufflevector(a, b, 0, 1, 2, 3, 4, 5, 6, 7); }
DEV float blo(unsigned u) { return __uint_as_float(u << 16); }
DEV float bhi(unsigned u) { return __uint_as_float(u & 0xffff0000u); }
DEV unsigned pk(float a, float b) { return __builtin_bit_cast(unsigned, __builtin_convertvector((f32x2){a, b}, bf2)); }
DEV float dot2(unsigned a, unsigned b, float c) { return __builtin_amdgcn_fdot2_f32_bf16(__builtin_bit_cast(bf2, a), __builtin_bit_cast(bf2, b), c, false); }
template <int CTRL> DEV float dpp(float x) { return __builtin_bit_cast(float, __builtin_amdgcn_mov_dpp(__builtin_bit_cast(int, x), CTRL, 0xf, 0xf, true)); }
constexpr int XOR1 = 0xB1, XOR2 = 0x4E, XOR7 = 0x141, XOR8 = 0x128;
DEV f32x2 gelu2(f32x2 x) {
  const f32x2 a = x * (x * x * -0.10294324f + -2.30220819f);
  f32x2 e; e.x = __builtin_amdgcn_exp2f(a.x); e.y = __builtin_amdgcn_exp2f(a.y);
  e = e + 1.0f;
  f32x2 r; r.x = __builtin_amdgcn_rcpf(e.x); r.y = __builtin_amdgcn_rcpf(e.y);
  return x * r;
}
DEV f32x4 gelu4(f32x4 x) { const f32x2 a = gelu2(x.xy), b = gelu2(x.zw); return (f32x4){a.x, a.y, b.x, b.y}; }
DEV u32x4 seal(float a, float b, u32x2 tag) { const unsigned ua = __float_as_uint(a), ub = __float_as_uint(b); return (u32x4){ua, ub, ua ^ tag.x, ub ^ tag.y}; }
DEV bool sealed(u32x4 g, u32x2 tag) { return (g.x ^ g.z) == tag.x && (g.y ^ g.w) == tag.y; }
DEV bool expired(u64 t0) { return __builtin_amdgcn_s_memrealtime() - t0 > 2000000ull; }
template <int NL> DEV void wait_flags(rsrc_t ws, unsigned voff, u32x2 tag) {
  const u64 t0 = __builtin_amdgcn_s_memrealtime();
  for (;;) {
    bool ok = true;
#pragma unroll
    for (int i = 0; i < NL; ++i) { const u32x4 f = ld16<SC1>(ws, voff, i * 1024); ok &= f.x == tag.x && f.y == tag.y && f.z == tag.x && f.w == tag.y; }
    if (__all(ok) || expired(t0)) break;
    FENCE();
  }
  FENCE();
}
DEV void set_flag(rsrc_t ws, unsigned off, u32x2 tag, int lane) { if (lane == 0) st8<SC1>(tag, ws, off, 0); }

struct Ids { int b, tp, ft, lane; };

DEV void role_L(const Params& P, LAS char* sm, const Ids id, const int lw STAMP_ARG) {
  int bcount = 0; (void)bcount;
  const int lane = id.lane, rr = lane >> 3, sl = lane & 7;
  const rsrc_t rx = mkrs(P.x, S * D * 2), rwin = mkrs(P.win, D * F * 2), rwsp = mkrs(P.wsp, S * S * 2), rws = mkrs(P.ws, W_END);
  const unsigned swz = (sl ^ (4 * lw + (rr >> 1))) * 16;
  const unsigned vxa = (16 * lw + 8 * (rr >> 2) + (rr & 3)) * (D * 2) + swz;
  unsigned vb[9];
#pragma unroll
  for (int i = 0; i < 9; ++i) { const int ci = 64 * (2 * i + lw) + lane, rho = ci / 18, ch = ci - rho * 18;
    const int kr = (rho & ~12) | ((rho & 4) << 1) | ((rho & 8) >> 1); vb[i] = kr * (F * 2) + ch * 16; }
  auto issue1 = [&](int n, int ks, bool passA, unsigned vx) {
    LAS char* a = sm + (n & 3) * ASLOT + lw * 1024; LAS char* bb = sm + BBASE + (n & 3) * BSLOT + lw * 1024;
    const unsigned sa = id.tp * 128 * (D * 2) + ks * 128, sb = ks * 64 * (F * 2) + ((passA ? FH : 0) + id.ft * 144) * 2;
#pragma unroll
    for (int i = 0; i < 9; ++i) {
      if (i < 8) dma<0>(rx, a + i * 2048, vx, sa + (passA ? 32 * (i >> 1) + 4 * (i & 1) : 16 * i) * (D * 2));
      dma<0>(rwin, bb + i * 2048, vb[i], sb);
    }
  };
  for (int n = 0; n < 3; ++n) issue1(n, n, true, vxa);
  STAMP(60);
  const unsigned vxb = (8 * lw + rr) * (D * 2) + swz, vw = (8 * lw + rr) * (S * 2) + swz, vl = lane * 16 + lw * 1024;
  auto issue2a = [&](int n) { LAS char* a = sm + (n & 3) * ASLOT + lw * 1024; const unsigned sa = id.tp * 128 * (S * 2) + (n - 18) * 128;
#pragma unroll
    for (int i = 0; i < 8; ++i) dma<0>(rwsp, a + i * 2048, vw, sa + 16 * i * (S * 2)); };
  auto issue2b = [&](int n) { LAS char* bb = sm + BBASE + (n & 3) * BSLOT + lw * 1024; const unsigned sb = W_ZF + (id.ft * 576 + (n - 18) * 18) * 1024;
#pragma unroll
    for (int i = 0; i < 9; ++i) dma<SC1>(rws, bb + i * 2048, vl, sb + i * 2048); };
#pragma unroll 1
  for (int n = 0; n < 6; ++n) { WAITV(34); LBAR(); issue1(n + 3, n + 3, true, vxa); }
#pragma unroll 1
  for (int n = 6; n < 15; ++n) { WAITV(34); LBAR(); issue1(n + 3, n - 6, false, vxb); }
  WAITV(34); LBAR(); issue2a(18);
  WAITV(25); LBAR(); issue2a(19);
  WAITV(16); LBAR(); issue2a(20);
  { const u32x2 tag = lds<u32x2>(sm + T_TAG); wait_flags<1>(rws, W_F2 + id.ft * 128 + (lane & 7) * 16, tag); }
  STAMP(61);
  issue2b(18); issue2b(19); issue2b(20);
  WAITV(18); LBAR(); issue2a(21); issue2b(21);
  WAITV(26); LBAR(); issue2a(22); issue2b(22);
#pragma unroll 1
  for (int n = 20; n < 47; ++n) { WAITV(34); LBAR(); issue2a(n + 3); issue2b(n + 3); }
  WAITV(34); LBAR(); WAITV(17); LBAR(); WAITV(0); LBAR();
  LBAR();
  const int g = id.ft & 7, u = g >> 1;
  auto issue3 = [&](int m, int slot) { LAS char* a = sm + slot * G3SLOT + lw * 1024; const unsigned sb = W_WOF + (u * 72 + 4 * m) * 9 * 1024;
#pragma unroll
    for (int i = 0; i < 10; ++i) { const int pc = 2 * i + lw, kk = pc / 5, tt = pc - kk * 5, tile = (tt == 4) ? 4 : tt + (g & 1) * 5;
      dma<SC1>(rws, a + i * 2048, lane * 16, sb + (kk * 9 + tile) * 1024); } };
  for (int m = 0; m < 5; ++m) issue3(m, m);
  LBAR(); LBAR();
  WAITV(40); LBAR();
  WAITV(30); LBAR(); issue3(5, 5); issue3(6, 0);
  int slot = 1;
#pragma unroll 1
  for (int m = 2; m < 13; ++m) { WAITV(40); LBAR(); issue3(m + 5, slot); slot = slot == 5 ? 0 : slot + 1; }
  WAITV(40); LBAR(); WAITV(30); LBAR(); WAITV(20); LBAR(); WAITV(10); LBAR(); WAITV(0); LBAR();
}

DEV void role_X(const Params& P, LAS char* sm, const Ids id, const int xw STAMP_ARG) {
  const int lane = id.lane;
  const rsrc_t rws = mkrs(P.ws, W_END);
  u32x2 tag; u64 epoch;
  { const u32x2 e = __builtin_amdgcn_raw_buffer_load_b64(rws, W_EPOCH, 0, SC1);
    epoch = ((u64)__builtin_amdgcn_readfirstlane(e.y) << 32) | __builtin_amdgcn_readfirstlane(e.x);
    const u64 t = ((epoch + 1) * 0x9E3779B97F4A7C15ull) | 1ull; tag.x = (unsigned)t; tag.y = (unsigned)(t >> 32); }
  if (xw == 0) {
    const rsrc_t rx = mkrs(P.x, S * D * 2);
    const int row = id.tp * 128 + id.ft * 8 + (lane >> 3);
    u32x4 v[9];
#pragma unroll
    for (int i = 0; i < 9; ++i) v[i] = ld16<0>(rx, row * (D * 2) + (lane & 7) * 16, i * 128);
    if (lane == 0) *(LAS u32x2*)(sm + T_TAG) = tag;
    WAITL(0); BAR();
    float s = 0.f, q = 0.f; unsigned ones = 0x3f803f80u; asm volatile("" : "+v"(ones));
#pragma unroll
    for (int i = 0; i < 9; ++i)
#pragma unroll
      for (int e = 0; e < 4; ++e) { s = dot2(v[i][e], ones, s); q = dot2(v[i][e], v[i][e], q); }
    s += dpp<XOR1>(s); q += dpp<XOR1>(q); s += dpp<XOR2>(s); q += dpp<XOR2>(q); s += dpp<XOR7>(s); q += dpp<XOR7>(q);
    const float m = s * (1.0f / D), var = fmaxf(q * (1.0f / D) - m * m, 0.f), r = __builtin_amdgcn_rsqf(var + 1e-5f);
    if ((lane & 7) == 0) st16<SC1>(seal(m, r, tag), rws, W_G0X + row * 16, 0);
    BAR();
  } else {
    const rsrc_t rwin = mkrs(P.win, D * F * 2);
    const int rg = lane / 18, ch = lane - rg * 18;
    const unsigned vo = lane < 54 ? ((id.tp >> 1) * 72 + rg) * (F * 2) + (((id.tp & 1) ? 0 : FH) + id.ft * 144) * 2 + ch * 16 : 0xF0000000u;
    u32x4 v[24];
#pragma unroll
    for (int i = 0; i < 24; ++i) v[i] = ld16<0>(rwin, vo, i * 3 * (F * 2));
    BAR();
    float a[8]; unsigned selo = 0x00003f80u, sehi = 0x3f800000u; asm volatile("" : "+v"(selo), "+v"(sehi));
#pragma unroll
    for (int e = 0; e < 8; ++e) a[e] = 0.f;
#pragma unroll
    for (int i = 0; i < 24; ++i)
#pragma unroll
      for (int e = 0; e < 4; ++e) { a[2 * e] = dot2(v[i][e], selo, a[2 * e]); a[2 * e + 1] = dot2(v[i][e], sehi, a[2 * e + 1]); }
#pragma unroll
    for (int e = 0; e < 8; ++e) a[e] += __shfl(a[e], lane + 18) + __shfl(a[e], lane + 36);
    if (lane < 18) {
#pragma unroll
      for (int e = 0; e < 4; ++e) st16<SC1>(seal(a[2 * e], a[2 * e + 1], tag), rws, W_G0C + ((id.ft * 16 + id.tp) * 72 + ch * 4 + e) * 16, 0);
    }
    BAR();
  }
  STAMP(0);
  BAR(); BAR(); BAR();
  if (xw == 0) {
    const u64 t0 = __builtin_amdgcn_s_memrealtime();
    int round = 0;
    for (;;) {
      u32x4 gx[2], gc[18];
#pragma unroll
      for (int i = 0; i < 2; ++i) gx[i] = ld16<SC1>(rws, W_G0X + (id.tp * 128 + lane) * 16, i * 1024);
#pragma unroll
      for (int i = 0; i < 18; ++i) gc[i] = ld16<SC1>(rws, W_G0C + (id.ft * 1152 + lane) * 16, i * 1024);
      if (round == 0) { FENCE(); BAR(); BAR(); FENCE(); }
      bool ok = sealed(gx[0], tag) && sealed(gx[1], tag);
#pragma unroll
      for (int i = 0; i < 18; ++i) ok &= sealed(gc[i], tag);
      ++round;
      if (__all(ok) || expired(t0)) {
#pragma unroll
        for (int i = 0; i < 2; ++i) { *(LAS unsigned*)(sm + T_M1 + (64 * i + lane) * 4) = gx[i].x; *(LAS unsigned*)(sm + T_R1 + (64 * i + lane) * 4) = gx[i].y; }
#pragma unroll
        for (int i = 0; i < 18; ++i) *(LAS u32x2*)(sm + STG + 4096 + (64 * i + lane) * 8) = gc[i].xy;
        break;
      }
      FENCE();
    }
    WAITL(0);
#pragma unroll
    for (int hz = 0; hz < 2; ++hz)
#pragma unroll
      for (int pass = 0; pass < 2; ++pass) { const int cp = pass * 64 + lane;
        if (cp < 72) { f32x2 a = {0.f, 0.f};
#pragma unroll
          for (int e = 0; e < 8; ++e) a += lds<f32x2>(sm + STG + 4096 + ((2 * e + hz) * 72 + cp) * 8);
          *(LAS f32x2*)(sm + (hz ? T_CS1 : T_CS2) + cp * 8) = a; } }
    WAITL(0); BAR();
  } else { BAR(); BAR(); BAR(); }
  STAMP(1);
  BAR();
  unsigned sink = 0;
  if (xw == 1) {
    auto touch = [&](const void* p, unsigned per, unsigned lines) { const rsrc_t r = mkrs(p, per * 256);
      for (unsigned l0 = 0; l0 < lines; l0 += 64) { const unsigned o = l0 + lane < lines ? id.b * per + (l0 + lane) * 128 : 0xF0000000u;
        asm volatile("buffer_load_dword %0, %1, %2, 0 offen" : "+v"(sink) : "v"(o), "s"(r) : "memory"); } };
    touch(P.wsp, 32768, 256); touch(P.k, 9216, 72); touch(P.v, 9216, 72); touch(P.wout, 10368, 81); touch(P.ssmk, 9216, 72);
  }
  BAR();
  { const int row = 64 * xw + lane;
    const f32x2 a = lds<f32x2>(sm + STG + row * 8) + lds<f32x2>(sm + STG + 1024 + row * 8);
    st16<SC1>(seal(a.x, a.y, tag), rws, W_G1S + ((id.tp * 16 + id.ft) * 128 + row) * 16, 0);
    BAR(); BAR();
    const u64 t0 = __builtin_amdgcn_s_memrealtime();
    int round = 0;
    for (;;) {
      u32x4 gs[16];
#pragma unroll
      for (int i = 0; i < 16; ++i) gs[i] = ld16<SC1>(rws, W_G1S + (id.tp * 16 * 128 + row) * 16, i * 2048);
      if (round == 0) { FENCE(); BAR(); FENCE(); }
      bool ok = true; float s = 0.f, q = 0.f;
#pragma unroll
      for (int i = 0; i < 16; ++i) { ok &= sealed(gs[i], tag); s += __uint_as_float(gs[i].x); q += __uint_as_float(gs[i].y); }
      ++round;
      if (__all(ok) || expired(t0)) {
        const float m = s * (1.0f / FH), var = fmaxf(q * (1.0f / FH) - m * m, 0.f), r = __builtin_amdgcn_rsqf(var + 1e-5f);
        *(LAS float*)(sm + T_R2 + row * 4) = r; *(LAS float*)(sm + T_N2 + row * 4) = -m * r;
        break;
      }
      FENCE();
    }
    asm volatile("s_waitcnt vmcnt(0) lgkmcnt(0)" : "+v"(sink) :: "memory"); BAR();
  }
  STAMP(2);
  BAR(); BAR(); BAR();
  if (xw == 0) set_flag(rws, W_F2 + (id.ft * 16 + id.tp) * 8, tag, lane);
  BAR(); BAR(); BAR();
  LAS char* stg = sm + STG + xw * STG_W;
  { const int tile = xw ? id.b : 256 + id.b - 32; const bool on = xw || (id.b >= 32 && id.b < 64);
    const int tb = tile / 9, db = tile - tb * 9, cc = lane & 7, rg = lane >> 3;
    u32x4 kk[11], vv[8], sh[4];
    if (on) {
      const rsrc_t rk = mkrs(P.k, S * D * 2), rv = mkrs(P.v, S * D * 2), rs = mkrs(P.shk, D * 4 * 2);
      const int t0 = 64 * tb + 8 * rg; const unsigned col = (64 * db + 8 * cc) * 2;
#pragma unroll
      for (int i = 0; i < 11; ++i) kk[i] = ld16<0>(rk, t0 + i - 3 >= 0 ? (unsigned)(t0 + i - 3) * (D * 2) + col : 0xF0000000u, 0);
#pragma unroll
      for (int i = 0; i < 8; ++i) vv[i] = ld16<0>(rv, t0 * (D * 2) + col, i * (D * 2));
#pragma unroll
      for (int i = 0; i < 4; ++i) sh[i] = ld16<0>(rs, col * 4, i * 16);
    }
    FENCE(); BAR(); BAR(); FENCE();
#pragma unroll
    for (int e = 0; e < 8; ++e) {
      unsigned o[4];
#pragma unroll
      for (int hf = 0; hf < 2; ++hf) {
        if (on) {
          float c[4], kf[7], y[4];
#pragma unroll
          for (int j = 0; j < 4; ++j) { const unsigned w = sh[e >> 1][(e & 1) * 2 + (j >> 1)]; c[j] = (j & 1) ? bhi(w) : blo(w); }
#pragma unroll
          for (int i = 0; i < 7; ++i) { const unsigned w = kk[4 * hf + i][e >> 1]; kf[i] = (e & 1) ? bhi(w) : blo(w); }
#pragma unroll
          for (int i = 0; i < 4; ++i) { const unsigned w = vv[4 * hf + i][e >> 1];
            float a = c[3] * kf[i]; asm volatile("" : "+v"(a)); a = fmaf(c[2], kf[i + 1], a); a = fmaf(c[1], kf[i + 2], a); a = fmaf(c[0], kf[i + 3], a);
            y[i] = a * ((e & 1) ? bhi(w) : blo(w)); asm volatile("" : "+v"(y[i])); }
          o[3 - 2 * hf] = pk(y[1], y[0]); o[2 - 2 * hf] = pk(y[3], y[2]);
          if (hf == 1) *(LAS u32x4*)(stg + (8 * cc + e) * 128 + (((7 - rg) ^ cc) * 16)) = (u32x4){o[0], o[1], o[2], o[3]};
        }
        FENCE(); BAR(); FENCE();
      }
    }
    if (on) {
#pragma unroll
      for (int i = 0; i < 8; ++i) { const u32x4 t = lds<u32x4>(stg + (8 * i + rg) * 128 + ((cc ^ i) * 16));
        st16<SC1>(t, rws, W_KVT + (64 * db + rg) * (S * 2) + (S - 64 * (tb + 1)) * 2 + cc * 16, 8 * i * (S * 2)); }
    }
    WAITL(0); BAR();
  }
  { const int unit = xw ? 256 + id.b : id.b; const bool on = !xw || id.b < 32;
    const int ks = unit >> 2, u = unit & 3;
    if (on) {
      const rsrc_t rwo = mkrs(P.wout, FH * D * 2);
#pragma unroll
      for (int i = 0; i < 9; ++i) { const int ci = 64 * i + lane, rho = ci / 18, ch = ci - rho * 18;
        const int f = ks < 64 ? 144 * (ks >> 2) + 32 * (ks & 3) + rho : 144 * (2 * (ks - 64) + (rho >> 4)) + 128 + (rho & 15);
        dma<0>(rwo, stg + i * 1024, f * (D * 2) + u * 288 + ch * 16, 0); }
    }
    FENCE(); BAR(); BAR();
    WAITV(0);
    if (on) {
      const int q = lane >> 4, qq = (lane & 15) >> 2, p = lane & 3;
      LAS char* base = stg + (4 * q + qq) * 288;
#pragma unroll
      for (int T = 0; T < 9; ++T) { const int cb = T < 4 ? 32 * (T >> 1) + 4 * (T & 1) : T == 4 ? 64 : 80 + 32 * ((T - 5) >> 1) + 4 * ((T - 5) & 1);
        LAS char* a = base + cb * 2 + p * (T == 4 ? 8 : 16);
        const bf16x8 fr = cat(ldtr(a), ldtr(a + 16 * 288));
        st16<SC1>(__builtin_bit_cast(u32x4, fr), rws, W_WOF + lane * 16, ((u * 72 + ks) * 9 + T) * 1024); }
    }
    FENCE(); BAR(); BAR();
    WAITV(0); BAR();
    if (xw == 0) set_flag(rws, W_F5 + id.b * 8, tag, lane);
  }
  STAMP(3);
  BAR(); BAR(); BAR(); BAR(); BAR();
  if (xw == 0) wait_flags<2>(rws, W_F5 + lane * 16, tag);
  BAR(); BAR();
  STAMP(4);
  { const rsrc_t rh = mkrs(P.ssmk, D * S * 2);
    const int nch = xw ? 1 : 2;
    for (int c = 0; c < nch; ++c) { const int ci = xw ? 1 : 2 * c, chn = ci == 2 ? 512 + (id.b >> 2) : 2 * id.b + ci;
      LAS char* cb = sm + CV + ci * CV_CH;
#pragma unroll
      for (int i = 0; i < 4; ++i) { dma<0>(rh, cb + 32 + i * 1024, lane * 16, chn * (S * 2) + i * 1024);
        dma<0>(rh, cb + CV_O + 32 + i * 1024, lane * 16 + 2, chn * (S * 2) + i * 1024);
        dma<SC1>(rws, cb + CV_U + i * 1024, lane * 16, W_KVT + chn * (S * 2) + i * 1024); }
      const unsigned h0 = P.ssmk[chn * S];
      if (lane < 8) { *(LAS unsigned*)(cb + lane * 4) = 0u; *(LAS unsigned*)(cb + CV_O + lane * 4) = lane == 7 ? h0 << 16 : 0u; }
      if (lane < 34) *(LAS u32x4*)(cb + CV_U + 4096 + lane * 16) = (u32x4){0u, 0u, 0u, 0u};
    }
    WAITV(0); WAITL(0); BAR();
  }
  BAR();
  if (xw == 0) { set_flag(rws, W_F3 + (id.tp * 16 + id.ft) * 8, tag, lane); wait_flags<1>(rws, W_F3 + id.tp * 128 + (lane & 7) * 16, tag); }
  STAMP(5);
  BAR();
  BAR(); BAR(); BAR();
  if (xw == 0) set_flag(rws, W_F4 + id.b * 8, tag, lane);
  BAR(); BAR(); BAR(); BAR(); BAR(); BAR();
  if (xw == 0) {
    wait_flags<2>(rws, W_F4 + lane * 16, tag);
    const int g = id.ft & 7, d0 = 144 * (g >> 1) + 64 * (g & 1);
#pragma unroll
    for (int i = 0; i < 10; ++i) dma<SC1>(rws, sm + YTILE + i * 1024, W_YT + (d0 + (lane >> 3)) * (S * 2) + (id.tp * 128 + 64 * (id.ft >> 3)) * 2 + (lane & 7) * 16, i * 8 * (S * 2));
    if (id.b == 0 && lane == 0) st8<SC1>((u32x2){(unsigned)(epoch + 1), (unsigned)((epoch + 1) >> 32)}, rws, W_EPOCH, 0);
  }
  STAMP(6);
  BAR(); BAR(); BAR(); BAR(); BAR(); BAR(); BAR();
  WAITV(0); BAR();
}

DEV void role_C(const Params& P, LAS char* sm, const Ids id, const int wid STAMP_ARG) {
  const int lane = id.lane, wt = wid >> 1, wf = wid & 1, c = lane & 15, q = lane >> 4;
  const rsrc_t rws = mkrs(P.ws, W_END);
  __builtin_amdgcn_s_setprio(1);
  f32x4 acc[4][4], accx[2];
  bf16x8 tf[2][4], ff[2][5];
  LAS char *ta[2][2], *fb[2], *fx[2];
#pragma unroll
  for (int s = 0; s < 2; ++s)
#pragma unroll
    for (int h = 0; h < 2; ++h) ta[s][h] = sm + (64 * wt + 32 * (h ^ wf) + c) * 128 + (((q + 4 * s) ^ ((c >> 1) & 7)) * 16);
  { const int o = (16 * (q >> 1) + 4 * (q & 1) + (c >> 2)) * 288 + 8 * (c & 3);
#pragma unroll
    for (int h = 0; h < 2; ++h) { fb[h] = sm + BBASE + h * 2 * BSLOT + o + wf * 128; fx[h] = sm + BBASE + h * 2 * BSLOT + o + 256; } }
  auto rd = [&](auto mode, int b, int slot, int s) {
    constexpr int MODE = decltype(mode)::value;
    if (MODE < 2) {
      const int o = (slot & 1) * BSLOT + 32 * s * 288;
#pragma unroll
      for (int j = 0; j < 4; ++j) ff[b][j] = cat(ldtr(fb[slot >> 1] + o + j * 32), ldtr(fb[slot >> 1] + o + j * 32 + 8 * 288));
    } else {
#pragma unroll
      for (int j = 0; j < 4; ++j) ff[b][j] = lds<bf16x8>(fb[slot >> 1] + (slot & 1) * BSLOT + (s * 9 + j) * 1024);
    }
#pragma unroll
    for (int i = 0; i < 4; ++i) tf[b][i] = lds<bf16x8>(ta[s][i >> 1] + slot * ASLOT + (i & 1) * 2048);
    if (MODE < 2) { const int o = (slot & 1) * BSLOT + 32 * s * 288; ff[b][4] = cat(ldtr(fx[slot >> 1] + o), ldtr(fx[slot >> 1] + o + 8 * 288)); }
    else ff[b][4] = lds<bf16x8>(fx[slot >> 1] + (slot & 1) * BSLOT + s * 9 * 1024);
  };
  auto mm = [&](auto mode, int b, int lo, int hi) {
    constexpr int MODE = decltype(mode)::value;
#pragma unroll
    for (int n = 0; n < 18; ++n) if (n >= lo && n < hi) {
      if (n < 16) { const int i = n >> 2, j = (i & 1) ? 3 - (n & 3) : (n & 3);
        acc[i][j] = MODE == 0 ? MFMA(tf[b][i], ff[b][j], acc[i][j]) : MFMA(ff[b][j], tf[b][i], acc[i][j]); }
      else accx[n - 16] = MODE == 0 ? MFMA(tf[b][n - 16], ff[b][4], accx[n - 16]) : MFMA(ff[b][4], tf[b][n - 16], accx[n - 16]);
    }
  };
  auto stage = [&](auto mode, int slot, bool drain) {
    constexpr int MODE = decltype(mode)::value;
    mm(mode, 1, 0, 4); SB();
    if (drain) WAITV(0);
    WAITL(0); BAR(); SB();
    rd(mode, 0, slot, 0); mm(mode, 1, 4, 18);
    if (MODE < 2) { for (int i = 0; i < 14; ++i) { SG(0x100, 1); SG(0x8, 1); } }
    else { for (int i = 0; i < 4; ++i) { SG(0x100, 1); SG(0x8, 1); SG(0x100, 1); SG(0x8, 2); } SG(0x100, 1); SG(0x8, 2); }
    SB();
    rd(mode, 1, slot, 1); mm(mode, 0, 0, 18);
    if (MODE < 2) { for (int i = 0; i < 4; ++i) { SG(0x100, 1); SG(0x8, 1); SG(0x100, 1); SG(0x8, 1); SG(0x100, 1); SG(0x8, 2); } SG(0x100, 1); SG(0x8, 1); SG(0x100, 1); SG(0x8, 1); }
    else { for (int i = 0; i < 9; ++i) { SG(0x100, 1); SG(0x8, 2); } }
    SB();
  };
  auto first = [&](auto mode, int slot) { WAITL(0); BAR(); SB(); rd(mode, 0, slot, 0); rd(mode, 1, slot, 1); mm(mode, 0, 0, 18); SB(); };
  auto zero = [&]() {
#pragma unroll
    for (int i = 0; i < 4; ++i)
#pragma unroll
      for (int j = 0; j < 4; ++j) acc[i][j] = (f32x4){0.f, 0.f, 0.f, 0.f};
    accx[0] = accx[1] = (f32x4){0.f, 0.f, 0.f, 0.f}; };
  const std::integral_constant<int, 0> M0; const std::integral_constant<int, 1> M1; const std::integral_constant<int, 2> M2;

  zero();
  first(M0, 0);
  STAMP(0);
#pragma unroll 1
  for (int it = 0; it < 2; ++it) { stage(M0, 1, false); stage(M0, 2, false); stage(M0, 3, false); stage(M0, 0, false); }
  mm(M0, 1, 0, 18); SB();
  STAMP(1);
  u32x4 z2p[2][4], z2x;
  { float cs[5];
#pragma unroll
    for (int j = 0; j < 5; ++j) cs[j] = lds<float>(sm + T_CS2 + ((j < 4 ? 4 * wf + j : 8) * 16 + c) * 4);
    float red[2][16];
#pragma unroll
    for (int i = 0; i < 4; ++i) {
      const int ro = (64 * wt + 32 * ((i >> 1) ^ wf) + 8 * q + 4 * (i & 1)) * 4;
      const f32x4 m = lds<f32x4>(sm + T_M1 + ro), r = lds<f32x4>(sm + T_R1 + ro);
      f32x4 su = {0.f, 0.f, 0.f, 0.f}, sq = {0.f, 0.f, 0.f, 0.f};
#pragma unroll
      for (int j = 0; j < (i < 2 ? 5 : 4); ++j) {
        const f32x4 z = gelu4(((j < 4 ? acc[i][j] : accx[i]) - m * cs[j]) * r);
        su += z; sq += z * z;
        u32x4& dst = j < 4 ? z2p[i >> 1][j] : z2x;
        dst[2 * (i & 1)] = pk(z[0], z[1]); dst[2 * (i & 1) + 1] = pk(z[2], z[3]);
      }
#pragma unroll
      for (int e = 0; e < 4; ++e) { red[0][4 * i + e] = su[e]; red[1][4 * i + e] = sq[e]; }
    }
    float r8[2][8], r4[2][4];
#pragma unroll
    for (int st = 0; st < 2; ++st) {
#pragma unroll
      for (int e = 0; e < 8; ++e) { const float a = red[st][e] + dpp<XOR8>(red[st][e]), b = red[st][8 + e] + dpp<XOR8>(red[st][8 + e]); r8[st][e] = (c & 8) ? b : a; }
#pragma unroll
      for (int e = 0; e < 4; ++e) { const float a = r8[st][e] + dpp<XOR7>(r8[st][e]), b = r8[st][4 + e] + dpp<XOR7>(r8[st][4 + e]); float t = (c & 4) ? b : a;
        t += dpp<XOR1>(t); t += dpp<XOR2>(t); r4[st][e] = t; }
    }
    if ((c & 3) == 0) { LAS char* pp = sm + STG + wf * 1024 + (64 * wt + 32 * ((c >> 3) ^ wf) + 8 * q + 4 * ((c >> 2) & 1)) * 8;
      *(LAS f32x4*)pp = (f32x4){r4[0][0], r4[1][0], r4[0][1], r4[1][1]}; *(LAS f32x4*)(pp + 16) = (f32x4){r4[0][2], r4[1][2], r4[0][3], r4[1][3]}; }
#pragma unroll
    for (int i = 0; i < 2; ++i) asm volatile("" : "+v"(z2p[i][0]), "+v"(z2p[i][1]), "+v"(z2p[i][2]), "+v"(z2p[i][3]));
    asm volatile("" : "+v"(z2x));
  }
  STAMP(2);
  zero();
  first(M1, 1);
  stage(M1, 2, false); stage(M1, 3, false); stage(M1, 0, false); stage(M1, 1, false);
  STAMP(3);
  {
#pragma unroll
    for (int i = 0; i < 2; ++i) asm volatile("" : "+v"(z2p[i][0]), "+v"(z2p[i][1]), "+v"(z2p[i][2]), "+v"(z2p[i][3]));
    asm volatile("" : "+v"(z2x));
#pragma unroll
    for (int ip = 0; ip < 2; ++ip) {
      const int ro = (64 * wt + 32 * (ip ^ wf) + 8 * q) * 4;
      const f32x4 r0 = lds<f32x4>(sm + T_R2 + ro), r1 = lds<f32x4>(sm + T_R2 + ro + 16), n0 = lds<f32x4>(sm + T_N2 + ro), n1 = lds<f32x4>(sm + T_N2 + ro + 16);
      const unsigned so = W_ZF + ((id.ft * 64 + 4 * id.tp + 2 * wt + (ip ^ wf)) * 9) * 1024;
#pragma unroll
      for (int j = 0; j < (ip == 0 ? 5 : 4); ++j) { const u32x4 z = j < 4 ? z2p[ip][j] : z2x;
        const f32x4 a = (f32x4){blo(z[0]), bhi(z[0]), blo(z[1]), bhi(z[1])} * r0 + n0, b = (f32x4){blo(z[2]), bhi(z[2]), blo(z[3]), bhi(z[3])} * r1 + n1;
        st16<SC1>((u32x4){pk(a[0], a[1]), pk(a[2], a[3]), pk(b[0], b[1]), pk(b[2], b[3])}, rws, lane * 16, so + (j < 4 ? 4 * wf + j : 8) * 1024); }
    }
    SB();
  }
  unsigned braw[4];
  { const rsrc_t rb = mkrs(P.bsp, S * 2);
#pragma unroll
    for (int i = 0; i < 4; ++i) braw[i] = __builtin_amdgcn_raw_buffer_load_b16(rb, (id.tp * 128 + 64 * wt + 16 * (i ^ (2 * wf)) + c) * 2, 0, 0); }
  STAMP(4);
  stage(M1, 2, false); stage(M1, 3, false); stage(M1, 0, true); stage(M1, 1, false);
  mm(M1, 1, 0, 18); SB();
  STAMP(5);
  u32x2 z1p[4][4], z1x[2];
  { float bias[4];
#pragma unroll
    for (int i = 0; i < 4; ++i) bias[i] = blo(braw[i]);
    f32x4 cs[5];
#pragma unroll
    for (int j = 0; j < 5; ++j) cs[j] = lds<f32x4>(sm + T_CS1 + ((j < 4 ? 4 * wf + j : 8) * 16 + 4 * q) * 4);
#pragma unroll
    for (int i = 0; i < 4; ++i) { const int ro = (64 * wt + 16 * (i ^ (2 * wf)) + c) * 4;
      const float m = lds<float>(sm + T_M1 + ro), r = lds<float>(sm + T_R1 + ro);
#pragma unroll
      for (int j = 0; j < (i < 2 ? 5 : 4); ++j) {
        f32x4& a = j < 4 ? acc[i][j] : accx[i];
        const f32x4 z = gelu4((a - m * cs[j]) * r);
        (j < 4 ? z1p[i][j] : z1x[i]) = (u32x2){pk(z[0], z[1]), pk(z[2], z[3])};
        a = (f32x4){bias[i], bias[i], bias[i], bias[i]};
      } }
#pragma unroll
    for (int h = 0; h < 2; ++h) { fb[h] = sm + BBASE + h * 2 * BSLOT + lane * 16 + wf * 4096; fx[h] = sm + BBASE + h * 2 * BSLOT + lane * 16 + 8192; }
#pragma unroll
    for (int i = 0; i < 4; ++i) asm volatile("" : "+v"(z1p[i][0]), "+v"(z1p[i][1]), "+v"(z1p[i][2]), "+v"(z1p[i][3]), "+v"(acc[i][0]), "+v"(acc[i][1]), "+v"(acc[i][2]), "+v"(acc[i][3]));
    asm volatile("" : "+v"(z1x[0]), "+v"(z1x[1]), "+v"(accx[0]), "+v"(accx[1]));
  }
  STAMP(6);
  first(M2, 2);
  STAMP(7);
#pragma unroll 1
  for (int it = 0; it < 7; ++it) { stage(M2, 3, false); stage(M2, 0, false); stage(M2, 1, false); stage(M2, 2, false); }
  stage(M2, 3, false); stage(M2, 0, false); stage(M2, 1, false);
  mm(M2, 1, 0, 4); SB(); WAITL(0); BAR(); SB();
  mm(M2, 1, 4, 18); SB();
  STAMP(8);
#pragma unroll
  for (int i = 0; i < 4; ++i) asm volatile("" : "+v"(z1p[i][0]), "+v"(z1p[i][1]), "+v"(z1p[i][2]), "+v"(z1p[i][3]));
  asm volatile("" : "+v"(z1x[0]), "+v"(z1x[1]));
#pragma unroll
  for (int i = 0; i < 4; ++i) {
    const unsigned so = W_HF + (id.tp * 8 + 4 * wt + (i ^ (2 * wf))) * 72 * 1024;
    auto gate = [&](u32x2 z, f32x4 a) { const f32x4 h = (f32x4){blo(z.x), bhi(z.x), blo(z.y), bhi(z.y)} * a; return (u32x2){pk(h[0], h[1]), pk(h[2], h[3])}; };
#pragma unroll
    for (int pp = 0; pp < 2; ++pp) { const u32x2 lo = gate(z1p[i][2 * pp], acc[i][2 * pp]), hi = gate(z1p[i][2 * pp + 1], acc[i][2 * pp + 1]);
      st16<SC1>((u32x4){lo.x, lo.y, hi.x, hi.y}, rws, lane * 16, so + (4 * id.ft + 2 * wf + pp) * 1024); }
    if (i < 2) st8<SC1>(gate(z1x[i], accx[i]), rws, lane * 16 + (id.ft & 1) * 8, so + (64 + (id.ft >> 1)) * 1024);
  }
  SB(); BAR(); SB();
  STAMP(9);
  f32x4 cm[8];
  { const unsigned dm = ((const unsigned*)P.ssmd)[id.b], dsh = ((const unsigned*)P.ssmd)[256 + (id.b >> 3)];
    const float dvm = wt ? bhi(dm) : blo(dm), dvs = (id.b & 4) ? bhi(dsh) : blo(dsh);
    const int aoff = ((c & 1) ? 0 : CV_O) + (4 * q + ((c + 1) >> 1)) * 4, boff = CV_U + (2032 - 16 * c + 8 * q) * 2;
    auto afrag = [&](LAS char* p) { const unsigned d0 = lds<unsigned>(p), d1 = lds<unsigned>(p + 4), d2 = lds<unsigned>(p + 8), d3 = lds<unsigned>(p + 12);
      return __builtin_bit_cast(bf16x8, (u32x4){d0, d1, d2, d3}); };
    { const int jq = id.b & 3; LAS char* cb = sm + CV + 2 * CV_CH;
      f32x4 s0 = {0.f, 0.f, 0.f, 0.f}, s1 = s0;
#pragma unroll
      for (int kk = 0; kk < 2; ++kk) { const int k0 = 2 * wid + kk;
#pragma unroll
        for (int e = 0; e < 8; ++e) { const bf16x8 a = afrag(cb + aoff + (k0 + 8 * e) * 64);
          const int o1 = 7 - jq - e >= 0 ? 64 * k0 - 512 * (7 - jq - e) : 512;
          s1 = MFMA(a, lds<bf16x8>(cb + boff + o1), s1);
          if (e < 4) { const int o0 = jq - e >= 0 ? 64 * k0 - 512 * (jq - e) : 512; s0 = MFMA(a, lds<bf16x8>(cb + boff + o0), s0); } } }
      *(LAS f32x4*)(sm + CV_PS + (wid * 2) * 1024 + lane * 16) = s0; *(LAS f32x4*)(sm + CV_PS + (wid * 2 + 1) * 1024 + lane * 16) = s1; }
    LAS char* cb = sm + CV + wt * CV_CH;
#pragma unroll
    for (int j = 0; j < 8; ++j) cm[j] = (f32x4){0.f, 0.f, 0.f, 0.f};
    auto block = [&](int k0) { bf16x8 a[8], bb[8];
#pragma unroll
      for (int e = 0; e < 8; ++e) { bb[e] = lds<bf16x8>(cb + boff + 64 * k0 - 512 * e); a[e] = afrag(cb + aoff + (k0 + 8 * e) * 64); }
#pragma unroll
      for (int e = 0; e < 8; ++e)
#pragma unroll
        for (int j = e; j < 8; ++j) cm[j] = MFMA(a[e], bb[j - e], cm[j]); };
    block(4 * wf);
    SB(); WAITV(0); WAITL(0); BAR(); SB();
    STAMP(10);
    f32x4 ys = {0.f, 0.f, 0.f, 0.f};
    if (wid < 2) {
#pragma unroll
      for (int w = 0; w < 4; ++w) ys += lds<f32x4>(sm + CV_PS + (w * 2 + wid) * 1024 + lane * 16);
    }
#pragma unroll 1
    for (int kk = 1; kk < 4; ++kk) block(4 * wf + kk);
    LAS char* pm = sm + (wt ? CV_PM23 : CV + 2 * CV_CH);
#pragma unroll
    for (int j = 0; j < 4; ++j) { f32x4 lo = cm[j], hi = cm[4 + j]; asm volatile("" : "+v"(lo), "+v"(hi));
      *(LAS f32x4*)(pm + wf * 4096 + j * 1024 + lane * 16) = wf ? lo : hi; cm[j] = wf ? hi : lo; }
    SB(); WAITL(0); BAR(); SB();
    STAMP(11);
    const int half = id.ft >> 3;
    const unsigned hso = W_HF + (id.tp * 8 + 4 * half + wid) * 72 * 1024;
    u32x4 hb[12];
#pragma unroll
    for (int i = 0; i < 12; ++i) hb[i] = ld16<SC1>(rws, lane * 16, hso + i * 1024);
    auto yout = [&](f32x4 a, int chn, LAS char* ub, int j, float dv) {
      const u32x2 u = lds<u32x2>(ub + CV_U + (2044 - 256 * j - 16 * c - 4 * q) * 2);
      const f32x4 y = a + (f32x4){bhi(u.y), blo(u.y), bhi(u.x), blo(u.x)} * dv;
      st8<SC1>((u32x2){pk(y[0], y[1]), pk(y[2], y[3])}, rws, (16 * c + 4 * q) * 2, W_YT + chn * (S * 2) + j * 512); };
#pragma unroll
    for (int j = 0; j < 4; ++j) yout(cm[j] + lds<f32x4>(pm + (wf ^ 1) * 4096 + j * 1024 + lane * 16), 2 * id.b + wt, cb, 4 * wf + j, dvm);
    if (wid < 2) yout(ys, 512 + (id.b >> 2), sm + CV + 2 * CV_CH, wid ? 7 - (id.b & 3) : (id.b & 3), dvs);
    const int g = id.ft & 7, dp = 144 * (g >> 1) + 80 * (g & 1), ds = 144 * (g >> 1) + 64, trow = 64 * half + 16 * wid + c;
    const rsrc_t rx = mkrs(P.x, S * D * 2);
    const unsigned xo = (id.tp * 128 + trow) * (D * 2);
    const u32x4 x0 = ld16<0>(rx, xo + q * 16, dp * 2), x1 = ld16<0>(rx, xo + q * 16, dp * 2 + 64);
    const u32x2 x2 = __builtin_amdgcn_raw_buffer_load_b64(rx, xo + q * 8, ds * 2, 0);
    SB();
    f32x4 o[5];
#pragma unroll
    for (int t = 0; t < 5; ++t) o[t] = (f32x4){0.f, 0.f, 0.f, 0.f};
    LAS char* wb[2] = {sm + lane * 16, sm + 3 * G3SLOT + lane * 16};
    bf16x8 wq[2][5];
    auto rdw = [&](int b, int slot, int kk) {
#pragma unroll
      for (int t = 0; t < 5; ++t) wq[b][t] = lds<bf16x8>(wb[slot / 3] + (slot % 3) * G3SLOT + (kk * 5 + t) * 1024); };
    rdw(0, 0, 0);
#pragma unroll 1
    for (int it = 0; it < 3; ++it) {
#pragma unroll
      for (int sl = 0; sl < 6; ++sl)
#pragma unroll
        for (int kk = 0; kk < 4; ++kk) { const int n = sl * 4 + kk;
          if (kk == 3) { if (sl == 5 && it == 2) {} else { SB(); if (sl == 2 && it == 0) WAITV(12); WAITL(0); BAR(); SB(); rdw((n + 1) & 1, sl == 5 ? 0 : sl + 1, 0); } }
          else rdw((n + 1) & 1, sl, kk + 1);
          const bf16x8 hf = __builtin_bit_cast(bf16x8, hb[n % 12]);
#pragma unroll
          for (int t = 0; t < 5; ++t) o[t] = MFMA(wq[n & 1][t], hf, o[t]);
          hb[n % 12] = ld16<SC1>(rws, lane * 16, it * 24 + n + 12 < 72 ? hso + (it * 24 + n + 12) * 1024 : 0xF0000000u);
          SB();
        }
    }
    STAMP(12);
    const float m = lds<float>(sm + T_M1 + trow * 4), r = lds<float>(sm + T_R1 + trow * 4);
    LAS char* yb = sm + YTILE + (c >> 2) * 128 + (16 * wid + 4 * (c & 3)) * 2;
    const rsrc_t ro = mkrs(P.out, S * D * 2);
    auto fin = [&](u32x2 xv, bf16x4 yv, f32x4 a) { const u32x2 yu = __builtin_bit_cast(u32x2, yv);
      const f32x4 xf = {blo(xv.x), bhi(xv.x), blo(xv.y), bhi(xv.y)}, yf = {blo(yu.x), bhi(yu.x), blo(yu.y), bhi(yu.y)};
      const f32x4 t = xf + (xf - m) * r * yf + a; return (u32x2){pk(t[0], t[1]), pk(t[2], t[3])}; };
    const int yp = 16 * (g & 1), ysh = 64 * ((g & 1) ^ 1);
#pragma unroll
    for (int pp = 0; pp < 2; ++pp) { const u32x4 xv = pp ? x1 : x0;
      const u32x2 lo = fin(xv.xy, ldtr(yb + (yp + 32 * pp + 8 * q) * 128), o[2 * pp]), hi = fin(xv.zw, ldtr(yb + (yp + 32 * pp + 8 * q + 4) * 128), o[2 * pp + 1]);
      st16<0>((u32x4){lo.x, lo.y, hi.x, hi.y}, ro, xo + q * 16, dp * 2 + 64 * pp); }
    const u32x2 ls = fin(x2, ldtr(yb + (ysh + 4 * q) * 128), o[4]);
    if (wt == (g & 1)) st8<0>(ls, ro, xo + q * 8, ds * 2);
    STAMP(13);
  }
}
}

extern "C" __global__ __launch_bounds__(512, 2) void h3_gmlp_fused(const Params P) {
  extern __shared__ __attribute__((aligned(16))) char smem_[];
  LAS char* sm = (LAS char*)smem_;
  const int wid = __builtin_amdgcn_readfirstlane(threadIdx.x >> 6);
  Ids id; id.b = blockIdx.x; id.lane = threadIdx.x & 63;
  const int xcd = id.b & 7, loc = id.b >> 3;
  id.tp = (xcd >> 1) * 4 + (loc >> 3); id.ft = (xcd & 1) * 8 + (loc & 7);
  unsigned stampv = 0; { const int lane = id.lane; STAMP(63); }
  if (wid == 3 && id.lane == 63) __hip_atomic_store((unsigned*)(P.ws + (41 << 20)) + blockIdx.x, stampv, __ATOMIC_RELAXED, __HIP_MEMORY_SCOPE_AGENT);
  if (wid == 0) {
    unsigned lo, hi, bytes, sink;
    asm volatile("s_getpc_b64 vcc\n1:\n\ts_sub_u32 %0, vcc_lo, 1b-h3_gmlp_fused\n\ts_subb_u32 %1, vcc_hi, 0\n\ts_mov_b32 %2, h3_gmlp_end-h3_gmlp_fused" : "=s"(lo), "=s"(hi), "=s"(bytes) :: "vcc", "scc");
    const rsrc_t rc = mkrs((const void*)(((u64)hi << 32) | lo), bytes);
    asm volatile("buffer_load_dword %0, %1, %2, 0 offen\n\ts_waitcnt vmcnt(0)" : "=&v"(sink) : "v"((id.lane < 16 ? id.lane * 32 + loc : 0x100000) * 128), "s"(rc) : "memory");
  }
  if (wid == 4 || wid == 5) role_L(P, sm, id, wid - 4 STAMP_PASS);
  else if (wid >= 6) role_X(P, sm, id, wid - 6 STAMP_PASS);
  else role_C(P, sm, id, wid STAMP_PASS);
  { const int lane = id.lane; STAMP(62); *(unsigned*)(P.ws + (40 << 20) + ((blockIdx.x * 8 + wid) * 64 + lane) * 4) = stampv; }
  if (wid == 5 && blockIdx.x == 0) {
    unsigned mn = 0xffffffffu, mx = 0;
    for (int i = 0; i < 4; ++i) { const unsigned v = __hip_atomic_load((unsigned*)(P.ws + (41 << 20)) + i * 64 + id.lane, __ATOMIC_RELAXED, __HIP_MEMORY_SCOPE_AGENT); mn = v < mn ? v : mn; mx = v > mx ? v : mx; }
    for (int o = 32; o; o >>= 1) { const unsigned a = __shfl_xor(mn, o), b = __shfl_xor(mx, o); mn = a < mn ? a : mn; mx = b > mx ? b : mx; }
    mn = __builtin_amdgcn_readfirstlane(mn); mx = __builtin_amdgcn_readfirstlane(mx);
    const unsigned t_e = __builtin_amdgcn_readlane(stampv, 62), t_s = __builtin_amdgcn_readlane(stampv, 63);
    const unsigned val = MK_AMP == 0 ? 0 : MK_AMP == 1 ? t_e - mn : MK_AMP == 2 ? mx - mn : MK_AMP == 3 ? t_s - mn : t_e - t_s;
    const unsigned long long d_ = (unsigned long long)val * MK_K, t0_ = __builtin_amdgcn_s_memrealtime();
    while (__builtin_amdgcn_s_memrealtime() - t0_ < (d_ > 200000 ? 200000 : d_)) __builtin_amdgcn_s_sleep(2); }
}
extern "C" __global__ void h3_gmlp_end() {}

extern "C" void kernel_launch(void* const* d_in, const int* in_sizes, int n_in, void* d_out, int out_size, void* d_ws, size_t ws_size, hipStream_t stream) {
  Params p;
  p.x = (const u16*)d_in[0]; p.k = (const u16*)d_in[1]; p.v = (const u16*)d_in[2]; p.shk = (const u16*)d_in[3]; p.ssmk = (const u16*)d_in[4]; p.ssmd = (const u16*)d_in[5];
  p.win = (const u16*)d_in[7]; p.wsp = (const u16*)d_in[8]; p.bsp = (const u16*)d_in[9]; p.wout = (const u16*)d_in[10];
  p.out = (u16*)d_out; p.ws = (char*)d_ws;
  (void)hipFuncSetAttribute((const void*)h3_gmlp_fused, hipFuncAttributeMaxDynamicSharedMemorySize, LDS_BYTES);
  h3_gmlp_fused<<<256, 512, LDS_BYTES, stream>>>(p);
}
```

```cpp
#define MK_STAMP
#define MK_AMP 0
#define MK_K 1
#define MK_PRE 0
#define MK_POST 1
#include <hip/hip_runtime.h>
#include <hip/hip_bf16.h>
#include <type_traits>

namespace {
typedef unsigned short u16;
typedef unsigned long long u64;
typedef short bf16x8 __attribute__((ext_vector_type(8)));
typedef short bf16x4 __attribute__((ext_vector_type(4)));
typedef __bf16 bf2 __attribute__((ext_vector_type(2)));
typedef float f32x2 __attribute__((ext_vector_type(2)));
typedef float f32x4 __attribute__((ext_vector_type(4)));
typedef unsigned u32x2 __attribute__((ext_vector_type(2)));
typedef unsigned u32x4 __attribute__((ext_vector_type(4)));
typedef __amdgpu_buffer_rsrc_t rsrc_t;
#define LAS __attribute__((address_space(3)))
#define DEV __device__ __forceinline__

constexpr int S = 2048, D = 576, F = 4608, FH = 2304;
constexpr int ASLOT = 16384, BSLOT = 18432, BBASE = 4 * ASLOT;
constexpr int STG = 139264, STG_W = 9984;
constexpr int T_M1 = 159232, T_R1 = T_M1 + 512, T_CS2 = T_R1 + 512, T_CS1 = T_CS2 + 576, T_N2 = T_CS1 + 576, T_R2 = T_N2 + 512, T_TAG = T_R2 + 512;
constexpr int LDS_BYTES = 163840;
constexpr int G3SLOT = 20480;
constexpr int CV = 102400, CV_CH = 13056, CV_O = 4160, CV_U = 8320;
constexpr int CV_PS = 141568, CV_PM23 = 149760;
constexpr int YTILE = 122880;
constexpr int W_EPOCH = 0, W_F2 = 4096, W_F3 = 8192, W_F4 = 12288, W_F5 = 16384;
constexpr int W_G0X = 0x8000, W_G0C = 0x10000, W_G1S = 0x60000;
constexpr int W_KVT = 1 << 20, W_YT = 4 << 20, W_WOF = 7 << 20, W_ZF = 10 << 20, W_HF = 20 << 20, W_END = 30 << 20;
constexpr int SC1 = 16;

#define BAR() do { asm volatile("" ::: "memory"); __builtin_amdgcn_s_barrier(); asm volatile("" ::: "memory"); } while (0)
#define SB() __builtin_amdgcn_sched_barrier(0)
#define SG(m, n) __builtin_amdgcn_sched_group_barrier(m, n, 0)
#define WAITV(n) asm volatile("s_waitcnt vmcnt(%0)" ::"i"(n) : "memory")
#define WAITL(n) asm volatile("s_waitcnt lgkmcnt(%0)" ::"i"(n) : "memory")
#define FENCE() asm volatile("" ::: "memory")
#define MFMA(a, b, c) __builtin_amdgcn_mfma_f32_16x16x32_bf16(a, b, c, 0, 0, 0)
#define STAMP(i) do { const unsigned t_ = (unsigned)__builtin_amdgcn_s_memrealtime(); stampv = lane == (i) ? t_ : stampv; asm volatile("" : "+v"(stampv)); } while (0)
#define LBAR() do { BAR(); const int bi_ = bcount - (lw ? 0 : 40); ++bcount; if (bi_ >= 0 && bi_ < 60) STAMP(bi_); } while (0)
#define STAMP_ARG , unsigned& stampv
#define STAMP_PASS , stampv

struct Params {
  const u16 *x, *k, *v, *shk, *ssmk, *ssmd, *win, *wsp, *bsp, *wout;
  u16* out; char* ws;
};

DEV rsrc_t mkrs(const void* p, unsigned bytes) { return __builtin_amdgcn_make_buffer_rsrc((void*)p, 0, bytes, 0x00020000); }
template <int AUX> DEV void dma(rsrc_t rs, LAS char* l, unsigned voff, unsigned soff) { __builtin_amdgcn_raw_ptr_buffer_load_lds(rs, (LAS void*)l, 16, voff, soff, 0, AUX); }
template <int AUX> DEV u32x4 ld16(rsrc_t rs, unsigned voff, unsigned soff) { return __builtin_amdgcn_raw_buffer_load_b128(rs, voff, soff, AUX); }
template <int AUX> DEV void st16(u32x4 v, rsrc_t rs, unsigned voff, unsigned soff) { __builtin_amdgcn_raw_buffer_store_b128(v, rs, voff, soff, AUX); }
template <int AUX> DEV void st8(u32x2 v, rsrc_t rs, unsigned voff, unsigned soff) { __builtin_amdgcn_raw_buffer_store_b64(v, rs, voff, soff, AUX); }
template <class T> DEV T lds(LAS char* p) { return *(LAS T*)p; }
DEV bf16x4 ldtr(LAS char* p) { return __builtin_amdgcn_ds_read_tr16_b64_v4i16((LAS bf16x4*)p); }
DEV bf16x8 cat(bf16x4 a, bf16x4 b) { return __builtin_shufflevector(a, b, 0, 1, 2, 3, 4, 5, 6, 7); }
DEV float blo(unsigned u) { return __uint_as_float(u << 16); }
DEV float bhi(unsigned u) { return __uint_as_float(u & 0xffff0000u); }
DEV unsigned pk(float a, float b) { return __builtin_bit_cast(unsigned, __builtin_convertvector((f32x2){a, b}, bf2)); }
DEV float dot2(unsigned a, unsigned b, float c) { return __builtin_amdgcn_fdot2_f32_bf16(__builtin_bit_cast(bf2, a), __builtin_bit_cast(bf2, b), c, false); }
template <int CTRL> DEV float dpp(float x) { return __builtin_bit_cast(float, __builtin_amdgcn_mov_dpp(__builtin_bit_cast(int, x), CTRL, 0xf, 0xf, true)); }
constexpr int XOR1 = 0xB1, XOR2 = 0x4E, XOR7 = 0x141, XOR8 = 0x128;
DEV f32x2 gelu2(f32x2 x) {
  const f32x2 a = x * (x * x * -0.10294324f + -2.30220819f);
  f32x2 e; e.x = __builtin_amdgcn_exp2f(a.x); e.y = __builtin_amdgcn_exp2f(a.y);
  e = e + 1.0f;
  f32x2 r; r.x = __builtin_amdgcn_rcpf(e.x); r.y = __builtin_amdgcn_rcpf(e.y);
  return x * r;
}
DEV f32x4 gelu4(f32x4 x) { const f32x2 a = gelu2(x.xy), b = gelu2(x.zw); return (f32x4){a.x, a.y, b.x, b.y}; }
DEV u32x4 seal(float a, float b, u32x2 tag) { const unsigned ua = __float_as_uint(a), ub = __float_as_uint(b); return (u32x4){ua, ub, ua ^ tag.x, ub ^ tag.y}; }
DEV bool sealed(u32x4 g, u32x2 tag) { return (g.x ^ g.z) == tag.x && (g.y ^ g.w) == tag.y; }
DEV bool expired(u64 t0) { return __builtin_amdgcn_s_memrealtime() - t0 > 2000000ull; }
template <int NL> DEV void wait_flags(rsrc_t ws, unsigned voff, u32x2 tag) {
  const u64 t0 = __builtin_amdgcn_s_memrealtime();
  for (;;) {
    bool ok = true;
#pragma unroll
    for (int i = 0; i < NL; ++i) { const u32x4 f = ld16<SC1>(ws, voff, i * 1024); ok &= f.x == tag.x && f.y == tag.y && f.z == tag.x && f.w == tag.y; }
    if (__all(ok) || expired(t0)) break;
    FENCE();
  }
  FENCE();
}
DEV void set_flag(rsrc_t ws, unsigned off, u32x2 tag, int lane) { if (lane == 0) st8<SC1>(tag, ws, off, 0); }

struct Ids { int b, tp, ft, lane; };

DEV void role_L(const Params& P, LAS char* sm, const Ids id, const int lw STAMP_ARG) {
  int bcount = 0; (void)bcount;
  const int lane = id.lane, rr = lane >> 3, sl = lane & 7;
  const rsrc_t rx = mkrs(P.x, S * D * 2), rwin = mkrs(P.win, D * F * 2), rwsp = mkrs(P.wsp, S * S * 2), rws = mkrs(P.ws, W_END);
  const unsigned swz = (sl ^ (4 * lw + (rr >> 1))) * 16;
  const unsigned vxa = (16 * lw + 8 * (rr >> 2) + (rr & 3)) * (D * 2) + swz;
  unsigned vb[9];
#pragma unroll
  for (int i = 0; i < 9; ++i) { const int ci = 64 * (2 * i + lw) + lane, rho = ci / 18, ch = ci - rho * 18;
    const int kr = (rho & ~12) | ((rho & 4) << 1) | ((rho & 8) >> 1); vb[i] = kr * (F * 2) + ch * 16; }
  auto issue1 = [&](int n, int ks, bool passA, unsigned vx) {
    LAS char* a = sm + (n & 3) * ASLOT + lw * 1024; LAS char* bb = sm + BBASE + (n & 3) * BSLOT + lw * 1024;
    const unsigned sa = id.tp * 128 * (D * 2) + ks * 128, sb = ks * 64 * (F * 2) + ((passA ? FH : 0) + id.ft * 144) * 2;
#pragma unroll
    for (int i = 0; i < 9; ++i) {
      if (i < 8) dma<0>(rx, a + i * 2048, vx, sa + (passA ? 32 * (i >> 1) + 4 * (i & 1) : 16 * i) * (D * 2));
      dma<0>(rwin, bb + i * 2048, vb[i], sb);
    }
  };
  for (int n = 0; n < 3; ++n) issue1(n, n, true, vxa);
  STAMP(60);
  const unsigned vxb = (8 * lw + rr) * (D * 2) + swz, vw = (8 * lw + rr) * (S * 2) + swz, vl = lane * 16 + lw * 1024;
  auto issue2a = [&](int n) { LAS char* a = sm + (n & 3) * ASLOT + lw * 1024; const unsigned sa = id.tp * 128 * (S * 2) + (n - 18) * 128;
#pragma unroll
    for (int i = 0; i < 8; ++i) dma<0>(rwsp, a + i * 2048, vw, sa + 16 * i * (S * 2)); };
  auto issue2b = [&](int n) { LAS char* bb = sm + BBASE + (n & 3) * BSLOT + lw * 1024; const unsigned sb = W_ZF + (id.ft * 576 + (n - 18) * 18) * 1024;
#pragma unroll
    for (int i = 0; i < 9; ++i) dma<SC1>(rws, bb + i * 2048, vl, sb + i * 2048); };
#pragma unroll 1
  for (int n = 0; n < 6; ++n) { WAITV(34); LBAR(); issue1(n + 3, n + 3, true, vxa); }
#pragma unroll 1
  for (int n = 6; n < 15; ++n) { WAITV(34); LBAR(); issue1(n + 3, n - 6, false, vxb); }
  WAITV(34); LBAR(); issue2a(18);
  WAITV(25); LBAR(); issue2a(19);
  WAITV(16); LBAR(); issue2a(20);
  { const u32x2 tag = lds<u32x2>(sm + T_TAG); wait_flags<1>(rws, W_F2 + id.ft * 128 + (lane & 7) * 16, tag); }
  STAMP(61);
  issue2b(18); issue2b(19); issue2b(20);
  WAITV(18); LBAR(); issue2a(21); issue2b(21);
  WAITV(26); LBAR(); issue2a(22); issue2b(22);
#pragma unroll 1
  for (int n = 20; n < 47; ++n) { WAITV(34); LBAR(); issue2a(n + 3); issue2b(n + 3); }
  WAITV(34); LBAR(); WAITV(17); LBAR(); WAITV(0); LBAR();
  LBAR();
  const int g = id.ft & 7, u = g >> 1;
  auto issue3 = [&](int m, int slot) { LAS char* a = sm + slot * G3SLOT + lw * 1024; const unsigned sb = W_WOF + (u * 72 + 4 * m) * 9 * 1024;
#pragma unroll
    for (int i = 0; i < 10; ++i) { const int pc = 2 * i + lw, kk = pc / 5, tt = pc - kk * 5, tile = (tt == 4) ? 4 : tt + (g & 1) * 5;
      dma<SC1>(rws, a + i * 2048, lane * 16, sb + (kk * 9 + tile) * 1024); } };
  for (int m = 0; m < 5; ++m) issue3(m, m);
  LBAR(); LBAR();
  WAITV(40); LBAR();
  WAITV(30); LBAR(); issue3(5, 5); issue3(6, 0);
  int slot = 1;
#pragma unroll 1
  for (int m = 2; m < 13; ++m) { WAITV(40); LBAR(); issue3(m + 5, slot); slot = slot == 5 ? 0 : slot + 1; }
  WAITV(40); LBAR(); WAITV(30); LBAR(); WAITV(20); LBAR(); WAITV(10); LBAR(); WAITV(0); LBAR();
}

DEV void role_X(const Params& P, LAS char* sm, const Ids id, const int xw STAMP_ARG) {
  const int lane = id.lane;
  const rsrc_t rws = mkrs(P.ws, W_END);
  u32x2 tag; u64 epoch;
  { const u32x2 e = __builtin_amdgcn_raw_buffer_load_b64(rws, W_EPOCH, 0, SC1);
    epoch = ((u64)__builtin_amdgcn_readfirstlane(e.y) << 32) | __builtin_amdgcn_readfirstlane(e.x);
    const u64 t = ((epoch + 1) * 0x9E3779B97F4A7C15ull) | 1ull; tag.x = (unsigned)t; tag.y = (unsigned)(t >> 32); }
  if (xw == 0) {
    const rsrc_t rx = mkrs(P.x, S * D * 2);
    const int row = id.tp * 128 + id.ft * 8 + (lane >> 3);
    u32x4 v[9];
#pragma unroll
    for (int i = 0; i < 9; ++i) v[i] = ld16<0>(rx, row * (D * 2) + (lane & 7) * 16, i * 128);
    if (lane == 0) *(LAS u32x2*)(sm + T_TAG) = tag;
    WAITL(0); BAR();
    float s = 0.f, q = 0.f; unsigned ones = 0x3f803f80u; asm volatile("" : "+v"(ones));
#pragma unroll
    for (int i = 0; i < 9; ++i)
#pragma unroll
      for (int e = 0; e < 4; ++e) { s = dot2(v[i][e], ones, s); q = dot2(v[i][e], v[i][e], q); }
    s += dpp<XOR1>(s); q += dpp<XOR1>(q); s += dpp<XOR2>(s); q += dpp<XOR2>(q); s += dpp<XOR7>(s); q += dpp<XOR7>(q);
    const float m = s * (1.0f / D), var = fmaxf(q * (1.0f / D) - m * m, 0.f), r = __builtin_amdgcn_rsqf(var + 1e-5f);
    if ((lane & 7) == 0) st16<SC1>(seal(m, r, tag), rws, W_G0X + row * 16, 0);
    BAR();
  } else {
    const rsrc_t rwin = mkrs(P.win, D * F * 2);
    const int rg = lane / 18, ch = lane - rg * 18;
    const unsigned vo = lane < 54 ? ((id.tp >> 1) * 72 + rg) * (F * 2) + (((id.tp & 1) ? 0 : FH) + id.ft * 144) * 2 + ch * 16 : 0xF0000000u;
    u32x4 v[24];
#pragma unroll
    for (int i = 0; i < 24; ++i) v[i] = ld16<0>(rwin, vo, i * 3 * (F * 2));
    BAR();
    float a[8]; unsigned selo = 0x00003f80u, sehi = 0x3f800000u; asm volatile("" : "+v"(selo), "+v"(sehi));
#pragma unroll
    for (int e = 0; e < 8; ++e) a[e] = 0.f;
#pragma unroll
    for (int i = 0; i < 24; ++i)
#pragma unroll
      for (int e = 0; e < 4; ++e) { a[2 * e] = dot2(v[i][e], selo, a[2 * e]); a[2 * e + 1] = dot2(v[i][e], sehi, a[2 * e + 1]); }
#pragma unroll
    for (int e = 0; e < 8; ++e) a[e] += __shfl(a[e], lane + 18) + __shfl(a[e], lane + 36);
    if (lane < 18) {
#pragma unroll
      for (int e = 0; e < 4; ++e) st16<SC1>(seal(a[2 * e], a[2 * e + 1], tag), rws, W_G0C + ((id.ft * 16 + id.tp) * 72 + ch * 4 + e) * 16, 0);
    }
    BAR();
  }
  STAMP(0);
  BAR(); BAR(); BAR();
  if (xw == 0) {
    const u64 t0 = __builtin_amdgcn_s_memrealtime();
    int round = 0;
    for (;;) {
      u32x4 gx[2], gc[18];
#pragma unroll
      for (int i = 0; i < 2; ++i) gx[i] = ld16<SC1>(rws, W_G0X + (id.tp * 128 + lane) * 16, i * 1024);
#pragma unroll
      for (int i = 0; i < 18; ++i) gc[i] = ld16<SC1>(rws, W_G0C + (id.ft * 1152 + lane) * 16, i * 1024);
      if (round == 0) { FENCE(); BAR(); BAR(); FENCE(); }
      bool ok = sealed(gx[0], tag) && sealed(gx[1], tag);
#pragma unroll
      for (int i = 0; i < 18; ++i) ok &= sealed(gc[i], tag);
      ++round;
      if (__all(ok) || expired(t0)) {
#pragma unroll
        for (int i = 0; i < 2; ++i) { *(LAS unsigned*)(sm + T_M1 + (64 * i + lane) * 4) = gx[i].x; *(LAS unsigned*)(sm + T_R1 + (64 * i + lane) * 4) = gx[i].y; }
#pragma unroll
        for (int i = 0; i < 18; ++i) *(LAS u32x2*)(sm + STG + 4096 + (64 * i + lane) * 8) = gc[i].xy;
        break;
      }
      FENCE();
    }
    WAITL(0);
#pragma unroll
    for (int hz = 0; hz < 2; ++hz)
#pragma unroll
      for (int pass = 0; pass < 2; ++pass) { const int cp = pass * 64 + lane;
        if (cp < 72) { f32x2 a = {0.f, 0.f};
#pragma unroll
          for (int e = 0; e < 8; ++e) a += lds<f32x2>(sm + STG + 4096 + ((2 * e + hz) * 72 + cp) * 8);
          *(LAS f32x2*)(sm + (hz ? T_CS1 : T_CS2) + cp * 8) = a; } }
    WAITL(0); BAR();
  } else { BAR(); BAR(); BAR(); }
  STAMP(1);
  BAR();
  unsigned sink = 0;
  if (xw == 1) {
    auto touch = [&](const void* p, unsigned per, unsigned lines) { const rsrc_t r = mkrs(p, per * 256);
      for (unsigned l0 = 0; l0 < lines; l0 += 64) { const unsigned o = l0 + lane < lines ? id.b * per + (l0 + lane) * 128 : 0xF0000000u;
        asm volatile("buffer_load_dword %0, %1, %2, 0 offen" : "+v"(sink) : "v"(o), "s"(r) : "memory"); } };
    touch(P.wsp, 32768, 256); touch(P.k, 9216, 72); touch(P.v, 9216, 72); touch(P.wout, 10368, 81); touch(P.ssmk, 9216, 72);
  }
  BAR();
  { const int row = 64 * xw + lane;
    const f32x2 a = lds<f32x2>(sm + STG + row * 8) + lds<f32x2>(sm + STG + 1024 + row * 8);
    st16<SC1>(seal(a.x, a.y, tag), rws, W_G1S + ((id.tp * 16 + id.ft) * 128 + row) * 16, 0);
    BAR(); BAR();
    const u64 t0 = __builtin_amdgcn_s_memrealtime();
    int round = 0;
    for (;;) {
      u32x4 gs[16];
#pragma unroll
      for (int i = 0; i < 16; ++i) gs[i] = ld16<SC1>(rws, W_G1S + (id.tp * 16 * 128 + row) * 16, i * 2048);
      if (round == 0) { FENCE(); BAR(); FENCE(); }
      bool ok = true; float s = 0.f, q = 0.f;
#pragma unroll
      for (int i = 0; i < 16; ++i) { ok &= sealed(gs[i], tag); s += __uint_as_float(gs[i].x); q += __uint_as_float(gs[i].y); }
      ++round;
      if (__all(ok) || expired(t0)) {
        const float m = s * (1.0f / FH), var = fmaxf(q * (1.0f / FH) - m * m, 0.f), r = __builtin_amdgcn_rsqf(var + 1e-5f);
        *(LAS float*)(sm + T_R2 + row * 4) = r; *(LAS float*)(sm + T_N2 + row * 4) = -m * r;
        break;
      }
      FENCE();
    }
    asm volatile("s_waitcnt vmcnt(0) lgkmcnt(0)" : "+v"(sink) :: "memory"); BAR();
  }
  STAMP(2);
  BAR(); BAR(); BAR();
  if (xw == 0) set_flag(rws, W_F2 + (id.ft * 16 + id.tp) * 8, tag, lane);
  BAR(); BAR(); BAR();
  LAS char* stg = sm + STG + xw * STG_W;
  { const int tile = xw ? id.b : 256 + id.b - 32; const bool on = xw || (id.b >= 32 && id.b < 64);
    const int tb = tile / 9, db = tile - tb * 9, cc = lane & 7, rg = lane >> 3;
    u32x4 kk[11], vv[8], sh[4];
    if (on) {
      const rsrc_t rk = mkrs(P.k, S * D * 2), rv = mkrs(P.v, S * D * 2), rs = mkrs(P.shk, D * 4 * 2);
      const int t0 = 64 * tb + 8 * rg; const unsigned col = (64 * db + 8 * cc) * 2;
#pragma unroll
      for (int i = 0; i < 11; ++i) kk[i] = ld16<0>(rk, t0 + i - 3 >= 0 ? (unsigned)(t0 + i - 3) * (D * 2) + col : 0xF0000000u, 0);
#pragma unroll
      for (int i = 0; i < 8; ++i) vv[i] = ld16<0>(rv, t0 * (D * 2) + col, i * (D * 2));
#pragma unroll
      for (int i = 0; i < 4; ++i) sh[i] = ld16<0>(rs, col * 4, i * 16);
    }
    FENCE(); BAR(); BAR(); FENCE();
#pragma unroll
    for (int e = 0; e < 8; ++e) {
      unsigned o[4];
#pragma unroll
      for (int hf = 0; hf < 2; ++hf) {
        if (on) {
          float c[4], kf[7], y[4];
#pragma unroll
          for (int j = 0; j < 4; ++j) { const unsigned w = sh[e >> 1][(e & 1) * 2 + (j >> 1)]; c[j] = (j & 1) ? bhi(w) : blo(w); }
#pragma unroll
          for (int i = 0; i < 7; ++i) { const unsigned w = kk[4 * hf + i][e >> 1]; kf[i] = (e & 1) ? bhi(w) : blo(w); }
#pragma unroll
          for (int i = 0; i < 4; ++i) { const unsigned w = vv[4 * hf + i][e >> 1];
            float a = c[3] * kf[i]; asm volatile("" : "+v"(a)); a = fmaf(c[2], kf[i + 1], a); a = fmaf(c[1], kf[i + 2], a); a = fmaf(c[0], kf[i + 3], a);
            y[i] = a * ((e & 1) ? bhi(w) : blo(w)); asm volatile("" : "+v"(y[i])); }
          o[3 - 2 * hf] = pk(y[1], y[0]); o[2 - 2 * hf] = pk(y[3], y[2]);
          if (hf == 1) *(LAS u32x4*)(stg + (8 * cc + e) * 128 + (((7 - rg) ^ cc) * 16)) = (u32x4){o[0], o[1], o[2], o[3]};
        }
        FENCE(); BAR(); FENCE();
      }
    }
    if (on) {
#pragma unroll
      for (int i = 0; i < 8; ++i) { const u32x4 t = lds<u32x4>(stg + (8 * i + rg) * 128 + ((cc ^ i) * 16));
        st16<SC1>(t, rws, W_KVT + (64 * db + rg) * (S * 2) + (S - 64 * (tb + 1)) * 2 + cc * 16, 8 * i * (S * 2)); }
    }
    WAITL(0); BAR();
  }
  { const int unit = xw ? 256 + id.b : id.b; const bool on = !xw || id.b < 32;
    const int ks = unit >> 2, u = unit & 3;
    if (on) {
      const rsrc_t rwo = mkrs(P.wout, FH * D * 2);
#pragma unroll
      for (int i = 0; i < 9; ++i) { const int ci = 64 * i + lane, rho = ci / 18, ch = ci - rho * 18;
        const int f = ks < 64 ? 144 * (ks >> 2) + 32 * (ks & 3) + rho : 144 * (2 * (ks - 64) + (rho >> 4)) + 128 + (rho & 15);
        dma<0>(rwo, stg + i * 1024, f * (D * 2) + u * 288 + ch * 16, 0); }
    }
    FENCE(); BAR(); BAR();
    WAITV(0);
    if (on) {
      const int q = lane >> 4, qq = (lane & 15) >> 2, p = lane & 3;
      LAS char* base = stg + (4 * q + qq) * 288;
#pragma unroll
      for (int T = 0; T < 9; ++T) { const int cb = T < 4 ? 32 * (T >> 1) + 4 * (T & 1) : T == 4 ? 64 : 80 + 32 * ((T - 5) >> 1) + 4 * ((T - 5) & 1);
        LAS char* a = base + cb * 2 + p * (T == 4 ? 8 : 16);
        const bf16x8 fr = cat(ldtr(a), ldtr(a + 16 * 288));
        st16<SC1>(__builtin_bit_cast(u32x4, fr), rws, W_WOF + lane * 16, ((u * 72 + ks) * 9 + T) * 1024); }
    }
    FENCE(); BAR(); BAR();
    WAITV(0); BAR();
    if (xw == 0) set_flag(rws, W_F5 + id.b * 8, tag, lane);
  }
  STAMP(3);
  BAR(); BAR(); BAR(); BAR(); BAR();
  if (xw == 0) wait_flags<2>(rws, W_F5 + lane * 16, tag);
  BAR(); BAR();
  STAMP(4);
  { const rsrc_t rh = mkrs(P.ssmk, D * S * 2);
    const int nch = xw ? 1 : 2;
    for (int c = 0; c < nch; ++c) { const int ci = xw ? 1 : 2 * c, chn = ci == 2 ? 512 + (id.b >> 2) : 2 * id.b + ci;
      LAS char* cb = sm + CV + ci * CV_CH;
#pragma unroll
      for (int i = 0; i < 4; ++i) { dma<0>(rh, cb + 32 + i * 1024, lane * 16, chn * (S * 2) + i * 1024);
        dma<0>(rh, cb + CV_O + 32 + i * 1024, lane * 16 + 2, chn * (S * 2) + i * 1024);
        dma<SC1>(rws, cb + CV_U + i * 1024, lane * 16, W_KVT + chn * (S * 2) + i * 1024); }
      const unsigned h0 = P.ssmk[chn * S];
      if (lane < 8) { *(LAS unsigned*)(cb + lane * 4) = 0u; *(LAS unsigned*)(cb + CV_O + lane * 4) = lane == 7 ? h0 << 16 : 0u; }
      if (lane < 34) *(LAS u32x4*)(cb + CV_U + 4096 + lane * 16) = (u32x4){0u, 0u, 0u, 0u};
    }
    WAITV(0); WAITL(0); BAR();
  }
  BAR();
  if (xw == 0) { set_flag(rws, W_F3 + (id.tp * 16 + id.ft) * 8, tag, lane); wait_flags<1>(rws, W_F3 + id.tp * 128 + (lane & 7) * 16, tag); }
  STAMP(5);
  BAR();
  BAR(); BAR(); BAR();
  if (xw == 0) set_flag(rws, W_F4 + id.b * 8, tag, lane);
  BAR(); BAR(); BAR(); BAR(); BAR(); BAR();
  if (xw == 0) {
    wait_flags<2>(rws, W_F4 + lane * 16, tag);
    const int g = id.ft & 7, d0 = 144 * (g >> 1) + 64 * (g & 1);
#pragma unroll
    for (int i = 0; i < 10; ++i) dma<SC1>(rws, sm + YTILE + i * 1024, W_YT + (d0 + (lane >> 3)) * (S * 2) + (id.tp * 128 + 64 * (id.ft >> 3)) * 2 + (lane & 7) * 16, i * 8 * (S * 2));
    if (id.b == 0 && lane == 0) st8<SC1>((u32x2){(unsigned)(epoch + 1), (unsigned)((epoch + 1) >> 32)}, rws, W_EPOCH, 0);
  }
  STAMP(6);
  BAR(); BAR(); BAR(); BAR(); BAR(); BAR(); BAR();
  WAITV(0); BAR();
}

DEV void role_C(const Params& P, LAS char* sm, const Ids id, const int wid STAMP_ARG) {
  const int lane = id.lane, wt = wid >> 1, wf = wid & 1, c = lane & 15, q = lane >> 4;
  const rsrc_t rws = mkrs(P.ws, W_END);
  __builtin_amdgcn_s_setprio(1);
  f32x4 acc[4][4], accx[2];
  bf16x8 tf[2][4], ff[2][5];
  LAS char *ta[2][2], *fb[2], *fx[2];
#pragma unroll
  for (int s = 0; s < 2; ++s)
#pragma unroll
    for (int h = 0; h < 2; ++h) ta[s][h] = sm + (64 * wt + 32 * (h ^ wf) + c) * 128 + (((q + 4 * s) ^ ((c >> 1) & 7)) * 16);
  { const int o = (16 * (q >> 1) + 4 * (q & 1) + (c >> 2)) * 288 + 8 * (c & 3);
#pragma unroll
    for (int h = 0; h < 2; ++h) { fb[h] = sm + BBASE + h * 2 * BSLOT + o + wf * 128; fx[h] = sm + BBASE + h * 2 * BSLOT + o + 256; } }
  auto rd = [&](auto mode, int b, int slot, int s) {
    constexpr int MODE = decltype(mode)::value;
    if (MODE < 2) {
      const int o = (slot & 1) * BSLOT + 32 * s * 288;
#pragma unroll
      for (int j = 0; j < 4; ++j) ff[b][j] = cat(ldtr(fb[slot >> 1] + o + j * 32), ldtr(fb[slot >> 1] + o + j * 32 + 8 * 288));
    } else {
#pragma unroll
      for (int j = 0; j < 4; ++j) ff[b][j] = lds<bf16x8>(fb[slot >> 1] + (slot & 1) * BSLOT + (s * 9 + j) * 1024);
    }
#pragma unroll
    for (int i = 0; i < 4; ++i) tf[b][i] = lds<bf16x8>(ta[s][i >> 1] + slot * ASLOT + (i & 1) * 2048);
    if (MODE < 2) { const int o = (slot & 1) * BSLOT + 32 * s * 288; ff[b][4] = cat(ldtr(fx[slot >> 1] + o), ldtr(fx[slot >> 1] + o + 8 * 288)); }
    else ff[b][4] = lds<bf16x8>(fx[slot >> 1] + (slot & 1) * BSLOT + s * 9 * 1024);
  };
  auto mm = [&](auto mode, int b, int lo, int hi) {
    constexpr int MODE = decltype(mode)::value;
#pragma unroll
    for (int n = 0; n < 18; ++n) if (n >= lo && n < hi) {
      if (n < 16) { const int i = n >> 2, j = (i & 1) ? 3 - (n & 3) : (n & 3);
        acc[i][j] = MODE == 0 ? MFMA(tf[b][i], ff[b][j], acc[i][j]) : MFMA(ff[b][j], tf[b][i], acc[i][j]); }
      else accx[n - 16] = MODE == 0 ? MFMA(tf[b][n - 16], ff[b][4], accx[n - 16]) : MFMA(ff[b][4], tf[b][n - 16], accx[n - 16]);
    }
  };
  auto stage = [&](auto mode, int slot, bool drain) {
    constexpr int MODE = decltype(mode)::value;
    mm(mode, 1, 0, 4); SB();
    if (drain) WAITV(0);
    WAITL(0); BAR(); SB();
    rd(mode, 0, slot, 0); mm(mode, 1, 4, 18);
    if (MODE < 2) { for (int i = 0; i < 14; ++i) { SG(0x100, 1); SG(0x8, 1); } }
    else { for (int i = 0; i < 4; ++i) { SG(0x100, 1); SG(0x8, 1); SG(0x100, 1); SG(0x8, 2); } SG(0x100, 1); SG(0x8, 2); }
    SB();
    rd(mode, 1, slot, 1); mm(mode, 0, 0, 18);
    if (MODE < 2) { for (int i = 0; i < 4; ++i) { SG(0x100, 1); SG(0x8, 1); SG(0x100, 1); SG(0x8, 1); SG(0x100, 1); SG(0x8, 2); } SG(0x100, 1); SG(0x8, 1); SG(0x100, 1); SG(0x8, 1); }
    else { for (int i = 0; i < 9; ++i) { SG(0x100, 1); SG(0x8, 2); } }
    SB();
  };
  auto first = [&](auto mode, int slot) { WAITL(0); BAR(); SB(); rd(mode, 0, slot, 0); rd(mode, 1, slot, 1); mm(mode, 0, 0, 18); SB(); };
  auto zero = [&]() {
#pragma unroll
    for (int i = 0; i < 4; ++i)
#pragma unroll
      for (int j = 0; j < 4; ++j) acc[i][j] = (f32x4){0.f, 0.f, 0.f, 0.f};
    accx[0] = accx[1] = (f32x4){0.f, 0.f, 0.f, 0.f}; };
  const std::integral_constant<int, 0> M0; const std::integral_constant<int, 1> M1; const std::integral_constant<int, 2> M2;

  zero();
  first(M0, 0);
  STAMP(0);
#pragma unroll 1
  for (int it = 0; it < 2; ++it) { stage(M0, 1, false); stage(M0, 2, false); stage(M0, 3, false); stage(M0, 0, false); }
  mm(M0, 1, 0, 18); SB();
  STAMP(1);
  u32x4 z2p[2][4], z2x;
  { float cs[5];
#pragma unroll
    for (int j = 0; j < 5; ++j) cs[j] = lds<float>(sm + T_CS2 + ((j < 4 ? 4 * wf + j : 8) * 16 + c) * 4);
    float red[2][16];
#pragma unroll
    for (int i = 0; i < 4; ++i) {
      const int ro = (64 * wt + 32 * ((i >> 1) ^ wf) + 8 * q + 4 * (i & 1)) * 4;
      const f32x4 m = lds<f32x4>(sm + T_M1 + ro), r = lds<f32x4>(sm + T_R1 + ro);
      f32x4 su = {0.f, 0.f, 0.f, 0.f}, sq = {0.f, 0.f, 0.f, 0.f};
#pragma unroll
      for (int j = 0; j < (i < 2 ? 5 : 4); ++j) {
        const f32x4 z = gelu4(((j < 4 ? acc[i][j] : accx[i]) - m * cs[j]) * r);
        su += z; sq += z * z;
        u32x4& dst = j < 4 ? z2p[i >> 1][j] : z2x;
        dst[2 * (i & 1)] = pk(z[0], z[1]); dst[2 * (i & 1) + 1] = pk(z[2], z[3]);
      }
#pragma unroll
      for (int e = 0; e < 4; ++e) { red[0][4 * i + e] = su[e]; red[1][4 * i + e] = sq[e]; }
    }
    float r8[2][8], r4[2][4];
#pragma unroll
    for (int st = 0; st < 2; ++st) {
#pragma unroll
      for (int e = 0; e < 8; ++e) { const float a = red[st][e] + dpp<XOR8>(red[st][e]), b = red[st][8 + e] + dpp<XOR8>(red[st][8 + e]); r8[st][e] = (c & 8) ? b : a; }
#pragma unroll
      for (int e = 0; e < 4; ++e) { const float a = r8[st][e] + dpp<XOR7>(r8[st][e]), b = r8[st][4 + e] + dpp<XOR7>(r8[st][4 + e]); float t = (c & 4) ? b : a;
        t += dpp<XOR1>(t); t += dpp<XOR2>(t); r4[st][e] = t; }
    }
    if ((c & 3) == 0) { LAS char* pp = sm + STG + wf * 1024 + (64 * wt + 32 * ((c >> 3) ^ wf) + 8 * q + 4 * ((c >> 2) & 1)) * 8;
      *(LAS f32x4*)pp = (f32x4){r4[0][0], r4[1][0], r4[0][1], r4[1][1]}; *(LAS f32x4*)(pp + 16) = (f32x4){r4[0][2], r4[1][2], r4[0][3], r4[1][3]}; }
#pragma unroll
    for (int i = 0; i < 2; ++i) asm volatile("" : "+v"(z2p[i][0]), "+v"(z2p[i][1]), "+v"(z2p[i][2]), "+v"(z2p[i][3]));
    asm volatile("" : "+v"(z2x));
  }
  STAMP(2);
  zero();
  first(M1, 1);
  stage(M1, 2, false); stage(M1, 3, false); stage(M1, 0, false); stage(M1, 1, false);
  STAMP(3);
  {
#pragma unroll
    for (int i = 0; i < 2; ++i) asm volatile("" : "+v"(z2p[i][0]), "+v"(z2p[i][1]), "+v"(z2p[i][2]), "+v"(z2p[i][3]));
    asm volatile("" : "+v"(z2x));
#pragma unroll
    for (int ip = 0; ip < 2; ++ip) {
      const int ro = (64 * wt + 32 * (ip ^ wf) + 8 * q) * 4;
      const f32x4 r0 = lds<f32x4>(sm + T_R2 + ro), r1 = lds<f32x4>(sm + T_R2 + ro + 16), n0 = lds<f32x4>(sm + T_N2 + ro), n1 = lds<f32x4>(sm + T_N2 + ro + 16);
      const unsigned so = W_ZF + ((id.ft * 64 + 4 * id.tp + 2 * wt + (ip ^ wf)) * 9) * 1024;
#pragma unroll
      for (int j = 0; j < (ip == 0 ? 5 : 4); ++j) { const u32x4 z = j < 4 ? z2p[ip][j] : z2x;
        const f32x4 a = (f32x4){blo(z[0]), bhi(z[0]), blo(z[1]), bhi(z[1])} * r0 + n0, b = (f32x4){blo(z[2]), bhi(z[2]), blo(z[3]), bhi(z[3])} * r1 + n1;
        st16<SC1>((u32x4){pk(a[0], a[1]), pk(a[2], a[3]), pk(b[0], b[1]), pk(b[2], b[3])}, rws, lane * 16, so + (j < 4 ? 4 * wf + j : 8) * 1024); }
    }
    SB();
  }
  unsigned braw[4];
  { const rsrc_t rb = mkrs(P.bsp, S * 2);
#pragma unroll
    for (int i = 0; i < 4; ++i) braw[i] = __builtin_amdgcn_raw_buffer_load_b16(rb, (id.tp * 128 + 64 * wt + 16 * (i ^ (2 * wf)) + c) * 2, 0, 0); }
  STAMP(4);
  stage(M1, 2, false); stage(M1, 3, false); stage(M1, 0, true); stage(M1, 1, false);
  mm(M1, 1, 0, 18); SB();
  STAMP(5);
  u32x2 z1p[4][4], z1x[2];
  { float bias[4];
#pragma unroll
    for (int i = 0; i < 4; ++i) bias[i] = blo(braw[i]);
    f32x4 cs[5];
#pragma unroll
    for (int j = 0; j < 5; ++j) cs[j] = lds<f32x4>(sm + T_CS1 + ((j < 4 ? 4 * wf + j : 8) * 16 + 4 * q) * 4);
#pragma unroll
    for (int i = 0; i < 4; ++i) { const int ro = (64 * wt + 16 * (i ^ (2 * wf)) + c) * 4;
      const float m = lds<float>(sm + T_M1 + ro), r = lds<float>(sm + T_R1 + ro);
#pragma unroll
      for (int j = 0; j < (i < 2 ? 5 : 4); ++j) {
        f32x4& a = j < 4 ? acc[i][j] : accx[i];
        const f32x4 z = gelu4((a - m * cs[j]) * r);
        (j < 4 ? z1p[i][j] : z1x[i]) = (u32x2){pk(z[0], z[1]), pk(z[2], z[3])};
        a = (f32x4){bias[i], bias[i], bias[i], bias[i]};
      } }
#pragma unroll
    for (int h = 0; h < 2; ++h) { fb[h] = sm + BBASE + h * 2 * BSLOT + lane * 16 + wf * 4096; fx[h] = sm + BBASE + h * 2 * BSLOT + lane * 16 + 8192; }
#pragma unroll
    for (int i = 0; i < 4; ++i) asm volatile("" : "+v"(z1p[i][0]), "+v"(z1p[i][1]), "+v"(z1p[i][2]), "+v"(z1p[i][3]), "+v"(acc[i][0]), "+v"(acc[i][1]), "+v"(acc[i][2]), "+v"(acc[i][3]));
    asm volatile("" : "+v"(z1x[0]), "+v"(z1x[1]), "+v"(accx[0]), "+v"(accx[1]));
  }
  STAMP(6);
  first(M2, 2);
  STAMP(7);
#pragma unroll 1
  for (int it = 0; it < 7; ++it) { stage(M2, 3, false); stage(M2, 0, false); stage(M2, 1, false); stage(M2, 2, false); }
  stage(M2, 3, false); stage(M2, 0, false); stage(M2, 1, false);
  mm(M2, 1, 0, 4); SB(); WAITL(0); BAR(); SB();
  mm(M2, 1, 4, 18); SB();
  STAMP(8);
#pragma unroll
  for (int i = 0; i < 4; ++i) asm volatile("" : "+v"(z1p[i][0]), "+v"(z1p[i][1]), "+v"(z1p[i][2]), "+v"(z1p[i][3]));
  asm volatile("" : "+v"(z1x[0]), "+v"(z1x[1]));
#pragma unroll
  for (int i = 0; i < 4; ++i) {
    const unsigned so = W_HF + (id.tp * 8 + 4 * wt + (i ^ (2 * wf))) * 72 * 1024;
    auto gate = [&](u32x2 z, f32x4 a) { const f32x4 h = (f32x4){blo(z.x), bhi(z.x), blo(z.y), bhi(z.y)} * a; return (u32x2){pk(h[0], h[1]), pk(h[2], h[3])}; };
#pragma unroll
    for (int pp = 0; pp < 2; ++pp) { const u32x2 lo = gate(z1p[i][2 * pp], acc[i][2 * pp]), hi = gate(z1p[i][2 * pp + 1], acc[i][2 * pp + 1]);
      st16<SC1>((u32x4){lo.x, lo.y, hi.x, hi.y}, rws, lane * 16, so + (4 * id.ft + 2 * wf + pp) * 1024); }
    if (i < 2) st8<SC1>(gate(z1x[i], accx[i]), rws, lane * 16 + (id.ft & 1) * 8, so + (64 + (id.ft >> 1)) * 1024);
  }
  SB(); BAR(); SB();
  STAMP(9);
  f32x4 cm[8];
  { const unsigned dm = ((const unsigned*)P.ssmd)[id.b], dsh = ((const unsigned*)P.ssmd)[256 + (id.b >> 3)];
    const float dvm = wt ? bhi(dm) : blo(dm), dvs = (id.b & 4) ? bhi(dsh) : blo(dsh);
    const int aoff = ((c & 1) ? 0 : CV_O) + (4 * q + ((c + 1) >> 1)) * 4, boff = CV_U + (2032 - 16 * c + 8 * q) * 2;
    auto afrag = [&](LAS char* p) { const unsigned d0 = lds<unsigned>(p), d1 = lds<unsigned>(p + 4), d2 = lds<unsigned>(p + 8), d3 = lds<unsigned>(p + 12);
      return __builtin_bit_cast(bf16x8, (u32x4){d0, d1, d2, d3}); };
    { const int jq = id.b & 3; LAS char* cb = sm + CV + 2 * CV_CH;
      f32x4 s0 = {0.f, 0.f, 0.f, 0.f}, s1 = s0;
#pragma unroll
      for (int kk = 0; kk < 2; ++kk) { const int k0 = 2 * wid + kk;
#pragma unroll
        for (int e = 0; e < 8; ++e) { const bf16x8 a = afrag(cb + aoff + (k0 + 8 * e) * 64);
          const int o1 = 7 - jq - e >= 0 ? 64 * k0 - 512 * (7 - jq - e) : 512;
          s1 = MFMA(a, lds<bf16x8>(cb + boff + o1), s1);
          if (e < 4) { const int o0 = jq - e >= 0 ? 64 * k0 - 512 * (jq - e) : 512; s0 = MFMA(a, lds<bf16x8>(cb + boff + o0), s0); } } }
      *(LAS f32x4*)(sm + CV_PS + (wid * 2) * 1024 + lane * 16) = s0; *(LAS f32x4*)(sm + CV_PS + (wid * 2 + 1) * 1024 + lane * 16) = s1; }
    LAS char* cb = sm + CV + wt * CV_CH;
#pragma unroll
    for (int j = 0; j < 8; ++j) cm[j] = (f32x4){0.f, 0.f, 0.f, 0.f};
    auto block = [&](int k0) { bf16x8 a[8], bb[8];
#pragma unroll
      for (int e = 0; e < 8; ++e) { bb[e] = lds<bf16x8>(cb + boff + 64 * k0 - 512 * e); a[e] = afrag(cb + aoff + (k0 + 8 * e) * 64); }
#pragma unroll
      for (int e = 0; e < 8; ++e)
#pragma unroll
        for (int j = e; j < 8; ++j) cm[j] = MFMA(a[e], bb[j - e], cm[j]); };
    block(4 * wf);
    SB(); WAITV(0); WAITL(0); BAR(); SB();
    STAMP(10);
    f32x4 ys = {0.f, 0.f, 0.f, 0.f};
    if (wid < 2) {
#pragma unroll
      for (int w = 0; w < 4; ++w) ys += lds<f32x4>(sm + CV_PS + (w * 2 + wid) * 1024 + lane * 16);
    }
#pragma unroll 1
    for (int kk = 1; kk < 4; ++kk) block(4 * wf + kk);
    LAS char* pm = sm + (wt ? CV_PM23 : CV + 2 * CV_CH);
#pragma unroll
    for (int j = 0; j < 4; ++j) { f32x4 lo = cm[j], hi = cm[4 + j]; asm volatile("" : "+v"(lo), "+v"(hi));
      *(LAS f32x4*)(pm + wf * 4096 + j * 1024 + lane * 16) = wf ? lo : hi; cm[j] = wf ? hi : lo; }
    SB(); WAITL(0); BAR(); SB();
    STAMP(11);
    const int half = id.ft >> 3;
    const unsigned hso = W_HF + (id.tp * 8 + 4 * half + wid) * 72 * 1024;
    u32x4 hb[12];
#pragma unroll
    for (int i = 0; i < 12; ++i) hb[i] = ld16<SC1>(rws, lane * 16, hso + i * 1024);
    auto yout = [&](f32x4 a, int chn, LAS char* ub, int j, float dv) {
      const u32x2 u = lds<u32x2>(ub + CV_U + (2044 - 256 * j - 16 * c - 4 * q) * 2);
      const f32x4 y = a + (f32x4){bhi(u.y), blo(u.y), bhi(u.x), blo(u.x)} * dv;
      st8<SC1>((u32x2){pk(y[0], y[1]), pk(y[2], y[3])}, rws, (16 * c + 4 * q) * 2, W_YT + chn * (S * 2) + j * 512); };
#pragma unroll
    for (int j = 0; j < 4; ++j) yout(cm[j] + lds<f32x4>(pm + (wf ^ 1) * 4096 + j * 1024 + lane * 16), 2 * id.b + wt, cb, 4 * wf + j, dvm);
    if (wid < 2) yout(ys, 512 + (id.b >> 2), sm + CV + 2 * CV_CH, wid ? 7 - (id.b & 3) : (id.b & 3), dvs);
    const int g = id.ft & 7, dp = 144 * (g >> 1) + 80 * (g & 1), ds = 144 * (g >> 1) + 64, trow = 64 * half + 16 * wid + c;
    const rsrc_t rx = mkrs(P.x, S * D * 2);
    const unsigned xo = (id.tp * 128 + trow) * (D * 2);
    const u32x4 x0 = ld16<0>(rx, xo + q * 16, dp * 2), x1 = ld16<0>(rx, xo + q * 16, dp * 2 + 64);
    const u32x2 x2 = __builtin_amdgcn_raw_buffer_load_b64(rx, xo + q * 8, ds * 2, 0);
    SB();
    f32x4 o[5];
#pragma unroll
    for (int t = 0; t < 5; ++t) o[t] = (f32x4){0.f, 0.f, 0.f, 0.f};
    LAS char* wb[2] = {sm + lane * 16, sm + 3 * G3SLOT + lane * 16};
    bf16x8 wq[2][5];
    auto rdw = [&](int b, int slot, int kk) {
#pragma unroll
      for (int t = 0; t < 5; ++t) wq[b][t] = lds<bf16x8>(wb[slot / 3] + (slot % 3) * G3SLOT + (kk * 5 + t) * 1024); };
    rdw(0, 0, 0);
#pragma unroll 1
    for (int it = 0; it < 3; ++it) {
#pragma unroll
      for (int sl = 0; sl < 6; ++sl)
#pragma unroll
        for (int kk = 0; kk < 4; ++kk) { const int n = sl * 4 + kk;
          if (kk == 3) { if (sl == 5 && it == 2) {} else { SB(); if (sl == 2 && it == 0) WAITV(12); WAITL(0); BAR(); SB(); rdw((n + 1) & 1, sl == 5 ? 0 : sl + 1, 0); } }
          else rdw((n + 1) & 1, sl, kk + 1);
          const bf16x8 hf = __builtin_bit_cast(bf16x8, hb[n % 12]);
#pragma unroll
          for (int t = 0; t < 5; ++t) o[t] = MFMA(wq[n & 1][t], hf, o[t]);
          hb[n % 12] = ld16<SC1>(rws, lane * 16, it * 24 + n + 12 < 72 ? hso + (it * 24 + n + 12) * 1024 : 0xF0000000u);
          SB();
        }
    }
    STAMP(12);
    const float m = lds<float>(sm + T_M1 + trow * 4), r = lds<float>(sm + T_R1 + trow * 4);
    LAS char* yb = sm + YTILE + (c >> 2) * 128 + (16 * wid + 4 * (c & 3)) * 2;
    const rsrc_t ro = mkrs(P.out, S * D * 2);
    auto fin = [&](u32x2 xv, bf16x4 yv, f32x4 a) { const u32x2 yu = __builtin_bit_cast(u32x2, yv);
      const f32x4 xf = {blo(xv.x), bhi(xv.x), blo(xv.y), bhi(xv.y)}, yf = {blo(yu.x), bhi(yu.x), blo(yu.y), bhi(yu.y)};
      const f32x4 t = xf + (xf - m) * r * yf + a; return (u32x2){pk(t[0], t[1]), pk(t[2], t[3])}; };
    const int yp = 16 * (g & 1), ysh = 64 * ((g & 1) ^ 1);
#pragma unroll
    for (int pp = 0; pp < 2; ++pp) { const u32x4 xv = pp ? x1 : x0;
      const u32x2 lo = fin(xv.xy, ldtr(yb + (yp + 32 * pp + 8 * q) * 128), o[2 * pp]), hi = fin(xv.zw, ldtr(yb + (yp + 32 * pp + 8 * q + 4) * 128), o[2 * pp + 1]);
      st16<0>((u32x4){lo.x, lo.y, hi.x, hi.y}, ro, xo + q * 16, dp * 2 + 64 * pp); }
    const u32x2 ls = fin(x2, ldtr(yb + (ysh + 4 * q) * 128), o[4]);
    if (wt == (g & 1)) st8<0>(ls, ro, xo + q * 8, ds * 2);
    STAMP(13);
  }
}
}

extern "C" __global__ __launch_bounds__(512, 2) void h3_gmlp_fused(const Params P) {
  extern __shared__ __attribute__((aligned(16))) char smem_[];
  LAS char* sm = (LAS char*)smem_;
  const int wid = __builtin_amdgcn_readfirstlane(threadIdx.x >> 6);
  Ids id; id.b = blockIdx.x; id.lane = threadIdx.x & 63;
  const int xcd = id.b & 7, loc = id.b >> 3;
  id.tp = (xcd >> 1) * 4 + (loc >> 3); id.ft = (xcd & 1) * 8 + (loc & 7);
  unsigned stampv = 0; { const int lane = id.lane; STAMP(63); }
  if (wid == 3 && id.lane == 63) __hip_atomic_store((unsigned*)(P.ws + (41 << 20)) + blockIdx.x, stampv, __ATOMIC_RELAXED, __HIP_MEMORY_SCOPE_AGENT);
  if (wid == 0) {
    unsigned lo, hi, bytes, sink;
    asm volatile("s_getpc_b64 vcc\n1:\n\ts_sub_u32 %0, vcc_lo, 1b-h3_gmlp_fused\n\ts_subb_u32 %1, vcc_hi, 0\n\ts_mov_b32 %2, h3_gmlp_end-h3_gmlp_fused" : "=s"(lo), "=s"(hi), "=s"(bytes) :: "vcc", "scc");
    const rsrc_t rc = mkrs((const void*)(((u64)hi << 32) | lo), bytes);
    asm volatile("buffer_load_dword %0, %1, %2, 0 offen\n\ts_waitcnt vmcnt(0)" : "=&v"(sink) : "v"((id.lane < 16 ? id.lane * 32 + loc : 0x100000) * 128), "s"(rc) : "memory");
  }
  if (wid == 4 || wid == 5) role_L(P, sm, id, wid - 4 STAMP_PASS);
  else if (wid >= 6) role_X(P, sm, id, wid - 6 STAMP_PASS);
  else role_C(P, sm, id, wid STAMP_PASS);
  { const int lane = id.lane; STAMP(62); *(unsigned*)(P.ws + (40 << 20) + ((blockIdx.x * 8 + wid) * 64 + lane) * 4) = stampv; }
  if (wid == 0) { asm volatile("s_waitcnt vmcnt(0)" ::: "memory"); const unsigned t_ = (unsigned)__builtin_amdgcn_s_memrealtime(); if (id.lane == 0) ((unsigned*)(P.ws + (42 << 20)))[blockIdx.x] = t_; }
}
extern "C" __global__ void h3_gmlp_end() {}

extern "C" __global__ void k_pre(unsigned* p) { const unsigned t_ = (unsigned)__builtin_amdgcn_s_memrealtime(); if (threadIdx.x == 0) p[0] = t_; }
extern "C" __global__ void k_post(const char* ws) { const unsigned tn = (unsigned)__builtin_amdgcn_s_memrealtime();
  unsigned mn = 0xffffffffu, mx = 0; const int lane = threadIdx.x;
  for (int i = 0; i < 4; ++i) { const unsigned v = ((const unsigned*)(ws + (41 << 20)))[i * 64 + lane], w = ((const unsigned*)(ws + (42 << 20)))[i * 64 + lane]; mn = v < mn ? v : mn; mx = w > mx ? w : mx; }
  for (int o = 32; o; o >>= 1) { const unsigned a = __shfl_xor(mn, o), b = __shfl_xor(mx, o); mn = a < mn ? a : mn; mx = b > mx ? b : mx; }
  const unsigned tp = ((const unsigned*)(ws + (43 << 20)))[0];
  const unsigned val = MK_AMP == 0 ? 0 : MK_AMP == 1 ? mn - tp : MK_AMP == 2 ? tn - mx : mx - mn;
  const unsigned long long d_ = (unsigned long long)val * MK_K, t0_ = __builtin_amdgcn_s_memrealtime();
  while (__builtin_amdgcn_s_memrealtime() - t0_ < (d_ > 200000 ? 200000 : d_)) __builtin_amdgcn_s_sleep(2); }
extern "C" void kernel_launch(void* const* d_in, const int* in_sizes, int n_in, void* d_out, int out_size, void* d_ws, size_t ws_size, hipStream_t stream) {
  Params p;
  p.x = (const u16*)d_in[0]; p.k = (const u16*)d_in[1]; p.v = (const u16*)d_in[2]; p.shk = (const u16*)d_in[3]; p.ssmk = (const u16*)d_in[4]; p.ssmd = (const u16*)d_in[5];
  p.win = (const u16*)d_in[7]; p.wsp = (const u16*)d_in[8]; p.bsp = (const u16*)d_in[9]; p.wout = (const u16*)d_in[10];
  p.out = (u16*)d_out; p.ws = (char*)d_ws;
  (void)hipFuncSetAttribute((const void*)h3_gmlp_fused, hipFuncAttributeMaxDynamicSharedMemorySize, LDS_BYTES);
  if (MK_PRE) k_pre<<<1, 64, 0, stream>>>((unsigned*)(p.ws + (43 << 20)));
  h3_gmlp_fused<<<256, 512, LDS_BYTES, stream>>>(p);
  if (MK_POST) k_post<<<1, 64, 0, stream>>>(p.ws);
}
```

```cpp
#include <hip/hip_runtime.h>
#include <hip/hip_bf16.h>
#include <type_traits>

namespace {
typedef unsigned short u16;
typedef unsigned long long u64;
typedef short bf16x8 __attribute__((ext_vector_type(8)));
typedef short bf16x4 __attribute__((ext_vector_type(4)));
typedef __bf16 bf2 __attribute__((ext_vector_type(2)));
typedef float f32x2 __attribute__((ext_vector_type(2)));
typedef float f32x4 __attribute__((ext_vector_type(4)));
typedef unsigned u32x2 __attribute__((ext_vector_type(2)));
typedef unsigned u32x4 __attribute__((ext_vector_type(4)));
typedef __amdgpu_buffer_rsrc_t rsrc_t;
#define LAS __attribute__((address_space(3)))
#define CAS __attribute__((address_space(4)))
#define DEV __device__ __forceinline__

constexpr int S = 2048, D = 576, F = 4608, FH = 2304;
constexpr int ASLOT = 16384, BSLOT = 18432, BBASE = 4 * ASLOT;
constexpr int STG = 139264, STG_W = 9216;
constexpr int T_M1 = 157952, T_R1 = T_M1 + 512, T_A0 = T_R1 + 512, T_A1 = T_A0 + 512, T_IR = T_A1 + 512;
constexpr int T_CS2 = T_IR + 512, T_CS1 = T_CS2;
constexpr int T_PART = T_CS1 + 640, T_N2 = T_PART, T_R2 = T_N2 + 512;
constexpr int T_TAG = T_PART + 2048, T_CNT = T_TAG + 16, T_GO = T_CNT + 4, T_YOK = T_CNT + 8;
constexpr int LDS_BYTES = 163840;
static_assert(T_CNT + 16 <= LDS_BYTES, "LDS map");
constexpr int G3W = 25600;
constexpr int CV = 102400, CV_CH = 13056, CV_O = 4160, CV_U = 8320;
constexpr int CV_PS = 143616, CV_PM23 = 149760;
constexpr int YTILE = 122880, XTILE = 133120;
constexpr int W_EPOCH = 0, W_F2 = 4096, W_F3 = 8192, W_F4 = 12288, W_F5 = 16384;
constexpr int W_G0X = 0x8000, W_G1F = 0x10000, W_G1S = 0x60000;
constexpr int W_KVT = 1 << 20, W_YT = 4 << 20, W_WOF = 7 << 20, W_ZF = 10 << 20, W_HF = 20 << 20, W_END = 30 << 20;
constexpr int SC1 = 16;
#define OPT_TWIN 0
#define OPT_TWSP 1
#define OPT_TG3 0
#define EXP 0
#define RH 0
#define RW 0
#define WD 3

#define BAR0() do { asm volatile("" ::: "memory"); __builtin_amdgcn_s_barrier(); asm volatile("" ::: "memory"); } while (0)
#define BAR() BAR0()
#define SB() __builtin_amdgcn_sched_barrier(0)
#define SG(m, n) __builtin_amdgcn_sched_group_barrier(m, n, 0)
#define WAITV(n) asm volatile("s_waitcnt vmcnt(%0)" ::"i"(n) : "memory")
#define WAITL(n) asm volatile("s_waitcnt lgkmcnt(%0)" ::"i"(n) : "memory")
#define FENCE() asm volatile("" ::: "memory")
#define MFMA(a, b, c) __builtin_amdgcn_mfma_f32_16x16x32_bf16(a, b, c, 0, 0, 0)
#define STAMP(i) do {} while (0)
#define LBAR() BAR()
#define STAMP_ARG
#define STAMP_PASS

struct Params {
  const u16 *x, *k, *v, *shk, *ssmk, *ssmd, *win, *wsp, *bsp, *wout;
  u16* out; char* ws;
};

DEV rsrc_t mkrs(const void* p, unsigned bytes) { return __builtin_amdgcn_make_buffer_rsrc((void*)p, 0, bytes, 0x00020000); }
template <int AUX> DEV void dma(rsrc_t rs, LAS char* l, unsigned voff, unsigned soff) { __builtin_amdgcn_raw_ptr_buffer_load_lds(rs, (LAS void*)l, 16, voff, soff, 0, AUX); }
DEV void dma_hidden(rsrc_t rs, LAS char* l, unsigned voff) { unsigned keep;
  asm volatile("s_mov_b32 %0, m0\n\ts_mov_b32 m0, %3\n\ts_nop 0\n\tbuffer_load_dwordx4 %1, %2, 0 offen lds\n\ts_mov_b32 m0, %0" : "=&s"(keep) : "v"(voff), "s"(rs), "s"((unsigned)(size_t)l) : "memory"); }
template <int AUX> DEV u32x4 ld16(rsrc_t rs, unsigned voff, unsigned soff) { return __builtin_amdgcn_raw_buffer_load_b128(rs, voff, soff, AUX); }
template <int AUX> DEV void st16(u32x4 v, rsrc_t rs, unsigned voff, unsigned soff) { __builtin_amdgcn_raw_buffer_store_b128(v, rs, voff, soff, AUX); }
template <int AUX> DEV void st8(u32x2 v, rsrc_t rs, unsigned voff, unsigned soff) { __builtin_amdgcn_raw_buffer_store_b64(v, rs, voff, soff, AUX); }
template <class T> DEV T lds(LAS char* p) { return *(LAS T*)p; }
DEV bf16x4 ldtr(LAS char* p) { return __builtin_amdgcn_ds_read_tr16_b64_v4i16((LAS bf16x4*)p); }
DEV bf16x8 cat(bf16x4 a, bf16x4 b) { return __builtin_shufflevector(a, b, 0, 1, 2, 3, 4, 5, 6, 7); }
DEV float blo(unsigned u) { return __uint_as_float(u << 16); }
DEV float bhi(unsigned u) { return __uint_as_float(u & 0xffff0000u); }
DEV unsigned pk(float a, float b) { return __builtin_bit_cast(unsigned, __builtin_convertvector((f32x2){a, b}, bf2)); }
DEV float dot2(unsigned a, unsigned b, float c) { return __builtin_amdgcn_fdot2_f32_bf16(__builtin_bit_cast(bf2, a), __builtin_bit_cast(bf2, b), c, false); }
template <int CTRL> DEV float dpp(float x) { return __builtin_bit_cast(float, __builtin_amdgcn_mov_dpp(__builtin_bit_cast(int, x), CTRL, 0xf, 0xf, true)); }
constexpr int XOR1 = 0xB1, XOR2 = 0x4E, XOR7 = 0x141, XOR8 = 0x128;
constexpr float GC0 = -2.30220819f, GC1 = -0.10294324f;
template <class T> DEV f32x2 gelu2(f32x2 a, T a0, T a1, T ir) {
  const f32x2 g = a * (a * a * a1 + a0);
  f32x2 e; e.x = __builtin_amdgcn_exp2f(g.x); e.y = __builtin_amdgcn_exp2f(g.y);
  e = e * ir + ir;
  f32x2 r; r.x = __builtin_amdgcn_rcpf(e.x); r.y = __builtin_amdgcn_rcpf(e.y);
  return a * r;
}
DEV f32x4 gelu4(f32x4 a, f32x4 a0, f32x4 a1, f32x4 ir) { const f32x2 p = gelu2(a.xy, a0.xy, a1.xy, ir.xy), q = gelu2(a.zw, a0.zw, a1.zw, ir.zw); return (f32x4){p.x, p.y, q.x, q.y}; }
DEV f32x4 gelu4(f32x4 a, float a0, float a1, float ir) { const f32x2 p = gelu2(a.xy, a0, a1, ir), q = gelu2(a.zw, a0, a1, ir); return (f32x4){p.x, p.y, q.x, q.y}; }
DEV u32x4 seal(float a, float b, u32x2 tag) { const unsigned ua = __float_as_uint(a), ub = __float_as_uint(b); return (u32x4){ua, ub, ua ^ tag.x, ub ^ tag.y}; }
DEV bool sealed(u32x4 g, u32x2 tag) { return (g.x ^ g.z) == tag.x && (g.y ^ g.w) == tag.y; }
DEV bool expired(u64 t0) { return __builtin_amdgcn_s_memrealtime() - t0 > 2000000ull; }
template <int NL, int NAP> DEV void wait_flags(rsrc_t ws, unsigned voff, u32x2 tag) {
  const u64 t0 = __builtin_amdgcn_s_memrealtime();
  for (;;) {
    bool ok = true;
#pragma unroll
    for (int i = 0; i < NL; ++i) { const u32x4 f = ld16<SC1>(ws, voff, i * 1024); ok &= f.x == tag.x && f.y == tag.y && f.z == tag.x && f.w == tag.y; }
    if (__all(ok) || expired(t0)) break;
    __builtin_amdgcn_s_sleep(NAP); FENCE();
  }
  FENCE();
}
DEV bool flags_ok(u32x4 f, u32x2 tag) { return f.x == tag.x && f.y == tag.y && f.z == tag.x && f.w == tag.y; }
DEV int prow(int T, int c) { return 32 * (T >> 1) + 8 * (c >> 2) + 4 * (T & 1) + (c & 3); }
DEV void set_flag(rsrc_t ws, unsigned off, u32x2 tag, int lane) { if (lane == 0) st8<SC1>(tag, ws, off, 0); }

struct Ids { int b, tp, ft, lane; };
DEV int krot(const Ids& id) { return (RH * (id.ft & 7) + ((RW * (id.tp & 3)) >> 1)) % 18; }
DEV int kwrap(int i, int r) { const int k = i + r; return k >= 18 ? k - 18 : k; }

DEV void role_L(const Params& P, LAS char* sm, const Ids id, const int lw STAMP_ARG) {
  int bcount = 0; (void)bcount;
  const int lane = id.lane, rr = lane >> 3, sl = lane & 7;
  const rsrc_t rx = mkrs(P.x, S * D * 2), rwin = mkrs(P.win, D * F * 2);
  const unsigned swz = (sl ^ (4 * lw + (rr >> 1))) * 16, prr = 16 * lw + 8 * (rr >> 2) + (rr & 3);
  const unsigned vx = prr * (D * 2) + swz;
  unsigned vb[9];
  auto issue1a = [&](int slot, int i, int ks) { dma<0>(rx, sm + slot * ASLOT + lw * 1024 + i * 2048, vx, (id.tp * 128 + 32 * (i >> 1) + 4 * (i & 1)) * (D * 2) + ks * 128); };
  auto issue1b = [&](int slot, int i, int ks, int col) { dma<0>(rwin, sm + BBASE + slot * BSLOT + lw * 1024 + i * 2048, vb[i], ks * 64 * (F * 2) + (col + id.ft * 144) * 2); };
#pragma unroll
  for (int i = 0; i < 9; ++i) {
    if (i < 8) issue1a(0, i, 0);
    SB();
    const unsigned ci = 64 * (2 * i + lw) + lane, rho = __umul24(ci, 3641) >> 16;
    vb[i] = __umul24(rho, F * 2 - 288) + 16 * ci + __mul24((int)(rho & 4) - (int)((rho >> 1) & 4), F * 2);
    issue1b(0, i, 0, FH);
    SB();
  }
  auto issueA = [&](int n) {
#pragma unroll
    for (int i = 0; i < 9; ++i) { if (i < 8) issue1a(n & 3, i, n); issue1b(n & 3, i, n, FH); } };
  auto issueB = [&](int n, bool with_x) {
#pragma unroll
    for (int i = 0; i < 9; ++i) { if (with_x && i < 8) issue1a((9 - n) & 3, i, 17 - n); issue1b(n & 3, i, 17 - n, 0); } };
  issueA(1); issueA(2);
  STAMP(60);
  const rsrc_t rwsp = mkrs(P.wsp, S * S * 2), rws = mkrs(P.ws, W_END);
  const unsigned vw = prr * (S * 2) + swz, vl = lane * 16 + lw * 1024;
  auto issue2a = [&](int n) { LAS char* a = sm + ((9 - n) & 3) * ASLOT + lw * 1024; const unsigned sa = id.tp * 128 * (S * 2) + (n - 18) * 128;
#pragma unroll
    for (int i = 0; i < 8; ++i) dma<0>(rwsp, a + i * 2048, vw, sa + (32 * (i >> 1) + 4 * (i & 1)) * (S * 2)); };
  auto issue2b = [&](int n) { LAS char* bb = sm + BBASE + (n & 3) * BSLOT + lw * 1024; const unsigned sb = W_ZF + (id.ft * 576 + (n - 18) * 18) * 1024;
#pragma unroll
    for (int i = 0; i < 9; ++i) dma<SC1>(rws, bb + i * 2048, vl, sb + i * 2048); };
#pragma unroll 1
  for (int n = 0; n < 6; ++n) { WAITV(34); LBAR(); issueA(n + 3); }
  WAITV(34); LBAR(); issueB(9, false);
  WAITV(26); LBAR(); issueB(10, false);
  WAITV(18); LBAR(); issueB(11, false);
  BAR();
  WAITV(18); LBAR(); issueB(12, false);
  WAITV(18); LBAR(); issueB(13, true);
  WAITV(26); LBAR();
#pragma unroll 1
  for (int n = 12; n < 16; ++n) { WAITV(17); LBAR(); issueB(n + 2, true); }
  WAITV(17); LBAR(); issue2a(18);
  WAITV(8); LBAR(); issue2a(19);
  BAR(); issue2a(20);
  { const u32x2 tag = lds<u32x2>(sm + T_TAG); wait_flags<1, 2>(rws, W_F2 + id.ft * 128 + (lane & 7) * 16, tag); }
  STAMP(61);
  issue2b(18); issue2b(19); issue2b(20);
  asm volatile("ds_write_b32 %0, %1" :: "v"((unsigned)(size_t)(sm + T_GO)), "v"(1u) : "memory");
  WAITV(18); LBAR(); issue2a(21); issue2b(21);
  WAITV(26); LBAR(); issue2a(22); issue2b(22);
#pragma unroll 1
  for (int n = 20; n < 47; ++n) { WAITV(34); LBAR(); issue2a(n + 3); issue2b(n + 3); }
  const int g = id.ft & 7;
  const unsigned wb0 = W_WOF + (g >> 1) * 72 * 9 * 1024, wb1 = wb0 + (g & 1) * 5 * 1024, vq = lane * 16;
  const int rot = krot(id);
  auto issue3 = [&](auto lo_, auto hi_) { constexpr int lo = decltype(lo_)::value, hi = decltype(hi_)::value;
#pragma unroll
    for (int p0 = lo; p0 < hi; p0 += 2) {
      auto off = [&](int pc) { return (pc % 5 == 4 ? wb0 : wb1) + (18 * (pc / 25) * 9 + pc % 5) * 1024 + kwrap((pc % 25) / 5, rot) * 9216; };
      dma<SC1>(rws, sm + p0 * 1024 + lw * 1024, vq, lw ? off(p0 + 1) : off(p0)); } };
#define IC(n) std::integral_constant<int, n>{}
  WAITV(34); LBAR(); issue3(IC(48), IC(64));
  WAITV(25); LBAR();
  WAITV(8); LBAR(); issue3(IC(16), IC(48));
  LBAR();
  LBAR(); issue3(IC(0), IC(16)); issue3(IC(64), IC(100));
  unsigned sink = 0;
  auto wtouch = [&]() {
    constexpr int K0 = 5 + WD, NK = 18 - K0;
#pragma unroll
    for (int i = 0; i < (NK * 40 + 127) / 128; ++i) { const unsigned n = (i * 2 + lw) * 64 + lane, blk = n >> 1, kw = __umul24(blk, 13108) >> 16, t = blk - kw * 5;
      const unsigned o = n < NK * 40 ? (t == 4 ? wb0 : wb1) + ((18 * (kw & 3) + K0 + (kw >> 2)) * 9 + t) * 1024 + ((id.tp & 3) * 2 + (n & 1)) * 128 : 0xF0000000u;
      asm volatile("buffer_load_dword %0, %1, %2, 0 offen sc1" : "+v"(sink) : "v"(o), "s"(rws) : "memory"); } };
  if ((OPT_TG3 & 3) == 1) wtouch();
  LBAR();
  if ((OPT_TG3 & 3) == 2) { __builtin_amdgcn_s_sleep(2); wtouch(); }
  WAITV((OPT_TG3 & 3) == 2 ? (( 18 - 5 - WD) * 40 + 127) / 128 : 0); LBAR();
  asm volatile("s_waitcnt vmcnt(0)" : "+v"(sink) :: "memory"); LBAR();
}

DEV void role_X(const Params& P, LAS char* sm, const Ids id, const int xw STAMP_ARG) {
  int bcount = 0; (void)bcount;
  const int lane = id.lane;
  const rsrc_t rws = mkrs(P.ws, W_END);
  unsigned sink = 0;
  const u32x2 ev = __builtin_amdgcn_raw_buffer_load_b64(rws, W_EPOCH, 0, SC1);
  const int srow = id.tp * 128 + id.ft * 8 + 4 * xw + (lane >> 4);
  u32x4 xv[5];
  { const rsrc_t rx = mkrs(P.x, S * D * 2);
#pragma unroll
    for (int i = 0; i < 5; ++i) xv[i] = ld16<0>(rx, i < 4 || (lane & 8) == 0 ? srow * (D * 2) + (lane & 15) * 16 : 0xF0000000u, i * 256); }
  const u64 epoch = ((u64)__builtin_amdgcn_readfirstlane(ev.y) << 32) | __builtin_amdgcn_readfirstlane(ev.x);
  u32x2 tag; { const u64 t = ((epoch + 1) * 0x9E3779B97F4A7C15ull) | 1ull; tag.x = (unsigned)t; tag.y = (unsigned)(t >> 32); }
  { float s = 0.f, q = 0.f; unsigned ones = 0x3f803f80u; asm volatile("" : "+v"(ones));
#pragma unroll
    for (int i = 0; i < 5; ++i)
#pragma unroll
      for (int e = 0; e < 4; ++e) { s = dot2(xv[i][e], ones, s); q = dot2(xv[i][e], xv[i][e], q); }
    s += dpp<XOR1>(s); q += dpp<XOR1>(q); s += dpp<XOR2>(s); q += dpp<XOR2>(q); s += dpp<XOR7>(s); q += dpp<XOR7>(q); s += dpp<XOR8>(s); q += dpp<XOR8>(q);
    const float m = s * (1.0f / D), var = fmaxf(q * (1.0f / D) - m * m, 0.f) + 1e-5f;
    if ((lane & 15) == 0) st16<SC1>(seal(m, var, tag), rws, W_G0X + srow * 16, 0);
    if (xw == 0 && lane == 0) { *(LAS u32x2*)(sm + T_TAG) = tag; *(LAS u32x4*)(sm + T_CNT) = (u32x4){0u, 0u, 0u, 0u}; }
  }
  STAMP(0);
  BAR(); BAR(); BAR(); BAR();
  LAS char* stg = sm + STG + xw * STG_W;
  const int unit = xw ? 256 + id.b : id.b, wks = unit >> 2, wu = unit & 3; const bool won = !xw || id.b < 32;
  const int tile = xw ? id.b : 256 + id.b - 32; const bool kon = xw || (id.b >= 32 && id.b < 64);
  const int tb = tile / 9, db = tile - tb * 9, cc = lane & 7, rg = lane >> 3;
  u32x4 kk[11], vv[8], sh[4];
  { const int row = 64 * xw + lane; const u64 t0 = __builtin_amdgcn_s_memrealtime();
    u32x4 gx = ld16<SC1>(rws, W_G0X + (id.tp * 128 + row) * 16, 0);
    BAR(); BAR();
    while (!__all(sealed(gx, tag)) && !expired(t0)) { __builtin_amdgcn_s_sleep(4); FENCE(); gx = ld16<SC1>(rws, W_G0X + (id.tp * 128 + row) * 16, 0); }
    const float var = __uint_as_float(gx.y), r = __builtin_amdgcn_rsqf(var);
    *(LAS unsigned*)(sm + T_M1 + row * 4) = gx.x; *(LAS float*)(sm + T_R1 + row * 4) = r;
    *(LAS float*)(sm + T_A0 + row * 4) = GC0 * r; *(LAS float*)(sm + T_A1 + row * 4) = GC1 * r * r * r; *(LAS float*)(sm + T_IR + row * 4) = var * r;
    WAITL(0); BAR();
  }
  STAMP(1);
  { const rsrc_t rk = mkrs(P.k, S * D * 2), rv = mkrs(P.v, S * D * 2), rs = mkrs(P.shk, D * 4 * 2), rwo = mkrs(P.wout, FH * D * 2);
    const int t0 = 64 * tb + 8 * rg; const unsigned col = (64 * db + 8 * cc) * 2;
    if (kon) {
#pragma unroll
      for (int i = 0; i < 11; ++i) kk[i] = ld16<0>(rk, t0 + i - 3 >= 0 ? (unsigned)(t0 + i - 3) * (D * 2) + col : 0xF0000000u, 0);
    }
    BAR();
    if (kon) {
#pragma unroll
      for (int i = 0; i < 8; ++i) vv[i] = ld16<0>(rv, t0 * (D * 2) + col, i * (D * 2));
#pragma unroll
      for (int i = 0; i < 4; ++i) sh[i] = ld16<0>(rs, col * 4, i * 16);
    }
    BAR();
    if (won) { const int fb0 = wks < 64 ? 144 * (wks >> 2) + 32 * (wks & 3) : 288 * (wks - 64) + 128, fst = wks < 64 ? 0 : 128;
#pragma unroll
      for (int i = 0; i < 9; ++i) { const int ci = 64 * i + lane, rho = __umul24(ci, 3641) >> 16, ch = ci - rho * 18;
        dma_hidden(rwo, stg + i * 1024, (fb0 + rho + (rho >> 4) * fst) * (D * 2) + wu * 288 + ch * 16); } }
    BAR();
    if (kon) {
#pragma unroll
      for (int i = 0; i < 4; ++i) asm volatile("" : "+v"(sh[i]), "+v"(vv[i]), "+v"(vv[4 + i]), "+v"(kk[i]), "+v"(kk[4 + i]), "+v"(kk[i == 3 ? 0 : 8 + i])); }
    if (OPT_TWSP) { const rsrc_t r = mkrs(P.wsp, S * S * 2);
#pragma unroll
      for (int i = 0; i < 2; ++i) asm volatile("buffer_load_dword %0, %1, %2, 0 offen" : "+v"(sink) : "v"(id.b * 32768 + ((2 * i + xw) * 64 + lane) * 128), "s"(r) : "memory"); }
  }
  BAR();
  u32x4 ko[8];
  auto kvstep = [&](int e) {
    if (kon) {
      float c[4], kf[11], y[8];
#pragma unroll
      for (int j = 0; j < 4; ++j) { const unsigned w = sh[e >> 1][(e & 1) * 2 + (j >> 1)]; c[j] = (j & 1) ? bhi(w) : blo(w); }
#pragma unroll
      for (int i = 0; i < 11; ++i) { const unsigned w = kk[i][e >> 1]; kf[i] = (e & 1) ? bhi(w) : blo(w); }
#pragma unroll
      for (int i = 0; i < 8; ++i) { const unsigned w = vv[i][e >> 1];
        float a = c[3] * kf[i]; asm volatile("" : "+v"(a)); a = fmaf(c[2], kf[i + 1], a); asm volatile("" : "+v"(a)); a = fmaf(c[1], kf[i + 2], a); asm volatile("" : "+v"(a)); a = fmaf(c[0], kf[i + 3], a);
        asm volatile("" : "+v"(a)); y[i] = a * ((e & 1) ? bhi(w) : blo(w)); asm volatile("" : "+v"(y[i])); }
      ko[e] = (u32x4){pk(y[7], y[6]), pk(y[5], y[4]), pk(y[3], y[2]), pk(y[1], y[0])};
      asm volatile("" : "+v"(ko[e]));
    } };
  { const int row = 64 * xw + lane;
    const f32x2 a = lds<f32x2>(sm + T_PART + row * 8) + lds<f32x2>(sm + T_PART + 1024 + row * 8);
    st16<SC1>(seal(a.x, a.y, tag), rws, W_G1S + ((id.tp * 16 + id.ft) * 128 + row) * 16, 0);
    kvstep(0); BAR(); kvstep(1); BAR();
    u32x4 gs[16];
    auto gather = [&]() {
#pragma unroll
      for (int i = 0; i < 16; ++i) gs[i] = ld16<SC1>(rws, W_G1S + (id.tp * 16 * 128 + row) * 16, i * 2048); };
    unsigned bad = 0; float s = 0.f, q = 0.f;
    auto sum = [&](int lo, int hi) {
#pragma unroll
      for (int i = lo; i < hi; ++i) { bad |= (gs[i].x ^ gs[i].z ^ tag.x) | (gs[i].y ^ gs[i].w ^ tag.y); s += __uint_as_float(gs[i].x); q += __uint_as_float(gs[i].y); }
      asm volatile("" : "+v"(bad), "+v"(s), "+v"(q)); };
    gather(); kvstep(2); BAR(); kvstep(3); BAR(); sum(0, 10); BAR();
    sum(10, 16);
    if (__builtin_expect(!__all(bad == 0), 0)) { const u64 t0 = __builtin_amdgcn_s_memrealtime();
      do { FENCE(); gather(); bad = 0; s = 0.f; q = 0.f; sum(0, 16); } while (!__all(bad == 0) && !expired(t0)); }
    const float m = s * (1.0f / FH), var = fmaxf(q * (1.0f / FH) - m * m, 0.f), r = __builtin_amdgcn_rsqf(var + 1e-5f);
    *(LAS float*)(sm + T_R2 + row * 4) = r; *(LAS float*)(sm + T_N2 + row * 4) = -m * r;
    asm volatile("s_waitcnt vmcnt(0) lgkmcnt(0)" : "+v"(sink) :: "memory"); BAR();
  }
  STAMP(2);
  kvstep(4); BAR(); kvstep(5); BAR();
  if (xw == 0) set_flag(rws, W_F2 + (id.ft * 16 + id.tp) * 8, tag, lane);
  kvstep(6); kvstep(7); BAR();
  { u32x4 wf[9];
    if (won) {
      const int q = lane >> 4, qq = (lane & 15) >> 2, p = lane & 3;
      LAS char* base = stg + (4 * q + qq) * 288;
#pragma unroll
      for (int T = 0; T < 9; ++T) { const int cb = T < 4 ? 32 * (T >> 1) + 4 * (T & 1) : T == 4 ? 64 : 80 + 32 * ((T - 5) >> 1) + 4 * ((T - 5) & 1);
        LAS char* a = base + cb * 2 + p * (T == 4 ? 8 : 16);
        wf[T] = __builtin_bit_cast(u32x4, cat(ldtr(a), ldtr(a + 16 * 288))); }
      WAITL(0);
    }
    if (kon) {
#pragma unroll
      for (int e = 0; e < 8; ++e) *(LAS u32x4*)(stg + (8 * cc + e) * 128 + (((7 - rg) ^ cc) * 16)) = ko[e];
#pragma unroll
      for (int i = 0; i < 8; ++i) ko[i] = lds<u32x4>(stg + (8 * i + rg) * 128 + ((cc ^ i) * 16));
    }
    const u64 t0 = __builtin_amdgcn_s_memrealtime();
    while (*(volatile LAS unsigned*)(sm + T_GO) == 0u && !expired(t0)) __builtin_amdgcn_s_sleep(2);
    FENCE();
    if (won) {
#pragma unroll
      for (int T = 0; T < 9; ++T) st16<SC1>(wf[T], rws, W_WOF + lane * 16, ((wu * 72 + wks) * 9 + T) * 1024); }
    if (kon) {
#pragma unroll
      for (int i = 0; i < 8; ++i) st16<SC1>(ko[i], rws, W_KVT + (64 * db + rg) * (S * 2) + (S - 64 * (tb + 1)) * 2 + cc * 16, 8 * i * (S * 2)); }
  }
  BAR(); BAR(); BAR();
  const unsigned h0a = ((const CAS unsigned*)P.ssmk)[id.b * S], h0b = ((const CAS unsigned*)P.ssmk)[(xw ? 2 * id.b + 1 : 512 + (id.b >> 2)) * (S / 2)];
  BAR(); WAITV(0); BAR();
  if (xw == 0) set_flag(rws, W_F5 + id.b * 8, tag, lane);
  STAMP(3);
#pragma unroll 1
  for (int i = 0; i < 22; ++i) BAR();
  { u32x4 f0 = {tag.x, tag.y, tag.x, tag.y}, f1 = f0;
    if (xw == 0) { f0 = ld16<SC1>(rws, W_F5 + lane * 16, 0); f1 = ld16<SC1>(rws, W_F5 + lane * 16, 1024); }
    BAR(); BAR();
    if (!__all(flags_ok(f0, tag) && flags_ok(f1, tag))) wait_flags<2, 8>(rws, W_F5 + lane * 16, tag); }
  BAR();
  STAMP(4);
  { const rsrc_t rh = mkrs(P.ssmk, D * S * 2);
    auto fetch = [&](int ci) { const int chn = ci == 2 ? 512 + (id.b >> 2) : 2 * id.b + ci; LAS char* cb = sm + CV + ci * CV_CH;
      const unsigned h0 = ci == 0 ? h0a : h0b;
      if (lane < 8) { *(LAS unsigned*)(cb + lane * 4) = 0u; *(LAS unsigned*)(cb + CV_O + lane * 4) = lane == 7 ? h0 << 16 : 0u; }
      if (lane < 38) *(LAS u32x4*)(cb + CV_U + 4096 + lane * 16) = (u32x4){0u, 0u, 0u, 0u};
      if (ci == 2) { *(LAS u32x4*)(cb + CV_U + 4704 + lane * 16) = (u32x4){0u, 0u, 0u, 0u}; *(LAS u32x4*)(cb + CV_U + 5728 + lane * 16) = (u32x4){0u, 0u, 0u, 0u}; }
      FENCE();
#pragma unroll
      for (int i = 0; i < 4; ++i) { dma<0>(rh, cb + 32 + i * 1024, lane * 16, chn * (S * 2) + i * 1024);
        dma<0>(rh, cb + CV_O + 32 + i * 1024, lane * 16 + 2, chn * (S * 2) + i * 1024);
        dma<SC1>(rws, cb + CV_U + i * 1024, lane * 16, W_KVT + chn * (S * 2) + i * 1024); } };
    if (xw == 0) fetch(0);
    BAR();
    fetch(2 - xw);
    BAR();
    WAITV(0); WAITL(0); BAR(); BAR();
  }
  BAR();
  if (xw == 0) { set_flag(rws, W_F3 + (id.tp * 16 + id.ft) * 8, tag, lane); wait_flags<1, 1>(rws, W_F3 + id.tp * 128 + (lane & 7) * 16, tag); }
  STAMP(5);
  BAR();
  if (xw == 1 && (OPT_TG3 & 4)) {
#pragma unroll
    for (int i = 0; i < 4; ++i) { const unsigned n = i * 64 + lane, o = n < 240 ? W_HF + ((id.tp * 8 + 4 * (id.ft >> 3) + (n & 3)) * 72 + 18 * ((n >> 2) & 3) + 3 + (n >> 4)) * 1024 + (id.ft & 7) * 128 : 0xF0000000u;
      asm volatile("buffer_load_dword %0, %1, %2, 0 offen sc1" : "+v"(sink) : "v"(o), "s"(rws) : "memory"); } }
  if (xw == 0) {
    const u64 t0 = __builtin_amdgcn_s_memrealtime();
    while (*(volatile LAS unsigned*)(sm + T_CNT) != 4u && !expired(t0)) __builtin_amdgcn_s_sleep(2);
    FENCE();
    set_flag(rws, W_F4 + id.b * 8, tag, lane);
    const int g = id.ft & 7, d0 = 144 * (g >> 1) + 64 * (g & 1);
    { const rsrc_t rx = mkrs(P.x, S * D * 2);
#pragma unroll
      for (int i = 0; i < 10; ++i) { const unsigned ci = 64 * i + lane, r = __umul24(ci, 6554) >> 16; dma<0>(rx, sm + XTILE + i * 1024, (id.tp * 128 + 64 * (id.ft >> 3) + r) * (D * 2) + d0 * 2 + (ci - 10 * r) * 16, 0); } }
    __builtin_amdgcn_s_sleep(24); wait_flags<2, 12>(rws, W_F4 + lane * 16, tag);
#pragma unroll
    for (int i = 0; i < 10; ++i) dma<SC1>(rws, sm + YTILE + i * 1024, W_YT + (d0 + (lane >> 3)) * (S * 2) + (id.tp * 128 + 64 * (id.ft >> 3)) * 2 + (lane & 7) * 16, i * 8 * (S * 2));
    WAITV(0);
    asm volatile("ds_write_b32 %0, %1" :: "v"((unsigned)(size_t)(sm + T_YOK)), "v"(1u) : "memory");
    if (id.b == 0 && lane == 0) st8<SC1>((u32x2){(unsigned)(epoch + 1), (unsigned)((epoch + 1) >> 32)}, rws, W_EPOCH, 0);
  }
  STAMP(6);
  asm volatile("s_waitcnt vmcnt(0)" : "+v"(sink) :: "memory"); BAR();
}

DEV void role_C(const Params& P, LAS char* sm, const Ids id, const int wid STAMP_ARG) {
  int bcount = 0; (void)bcount;
  const int lane = id.lane, wt = wid >> 1, wf = wid & 1, c = lane & 15, q = lane >> 4;
  const rsrc_t rws = mkrs(P.ws, W_END);
  __builtin_amdgcn_s_setprio(1);
  unsigned dm = ((const CAS unsigned*)P.ssmd)[id.b], dsh = ((const CAS unsigned*)P.ssmd)[256 + (id.b >> 3)];
  asm volatile("" : "+s"(dm), "+s"(dsh));
  f32x4 acc[4][4], accx[2], csa[3];
  bf16x8 tf[2][4], ff[2][5], ones;
  LAS char *ta[2][2], *fb[2][2], *fx[2];
#pragma unroll
  for (int s = 0; s < 2; ++s) {
    const int o = (32 * (s ^ wt) + 16 * (q >> 1) + 4 * (q & 1) + (c >> 2)) * 288 + 8 * (c & 3);
    fx[s] = sm + BBASE + o + 256;
#pragma unroll
    for (int h = 0; h < 2; ++h) { ta[s][h] = sm + (64 * wt + 32 * (h ^ wf) + c) * 128 + (((q + 4 * (s ^ wt)) ^ ((c >> 1) & 7)) * 16); fb[s][h] = sm + BBASE + o + wf * 128 + (h ^ wt) * 64; }
  }
#pragma unroll
  for (int e = 0; e < 8; ++e) ones[e] = 0x3f80;
  asm volatile("" : "+v"(ones));
  auto rd = [&](auto mode, int b, int as, int bs, int s) {
    constexpr int MODE = decltype(mode)::value;
#pragma unroll
    for (int j = 0; j < 4; ++j) { LAS char* p = fb[s][j >> 1] + bs * BSLOT;
      ff[b][j] = MODE < 2 ? cat(ldtr(p + (j & 1) * 32), ldtr(p + (j & 1) * 32 + 8 * 288)) : lds<bf16x8>(p + (j & 1) * 1024); }
#pragma unroll
    for (int i = 0; i < 4; ++i) tf[b][i] = lds<bf16x8>(ta[s][i >> 1] + as * ASLOT + (i & 1) * 2048);
    ff[b][4] = MODE < 2 ? cat(ldtr(fx[s] + bs * BSLOT), ldtr(fx[s] + bs * BSLOT + 8 * 288)) : lds<bf16x8>(fx[s] + bs * BSLOT);
  };
  auto mm = [&](auto mode, int b, int lo, int hi) {
    constexpr int MODE = decltype(mode)::value;
#pragma unroll
    for (int n = 0; n < (MODE == 2 ? 18 : 21 - b); ++n) if (n >= lo && n < hi) {
      if (n < 16) { const int i = n >> 2, j = (i & 1) ? 3 - (n & 3) : (n & 3);
        acc[i][j] = MODE == 0 ? MFMA(tf[b][i], ff[b][j], acc[i][j]) : MFMA(ff[b][j], tf[b][i], acc[i][j]); }
      else if (n < 18) accx[n - 16] = MODE == 0 ? MFMA(tf[b][n - 16], ff[b][4], accx[n - 16]) : MFMA(ff[b][4], tf[b][n - 16], accx[n - 16]);
      else { const int j = n == 20 ? 4 : n - 18; csa[n - 18] = MODE == 0 ? MFMA(ones, ff[b][j], csa[n - 18]) : MFMA(ff[b][j], ones, csa[n - 18]); }
    }
  };
  auto stage = [&](auto mode, int as, int bs, bool drain) {
    constexpr int MODE = decltype(mode)::value;
    mm(mode, 1, 0, 4); SB();
    if (drain) WAITV(0);
    WAITL(0); BAR(); SB();
    rd(mode, 0, as, bs, 0); mm(mode, 1, 4, 21);
    if (MODE < 2) { for (int i = 0; i < 14; ++i) { SG(0x100, 1); SG(0x8, 1); } SG(0x8, 2); }
    else { for (int i = 0; i < 4; ++i) { SG(0x100, 1); SG(0x8, 1); SG(0x100, 1); SG(0x8, 2); } SG(0x100, 1); SG(0x8, 2); }
    SB();
    rd(mode, 1, as, bs, 1); mm(mode, 0, 0, 21);
    if (MODE < 2) { for (int i = 0; i < 7; ++i) { SG(0x100, 1); SG(0x8, 1); SG(0x100, 1); SG(0x8, 2); } }
    else { for (int i = 0; i < 9; ++i) { SG(0x100, 1); SG(0x8, 2); } }
    SB();
  };
  auto first = [&](auto mode, int as, int bs) { WAITL(0); BAR(); SB(); rd(mode, 0, as, bs, 0); rd(mode, 1, as, bs, 1); mm(mode, 0, 0, 21); SB(); };
  auto zero = [&]() {
#pragma unroll
    for (int i = 0; i < 4; ++i)
#pragma unroll
      for (int j = 0; j < 4; ++j) acc[i][j] = (f32x4){0.f, 0.f, 0.f, 0.f};
    accx[0] = accx[1] = csa[0] = csa[1] = csa[2] = (f32x4){0.f, 0.f, 0.f, 0.f}; };
  const std::integral_constant<int, 0> M0; const std::integral_constant<int, 1> M1; const std::integral_constant<int, 2> M2;
  const int jg0 = 4 * wf + 2 * wt, jg2 = 4 * wf + 2 - 2 * wt;

  zero();
  first(M0, 0, 0);
  STAMP(0);
#pragma unroll 1
  for (int it = 0; it < 2; ++it) { stage(M0, 1, 1, false); stage(M0, 2, 2, false); stage(M0, 3, 3, false); stage(M0, 0, 0, false); }
  mm(M0, 1, 18, 21); mm(M0, 1, 0, 8); SB();
  if (q == 0) { *(LAS float*)(sm + T_CS2 + (jg0 * 16 + c) * 4) = csa[0][0]; *(LAS float*)(sm + T_CS2 + (jg0 * 16 + 16 + c) * 4) = csa[1][0]; *(LAS float*)(sm + T_CS2 + (128 + 16 * wt + c) * 4) = csa[2][0]; }
  mm(M0, 1, 8, 18); SB();
  WAITL(0); BAR();
  STAMP(1);
  u32x4 z2p[2][4], z2x;
  { float cs[5];
    cs[0] = csa[0][0]; cs[1] = csa[1][0]; cs[2] = lds<float>(sm + T_CS2 + (jg2 * 16 + c) * 4); cs[3] = lds<float>(sm + T_CS2 + (jg2 * 16 + 16 + c) * 4);
    cs[4] = csa[2][0] + lds<float>(sm + T_CS2 + (144 - 16 * wt + c) * 4);
    float red[2][16];
#pragma unroll
    for (int i = 0; i < 4; ++i) {
      const int ro = (64 * wt + 32 * ((i >> 1) ^ wf) + 8 * q + 4 * (i & 1)) * 4;
      const f32x4 m = lds<f32x4>(sm + T_M1 + ro), a0 = lds<f32x4>(sm + T_A0 + ro), a1 = lds<f32x4>(sm + T_A1 + ro), ir = lds<f32x4>(sm + T_IR + ro);
      f32x4 su = {0.f, 0.f, 0.f, 0.f}, sq = {0.f, 0.f, 0.f, 0.f};
#pragma unroll
      for (int j = 0; j < (i < 2 ? 5 : 4); ++j) {
        const f32x4 z = gelu4((j < 4 ? acc[i][j] : accx[i]) - m * cs[j], a0, a1, ir);
        su = j ? su + z : z; sq = j ? sq + z * z : z * z;
        u32x4& dst = j < 4 ? z2p[i >> 1][j] : z2x;
        dst[2 * (i & 1)] = pk(z[0], z[1]); dst[2 * (i & 1) + 1] = pk(z[2], z[3]);
      }
#pragma unroll
      for (int e = 0; e < 4; ++e) { red[0][4 * i + e] = su[e]; red[1][4 * i + e] = sq[e]; }
    }
    float r8[2][8], r4[2][4];
#pragma unroll
    for (int st = 0; st < 2; ++st) {
#pragma unroll
      for (int e = 0; e < 8; e += 4)
        asm volatile("s_nop 1\n\t"
                     "v_add_f32_dpp %0, %4, %4 row_ror:8 row_mask:0xf bank_mask:0x3\n\tv_add_f32_dpp %1, %5, %5 row_ror:8 row_mask:0xf bank_mask:0x3\n\t"
                     "v_add_f32_dpp %2, %6, %6 row_ror:8 row_mask:0xf bank_mask:0x3\n\tv_add_f32_dpp %3, %7, %7 row_ror:8 row_mask:0xf bank_mask:0x3\n\t"
                     "v_add_f32_dpp %0, %8, %8 row_ror:8 row_mask:0xf bank_mask:0xc\n\tv_add_f32_dpp %1, %9, %9 row_ror:8 row_mask:0xf bank_mask:0xc\n\t"
                     "v_add_f32_dpp %2, %10, %10 row_ror:8 row_mask:0xf bank_mask:0xc\n\tv_add_f32_dpp %3, %11, %11 row_ror:8 row_mask:0xf bank_mask:0xc"
                     : "=&v"(r8[st][e]), "=&v"(r8[st][e + 1]), "=&v"(r8[st][e + 2]), "=&v"(r8[st][e + 3])
                     : "v"(red[st][e]), "v"(red[st][e + 1]), "v"(red[st][e + 2]), "v"(red[st][e + 3]), "v"(red[st][8 + e]), "v"(red[st][9 + e]), "v"(red[st][10 + e]), "v"(red[st][11 + e]));
      asm volatile("s_nop 1\n\t"
                   "v_add_f32_dpp %0, %4, %4 row_half_mirror row_mask:0xf bank_mask:0x5\n\tv_add_f32_dpp %1, %5, %5 row_half_mirror row_mask:0xf bank_mask:0x5\n\t"
                   "v_add_f32_dpp %2, %6, %6 row_half_mirror row_mask:0xf bank_mask:0x5\n\tv_add_f32_dpp %3, %7, %7 row_half_mirror row_mask:0xf bank_mask:0x5\n\t"
                   "v_add_f32_dpp %0, %8, %8 row_half_mirror row_mask:0xf bank_mask:0xa\n\tv_add_f32_dpp %1, %9, %9 row_half_mirror row_mask:0xf bank_mask:0xa\n\t"
                   "v_add_f32_dpp %2, %10, %10 row_half_mirror row_mask:0xf bank_mask:0xa\n\tv_add_f32_dpp %3, %11, %11 row_half_mirror row_mask:0xf bank_mask:0xa\n\t"
                   "v_add_f32_dpp %0, %0, %0 quad_perm:[1,0,3,2] row_mask:0xf bank_mask:0xf\n\tv_add_f32_dpp %1, %1, %1 quad_perm:[1,0,3,2] row_mask:0xf bank_mask:0xf\n\t"
                   "v_add_f32_dpp %2, %2, %2 quad_perm:[1,0,3,2] row_mask:0xf bank_mask:0xf\n\tv_add_f32_dpp %3, %3, %3 quad_perm:[1,0,3,2] row_mask:0xf bank_mask:0xf\n\t"
                   "v_add_f32_dpp %0, %0, %0 quad_perm:[2,3,0,1] row_mask:0xf bank_mask:0xf\n\tv_add_f32_dpp %1, %1, %1 quad_perm:[2,3,0,1] row_mask:0xf bank_mask:0xf\n\t"
                   "v_add_f32_dpp %2, %2, %2 quad_perm:[2,3,0,1] row_mask:0xf bank_mask:0xf\n\tv_add_f32_dpp %3, %3, %3 quad_perm:[2,3,0,1] row_mask:0xf bank_mask:0xf"
                   : "=&v"(r4[st][0]), "=&v"(r4[st][1]), "=&v"(r4[st][2]), "=&v"(r4[st][3])
                   : "v"(r8[st][0]), "v"(r8[st][1]), "v"(r8[st][2]), "v"(r8[st][3]), "v"(r8[st][4]), "v"(r8[st][5]), "v"(r8[st][6]), "v"(r8[st][7]));
    }
    if ((c & 3) == 0) { LAS char* pp = sm + T_PART + wf * 1024 + (64 * wt + 32 * ((c >> 3) ^ wf) + 8 * q + 4 * ((c >> 2) & 1)) * 8;
      *(LAS f32x4*)pp = (f32x4){r4[0][0], r4[1][0], r4[0][1], r4[1][1]}; *(LAS f32x4*)(pp + 16) = (f32x4){r4[0][2], r4[1][2], r4[0][3], r4[1][3]}; }
#pragma unroll
    for (int i = 0; i < 2; ++i) asm volatile("" : "+v"(z2p[i][0]), "+v"(z2p[i][1]), "+v"(z2p[i][2]), "+v"(z2p[i][3]));
    asm volatile("" : "+v"(z2x));
  }
  STAMP(2);
  zero();
  first(M1, 0, 1);
  stage(M1, 3, 2, false); stage(M1, 2, 3, false); stage(M1, 1, 0, false);
  stage(M1, 0, 1, false); stage(M1, 3, 2, false); stage(M1, 2, 3, false);
  STAMP(3);
  {
#pragma unroll
    for (int i = 0; i < 2; ++i) asm volatile("" : "+v"(z2p[i][0]), "+v"(z2p[i][1]), "+v"(z2p[i][2]), "+v"(z2p[i][3]));
    asm volatile("" : "+v"(z2x));
#pragma unroll
    for (int ip = 0; ip < 2; ++ip) {
      const int ro = (64 * wt + 32 * (ip ^ wf) + 8 * q) * 4;
      const f32x4 r0 = lds<f32x4>(sm + T_R2 + ro), r1 = lds<f32x4>(sm + T_R2 + ro + 16), n0 = lds<f32x4>(sm + T_N2 + ro), n1 = lds<f32x4>(sm + T_N2 + ro + 16);
      const unsigned so = W_ZF + ((id.ft * 64 + 4 * id.tp + 2 * wt + (ip ^ wf)) * 9) * 1024;
#pragma unroll
      for (int j = 0; j < (ip == 0 ? 5 : 4); ++j) { const u32x4 z = j < 4 ? z2p[ip][j] : z2x;
        const f32x4 a = (f32x4){blo(z[0]), bhi(z[0]), blo(z[1]), bhi(z[1])} * r0 + n0, b = (f32x4){blo(z[2]), bhi(z[2]), blo(z[3]), bhi(z[3])} * r1 + n1;
        st16<SC1>((u32x4){pk(a[0], a[1]), pk(a[2], a[3]), pk(b[0], b[1]), pk(b[2], b[3])}, rws, lane * 16, so + (j < 2 ? jg0 + j : j < 4 ? jg2 + j - 2 : 8) * 1024); }
    }
    SB();
  }
  unsigned braw[4];
  { const rsrc_t rb = mkrs(P.bsp, S * 2);
#pragma unroll
    for (int i = 0; i < 4; ++i) braw[i] = __builtin_amdgcn_raw_buffer_load_b16(rb, (id.tp * 128 + 64 * wt + prow(i ^ (2 * wf), c)) * 2, 0, 0); }
  STAMP(4);
  stage(M1, 1, 0, false); stage(M1, 0, 1, true);
  mm(M1, 1, 18, 21); mm(M1, 1, 0, 8); SB();
  if (c == 0) { *(LAS f32x4*)(sm + T_CS1 + (jg0 * 16 + 4 * q) * 4) = csa[0]; *(LAS f32x4*)(sm + T_CS1 + (jg0 * 16 + 16 + 4 * q) * 4) = csa[1]; *(LAS f32x4*)(sm + T_CS1 + (128 + 16 * wt + 4 * q) * 4) = csa[2]; }
  mm(M1, 1, 8, 18); SB();
  WAITL(0); BAR();
  STAMP(5);
  u32x2 z1p[4][4], z1x[2];
  { f32x4 cs[5];
    cs[0] = csa[0]; cs[1] = csa[1]; cs[2] = lds<f32x4>(sm + T_CS1 + (jg2 * 16 + 4 * q) * 4); cs[3] = lds<f32x4>(sm + T_CS1 + (jg2 * 16 + 16 + 4 * q) * 4);
    cs[4] = csa[2] + lds<f32x4>(sm + T_CS1 + (144 - 16 * wt + 4 * q) * 4);
#pragma unroll
    for (int i = 0; i < 4; ++i) { const int ro = (64 * wt + prow(i ^ (2 * wf), c)) * 4;
      const float m = lds<float>(sm + T_M1 + ro), a0 = lds<float>(sm + T_A0 + ro), a1 = lds<float>(sm + T_A1 + ro), ir = lds<float>(sm + T_IR + ro), bias = blo(braw[i]);
#pragma unroll
      for (int j = 0; j < (i < 2 ? 5 : 4); ++j) {
        f32x4& a = j < 4 ? acc[i][j] : accx[i];
        const f32x4 z = gelu4(a - m * cs[j], a0, a1, ir);
        (j < 4 ? z1p[i][j] : z1x[i]) = (u32x2){pk(z[0], z[1]), pk(z[2], z[3])};
        a = (f32x4){bias, bias, bias, bias};
      } }
#pragma unroll
    for (int s = 0; s < 2; ++s) { fx[s] = sm + BBASE + (s * 9 + 8) * 1024 + lane * 16;
#pragma unroll
      for (int h = 0; h < 2; ++h) { fb[s][h] = sm + BBASE + (s * 9 + (h ? jg2 : jg0)) * 1024 + lane * 16; ta[s][h] = sm + (64 * wt + 32 * (h ^ wf) + c) * 128 + (((q + 4 * s) ^ ((c >> 1) & 7)) * 16); } }
#pragma unroll
    for (int i = 0; i < 4; ++i) asm volatile("" : "+v"(z1p[i][0]), "+v"(z1p[i][1]), "+v"(z1p[i][2]), "+v"(z1p[i][3]), "+v"(acc[i][0]), "+v"(acc[i][1]), "+v"(acc[i][2]), "+v"(acc[i][3]));
    asm volatile("" : "+v"(z1x[0]), "+v"(z1x[1]), "+v"(accx[0]), "+v"(accx[1]));
  }
  STAMP(6);
  const int jq = id.b & 3;
  LAS char *cs_ = sm + CV + 2 * CV_CH, *cb = sm + CV + wt * CV_CH;
  LAS char *am[4], *um = cb + CV_U + (2032 - 16 * c + 8 * q) * 2 + 256 * wf - 3584;
#pragma unroll
  for (int i = 0; i < 4; ++i) { am[i] = cb + ((c & 1) ? 0 : CV_O) + (4 * q + ((c + 1) >> 1)) * 4 + 256 * wf + 1024 * i; asm volatile("" : "+v"(am[i])); }
  asm volatile("" : "+v"(um));
  bf16x8 ca[2][8], cu[2][12];
  auto afrag = [&](LAS char* p) { const unsigned d0 = lds<unsigned>(p), d1 = lds<unsigned>(p + 4), d2 = lds<unsigned>(p + 8), d3 = lds<unsigned>(p + 12);
    return __builtin_bit_cast(bf16x8, (u32x4){d0, d1, d2, d3}); };
  auto lds_mn = [&](int b, int kk) {
#pragma unroll
    for (int e = 0; e < 8; ++e) { cu[b][e] = lds<bf16x8>(um + kk * 64 + 512 * (7 - e)); if (e == 0) ca[b][0] = afrag(am[0] + kk * 64); }
#pragma unroll
    for (int e = 1; e < 8; ++e) ca[b][e] = afrag(am[e >> 1] + kk * 64 + (e & 1) * 512); };
  first(M2, 3, 2);
  STAMP(7);
#pragma unroll 1
  for (int it = 0; it < 7; ++it) { stage(M2, 2, 3, false); stage(M2, 1, 0, false); stage(M2, 0, 1, false); stage(M2, 3, 2, false); }
  stage(M2, 2, 3, false); stage(M2, 1, 0, false); stage(M2, 0, 1, false);
  mm(M2, 1, 0, 4); SB(); WAITL(0); BAR(); SB();
  mm(M2, 1, 4, 18); SB();
  lds_mn(0, 0); SB();
  STAMP(8);
#pragma unroll
  for (int i = 0; i < 4; ++i) asm volatile("" : "+v"(z1p[i][0]), "+v"(z1p[i][1]), "+v"(z1p[i][2]), "+v"(z1p[i][3]));
  asm volatile("" : "+v"(z1x[0]), "+v"(z1x[1]));
#pragma unroll
  for (int i = 0; i < 4; ++i) {
    const unsigned so = W_HF + (id.tp * 8 + 4 * wt + (i ^ (2 * wf))) * 72 * 1024;
    auto gate = [&](u32x2 z, f32x4 a) { const f32x4 h = (f32x4){blo(z.x), bhi(z.x), blo(z.y), bhi(z.y)} * a; return (u32x2){pk(h[0], h[1]), pk(h[2], h[3])}; };
#pragma unroll
    for (int pp = 0; pp < 2; ++pp) { const u32x2 lo = gate(z1p[i][2 * pp], acc[i][2 * pp]), hi = gate(z1p[i][2 * pp + 1], acc[i][2 * pp + 1]);
      st16<SC1>((u32x4){lo.x, lo.y, hi.x, hi.y}, rws, lane * 16, so + (4 * id.ft + 2 * wf + (pp ^ wt)) * 1024); }
    if (i < 2) st8<SC1>(gate(z1x[i], accx[i]), rws, lane * 16 + (id.ft & 1) * 8, so + (64 + (id.ft >> 1)) * 1024);
  }
  const int g = id.ft & 7, gp = g & 1, half = id.ft >> 3, rot = krot(id);
  const unsigned wso = W_WOF + ((((EXP & 4) ? 0 : g >> 1) * 72 + 18 * wid) * 9) * 1024;
  u32x4 wr[WD][5];
  auto ldw = [&](int slot, int i) {
#pragma unroll
    for (int t = 0; t < 5; ++t) wr[slot][t] = ld16<(EXP & 8) ? 0 : SC1>(rws, lane * 16, wso + kwrap(i, rot) * 9216 + (t == 4 ? 4 : t + gp * 5) * 1024); };
  SB();
  SB(); BAR(); SB();
  STAMP(9);
  u32x4 hr[3][4];
  const unsigned hso = W_HF + (((EXP & 2) ? (id.b & 7) * 4 : id.tp * 8 + 4 * half) * 72 + 18 * wid) * 1024;
  auto ldh = [&](int slot, int i) {
#pragma unroll
    for (int t = 0; t < 4; ++t) hr[slot][t] = ld16<(EXP & 8) ? 0 : SC1>(rws, lane * 16, hso + (((unsigned)t ^ (wf ^ (2 * gp))) * 72 + kwrap(i, rot)) * 1024); };
  { const float dvm = wt ? bhi(dm) : blo(dm), dvs = (id.b & 4) ? bhi(dsh) : blo(dsh);
    f32x4 cm[8], s0 = {0.f, 0.f, 0.f, 0.f}, s1 = s0;
#pragma unroll
    for (int j = 0; j < 8; ++j) cm[j] = s0;
    LAS char *as[4], *u1, *u0;
    auto addr_sh = [&]() {
      const int da = (2 - wt) * CV_CH + 128 * wid - 256 * wf;
#pragma unroll
      for (int i = 0; i < 4; ++i) { as[i] = am[i] + da; asm volatile("" : "+v"(as[i])); }
      u1 = um + da + 512 * jq; u0 = um + da + 512 * (7 - jq); asm volatile("" : "+v"(u1), "+v"(u0)); };
    auto lds_sh = [&](int b, int kk) {
#pragma unroll
      for (int e = 0; e < 8; ++e) { ca[b][e] = afrag(as[e >> 1] + kk * 64 + (e & 1) * 512); cu[b][e] = lds<bf16x8>(u1 + kk * 64 + 512 * e);
        if (e < 4) cu[b][8 + e] = lds<bf16x8>(u0 + kk * 64 + 512 * e); } };
    auto mm_sh = [&](int b) {
#pragma unroll
      for (int e = 0; e < 8; ++e) { s1 = MFMA(ca[b][e], cu[b][e], s1); if (e < 4) s0 = MFMA(ca[b][e], cu[b][8 + e], s0); } };
    auto mm_mn = [&](int b) {
#pragma unroll
      for (int e = 0; e < 8; ++e)
#pragma unroll
        for (int j = e; j < 8; ++j) cm[j] = MFMA(ca[b][e], cu[b][j - e], cm[j]); };
#define il(nr, per) do { for (int i_ = 0; i_ < nr; ++i_) { SG(0x100, per); SG(0x8, 1); } SG(0x8, 36); SB(); } while (0)
#define ilv() do { for (int i_ = 0; i_ < 5; ++i_) { SG(0x100, 1); SG(0x8, 1); SG(0x100, 1); SG(0x8, 1); SG(0x100, 1); SG(0x8, 1); SG(0x100, 1); SG(0x8, 1); SG(0x100, 2); SG(0x8, 2); SG(0x20, 1); SG(0x8, 1); } SG(0x8, 36); SB(); } while (0)
    lds_mn(1, 1); mm_mn(0); il(24, 1); STAMP(15);
    lds_mn(0, 2); mm_mn(1); il(24, 1); STAMP(16); addr_sh(); SB();
    lds_sh(1, 0); mm_mn(0); il(28, 1);
    lds_sh(0, 1); mm_sh(1); il(10, 3);
    lds_mn(1, 3); mm_sh(0); il(12, 2);
    if (wid != 0) *(LAS f32x4*)(sm + CV_PS + (wid - 1) * 1024 + lane * 16) = s0;
    if (wid != 1) *(LAS f32x4*)(sm + CV_PS + (3 + (wid ? wid - 1 : 0)) * 1024 + lane * 16) = s1;
    SB(); STAMP(17); WAITV(0); WAITL(0); BAR(); SB();
    STAMP(10);
    f32x4 ys = wf ? s1 : s0;
    if (wid < 2) {
#pragma unroll
      for (int w = 0; w < 3; ++w) ys += lds<f32x4>(sm + CV_PS + (3 * wid + w) * 1024 + lane * 16);
    }
    SB(); ldw(0, 5); ldw(1, 6); if (WD > 2) ldw(2, 7); mm_mn(1); SG(0x8, 4); for (int i_ = 0; i_ < 15; ++i_) { SG(0x8, 2); SG(0x20, 1); } SG(0x8, 36); SB();
    LAS char* pm = sm + (wt ? CV_PM23 : CV + 2 * CV_CH);
#pragma unroll
    for (int j = 0; j < 4; ++j) { f32x4 lo = cm[j], hi = cm[4 + j]; asm volatile("" : "+v"(lo), "+v"(hi));
      *(LAS f32x4*)(pm + wf * 4096 + j * 1024 + lane * 16) = wf ? lo : hi; cm[j] = wf ? hi : lo; }
    SB(); WAITL(0); BAR(); SB();
    STAMP(11);
    ldh(0, 0); ldh(1, 1); ldh(2, 2);
    auto yout = [&](f32x4 a, int chn, LAS char* ub, int j, float dv) {
      const u32x2 u = lds<u32x2>(ub + CV_U + (2044 - 256 * j - 16 * c - 4 * q) * 2);
      const f32x4 y = a + (f32x4){bhi(u.y), blo(u.y), bhi(u.x), blo(u.x)} * dv;
      st8<SC1>((u32x2){pk(y[0], y[1]), pk(y[2], y[3])}, rws, (16 * c + 4 * q) * 2, W_YT + chn * (S * 2) + j * 512); };
#pragma unroll
    for (int j = 0; j < 4; ++j) yout(cm[j] + lds<f32x4>(pm + (wf ^ 1) * 4096 + j * 1024 + lane * 16), 2 * id.b + wt, cb, 4 * wf + j, dvm);
    if (wid < 2) yout(ys, 512 + (id.b >> 2), cs_, wid ? 7 - jq : jq, dvs);
    SB();
  }
  STAMP(19);
  f32x4 o[4][4], ox[2];
#pragma unroll
  for (int i = 0; i < 4; ++i)
#pragma unroll
    for (int t = 0; t < 4; ++t) o[i][t] = (f32x4){0.f, 0.f, 0.f, 0.f};
  ox[0] = ox[1] = (f32x4){0.f, 0.f, 0.f, 0.f};
  const int dp = 144 * (g >> 1) + 80 * gp, ds = 144 * (g >> 1) + 64, trow = 64 * half + prow(wid, c);
  const unsigned xo = (id.tp * 128 + trow) * (D * 2);
  f32x4 rs[5];
  { LAS char* wl = sm + wid * G3W + lane * 16;
    bf16x8 wq[2][5];
#pragma unroll
    for (int t = 0; t < 5; ++t) wq[0][t] = lds<bf16x8>(wl + t * 1024);
#pragma unroll
    for (int i = 0; i < 18; ++i) {
      if (i < 4) {
#pragma unroll
        for (int t = 0; t < 5; ++t) wq[(i + 1) & 1][t] = lds<bf16x8>(wl + ((i + 1) * 5 + t) * 1024); }
#pragma unroll
      for (int r = 0; r < 4; ++r) { const bf16x8 hf = __builtin_bit_cast(bf16x8, hr[i % 3][r]);
#pragma unroll
        for (int t = 0; t < 5; ++t) if (t < 4 || r < 2) { const int tt = (r & 1) && t < 4 ? 3 - t : t;
          const bf16x8 wf_ = i < 5 ? wq[i & 1][tt] : __builtin_bit_cast(bf16x8, wr[(i - 5) % WD][tt]);
          if (tt < 4) o[r][tt] = MFMA(wf_, hf, o[r][tt]); else ox[r] = MFMA(wf_, hf, ox[r]); } }
      SB();
      if (i == 0 || i == 2 || i == 4 || i == 7 || i == 10 || i == 13 || i == 15) STAMP(20 + i);
      if (i + 3 < 18) ldh(i % 3, i + 3);
      if (i >= 5 && i + WD < 18) ldw((i - 5) % WD, i + WD);
      if (i == 2) { WAITV(12); if (lane == 0) __hip_atomic_fetch_add((LAS unsigned*)(sm + T_CNT), 1u, __ATOMIC_RELAXED, __HIP_MEMORY_SCOPE_WORKGROUP); }
      if (i == 16) {
        const u64 t0 = __builtin_amdgcn_s_memrealtime();
        while (*(volatile LAS unsigned*)(sm + T_YOK) == 0u && !expired(t0)) __builtin_amdgcn_s_sleep(1);
        FENCE();
        const float m = lds<float>(sm + T_M1 + trow * 4), r = lds<float>(sm + T_R1 + trow * 4);
        LAS char* yb = sm + YTILE + (c >> 2) * 128 + (32 * wt + 4 * wf + 8 * (c & 3)) * 2;
        auto res = [&](u32x2 xv, bf16x4 yv) { const u32x2 yu = __builtin_bit_cast(u32x2, yv);
          const f32x4 xf = {blo(xv.x), bhi(xv.x), blo(xv.y), bhi(xv.y)}, yf = {blo(yu.x), bhi(yu.x), blo(yu.y), bhi(yu.y)};
          return xf + (xf - m) * r * yf; };
        const int yp = 16 * gp, ysh = 64 * (gp ^ 1);
        LAS char* xb = sm + XTILE + prow(wid, c) * 160;
        const u32x4 x0 = lds<u32x4>(xb + yp * 2 + q * 16), x1 = lds<u32x4>(xb + yp * 2 + 64 + q * 16); const u32x2 x2 = lds<u32x2>(xb + ysh * 2 + q * 8);
        rs[0] = res(x0.xy, ldtr(yb + (yp + 8 * q) * 128)); rs[1] = res(x0.zw, ldtr(yb + (yp + 8 * q + 4) * 128));
        rs[2] = res(x1.xy, ldtr(yb + (yp + 32 + 8 * q) * 128)); rs[3] = res(x1.zw, ldtr(yb + (yp + 36 + 8 * q) * 128));
        rs[4] = res(x2, ldtr(yb + (ysh + 4 * q) * 128));
        asm volatile("" : "+v"(rs[0]), "+v"(rs[1]), "+v"(rs[2]), "+v"(rs[3]), "+v"(rs[4])); }
      SB();
    }
  }
  STAMP(12);
  const bool own0 = wt == gp;
  { LAS char* pw = sm + wid * G3W + lane * 16;
    auto put = [&](int r) { LAS char* p = pw + ((unsigned)r ^ (wf ^ (2 * gp))) * 5120;
#pragma unroll
      for (int t = 0; t < 4; ++t) *(LAS f32x4*)(p + t * 1024) = o[r][t];
      if (r < 2) *(LAS f32x4*)(p + 4096) = ox[r]; };
    put(1); put(3);
    if (own0) { put(2);
#pragma unroll
      for (int t = 0; t < 4; ++t) rs[t] += o[0][t];
      rs[4] += ox[0]; }
    else { put(0);
#pragma unroll
      for (int t = 0; t < 4; ++t) rs[t] += o[2][t]; }
  }
  SB(); WAITL(0); BAR(); SB();
  STAMP(13);
  { f32x4 pt[3][5];
#pragma unroll
    for (int t = 0; t < 5; ++t)
#pragma unroll
      for (int w = 0; w < 3; ++w) pt[w][t] = lds<f32x4>(sm + (wid ^ (w + 1)) * G3W + wid * 5120 + lane * 16 + t * 1024);
    const rsrc_t ro = mkrs(P.out, S * D * 2);
    auto fin = [&](int t) { const f32x4 v = (rs[t] + pt[0][t]) + (pt[1][t] + pt[2][t]); return (u32x2){pk(v[0], v[1]), pk(v[2], v[3])}; };
#pragma unroll
    for (int pp = 0; pp < 2; ++pp) { const u32x2 lo = fin(2 * pp), hi = fin(2 * pp + 1); st16<0>((u32x4){lo.x, lo.y, hi.x, hi.y}, ro, xo + q * 16, dp * 2 + 64 * pp); }
    const u32x2 ls = fin(4);
    if (own0) st8<0>(ls, ro, xo + q * 8, ds * 2);
  }
  STAMP(14);
}
}

extern "C" __global__ __launch_bounds__(512, 2) void h3_gmlp_fused(const Params P) {
  extern __shared__ __attribute__((aligned(16))) char smem_[];
  LAS char* sm = (LAS char*)smem_;
  const int wid = __builtin_amdgcn_readfirstlane(threadIdx.x >> 6);
  Ids id; id.b = blockIdx.x; id.lane = threadIdx.x & 63;
  const int xcd = id.b & 7, loc = id.b >> 3;
  id.tp = (xcd >> 1) * 4 + (loc >> 3); id.ft = (xcd & 1) * 8 + (loc & 7);
  if (wid == 0) {
    unsigned lo, hi, bytes, sink;
    asm volatile("s_getpc_b64 vcc\n1:\n\ts_sub_u32 %0, vcc_lo, 1b-h3_gmlp_fused\n\ts_subb_u32 %1, vcc_hi, 0\n\ts_mov_b32 %2, h3_gmlp_end-h3_gmlp_fused" : "=s"(lo), "=s"(hi), "=s"(bytes) :: "vcc", "scc");
    const rsrc_t rc = mkrs((const void*)(((u64)hi << 32) | lo), bytes);
    asm volatile("buffer_load_dword %0, %1, %2, 0 offen\n\ts_waitcnt vmcnt(0)" : "=&v"(sink) : "v"((id.lane < 16 ? id.lane * 32 + loc : 0x100000) * 128), "s"(rc) : "memory");
  }
  if (__builtin_expect(wid == 4 || wid == 5, 1)) role_L(P, sm, id, wid - 4 STAMP_PASS);
  else if (wid >= 6) role_X(P, sm, id, wid - 6 STAMP_PASS);
  else role_C(P, sm, id, wid STAMP_PASS);
}
extern "C" __global__ void h3_gmlp_end() {}

extern "C" void kernel_launch(void* const* d_in, const int* in_sizes, int n_in, void* d_out, int out_size, void* d_ws, size_t ws_size, hipStream_t stream) {
  Params p;
  p.x = (const u16*)d_in[0]; p.k = (const u16*)d_in[1]; p.v = (const u16*)d_in[2]; p.shk = (const u16*)d_in[3]; p.ssmk = (const u16*)d_in[4]; p.ssmd = (const u16*)d_in[5];
  p.win = (const u16*)d_in[7]; p.wsp = (const u16*)d_in[8]; p.bsp = (const u16*)d_in[9]; p.wout = (const u16*)d_in[10];
  p.out = (u16*)d_out; p.ws = (char*)d_ws;
  (void)hipFuncSetAttribute((const void*)h3_gmlp_fused, hipFuncAttributeMaxDynamicSharedMemorySize, LDS_BYTES);
  h3_gmlp_fused<<<256, 512, LDS_BYTES, stream>>>(p);
}
```

```cpp
#include <hip/hip_runtime.h>
#include <hip/hip_bf16.h>
#include <type_traits>

namespace {
typedef unsigned short u16;
typedef unsigned long long u64;
typedef short bf16x8 __attribute__((ext_vector_type(8)));
typedef short bf16x4 __attribute__((ext_vector_type(4)));
typedef __bf16 bf2 __attribute__((ext_vector_type(2)));
typedef float f32x2 __attribute__((ext_vector_type(2)));
typedef float f32x4 __attribute__((ext_vector_type(4)));
typedef unsigned u32x2 __attribute__((ext_vector_type(2)));
typedef unsigned u32x4 __attribute__((ext_vector_type(4)));
typedef __amdgpu_buffer_rsrc_t rsrc_t;
#define LAS __attribute__((address_space(3)))
#define CAS __attribute__((address_space(4)))
#define DEV __device__ __forceinline__

constexpr int S = 2048, D = 576, F = 4608, FH = 2304;
constexpr int ASLOT = 16384, BSLOT = 18432, BBASE = 4 * ASLOT;
constexpr int STG = 139264, STG_W = 9216;
constexpr int T_M1 = 157952, T_R1 = T_M1 + 512, T_A0 = T_R1 + 512, T_A1 = T_A0 + 512, T_IR = T_A1 + 512;
constexpr int T_CS2 = T_IR + 512, T_CS1 = T_CS2;
constexpr int T_PART = T_CS1 + 640, T_N2 = T_PART, T_R2 = T_N2 + 512;
constexpr int T_TAG = T_PART + 2048, T_CNT = T_TAG + 16, T_GO = T_CNT + 4, T_YOK = T_CNT + 8;
constexpr int LDS_BYTES = 163840;
static_assert(T_CNT + 16 <= LDS_BYTES, "LDS map");
constexpr int G3W = 25600;
constexpr int CV = 102400, CV_CH = 13056, CV_O = 4160, CV_U = 8320;
constexpr int CV_PS = 141568, CV_PM23 = 149760;
constexpr int YTILE = 122880;
constexpr int W_EPOCH = 0, W_F2 = 4096, W_F3 = 8192, W_F4 = 12288, W_F5 = 16384;
constexpr int W_G0X = 0x8000, W_G1F = 0x10000, W_G1S = 0x60000;
constexpr int W_KVT = 1 << 20, W_YT = 4 << 20, W_WOF = 7 << 20, W_ZF = 10 << 20, W_HF = 20 << 20, W_END = 30 << 20;
constexpr int SC1 = 16;
#define OPT_TWIN 0
#define OPT_TWSP 1
#define OPT_TG3 0
#define EXP 0
#define WD 3

#define BAR0() do { asm volatile("" ::: "memory"); __builtin_amdgcn_s_barrier(); asm volatile("" ::: "memory"); } while (0)
#define BAR() BAR0()
#define SB() __builtin_amdgcn_sched_barrier(0)
#define SG(m, n) __builtin_amdgcn_sched_group_barrier(m, n, 0)
#define WAITV(n) asm volatile("s_waitcnt vmcnt(%0)" ::"i"(n) : "memory")
#define WAITL(n) asm volatile("s_waitcnt lgkmcnt(%0)" ::"i"(n) : "memory")
#define FENCE() asm volatile("" ::: "memory")
#define MFMA(a, b, c) __builtin_amdgcn_mfma_f32_16x16x32_bf16(a, b, c, 0, 0, 0)
#define STAMP(i) do {} while (0)
#define LBAR() BAR()
#define STAMP_ARG
#define STAMP_PASS

struct Params {
  const u16 *x, *k, *v, *shk, *ssmk, *ssmd, *win, *wsp, *bsp, *wout;
  u16* out; char* ws;
};

DEV rsrc_t mkrs(const void* p, unsigned bytes) { return __builtin_amdgcn_make_buffer_rsrc((void*)p, 0, bytes, 0x00020000); }
template <int AUX> DEV void dma(rsrc_t rs, LAS char* l, unsigned voff, unsigned soff) { __builtin_amdgcn_raw_ptr_buffer_load_lds(rs, (LAS void*)l, 16, voff, soff, 0, AUX); }
DEV void dma_hidden(rsrc_t rs, LAS char* l, unsigned voff) { unsigned keep;
  asm volatile("s_mov_b32 %0, m0\n\ts_mov_b32 m0, %3\n\ts_nop 0\n\tbuffer_load_dwordx4 %1, %2, 0 offen lds\n\ts_mov_b32 m0, %0" : "=&s"(keep) : "v"(voff), "s"(rs), "s"((unsigned)(size_t)l) : "memory"); }
template <int AUX> DEV u32x4 ld16(rsrc_t rs, unsigned voff, unsigned soff) { return __builtin_amdgcn_raw_buffer_load_b128(rs, voff, soff, AUX); }
template <int AUX> DEV void st16(u32x4 v, rsrc_t rs, unsigned voff, unsigned soff) { __builtin_amdgcn_raw_buffer_store_b128(v, rs, voff, soff, AUX); }
template <int AUX> DEV void st8(u32x2 v, rsrc_t rs, unsigned voff, unsigned soff) { __builtin_amdgcn_raw_buffer_store_b64(v, rs, voff, soff, AUX); }
template <class T> DEV T lds(LAS char* p) { return *(LAS T*)p; }
DEV bf16x4 ldtr(LAS char* p) { return __builtin_amdgcn_ds_read_tr16_b64_v4i16((LAS bf16x4*)p); }
DEV bf16x8 cat(bf16x4 a, bf16x4 b) { return __builtin_shufflevector(a, b, 0, 1, 2, 3, 4, 5, 6, 7); }
DEV float blo(unsigned u) { return __uint_as_float(u << 16); }
DEV float bhi(unsigned u) { return __uint_as_float(u & 0xffff0000u); }
DEV unsigned pk(float a, float b) { return __builtin_bit_cast(unsigned, __builtin_convertvector((f32x2){a, b}, bf2)); }
DEV float dot2(unsigned a, unsigned b, float c) { return __builtin_amdgcn_fdot2_f32_bf16(__builtin_bit_cast(bf2, a), __builtin_bit_cast(bf2, b), c, false); }
template <int CTRL> DEV float dpp(float x) { return __builtin_bit_cast(float, __builtin_amdgcn_mov_dpp(__builtin_bit_cast(int, x), CTRL, 0xf, 0xf, true)); }
constexpr int XOR1 = 0xB1, XOR2 = 0x4E, XOR7 = 0x141, XOR8 = 0x128;
constexpr float GC0 = -2.30220819f, GC1 = -0.10294324f;
template <class T> DEV f32x2 gelu2(f32x2 a, T a0, T a1, T ir) {
  const f32x2 g = a * (a * a * a1 + a0);
  f32x2 e; e.x = __builtin_amdgcn_exp2f(g.x); e.y = __builtin_amdgcn_exp2f(g.y);
  e = e * ir + ir;
  f32x2 r; r.x = __builtin_amdgcn_rcpf(e.x); r.y = __builtin_amdgcn_rcpf(e.y);
  return a * r;
}
DEV f32x4 gelu4(f32x4 a, f32x4 a0, f32x4 a1, f32x4 ir) { const f32x2 p = gelu2(a.xy, a0.xy, a1.xy, ir.xy), q = gelu2(a.zw, a0.zw, a1.zw, ir.zw); return (f32x4){p.x, p.y, q.x, q.y}; }
DEV f32x4 gelu4(f32x4 a, float a0, float a1, float ir) { const f32x2 p = gelu2(a.xy, a0, a1, ir), q = gelu2(a.zw, a0, a1, ir); return (f32x4){p.x, p.y, q.x, q.y}; }
DEV u32x4 seal(float a, float b, u32x2 tag) { const unsigned ua = __float_as_uint(a), ub = __float_as_uint(b); return (u32x4){ua, ub, ua ^ tag.x, ub ^ tag.y}; }
DEV bool sealed(u32x4 g, u32x2 tag) { return (g.x ^ g.z) == tag.x && (g.y ^ g.w) == tag.y; }
DEV bool expired(u64 t0) { return __builtin_amdgcn_s_memrealtime() - t0 > 2000000ull; }
template <int NL, int NAP> DEV void wait_flags(rsrc_t ws, unsigned voff, u32x2 tag) {
  const u64 t0 = __builtin_amdgcn_s_memrealtime();
  for (;;) {
    bool ok = true;
#pragma unroll
    for (int i = 0; i < NL; ++i) { const u32x4 f = ld16<SC1>(ws, voff, i * 1024); ok &= f.x == tag.x && f.y == tag.y && f.z == tag.x && f.w == tag.y; }
    if (__all(ok) || expired(t0)) break;
    __builtin_amdgcn_s_sleep(NAP); FENCE();
  }
  FENCE();
}
DEV bool flags_ok(u32x4 f, u32x2 tag) { return f.x == tag.x && f.y == tag.y && f.z == tag.x && f.w == tag.y; }
DEV int prow(int T, int c) { return 32 * (T >> 1) + 8 * (c >> 2) + 4 * (T & 1) + (c & 3); }
DEV void set_flag(rsrc_t ws, unsigned off, u32x2 tag, int lane) { if (lane == 0) st8<SC1>(tag, ws, off, 0); }

struct Ids { int b, tp, ft, lane; };

DEV void role_L(const Params& P, LAS char* sm, const Ids id, const int lw STAMP_ARG) {
  int bcount = 0; (void)bcount;
  const int lane = id.lane, rr = lane >> 3, sl = lane & 7;
  const rsrc_t rx = mkrs(P.x, S * D * 2), rwin = mkrs(P.win, D * F * 2);
  const unsigned swz = (sl ^ (4 * lw + (rr >> 1))) * 16, prr = 16 * lw + 8 * (rr >> 2) + (rr & 3);
  const unsigned vx = prr * (D * 2) + swz;
  unsigned vb[9];
  auto issue1a = [&](int slot, int i, int ks) { dma<0>(rx, sm + slot * ASLOT + lw * 1024 + i * 2048, vx, (id.tp * 128 + 32 * (i >> 1) + 4 * (i & 1)) * (D * 2) + ks * 128); };
  auto issue1b = [&](int slot, int i, int ks, int col) { dma<0>(rwin, sm + BBASE + slot * BSLOT + lw * 1024 + i * 2048, vb[i], ks * 64 * (F * 2) + (col + id.ft * 144) * 2); };
#pragma unroll
  for (int i = 0; i < 9; ++i) {
    if (i < 8) issue1a(0, i, 0);
    SB();
    const unsigned ci = 64 * (2 * i + lw) + lane, rho = __umul24(ci, 3641) >> 16;
    vb[i] = __umul24(rho, F * 2 - 288) + 16 * ci + __mul24((int)(rho & 4) - (int)((rho >> 1) & 4), F * 2);
    issue1b(0, i, 0, FH);
    SB();
  }
  auto issueA = [&](int n) {
#pragma unroll
    for (int i = 0; i < 9; ++i) { if (i < 8) issue1a(n & 3, i, n); issue1b(n & 3, i, n, FH); } };
  auto issueB = [&](int n, bool with_x) {
#pragma unroll
    for (int i = 0; i < 9; ++i) { if (with_x && i < 8) issue1a((9 - n) & 3, i, 17 - n); issue1b(n & 3, i, 17 - n, 0); } };
  issueA(1); issueA(2);
  STAMP(60);
  const rsrc_t rwsp = mkrs(P.wsp, S * S * 2), rws = mkrs(P.ws, W_END);
  const unsigned vw = prr * (S * 2) + swz, vl = lane * 16 + lw * 1024;
  auto issue2a = [&](int n) { LAS char* a = sm + ((9 - n) & 3) * ASLOT + lw * 1024; const unsigned sa = id.tp * 128 * (S * 2) + (n - 18) * 128;
#pragma unroll
    for (int i = 0; i < 8; ++i) dma<0>(rwsp, a + i * 2048, vw, sa + (32 * (i >> 1) + 4 * (i & 1)) * (S * 2)); };
  auto issue2b = [&](int n) { LAS char* bb = sm + BBASE + (n & 3) * BSLOT + lw * 1024; const unsigned sb = W_ZF + (id.ft * 576 + (n - 18) * 18) * 1024;
#pragma unroll
    for (int i = 0; i < 9; ++i) dma<SC1>(rws, bb + i * 2048, vl, sb + i * 2048); };
#pragma unroll 1
  for (int n = 0; n < 6; ++n) { WAITV(34); LBAR(); issueA(n + 3); }
  WAITV(34); LBAR(); issueB(9, false);
  WAITV(26); LBAR(); issueB(10, false);
  WAITV(18); LBAR(); issueB(11, false);
  BAR();
  WAITV(18); LBAR(); issueB(12, false);
  WAITV(18); LBAR(); issueB(13, true);
  WAITV(26); LBAR(); issueB(14, true);
#pragma unroll 1
  for (int n = 12; n < 15; ++n) { WAITV(34); LBAR(); issueB(n + 3, true); }
  WAITV(34); LBAR(); issue2a(18);
  WAITV(25); LBAR(); issue2a(19);
  WAITV(16); LBAR(); issue2a(20);
  BAR();
  { const u32x2 tag = lds<u32x2>(sm + T_TAG); wait_flags<1, 2>(rws, W_F2 + id.ft * 128 + (lane & 7) * 16, tag); }
  STAMP(61);
  issue2b(18); issue2b(19); issue2b(20);
  asm volatile("ds_write_b32 %0, %1" :: "v"((unsigned)(size_t)(sm + T_GO)), "v"(1u) : "memory");
  WAITV(18); LBAR(); issue2a(21); issue2b(21);
  WAITV(26); LBAR(); issue2a(22); issue2b(22);
#pragma unroll 1
  for (int n = 20; n < 47; ++n) { WAITV(34); LBAR(); issue2a(n + 3); issue2b(n + 3); }
  const int g = id.ft & 7;
  const unsigned wb0 = W_WOF + (g >> 1) * 72 * 9 * 1024, wb1 = wb0 + (g & 1) * 5 * 1024, vq = lane * 16;
  auto issue3 = [&](auto lo_, auto hi_) { constexpr int lo = decltype(lo_)::value, hi = decltype(hi_)::value;
#pragma unroll
    for (int p0 = lo; p0 < hi; p0 += 2) {
      constexpr auto off = [](int pc) { return ((18 * (pc / 25) + (pc % 25) / 5) * 9 + pc % 5) * 1024; };
      const unsigned o0 = (p0 % 5 == 4 ? wb0 : wb1) + off(p0), o1 = ((p0 + 1) % 5 == 4 ? wb0 : wb1) + off(p0 + 1);
      dma<SC1>(rws, sm + p0 * 1024 + lw * 1024, vq, lw ? o1 : o0); } };
#define IC(n) std::integral_constant<int, n>{}
  WAITV(34); LBAR(); issue3(IC(48), IC(64));
  WAITV(25); LBAR(); issue3(IC(32), IC(48));
  WAITV(16); LBAR(); issue3(IC(16), IC(32));
  LBAR();
  LBAR(); issue3(IC(0), IC(16)); issue3(IC(64), IC(100));
  LBAR();
  WAITV(0); LBAR();
  LBAR();
}

DEV void role_X(const Params& P, LAS char* sm, const Ids id, const int xw STAMP_ARG) {
  int bcount = 0; (void)bcount;
  const int lane = id.lane;
  const rsrc_t rws = mkrs(P.ws, W_END);
  unsigned sink = 0;
  const u32x2 ev = __builtin_amdgcn_raw_buffer_load_b64(rws, W_EPOCH, 0, SC1);
  const int srow = id.tp * 128 + id.ft * 8 + 4 * xw + (lane >> 4);
  u32x4 xv[5];
  { const rsrc_t rx = mkrs(P.x, S * D * 2);
#pragma unroll
    for (int i = 0; i < 5; ++i) xv[i] = ld16<0>(rx, i < 4 || (lane & 8) == 0 ? srow * (D * 2) + (lane & 15) * 16 : 0xF0000000u, i * 256); }
  BAR();
  const u64 epoch = ((u64)__builtin_amdgcn_readfirstlane(ev.y) << 32) | __builtin_amdgcn_readfirstlane(ev.x);
  u32x2 tag; { const u64 t = ((epoch + 1) * 0x9E3779B97F4A7C15ull) | 1ull; tag.x = (unsigned)t; tag.y = (unsigned)(t >> 32); }
  { float s = 0.f, q = 0.f; unsigned ones = 0x3f803f80u; asm volatile("" : "+v"(ones));
#pragma unroll
    for (int i = 0; i < 5; ++i)
#pragma unroll
      for (int e = 0; e < 4; ++e) { s = dot2(xv[i][e], ones, s); q = dot2(xv[i][e], xv[i][e], q); }
    s += dpp<XOR1>(s); q += dpp<XOR1>(q); s += dpp<XOR2>(s); q += dpp<XOR2>(q); s += dpp<XOR7>(s); q += dpp<XOR7>(q); s += dpp<XOR8>(s); q += dpp<XOR8>(q);
    const float m = s * (1.0f / D), var = fmaxf(q * (1.0f / D) - m * m, 0.f) + 1e-5f;
    if ((lane & 15) == 0) st16<SC1>(seal(m, var, tag), rws, W_G0X + srow * 16, 0);
    if (xw == 0 && lane == 0) { *(LAS u32x2*)(sm + T_TAG) = tag; *(LAS u32x4*)(sm + T_CNT) = (u32x4){0u, 0u, 0u, 0u}; }
  }
  STAMP(0);
  BAR(); BAR(); BAR();
  LAS char* stg = sm + STG + xw * STG_W;
  const int unit = xw ? 256 + id.b : id.b, wks = unit >> 2, wu = unit & 3; const bool won = !xw || id.b < 32;
  const int tile = xw ? id.b : 256 + id.b - 32; const bool kon = xw || (id.b >= 32 && id.b < 64);
  const int tb = tile / 9, db = tile - tb * 9, cc = lane & 7, rg = lane >> 3;
  u32x4 kk[11], vv[8], sh[4];
  { const int row = 64 * xw + lane; const u64 t0 = __builtin_amdgcn_s_memrealtime();
    u32x4 gx = ld16<SC1>(rws, W_G0X + (id.tp * 128 + row) * 16, 0);
    BAR(); BAR();
    while (!__all(sealed(gx, tag)) && !expired(t0)) { __builtin_amdgcn_s_sleep(4); FENCE(); gx = ld16<SC1>(rws, W_G0X + (id.tp * 128 + row) * 16, 0); }
    const float var = __uint_as_float(gx.y), r = __builtin_amdgcn_rsqf(var);
    *(LAS unsigned*)(sm + T_M1 + row * 4) = gx.x; *(LAS float*)(sm + T_R1 + row * 4) = r;
    *(LAS float*)(sm + T_A0 + row * 4) = GC0 * r; *(LAS float*)(sm + T_A1 + row * 4) = GC1 * r * r * r; *(LAS float*)(sm + T_IR + row * 4) = var * r;
    WAITL(0); BAR();
  }
  STAMP(1);
  { const rsrc_t rk = mkrs(P.k, S * D * 2), rv = mkrs(P.v, S * D * 2), rs = mkrs(P.shk, D * 4 * 2), rwo = mkrs(P.wout, FH * D * 2);
    const int t0 = 64 * tb + 8 * rg; const unsigned col = (64 * db + 8 * cc) * 2;
    if (kon) {
#pragma unroll
      for (int i = 0; i < 11; ++i) kk[i] = ld16<0>(rk, t0 + i - 3 >= 0 ? (unsigned)(t0 + i - 3) * (D * 2) + col : 0xF0000000u, 0);
    }
    BAR();
    if (kon) {
#pragma unroll
      for (int i = 0; i < 8; ++i) vv[i] = ld16<0>(rv, t0 * (D * 2) + col, i * (D * 2));
#pragma unroll
      for (int i = 0; i < 4; ++i) sh[i] = ld16<0>(rs, col * 4, i * 16);
    }
    BAR();
    if (won) { const int fb0 = wks < 64 ? 144 * (wks >> 2) + 32 * (wks & 3) : 288 * (wks - 64) + 128, fst = wks < 64 ? 0 : 128;
#pragma unroll
      for (int i = 0; i < 9; ++i) { const int ci = 64 * i + lane, rho = __umul24(ci, 3641) >> 16, ch = ci - rho * 18;
        dma_hidden(rwo, stg + i * 1024, (fb0 + rho + (rho >> 4) * fst) * (D * 2) + wu * 288 + ch * 16); } }
    BAR();
    if (kon) {
#pragma unroll
      for (int i = 0; i < 4; ++i) asm volatile("" : "+v"(sh[i]), "+v"(vv[i]), "+v"(vv[4 + i]), "+v"(kk[i]), "+v"(kk[4 + i]), "+v"(kk[i == 3 ? 0 : 8 + i])); }
    if (OPT_TWSP) { const rsrc_t r = mkrs(P.wsp, S * S * 2);
#pragma unroll
      for (int i = 0; i < 2; ++i) asm volatile("buffer_load_dword %0, %1, %2, 0 offen" : "+v"(sink) : "v"(id.b * 32768 + ((2 * i + xw) * 64 + lane) * 128), "s"(r) : "memory"); }
  }
  BAR();
  u32x4 ko[8];
  auto kvstep = [&](int e) {
    if (kon) {
      const unsigned c01 = sh[e >> 1][(e & 1) * 2], c23 = sh[e >> 1][(e & 1) * 2 + 1];
      unsigned pr[10]; float y[8];
#pragma unroll
      for (int i = 0; i < 10; ++i) pr[i] = __builtin_amdgcn_perm(kk[i][e >> 1], kk[i + 1][e >> 1], (e & 1) ? 0x07060302u : 0x05040100u);
#pragma unroll
      for (int i = 0; i < 8; ++i) { const unsigned w = vv[i][e >> 1];
        y[i] = dot2(pr[i + 2], c01, dot2(pr[i], c23, 0.f)) * ((e & 1) ? bhi(w) : blo(w)); asm volatile("" : "+v"(y[i])); }
      ko[e] = (u32x4){pk(y[7], y[6]), pk(y[5], y[4]), pk(y[3], y[2]), pk(y[1], y[0])};
      asm volatile("" : "+v"(ko[e]));
    } };
  { const int row = 64 * xw + lane;
    const f32x2 a = lds<f32x2>(sm + T_PART + row * 8) + lds<f32x2>(sm + T_PART + 1024 + row * 8);
    st16<SC1>(seal(a.x, a.y, tag), rws, W_G1S + ((id.tp * 16 + id.ft) * 128 + row) * 16, 0);
    kvstep(0); BAR(); kvstep(1); BAR();
    u32x4 gs[16];
    auto gather = [&]() {
#pragma unroll
      for (int i = 0; i < 16; ++i) gs[i] = ld16<SC1>(rws, W_G1S + (id.tp * 16 * 128 + row) * 16, i * 2048); };
    unsigned bad = 0; float s = 0.f, q = 0.f;
    auto sum = [&](int lo, int hi) {
#pragma unroll
      for (int i = lo; i < hi; ++i) { bad |= (gs[i].x ^ gs[i].z ^ tag.x) | (gs[i].y ^ gs[i].w ^ tag.y); s += __uint_as_float(gs[i].x); q += __uint_as_float(gs[i].y); }
      asm volatile("" : "+v"(bad), "+v"(s), "+v"(q)); };
    gather(); kvstep(2); BAR(); kvstep(3); BAR(); sum(0, 10); BAR();
    sum(10, 16);
    if (__builtin_expect(!__all(bad == 0), 0)) { const u64 t0 = __builtin_amdgcn_s_memrealtime();
      do { FENCE(); gather(); bad = 0; s = 0.f; q = 0.f; sum(0, 16); } while (!__all(bad == 0) && !expired(t0)); }
    const float m = s * (1.0f / FH), var = fmaxf(q * (1.0f / FH) - m * m, 0.f), r = __builtin_amdgcn_rsqf(var + 1e-5f);
    *(LAS float*)(sm + T_R2 + row * 4) = r; *(LAS float*)(sm + T_N2 + row * 4) = -m * r;
    asm volatile("s_waitcnt vmcnt(0) lgkmcnt(0)" : "+v"(sink) :: "memory"); BAR();
  }
  STAMP(2);
  kvstep(4); BAR(); kvstep(5); BAR();
  if (xw == 0) set_flag(rws, W_F2 + (id.ft * 16 + id.tp) * 8, tag, lane);
  kvstep(6); kvstep(7); BAR();
  { u32x4 wf[9];
    if (won) {
      const int q = lane >> 4, qq = (lane & 15) >> 2, p = lane & 3;
      LAS char* base = stg + (4 * q + qq) * 288;
#pragma unroll
      for (int T = 0; T < 9; ++T) { const int cb = T < 4 ? 32 * (T >> 1) + 4 * (T & 1) : T == 4 ? 64 : 80 + 32 * ((T - 5) >> 1) + 4 * ((T - 5) & 1);
        LAS char* a = base + cb * 2 + p * (T == 4 ? 8 : 16);
        wf[T] = __builtin_bit_cast(u32x4, cat(ldtr(a), ldtr(a + 16 * 288))); }
      WAITL(0);
    }
    if (kon) {
#pragma unroll
      for (int e = 0; e < 8; ++e) *(LAS u32x4*)(stg + (8 * cc + e) * 128 + (((7 - rg) ^ cc) * 16)) = ko[e];
#pragma unroll
      for (int i = 0; i < 8; ++i) ko[i] = lds<u32x4>(stg + (8 * i + rg) * 128 + ((cc ^ i) * 16));
    }
    const u64 t0 = __builtin_amdgcn_s_memrealtime();
    while (*(volatile LAS unsigned*)(sm + T_GO) == 0u && !expired(t0)) __builtin_amdgcn_s_sleep(2);
    FENCE();
    if (won) {
#pragma unroll
      for (int T = 0; T < 9; ++T) st16<SC1>(wf[T], rws, W_WOF + lane * 16, ((wu * 72 + wks) * 9 + T) * 1024); }
    if (kon) {
#pragma unroll
      for (int i = 0; i < 8; ++i) st16<SC1>(ko[i], rws, W_KVT + (64 * db + rg) * (S * 2) + (S - 64 * (tb + 1)) * 2 + cc * 16, 8 * i * (S * 2)); }
  }
  BAR(); BAR(); BAR();
  const unsigned h0a = ((const CAS unsigned*)P.ssmk)[id.b * S], h0b = ((const CAS unsigned*)P.ssmk)[(xw ? 2 * id.b + 1 : 512 + (id.b >> 2)) * (S / 2)];
  BAR(); WAITV(0); BAR();
  if (xw == 0) set_flag(rws, W_F5 + id.b * 8, tag, lane);
  STAMP(3);
#pragma unroll 1
  for (int i = 0; i < 22; ++i) BAR();
  { u32x4 f0 = {tag.x, tag.y, tag.x, tag.y}, f1 = f0;
    if (xw == 0) { f0 = ld16<SC1>(rws, W_F5 + lane * 16, 0); f1 = ld16<SC1>(rws, W_F5 + lane * 16, 1024); }
    BAR(); BAR();
    if (!__all(flags_ok(f0, tag) && flags_ok(f1, tag))) wait_flags<2, 8>(rws, W_F5 + lane * 16, tag); }
  BAR();
  STAMP(4);
  { const rsrc_t rh = mkrs(P.ssmk, D * S * 2);
    auto fetch = [&](int ci) { const int chn = ci == 2 ? 512 + (id.b >> 2) : 2 * id.b + ci; LAS char* cb = sm + CV + ci * CV_CH;
      const unsigned h0 = ci == 0 ? h0a : h0b;
      if (lane < 8) { *(LAS unsigned*)(cb + lane * 4) = 0u; *(LAS unsigned*)(cb + CV_O + lane * 4) = lane == 7 ? h0 << 16 : 0u; }
      if (lane < 38) *(LAS u32x4*)(cb + CV_U + 4096 + lane * 16) = (u32x4){0u, 0u, 0u, 0u};
      FENCE();
#pragma unroll
      for (int i = 0; i < 4; ++i) { dma<0>(rh, cb + 32 + i * 1024, lane * 16, chn * (S * 2) + i * 1024);
        dma<0>(rh, cb + CV_O + 32 + i * 1024, lane * 16 + 2, chn * (S * 2) + i * 1024);
        dma<SC1>(rws, cb + CV_U + i * 1024, lane * 16, W_KVT + chn * (S * 2) + i * 1024); } };
    if (xw == 0) fetch(0);
    BAR();
    fetch(2 - xw);
    BAR(); BAR();
    WAITV(0); WAITL(0); BAR();
  }
  BAR();
  if (xw == 0) { set_flag(rws, W_F3 + (id.tp * 16 + id.ft) * 8, tag, lane); wait_flags<1, 1>(rws, W_F3 + id.tp * 128 + (lane & 7) * 16, tag); }
  STAMP(5);
  BAR();
  if (xw == 0) {
    const u64 t0 = __builtin_amdgcn_s_memrealtime();
    while (*(volatile LAS unsigned*)(sm + T_CNT) != 4u && !expired(t0)) __builtin_amdgcn_s_sleep(2);
    FENCE();
    set_flag(rws, W_F4 + id.b * 8, tag, lane);
    __builtin_amdgcn_s_sleep((EXP & 1) ? 127 : 40); wait_flags<2, 16>(rws, W_F4 + lane * 16, tag);
    const int g = id.ft & 7, d0 = 144 * (g >> 1) + 64 * (g & 1);
#pragma unroll
    for (int i = 0; i < 10; ++i) dma<SC1>(rws, sm + YTILE + i * 1024, W_YT + (d0 + (lane >> 3)) * (S * 2) + (id.tp * 128 + 64 * (id.ft >> 3)) * 2 + (lane & 7) * 16, i * 8 * (S * 2));
    WAITV(0);
    asm volatile("ds_write_b32 %0, %1" :: "v"((unsigned)(size_t)(sm + T_YOK)), "v"(1u) : "memory");
    if (id.b == 0 && lane == 0) st8<SC1>((u32x2){(unsigned)(epoch + 1), (unsigned)((epoch + 1) >> 32)}, rws, W_EPOCH, 0);
  }
  STAMP(6);
  asm volatile("s_waitcnt vmcnt(0)" : "+v"(sink) :: "memory"); BAR();
}

DEV void role_C(const Params& P, LAS char* sm, const Ids id, const int wid STAMP_ARG) {
  int bcount = 0; (void)bcount;
  const int lane = id.lane, wt = wid >> 1, wf = wid & 1, c = lane & 15, q = lane >> 4;
  const rsrc_t rws = mkrs(P.ws, W_END);
  __builtin_amdgcn_s_setprio(1);
  unsigned dm = ((const CAS unsigned*)P.ssmd)[id.b], dsh = ((const CAS unsigned*)P.ssmd)[256 + (id.b >> 3)];
  asm volatile("" : "+s"(dm), "+s"(dsh));
  f32x4 acc[4][4], accx[2], csa[3];
  bf16x8 tf[2][4], ff[2][5], ones;
  LAS char *ta[2][2], *fb[2][2], *fx[2];
#pragma unroll
  for (int s = 0; s < 2; ++s) {
    const int o = (32 * (s ^ wt) + 16 * (q >> 1) + 4 * (q & 1) + (c >> 2)) * 288 + 8 * (c & 3);
    fx[s] = sm + BBASE + o + 256;
#pragma unroll
    for (int h = 0; h < 2; ++h) { ta[s][h] = sm + (64 * wt + 32 * (h ^ wf) + c) * 128 + (((q + 4 * (s ^ wt)) ^ ((c >> 1) & 7)) * 16); fb[s][h] = sm + BBASE + o + wf * 128 + (h ^ wt) * 64; }
  }
#pragma unroll
  for (int e = 0; e < 8; ++e) ones[e] = 0x3f80;
  asm volatile("" : "+v"(ones));
  auto rd = [&](auto mode, int b, int as, int bs, int s) {
    constexpr int MODE = decltype(mode)::value;
#pragma unroll
    for (int j = 0; j < 4; ++j) { LAS char* p = fb[s][j >> 1] + bs * BSLOT;
      ff[b][j] = MODE < 2 ? cat(ldtr(p + (j & 1) * 32), ldtr(p + (j & 1) * 32 + 8 * 288)) : lds<bf16x8>(p + (j & 1) * 1024); }
#pragma unroll
    for (int i = 0; i < 4; ++i) tf[b][i] = lds<bf16x8>(ta[s][i >> 1] + as * ASLOT + (i & 1) * 2048);
    ff[b][4] = MODE < 2 ? cat(ldtr(fx[s] + bs * BSLOT), ldtr(fx[s] + bs * BSLOT + 8 * 288)) : lds<bf16x8>(fx[s] + bs * BSLOT);
  };
  auto mm = [&](auto mode, int b, int lo, int hi) {
    constexpr int MODE = decltype(mode)::value;
#pragma unroll
    for (int n = 0; n < (MODE == 2 ? 18 : 21 - b); ++n) if (n >= lo && n < hi) {
      if (n < 16) { const int i = n >> 2, j = (i & 1) ? 3 - (n & 3) : (n & 3);
        acc[i][j] = MODE == 0 ? MFMA(tf[b][i], ff[b][j], acc[i][j]) : MFMA(ff[b][j], tf[b][i], acc[i][j]); }
      else if (n < 18) accx[n - 16] = MODE == 0 ? MFMA(tf[b][n - 16], ff[b][4], accx[n - 16]) : MFMA(ff[b][4], tf[b][n - 16], accx[n - 16]);
      else { const int j = n == 20 ? 4 : n - 18; csa[n - 18] = MODE == 0 ? MFMA(ones, ff[b][j], csa[n - 18]) : MFMA(ff[b][j], ones, csa[n - 18]); }
    }
  };
  auto stage = [&](auto mode, int as, int bs, bool drain) {
    constexpr int MODE = decltype(mode)::value;
    mm(mode, 1, 0, 4); SB();
    if (drain) WAITV(0);
    WAITL(0); BAR(); SB();
    rd(mode, 0, as, bs, 0); mm(mode, 1, 4, 21);
    if (MODE < 2) { for (int i = 0; i < 14; ++i) { SG(0x100, 1); SG(0x8, 1); } SG(0x8, 2); }
    else { for (int i = 0; i < 4; ++i) { SG(0x100, 1); SG(0x8, 1); SG(0x100, 1); SG(0x8, 2); } SG(0x100, 1); SG(0x8, 2); }
    SB();
    rd(mode, 1, as, bs, 1); mm(mode, 0, 0, 21);
    if (MODE < 2) { for (int i = 0; i < 7; ++i) { SG(0x100, 1); SG(0x8, 1); SG(0x100, 1); SG(0x8, 2); } }
    else { for (int i = 0; i < 9; ++i) { SG(0x100, 1); SG(0x8, 2); } }
    SB();
  };
  auto first = [&](auto mode, int as, int bs) { WAITL(0); BAR(); SB(); rd(mode, 0, as, bs, 0); rd(mode, 1, as, bs, 1); mm(mode, 0, 0, 21); SB(); };
  auto zero = [&]() {
#pragma unroll
    for (int i = 0; i < 4; ++i)
#pragma unroll
      for (int j = 0; j < 4; ++j) acc[i][j] = (f32x4){0.f, 0.f, 0.f, 0.f};
    accx[0] = accx[1] = csa[0] = csa[1] = csa[2] = (f32x4){0.f, 0.f, 0.f, 0.f}; };
  const std::integral_constant<int, 0> M0; const std::integral_constant<int, 1> M1; const std::integral_constant<int, 2> M2;
  const int jg0 = 4 * wf + 2 * wt, jg2 = 4 * wf + 2 - 2 * wt;

  zero();
  first(M0, 0, 0);
  STAMP(0);
#pragma unroll 1
  for (int it = 0; it < 2; ++it) { stage(M0, 1, 1, false); stage(M0, 2, 2, false); stage(M0, 3, 3, false); stage(M0, 0, 0, false); }
  mm(M0, 1, 18, 21); mm(M0, 1, 0, 8); SB();
  if (q == 0) { *(LAS float*)(sm + T_CS2 + (jg0 * 16 + c) * 4) = csa[0][0]; *(LAS float*)(sm + T_CS2 + (jg0 * 16 + 16 + c) * 4) = csa[1][0]; *(LAS float*)(sm + T_CS2 + (128 + 16 * wt + c) * 4) = csa[2][0]; }
  mm(M0, 1, 8, 18); SB();
  WAITL(0); BAR();
  STAMP(1);
  u32x4 z2p[2][4], z2x;
  { float cs[5];
    cs[0] = csa[0][0]; cs[1] = csa[1][0]; cs[2] = lds<float>(sm + T_CS2 + (jg2 * 16 + c) * 4); cs[3] = lds<float>(sm + T_CS2 + (jg2 * 16 + 16 + c) * 4);
    cs[4] = csa[2][0] + lds<float>(sm + T_CS2 + (144 - 16 * wt + c) * 4);
    float red[2][16];
#pragma unroll
    for (int i = 0; i < 4; ++i) {
      const int ro = (64 * wt + 32 * ((i >> 1) ^ wf) + 8 * q + 4 * (i & 1)) * 4;
      const f32x4 m = lds<f32x4>(sm + T_M1 + ro), a0 = lds<f32x4>(sm + T_A0 + ro), a1 = lds<f32x4>(sm + T_A1 + ro), ir = lds<f32x4>(sm + T_IR + ro);
      f32x4 su = {0.f, 0.f, 0.f, 0.f}, sq = {0.f, 0.f, 0.f, 0.f};
#pragma unroll
      for (int j = 0; j < (i < 2 ? 5 : 4); ++j) {
        const f32x4 z = gelu4((j < 4 ? acc[i][j] : accx[i]) - m * cs[j], a0, a1, ir);
        su = j ? su + z : z; sq = j ? sq + z * z : z * z;
        u32x4& dst = j < 4 ? z2p[i >> 1][j] : z2x;
        dst[2 * (i & 1)] = pk(z[0], z[1]); dst[2 * (i & 1) + 1] = pk(z[2], z[3]);
      }
#pragma unroll
      for (int e = 0; e < 4; ++e) { red[0][4 * i + e] = su[e]; red[1][4 * i + e] = sq[e]; }
    }
    float r8[2][8], r4[2][4];
#pragma unroll
    for (int st = 0; st < 2; ++st) {
#pragma unroll
      for (int e = 0; e < 8; e += 4)
        asm volatile("s_nop 1\n\t"
                     "v_add_f32_dpp %0, %4, %4 row_ror:8 row_mask:0xf bank_mask:0x3\n\tv_add_f32_dpp %1, %5, %5 row_ror:8 row_mask:0xf bank_mask:0x3\n\t"
                     "v_add_f32_dpp %2, %6, %6 row_ror:8 row_mask:0xf bank_mask:0x3\n\tv_add_f32_dpp %3, %7, %7 row_ror:8 row_mask:0xf bank_mask:0x3\n\t"
                     "v_add_f32_dpp %0, %8, %8 row_ror:8 row_mask:0xf bank_mask:0xc\n\tv_add_f32_dpp %1, %9, %9 row_ror:8 row_mask:0xf bank_mask:0xc\n\t"
                     "v_add_f32_dpp %2, %10, %10 row_ror:8 row_mask:0xf bank_mask:0xc\n\tv_add_f32_dpp %3, %11, %11 row_ror:8 row_mask:0xf bank_mask:0xc"
                     : "=&v"(r8[st][e]), "=&v"(r8[st][e + 1]), "=&v"(r8[st][e + 2]), "=&v"(r8[st][e + 3])
                     : "v"(red[st][e]), "v"(red[st][e + 1]), "v"(red[st][e + 2]), "v"(red[st][e + 3]), "v"(red[st][8 + e]), "v"(red[st][9 + e]), "v"(red[st][10 + e]), "v"(red[st][11 + e]));
      asm volatile("s_nop 1\n\t"
                   "v_add_f32_dpp %0, %4, %4 row_half_mirror row_mask:0xf bank_mask:0x5\n\tv_add_f32_dpp %1, %5, %5 row_half_mirror row_mask:0xf bank_mask:0x5\n\t"
                   "v_add_f32_dpp %2, %6, %6 row_half_mirror row_mask:0xf bank_mask:0x5\n\tv_add_f32_dpp %3, %7, %7 row_half_mirror row_mask:0xf bank_mask:0x5\n\t"
                   "v_add_f32_dpp %0, %8, %8 row_half_mirror row_mask:0xf bank_mask:0xa\n\tv_add_f32_dpp %1, %9, %9 row_half_mirror row_mask:0xf bank_mask:0xa\n\t"
                   "v_add_f32_dpp %2, %10, %10 row_half_mirror row_mask:0xf bank_mask:0xa\n\tv_add_f32_dpp %3, %11, %11 row_half_mirror row_mask:0xf bank_mask:0xa\n\t"
                   "v_add_f32_dpp %0, %0, %0 quad_perm:[1,0,3,2] row_mask:0xf bank_mask:0xf\n\tv_add_f32_dpp %1, %1, %1 quad_perm:[1,0,3,2] row_mask:0xf bank_mask:0xf\n\t"
                   "v_add_f32_dpp %2, %2, %2 quad_perm:[1,0,3,2] row_mask:0xf bank_mask:0xf\n\tv_add_f32_dpp %3, %3, %3 quad_perm:[1,0,3,2] row_mask:0xf bank_mask:0xf\n\t"
                   "v_add_f32_dpp %0, %0, %0 quad_perm:[2,3,0,1] row_mask:0xf bank_mask:0xf\n\tv_add_f32_dpp %1, %1, %1 quad_perm:[2,3,0,1] row_mask:0xf bank_mask:0xf\n\t"
                   "v_add_f32_dpp %2, %2, %2 quad_perm:[2,3,0,1] row_mask:0xf bank_mask:0xf\n\tv_add_f32_dpp %3, %3, %3 quad_perm:[2,3,0,1] row_mask:0xf bank_mask:0xf"
                   : "=&v"(r4[st][0]), "=&v"(r4[st][1]), "=&v"(r4[st][2]), "=&v"(r4[st][3])
                   : "v"(r8[st][0]), "v"(r8[st][1]), "v"(r8[st][2]), "v"(r8[st][3]), "v"(r8[st][4]), "v"(r8[st][5]), "v"(r8[st][6]), "v"(r8[st][7]));
    }
    if ((c & 3) == 0) { LAS char* pp = sm + T_PART + wf * 1024 + (64 * wt + 32 * ((c >> 3) ^ wf) + 8 * q + 4 * ((c >> 2) & 1)) * 8;
      *(LAS f32x4*)pp = (f32x4){r4[0][0], r4[1][0], r4[0][1], r4[1][1]}; *(LAS f32x4*)(pp + 16) = (f32x4){r4[0][2], r4[1][2], r4[0][3], r4[1][3]}; }
#pragma unroll
    for (int i = 0; i < 2; ++i) asm volatile("" : "+v"(z2p[i][0]), "+v"(z2p[i][1]), "+v"(z2p[i][2]), "+v"(z2p[i][3]));
    asm volatile("" : "+v"(z2x));
  }
  STAMP(2);
  zero();
  first(M1, 0, 1);
  stage(M1, 3, 2, false); stage(M1, 2, 3, false); stage(M1, 1, 0, false);
  stage(M1, 0, 1, false); stage(M1, 3, 2, false); stage(M1, 2, 3, false);
  STAMP(3);
  {
#pragma unroll
    for (int i = 0; i < 2; ++i) asm volatile("" : "+v"(z2p[i][0]), "+v"(z2p[i][1]), "+v"(z2p[i][2]), "+v"(z2p[i][3]));
    asm volatile("" : "+v"(z2x));
#pragma unroll
    for (int ip = 0; ip < 2; ++ip) {
      const int ro = (64 * wt + 32 * (ip ^ wf) + 8 * q) * 4;
      const f32x4 r0 = lds<f32x4>(sm + T_R2 + ro), r1 = lds<f32x4>(sm + T_R2 + ro + 16), n0 = lds<f32x4>(sm + T_N2 + ro), n1 = lds<f32x4>(sm + T_N2 + ro + 16);
      const unsigned so = W_ZF + ((id.ft * 64 + 4 * id.tp + 2 * wt + (ip ^ wf)) * 9) * 1024;
#pragma unroll
      for (int j = 0; j < (ip == 0 ? 5 : 4); ++j) { const u32x4 z = j < 4 ? z2p[ip][j] : z2x;
        const f32x4 a = (f32x4){blo(z[0]), bhi(z[0]), blo(z[1]), bhi(z[1])} * r0 + n0, b = (f32x4){blo(z[2]), bhi(z[2]), blo(z[3]), bhi(z[3])} * r1 + n1;
        st16<SC1>((u32x4){pk(a[0], a[1]), pk(a[2], a[3]), pk(b[0], b[1]), pk(b[2], b[3])}, rws, lane * 16, so + (j < 2 ? jg0 + j : j < 4 ? jg2 + j - 2 : 8) * 1024); }
    }
    SB();
  }
  unsigned braw[4];
  { const rsrc_t rb = mkrs(P.bsp, S * 2);
#pragma unroll
    for (int i = 0; i < 4; ++i) braw[i] = __builtin_amdgcn_raw_buffer_load_b16(rb, (id.tp * 128 + 64 * wt + prow(i ^ (2 * wf), c)) * 2, 0, 0); }
  STAMP(4);
  stage(M1, 1, 0, false); stage(M1, 0, 1, true);
  mm(M1, 1, 18, 21); mm(M1, 1, 0, 8); SB();
  if (c == 0) { *(LAS f32x4*)(sm + T_CS1 + (jg0 * 16 + 4 * q) * 4) = csa[0]; *(LAS f32x4*)(sm + T_CS1 + (jg0 * 16 + 16 + 4 * q) * 4) = csa[1]; *(LAS f32x4*)(sm + T_CS1 + (128 + 16 * wt + 4 * q) * 4) = csa[2]; }
  mm(M1, 1, 8, 18); SB();
  WAITL(0); BAR();
  STAMP(5);
  u32x2 z1p[4][4], z1x[2];
  { f32x4 cs[5];
    cs[0] = csa[0]; cs[1] = csa[1]; cs[2] = lds<f32x4>(sm + T_CS1 + (jg2 * 16 + 4 * q) * 4); cs[3] = lds<f32x4>(sm + T_CS1 + (jg2 * 16 + 16 + 4 * q) * 4);
    cs[4] = csa[2] + lds<f32x4>(sm + T_CS1 + (144 - 16 * wt + 4 * q) * 4);
#pragma unroll
    for (int i = 0; i < 4; ++i) { const int ro = (64 * wt + prow(i ^ (2 * wf), c)) * 4;
      const float m = lds<float>(sm + T_M1 + ro), a0 = lds<float>(sm + T_A0 + ro), a1 = lds<float>(sm + T_A1 + ro), ir = lds<float>(sm + T_IR + ro), bias = blo(braw[i]);
#pragma unroll
      for (int j = 0; j < (i < 2 ? 5 : 4); ++j) {
        f32x4& a = j < 4 ? acc[i][j] : accx[i];
        const f32x4 z = gelu4(a - m * cs[j], a0, a1, ir);
        (j < 4 ? z1p[i][j] : z1x[i]) = (u32x2){pk(z[0], z[1]), pk(z[2], z[3])};
        a = (f32x4){bias, bias, bias, bias};
      } }
#pragma unroll
    for (int s = 0; s < 2; ++s) { fx[s] = sm + BBASE + (s * 9 + 8) * 1024 + lane * 16;
#pragma unroll
      for (int h = 0; h < 2; ++h) { fb[s][h] = sm + BBASE + (s * 9 + (h ? jg2 : jg0)) * 1024 + lane * 16; ta[s][h] = sm + (64 * wt + 32 * (h ^ wf) + c) * 128 + (((q + 4 * s) ^ ((c >> 1) & 7)) * 16); } }
#pragma unroll
    for (int i = 0; i < 4; ++i) asm volatile("" : "+v"(z1p[i][0]), "+v"(z1p[i][1]), "+v"(z1p[i][2]), "+v"(z1p[i][3]), "+v"(acc[i][0]), "+v"(acc[i][1]), "+v"(acc[i][2]), "+v"(acc[i][3]));
    asm volatile("" : "+v"(z1x[0]), "+v"(z1x[1]), "+v"(accx[0]), "+v"(accx[1]));
  }
  STAMP(6);
  first(M2, 3, 2);
  STAMP(7);
#pragma unroll 1
  for (int it = 0; it < 7; ++it) { stage(M2, 2, 3, false); stage(M2, 1, 0, false); stage(M2, 0, 1, false); stage(M2, 3, 2, false); }
  stage(M2, 2, 3, false); stage(M2, 1, 0, false); stage(M2, 0, 1, false);
  mm(M2, 1, 0, 4); SB(); WAITL(0); BAR(); SB();
  mm(M2, 1, 4, 18); SB();
  STAMP(8);
#pragma unroll
  for (int i = 0; i < 4; ++i) asm volatile("" : "+v"(z1p[i][0]), "+v"(z1p[i][1]), "+v"(z1p[i][2]), "+v"(z1p[i][3]));
  asm volatile("" : "+v"(z1x[0]), "+v"(z1x[1]));
#pragma unroll
  for (int i = 0; i < 4; ++i) {
    const unsigned so = W_HF + (id.tp * 8 + 4 * wt + (i ^ (2 * wf))) * 72 * 1024;
    auto gate = [&](u32x2 z, f32x4 a) { const f32x4 h = (f32x4){blo(z.x), bhi(z.x), blo(z.y), bhi(z.y)} * a; return (u32x2){pk(h[0], h[1]), pk(h[2], h[3])}; };
#pragma unroll
    for (int pp = 0; pp < 2; ++pp) { const u32x2 lo = gate(z1p[i][2 * pp], acc[i][2 * pp]), hi = gate(z1p[i][2 * pp + 1], acc[i][2 * pp + 1]);
      st16<SC1>((u32x4){lo.x, lo.y, hi.x, hi.y}, rws, lane * 16, so + (4 * id.ft + 2 * wf + (pp ^ wt)) * 1024); }
    if (i < 2) st8<SC1>(gate(z1x[i], accx[i]), rws, lane * 16 + (id.ft & 1) * 8, so + (64 + (id.ft >> 1)) * 1024);
  }
  const int g = id.ft & 7, gp = g & 1, half = id.ft >> 3;
  const unsigned wso = W_WOF + ((((EXP & 4) ? 0 : g >> 1) * 72 + 18 * wid) * 9) * 1024;
  u32x4 wr[WD][5];
  auto ldw = [&](int slot, int i) {
#pragma unroll
    for (int t = 0; t < 5; ++t) wr[slot][t] = ld16<(EXP & 8) ? 0 : SC1>(rws, lane * 16, wso + (i * 9 + (t == 4 ? 4 : t + gp * 5)) * 1024); };
  SB();
  SB(); BAR(); SB();
  STAMP(9);
  u32x4 hr[3][4];
  const unsigned hso = W_HF + (((EXP & 2) ? (id.b & 7) * 4 : id.tp * 8 + 4 * half) * 72 + 18 * wid) * 1024;
  auto ldh = [&](int slot, int i) {
#pragma unroll
    for (int t = 0; t < 4; ++t) hr[slot][t] = ld16<(EXP & 8) ? 0 : SC1>(rws, lane * 16, hso + (((unsigned)t ^ (wf ^ (2 * gp))) * 72 + i) * 1024); };
  { const float dvm = wt ? bhi(dm) : blo(dm), dvs = (id.b & 4) ? bhi(dsh) : blo(dsh);
    const int aoff = ((c & 1) ? 0 : CV_O) + (4 * q + ((c + 1) >> 1)) * 4, boff = CV_U + (2032 - 16 * c + 8 * q) * 2, jq = id.b & 3;
    auto afrag = [&](LAS char* p) { const unsigned d0 = lds<unsigned>(p), d1 = lds<unsigned>(p + 4), d2 = lds<unsigned>(p + 8), d3 = lds<unsigned>(p + 12);
      return __builtin_bit_cast(bf16x8, (u32x4){d0, d1, d2, d3}); };
    LAS char *cs_ = sm + CV + 2 * CV_CH, *cb = sm + CV + wt * CV_CH;
    LAS char *am[4], *as[4], *um = cb + boff + 256 * wf - 3584, *us[12];
#pragma unroll
    for (int i = 0; i < 4; ++i) { am[i] = cb + aoff + 256 * wf + 1024 * i; asm volatile("" : "+v"(am[i])); }
    asm volatile("" : "+v"(um));
    bf16x8 ca[2][8], cu[2][12];
    f32x4 cm[8], s0 = {0.f, 0.f, 0.f, 0.f}, s1 = s0;
#pragma unroll
    for (int j = 0; j < 8; ++j) cm[j] = s0;
    auto addr_sh = [&]() {
#pragma unroll
      for (int i = 0; i < 4; ++i) { as[i] = cs_ + aoff + 128 * wid + 1024 * i; asm volatile("" : "+v"(as[i])); }
#pragma unroll
      for (int e = 0; e < 12; ++e) { const int d = e < 8 ? 7 - jq - e : jq - (e - 8); us[e] = cs_ + boff + (d >= 0 ? 128 * wid - 512 * d : 512); asm volatile("" : "+v"(us[e])); } };
    auto lds_sh = [&](int b, int kk) {
#pragma unroll
      for (int e = 0; e < 8; ++e) { ca[b][e] = afrag(as[e >> 1] + kk * 64 + (e & 1) * 512); cu[b][e] = lds<bf16x8>(us[e] + kk * 64);
        if (e < 4) cu[b][8 + e] = lds<bf16x8>(us[8 + e] + kk * 64); } };
    auto mm_sh = [&](int b) {
#pragma unroll
      for (int e = 0; e < 8; ++e) { s1 = MFMA(ca[b][e], cu[b][e], s1); if (e < 4) s0 = MFMA(ca[b][e], cu[b][8 + e], s0); } };
    auto lds_mn = [&](int b, int kk) {
#pragma unroll
      for (int e = 0; e < 8; ++e) { cu[b][e] = lds<bf16x8>(um + kk * 64 + 512 * (7 - e)); if (e == 0) ca[b][0] = afrag(am[0] + kk * 64); }
#pragma unroll
      for (int e = 1; e < 8; ++e) ca[b][e] = afrag(am[e >> 1] + kk * 64 + (e & 1) * 512); };
    auto mm_mn = [&](int b) {
#pragma unroll
      for (int e = 0; e < 8; ++e)
#pragma unroll
        for (int j = e; j < 8; ++j) cm[j] = MFMA(ca[b][e], cu[b][j - e], cm[j]); };
#define il(nr, per) do { for (int i_ = 0; i_ < nr; ++i_) { SG(0x100, per); SG(0x8, 1); } SG(0x8, 36); SB(); } while (0)
#define ilv() do { for (int i_ = 0; i_ < 5; ++i_) { SG(0x100, 1); SG(0x8, 1); SG(0x100, 1); SG(0x8, 1); SG(0x100, 1); SG(0x8, 1); SG(0x100, 1); SG(0x8, 1); SG(0x100, 2); SG(0x8, 2); SG(0x20, 1); SG(0x8, 1); } SG(0x8, 36); SB(); } while (0)
    lds_mn(0, 0); SB();
    lds_mn(1, 1); mm_mn(0); il(24, 1); STAMP(15);
    lds_mn(0, 2); mm_mn(1); il(24, 1); STAMP(16); addr_sh(); SB();
    lds_sh(1, 0); mm_mn(0); il(28, 1);
    lds_sh(0, 1); mm_sh(1); il(10, 3);
    lds_mn(1, 3); mm_sh(0); il(12, 2);
    *(LAS f32x4*)(sm + CV_PS + (wid * 2) * 1024 + lane * 16) = s0; *(LAS f32x4*)(sm + CV_PS + (wid * 2 + 1) * 1024 + lane * 16) = s1;
    SB(); STAMP(17); WAITV(0); WAITL(0); BAR(); SB();
    STAMP(10);
    f32x4 ys = {0.f, 0.f, 0.f, 0.f};
    if (wid < 2) {
#pragma unroll
      for (int w = 0; w < 4; ++w) ys += lds<f32x4>(sm + CV_PS + (w * 2 + wid) * 1024 + lane * 16);
    }
    SB(); ldw(0, 5); ldw(1, 6); if (WD > 2) ldw(2, 7); mm_mn(1); SG(0x8, 4); for (int i_ = 0; i_ < 15; ++i_) { SG(0x8, 2); SG(0x20, 1); } SG(0x8, 36); SB();
    LAS char* pm = sm + (wt ? CV_PM23 : CV + 2 * CV_CH);
#pragma unroll
    for (int j = 0; j < 4; ++j) { f32x4 lo = cm[j], hi = cm[4 + j]; asm volatile("" : "+v"(lo), "+v"(hi));
      *(LAS f32x4*)(pm + wf * 4096 + j * 1024 + lane * 16) = wf ? lo : hi; cm[j] = wf ? hi : lo; }
    SB(); WAITL(0); BAR(); SB();
    STAMP(11);
    ldh(0, 0); ldh(1, 1); ldh(2, 2);
    auto yout = [&](f32x4 a, int chn, LAS char* ub, int j, float dv) {
      const u32x2 u = lds<u32x2>(ub + CV_U + (2044 - 256 * j - 16 * c - 4 * q) * 2);
      const f32x4 y = a + (f32x4){bhi(u.y), blo(u.y), bhi(u.x), blo(u.x)} * dv;
      st8<SC1>((u32x2){pk(y[0], y[1]), pk(y[2], y[3])}, rws, (16 * c + 4 * q) * 2, W_YT + chn * (S * 2) + j * 512); };
#pragma unroll
    for (int j = 0; j < 4; ++j) yout(cm[j] + lds<f32x4>(pm + (wf ^ 1) * 4096 + j * 1024 + lane * 16), 2 * id.b + wt, cb, 4 * wf + j, dvm);
    if (wid < 2) yout(ys, 512 + (id.b >> 2), cs_, wid ? 7 - jq : jq, dvs);
    SB();
  }
  STAMP(19);
  f32x4 o[4][4], ox[2];
#pragma unroll
  for (int i = 0; i < 4; ++i)
#pragma unroll
    for (int t = 0; t < 4; ++t) o[i][t] = (f32x4){0.f, 0.f, 0.f, 0.f};
  ox[0] = ox[1] = (f32x4){0.f, 0.f, 0.f, 0.f};
  const int dp = 144 * (g >> 1) + 80 * gp, ds = 144 * (g >> 1) + 64, trow = 64 * half + prow(wid, c);
  const unsigned xo = (id.tp * 128 + trow) * (D * 2);
  u32x4 x0, x1; u32x2 x2; f32x4 rs[5];
  { LAS char* wl = sm + wid * G3W + lane * 16;
    bf16x8 wq[2][5];
#pragma unroll
    for (int t = 0; t < 5; ++t) wq[0][t] = lds<bf16x8>(wl + t * 1024);
#pragma unroll
    for (int i = 0; i < 18; ++i) {
      if (i < 4) {
#pragma unroll
        for (int t = 0; t < 5; ++t) wq[(i + 1) & 1][t] = lds<bf16x8>(wl + ((i + 1) * 5 + t) * 1024); }
#pragma unroll
      for (int r = 0; r < 4; ++r) { const bf16x8 hf = __builtin_bit_cast(bf16x8, hr[i % 3][r]);
#pragma unroll
        for (int t = 0; t < 5; ++t) if (t < 4 || r < 2) { const int tt = (r & 1) && t < 4 ? 3 - t : t;
          const bf16x8 wf_ = i < 5 ? wq[i & 1][tt] : __builtin_bit_cast(bf16x8, wr[(i - 5) % WD][tt]);
          if (tt < 4) o[r][tt] = MFMA(wf_, hf, o[r][tt]); else ox[r] = MFMA(wf_, hf, ox[r]); } }
      SB();
      if (i == 0 || i == 2 || i == 4 || i == 7 || i == 10 || i == 13 || i == 15) STAMP(20 + i);
      if (i + 3 < 18) ldh(i % 3, i + 3);
      if (i >= 5 && i + WD < 18) ldw((i - 5) % WD, i + WD);
      if (i == 2) { WAITV(12); if (lane == 0) __hip_atomic_fetch_add((LAS unsigned*)(sm + T_CNT), 1u, __ATOMIC_RELAXED, __HIP_MEMORY_SCOPE_WORKGROUP); }
      if (i == 9) { const rsrc_t rx = mkrs(P.x, S * D * 2);
        x0 = ld16<0>(rx, xo + q * 16, dp * 2); x1 = ld16<0>(rx, xo + q * 16, dp * 2 + 64); x2 = __builtin_amdgcn_raw_buffer_load_b64(rx, xo + q * 8, ds * 2, 0); }
      if (i == 15) {
        const u64 t0 = __builtin_amdgcn_s_memrealtime();
        while (*(volatile LAS unsigned*)(sm + T_YOK) == 0u && !expired(t0)) __builtin_amdgcn_s_sleep(1);
        FENCE();
        const float m = lds<float>(sm + T_M1 + trow * 4), r = lds<float>(sm + T_R1 + trow * 4);
        LAS char* yb = sm + YTILE + (c >> 2) * 128 + (32 * wt + 4 * wf + 8 * (c & 3)) * 2;
        auto res = [&](u32x2 xv, bf16x4 yv) { const u32x2 yu = __builtin_bit_cast(u32x2, yv);
          const f32x4 xf = {blo(xv.x), bhi(xv.x), blo(xv.y), bhi(xv.y)}, yf = {blo(yu.x), bhi(yu.x), blo(yu.y), bhi(yu.y)};
          return xf + (xf - m) * r * yf; };
        const int yp = 16 * gp, ysh = 64 * (gp ^ 1);
        rs[0] = res(x0.xy, ldtr(yb + (yp + 8 * q) * 128)); rs[1] = res(x0.zw, ldtr(yb + (yp + 8 * q + 4) * 128));
        rs[2] = res(x1.xy, ldtr(yb + (yp + 32 + 8 * q) * 128)); rs[3] = res(x1.zw, ldtr(yb + (yp + 36 + 8 * q) * 128));
        rs[4] = res(x2, ldtr(yb + (ysh + 4 * q) * 128));
        asm volatile("" : "+v"(rs[0]), "+v"(rs[1]), "+v"(rs[2]), "+v"(rs[3]), "+v"(rs[4])); }
      SB();
    }
  }
  STAMP(12);
  const bool own0 = wt == gp;
  { LAS char* pw = sm + wid * G3W + lane * 16;
    auto put = [&](int r) { LAS char* p = pw + ((unsigned)r ^ (wf ^ (2 * gp))) * 5120;
#pragma unroll
      for (int t = 0; t < 4; ++t) *(LAS f32x4*)(p + t * 1024) = o[r][t];
      if (r < 2) *(LAS f32x4*)(p + 4096) = ox[r]; };
    put(1); put(3);
    if (own0) { put(2);
#pragma unroll
      for (int t = 0; t < 4; ++t) rs[t] += o[0][t];
      rs[4] += ox[0]; }
    else { put(0);
#pragma unroll
      for (int t = 0; t < 4; ++t) rs[t] += o[2][t]; }
  }
  SB(); WAITL(0); BAR(); SB();
  STAMP(13);
  { f32x4 pt[3][5];
#pragma unroll
    for (int t = 0; t < 5; ++t)
#pragma unroll
      for (int w = 0; w < 3; ++w) pt[w][t] = lds<f32x4>(sm + (wid ^ (w + 1)) * G3W + wid * 5120 + lane * 16 + t * 1024);
    const rsrc_t ro = mkrs(P.out, S * D * 2);
    auto fin = [&](int t) { const f32x4 v = (rs[t] + pt[0][t]) + (pt[1][t] + pt[2][t]); return (u32x2){pk(v[0], v[1]), pk(v[2], v[3])}; };
#pragma unroll
    for (int pp = 0; pp < 2; ++pp) { const u32x2 lo = fin(2 * pp), hi = fin(2 * pp + 1); st16<0>((u32x4){lo.x, lo.y, hi.x, hi.y}, ro, xo + q * 16, dp * 2 + 64 * pp); }
    const u32x2 ls = fin(4);
    if (own0) st8<0>(ls, ro, xo + q * 8, ds * 2);
  }
  STAMP(14);
}
}

extern "C" __global__ __launch_bounds__(512, 2) void h3_gmlp_fused(const Params P) {
  extern __shared__ __attribute__((aligned(16))) char smem_[];
  LAS char* sm = (LAS char*)smem_;
  const int wid = __builtin_amdgcn_readfirstlane(threadIdx.x >> 6);
  Ids id; id.b = blockIdx.x; id.lane = threadIdx.x & 63;
  const int xcd = id.b & 7, loc = id.b >> 3;
  id.tp = (xcd >> 1) * 4 + (loc >> 3); id.ft = (xcd & 1) * 8 + (loc & 7);
  if (wid == 0) {
    unsigned lo, hi, bytes, sink;
    asm volatile("s_getpc_b64 vcc\n1:\n\ts_sub_u32 %0, vcc_lo, 1b-h3_gmlp_fused\n\ts_subb_u32 %1, vcc_hi, 0\n\ts_mov_b32 %2, h3_gmlp_end-h3_gmlp_fused" : "=s"(lo), "=s"(hi), "=s"(bytes) :: "vcc", "scc");
    const rsrc_t rc = mkrs((const void*)(((u64)hi << 32) | lo), bytes);
    asm volatile("buffer_load_dword %0, %1, %2, 0 offen\n\ts_waitcnt vmcnt(0)" : "=&v"(sink) : "v"((id.lane < 16 ? id.lane * 32 + loc : 0x100000) * 128), "s"(rc) : "memory");
  }
  if (__builtin_expect(wid == 4 || wid == 5, 1)) role_L(P, sm, id, wid - 4 STAMP_PASS);
  else if (wid >= 6) role_X(P, sm, id, wid - 6 STAMP_PASS);
  else role_C(P, sm, id, wid STAMP_PASS);
}
extern "C" __global__ void h3_gmlp_end() {}

extern "C" void kernel_launch(void* const* d_in, const int* in_sizes, int n_in, void* d_out, int out_size, void* d_ws, size_t ws_size, hipStream_t stream) {
  Params p;
  p.x = (const u16*)d_in[0]; p.k = (const u16*)d_in[1]; p.v = (const u16*)d_in[2]; p.shk = (const u16*)d_in[3]; p.ssmk = (const u16*)d_in[4]; p.ssmd = (const u16*)d_in[5];
  p.win = (const u16*)d_in[7]; p.wsp = (const u16*)d_in[8]; p.bsp = (const u16*)d_in[9]; p.wout = (const u16*)d_in[10];
  p.out = (u16*)d_out; p.ws = (char*)d_ws;
  (void)hipFuncSetAttribute((const void*)h3_gmlp_fused, hipFuncAttributeMaxDynamicSharedMemorySize, LDS_BYTES);
  h3_gmlp_fused<<<256, 512, LDS_BYTES, stream>>>(p);
}
```

```cpp
#define MK_STAMP
#define MK_AMP 0
#include <hip/hip_runtime.h>
#include <hip/hip_bf16.h>
#include <type_traits>

namespace {
typedef unsigned short u16;
typedef unsigned long long u64;
typedef short bf16x8 __attribute__((ext_vector_type(8)));
typedef short bf16x4 __attribute__((ext_vector_type(4)));
typedef __bf16 bf2 __attribute__((ext_vector_type(2)));
typedef float f32x2 __attribute__((ext_vector_type(2)));
typedef float f32x4 __attribute__((ext_vector_type(4)));
typedef unsigned u32x2 __attribute__((ext_vector_type(2)));
typedef unsigned u32x4 __attribute__((ext_vector_type(4)));
typedef __amdgpu_buffer_rsrc_t rsrc_t;
#define LAS __attribute__((address_space(3)))
#define CAS __attribute__((address_space(4)))
#define DEV __device__ __forceinline__

constexpr int S = 2048, D = 576, F = 4608, FH = 2304;
constexpr int ASLOT = 16384, BSLOT = 18432, BBASE = 4 * ASLOT;
constexpr int STG = 139264, STG_W = 9216;
constexpr int T_M1 = 157952, T_R1 = T_M1 + 512, T_A0 = T_R1 + 512, T_A1 = T_A0 + 512, T_IR = T_A1 + 512;
constexpr int T_CS2 = T_IR + 512, T_CS1 = T_CS2;
constexpr int T_PART = T_CS1 + 640, T_N2 = T_PART, T_R2 = T_N2 + 512;
constexpr int T_TAG = T_PART + 2048, T_CNT = T_TAG + 16, T_GO = T_CNT + 4, T_YOK = T_CNT + 8;
constexpr int LDS_BYTES = 163840;
static_assert(T_CNT + 16 <= LDS_BYTES, "LDS map");
constexpr int G3W = 25600;
constexpr int CV = 102400, CV_CH = 13056, CV_O = 4160, CV_U = 8320;
constexpr int CV_PS = 143616, CV_PM23 = 149760;
constexpr int YTILE = 122880, XTILE = 133120;
constexpr int W_EPOCH = 0, W_F2 = 4096, W_F3 = 8192, W_F4 = 12288, W_F5 = 16384;
constexpr int W_G0X = 0x8000, W_G1F = 0x10000, W_G1S = 0x60000;
constexpr int W_KVT = 1 << 20, W_YT = 4 << 20, W_WOF = 7 << 20, W_ZF = 10 << 20, W_HF = 20 << 20, W_END = 30 << 20;
constexpr int SC1 = 16;
#define OPT_TWIN 0
#define OPT_TWSP 1
#define OPT_TG3 0
#define EXP 0
#define RH 0
#define RW 0
#define WD 3

#define BAR0() do { asm volatile("" ::: "memory"); __builtin_amdgcn_s_barrier(); asm volatile("" ::: "memory"); } while (0)
#define BAR() BAR0()
#define SB() __builtin_amdgcn_sched_barrier(0)
#define SG(m, n) __builtin_amdgcn_sched_group_barrier(m, n, 0)
#define WAITV(n) asm volatile("s_waitcnt vmcnt(%0)" ::"i"(n) : "memory")
#define WAITL(n) asm volatile("s_waitcnt lgkmcnt(%0)" ::"i"(n) : "memory")
#define FENCE() asm volatile("" ::: "memory")
#define MFMA(a, b, c) __builtin_amdgcn_mfma_f32_16x16x32_bf16(a, b, c, 0, 0, 0)
#define CLK __builtin_amdgcn_s_memrealtime
#define STAMP(i) do { const unsigned t_ = (unsigned)CLK(); stampv = lane == (i) ? t_ : stampv; asm volatile("" : "+v"(stampv)); } while (0)
#define LBAR() do { BAR(); const int bi_ = bcount - (lw ? 0 : 40); ++bcount; if (bi_ >= 0 && bi_ < 60) STAMP(bi_); } while (0)
#define STAMP_ARG , unsigned& stampv
#define STAMP_PASS , stampv

struct Params {
  const u16 *x, *k, *v, *shk, *ssmk, *ssmd, *win, *wsp, *bsp, *wout;
  u16* out; char* ws;
};

DEV rsrc_t mkrs(const void* p, unsigned bytes) { return __builtin_amdgcn_make_buffer_rsrc((void*)p, 0, bytes, 0x00020000); }
template <int AUX> DEV void dma(rsrc_t rs, LAS char* l, unsigned voff, unsigned soff) { __builtin_amdgcn_raw_ptr_buffer_load_lds(rs, (LAS void*)l, 16, voff, soff, 0, AUX); }
DEV void dma_hidden(rsrc_t rs, LAS char* l, unsigned voff) { unsigned keep;
  asm volatile("s_mov_b32 %0, m0\n\ts_mov_b32 m0, %3\n\ts_nop 0\n\tbuffer_load_dwordx4 %1, %2, 0 offen lds\n\ts_mov_b32 m0, %0" : "=&s"(keep) : "v"(voff), "s"(rs), "s"((unsigned)(size_t)l) : "memory"); }
template <int AUX> DEV u32x4 ld16(rsrc_t rs, unsigned voff, unsigned soff) { return __builtin_amdgcn_raw_buffer_load_b128(rs, voff, soff, AUX); }
template <int AUX> DEV void st16(u32x4 v, rsrc_t rs, unsigned voff, unsigned soff) { __builtin_amdgcn_raw_buffer_store_b128(v, rs, voff, soff, AUX); }
template <int AUX> DEV void st8(u32x2 v, rsrc_t rs, unsigned voff, unsigned soff) { __builtin_amdgcn_raw_buffer_store_b64(v, rs, voff, soff, AUX); }
template <class T> DEV T lds(LAS char* p) { return *(LAS T*)p; }
DEV bf16x4 ldtr(LAS char* p) { return __builtin_amdgcn_ds_read_tr16_b64_v4i16((LAS bf16x4*)p); }
DEV bf16x8 cat(bf16x4 a, bf16x4 b) { return __builtin_shufflevector(a, b, 0, 1, 2, 3, 4, 5, 6, 7); }
DEV float blo(unsigned u) { return __uint_as_float(u << 16); }
DEV float bhi(unsigned u) { return __uint_as_float(u & 0xffff0000u); }
DEV unsigned pk(float a, float b) { return __builtin_bit_cast(unsigned, __builtin_convertvector((f32x2){a, b}, bf2)); }
DEV float dot2(unsigned a, unsigned b, float c) { return __builtin_amdgcn_fdot2_f32_bf16(__builtin_bit_cast(bf2, a), __builtin_bit_cast(bf2, b), c, false); }
template <int CTRL> DEV float dpp(float x) { return __builtin_bit_cast(float, __builtin_amdgcn_mov_dpp(__builtin_bit_cast(int, x), CTRL, 0xf, 0xf, true)); }
constexpr int XOR1 = 0xB1, XOR2 = 0x4E, XOR7 = 0x141, XOR8 = 0x128;
constexpr float GC0 = -2.30220819f, GC1 = -0.10294324f;
template <class T> DEV f32x2 gelu2(f32x2 a, T a0, T a1, T ir) {
  const f32x2 g = a * (a * a * a1 + a0);
  f32x2 e; e.x = __builtin_amdgcn_exp2f(g.x); e.y = __builtin_amdgcn_exp2f(g.y);
  e = e * ir + ir;
  f32x2 r; r.x = __builtin_amdgcn_rcpf(e.x); r.y = __builtin_amdgcn_rcpf(e.y);
  return a * r;
}
DEV f32x4 gelu4(f32x4 a, f32x4 a0, f32x4 a1, f32x4 ir) { const f32x2 p = gelu2(a.xy, a0.xy, a1.xy, ir.xy), q = gelu2(a.zw, a0.zw, a1.zw, ir.zw); return (f32x4){p.x, p.y, q.x, q.y}; }
DEV f32x4 gelu4(f32x4 a, float a0, float a1, float ir) { const f32x2 p = gelu2(a.xy, a0, a1, ir), q = gelu2(a.zw, a0, a1, ir); return (f32x4){p.x, p.y, q.x, q.y}; }
DEV u32x4 seal(float a, float b, u32x2 tag) { const unsigned ua = __float_as_uint(a), ub = __float_as_uint(b); return (u32x4){ua, ub, ua ^ tag.x, ub ^ tag.y}; }
DEV bool sealed(u32x4 g, u32x2 tag) { return (g.x ^ g.z) == tag.x && (g.y ^ g.w) == tag.y; }
DEV bool expired(u64 t0) { return __builtin_amdgcn_s_memrealtime() - t0 > 2000000ull; }
template <int NL, int NAP> DEV void wait_flags(rsrc_t ws, unsigned voff, u32x2 tag) {
  const u64 t0 = __builtin_amdgcn_s_memrealtime();
  for (;;) {
    bool ok = true;
#pragma unroll
    for (int i = 0; i < NL; ++i) { const u32x4 f = ld16<SC1>(ws, voff, i * 1024); ok &= f.x == tag.x && f.y == tag.y && f.z == tag.x && f.w == tag.y; }
    if (__all(ok) || expired(t0)) break;
    __builtin_amdgcn_s_sleep(NAP); FENCE();
  }
  FENCE();
}
DEV bool flags_ok(u32x4 f, u32x2 tag) { return f.x == tag.x && f.y == tag.y && f.z == tag.x && f.w == tag.y; }
DEV int prow(int T, int c) { return 32 * (T >> 1) + 8 * (c >> 2) + 4 * (T & 1) + (c & 3); }
DEV void set_flag(rsrc_t ws, unsigned off, u32x2 tag, int lane) { if (lane == 0) st8<SC1>(tag, ws, off, 0); }

struct Ids { int b, tp, ft, lane; };
DEV int krot(const Ids& id) { return (RH * (id.ft & 7) + ((RW * (id.tp & 3)) >> 1)) % 18; }
DEV int kwrap(int i, int r) { const int k = i + r; return k >= 18 ? k - 18 : k; }

DEV void role_L(const Params& P, LAS char* sm, const Ids id, const int lw STAMP_ARG) {
  int bcount = 0; (void)bcount;
  const int lane = id.lane, rr = lane >> 3, sl = lane & 7;
  const rsrc_t rx = mkrs(P.x, S * D * 2), rwin = mkrs(P.win, D * F * 2);
  const unsigned swz = (sl ^ (4 * lw + (rr >> 1))) * 16, prr = 16 * lw + 8 * (rr >> 2) + (rr & 3);
  const unsigned vx = prr * (D * 2) + swz;
  unsigned vb[9];
  auto issue1a = [&](int slot, int i, int ks) { dma<0>(rx, sm + slot * ASLOT + lw * 1024 + i * 2048, vx, (id.tp * 128 + 32 * (i >> 1) + 4 * (i & 1)) * (D * 2) + ks * 128); };
  auto issue1b = [&](int slot, int i, int ks, int col) { dma<0>(rwin, sm + BBASE + slot * BSLOT + lw * 1024 + i * 2048, vb[i], ks * 64 * (F * 2) + (col + id.ft * 144) * 2); };
#pragma unroll
  for (int i = 0; i < 9; ++i) {
    if (i < 8) issue1a(0, i, 0);
    SB();
    const unsigned ci = 64 * (2 * i + lw) + lane, rho = __umul24(ci, 3641) >> 16;
    vb[i] = __umul24(rho, F * 2 - 288) + 16 * ci + __mul24((int)(rho & 4) - (int)((rho >> 1) & 4), F * 2);
    issue1b(0, i, 0, FH);
    SB();
  }
  auto issueA = [&](int n) {
#pragma unroll
    for (int i = 0; i < 9; ++i) { if (i < 8) issue1a(n & 3, i, n); issue1b(n & 3, i, n, FH); } };
  auto issueB = [&](int n, bool with_x) {
#pragma unroll
    for (int i = 0; i < 9; ++i) { if (with_x && i < 8) issue1a((9 - n) & 3, i, 17 - n); issue1b(n & 3, i, 17 - n, 0); } };
  issueA(1); issueA(2);
  STAMP(60);
  const rsrc_t rwsp = mkrs(P.wsp, S * S * 2), rws = mkrs(P.ws, W_END);
  const unsigned vw = prr * (S * 2) + swz, vl = lane * 16 + lw * 1024;
  auto issue2a = [&](int n) { LAS char* a = sm + ((9 - n) & 3) * ASLOT + lw * 1024; const unsigned sa = id.tp * 128 * (S * 2) + (n - 18) * 128;
#pragma unroll
    for (int i = 0; i < 8; ++i) dma<0>(rwsp, a + i * 2048, vw, sa + (32 * (i >> 1) + 4 * (i & 1)) * (S * 2)); };
  auto issue2b = [&](int n) { LAS char* bb = sm + BBASE + (n & 3) * BSLOT + lw * 1024; const unsigned sb = W_ZF + (id.ft * 576 + (n - 18) * 18) * 1024;
#pragma unroll
    for (int i = 0; i < 9; ++i) dma<SC1>(rws, bb + i * 2048, vl, sb + i * 2048); };
#pragma unroll 1
  for (int n = 0; n < 6; ++n) { WAITV(34); LBAR(); issueA(n + 3); }
  WAITV(34); LBAR(); issueB(9, false);
  WAITV(26); LBAR(); issueB(10, false);
  WAITV(18); LBAR(); issueB(11, false);
  BAR();
  WAITV(18); LBAR(); issueB(12, false);
  WAITV(18); LBAR(); issueB(13, true);
  WAITV(26); LBAR();
#pragma unroll 1
  for (int n = 12; n < 16; ++n) { WAITV(17); LBAR(); issueB(n + 2, true); }
  WAITV(17); LBAR(); issue2a(18);
  WAITV(8); LBAR(); issue2a(19);
  BAR(); issue2a(20);
  { const u32x2 tag = lds<u32x2>(sm + T_TAG); wait_flags<1, 2>(rws, W_F2 + id.ft * 128 + (lane & 7) * 16, tag); }
  STAMP(61);
  issue2b(18); issue2b(19); issue2b(20);
  asm volatile("ds_write_b32 %0, %1" :: "v"((unsigned)(size_t)(sm + T_GO)), "v"(1u) : "memory");
  WAITV(18); LBAR(); issue2a(21); issue2b(21);
  WAITV(26); LBAR(); issue2a(22); issue2b(22);
#pragma unroll 1
  for (int n = 20; n < 47; ++n) { WAITV(34); LBAR(); issue2a(n + 3); issue2b(n + 3); }
  const int g = id.ft & 7;
  const unsigned wb0 = W_WOF + (g >> 1) * 72 * 9 * 1024, wb1 = wb0 + (g & 1) * 5 * 1024, vq = lane * 16;
  const int rot = krot(id);
  auto issue3 = [&](auto lo_, auto hi_) { constexpr int lo = decltype(lo_)::value, hi = decltype(hi_)::value;
#pragma unroll
    for (int p0 = lo; p0 < hi; p0 += 2) {
      auto off = [&](int pc) { return (pc % 5 == 4 ? wb0 : wb1) + (18 * (pc / 25) * 9 + pc % 5) * 1024 + kwrap((pc % 25) / 5, rot) * 9216; };
      dma<SC1>(rws, sm + p0 * 1024 + lw * 1024, vq, lw ? off(p0 + 1) : off(p0)); } };
#define IC(n) std::integral_constant<int, n>{}
  WAITV(34); LBAR(); issue3(IC(48), IC(64));
  WAITV(25); LBAR();
  WAITV(8); LBAR(); issue3(IC(16), IC(48));
  LBAR();
  LBAR(); issue3(IC(0), IC(16)); issue3(IC(64), IC(100));
  unsigned sink = 0;
  auto wtouch = [&]() {
    constexpr int K0 = 5 + WD, NK = 18 - K0;
#pragma unroll
    for (int i = 0; i < (NK * 40 + 127) / 128; ++i) { const unsigned n = (i * 2 + lw) * 64 + lane, blk = n >> 1, kw = __umul24(blk, 13108) >> 16, t = blk - kw * 5;
      const unsigned o = n < NK * 40 ? (t == 4 ? wb0 : wb1) + ((18 * (kw & 3) + K0 + (kw >> 2)) * 9 + t) * 1024 + ((id.tp & 3) * 2 + (n & 1)) * 128 : 0xF0000000u;
      asm volatile("buffer_load_dword %0, %1, %2, 0 offen sc1" : "+v"(sink) : "v"(o), "s"(rws) : "memory"); } };
  if ((OPT_TG3 & 3) == 1) wtouch();
  LBAR();
  if ((OPT_TG3 & 3) == 2) { __builtin_amdgcn_s_sleep(2); wtouch(); }
  WAITV((OPT_TG3 & 3) == 2 ? (( 18 - 5 - WD) * 40 + 127) / 128 : 0); LBAR();
  asm volatile("s_waitcnt vmcnt(0)" : "+v"(sink) :: "memory"); LBAR();
}

DEV void role_X(const Params& P, LAS char* sm, const Ids id, const int xw STAMP_ARG) {
  int bcount = 0; (void)bcount;
  const int lane = id.lane;
  const rsrc_t rws = mkrs(P.ws, W_END);
  unsigned sink = 0;
  const u32x2 ev = __builtin_amdgcn_raw_buffer_load_b64(rws, W_EPOCH, 0, SC1);
  const int srow = id.tp * 128 + id.ft * 8 + 4 * xw + (lane >> 4);
  u32x4 xv[5];
  { const rsrc_t rx = mkrs(P.x, S * D * 2);
#pragma unroll
    for (int i = 0; i < 5; ++i) xv[i] = ld16<0>(rx, i < 4 || (lane & 8) == 0 ? srow * (D * 2) + (lane & 15) * 16 : 0xF0000000u, i * 256); }
  const u64 epoch = ((u64)__builtin_amdgcn_readfirstlane(ev.y) << 32) | __builtin_amdgcn_readfirstlane(ev.x);
  u32x2 tag; { const u64 t = ((epoch + 1) * 0x9E3779B97F4A7C15ull) | 1ull; tag.x = (unsigned)t; tag.y = (unsigned)(t >> 32); }
  { float s = 0.f, q = 0.f; unsigned ones = 0x3f803f80u; asm volatile("" : "+v"(ones));
#pragma unroll
    for (int i = 0; i < 5; ++i)
#pragma unroll
      for (int e = 0; e < 4; ++e) { s = dot2(xv[i][e], ones, s); q = dot2(xv[i][e], xv[i][e], q); }
    s += dpp<XOR1>(s); q += dpp<XOR1>(q); s += dpp<XOR2>(s); q += dpp<XOR2>(q); s += dpp<XOR7>(s); q += dpp<XOR7>(q); s += dpp<XOR8>(s); q += dpp<XOR8>(q);
    const float m = s * (1.0f / D), var = fmaxf(q * (1.0f / D) - m * m, 0.f) + 1e-5f;
    if ((lane & 15) == 0) st16<SC1>(seal(m, var, tag), rws, W_G0X + srow * 16, 0);
    if (xw == 0 && lane == 0) { *(LAS u32x2*)(sm + T_TAG) = tag; *(LAS u32x4*)(sm + T_CNT) = (u32x4){0u, 0u, 0u, 0u}; }
  }
  STAMP(0);
  BAR(); BAR(); BAR(); BAR();
  LAS char* stg = sm + STG + xw * STG_W;
  const int unit = xw ? 256 + id.b : id.b, wks = unit >> 2, wu = unit & 3; const bool won = !xw || id.b < 32;
  const int tile = xw ? id.b : 256 + id.b - 32; const bool kon = xw || (id.b >= 32 && id.b < 64);
  const int tb = tile / 9, db = tile - tb * 9, cc = lane & 7, rg = lane >> 3;
  u32x4 kk[11], vv[8], sh[4];
  { const int row = 64 * xw + lane; const u64 t0 = __builtin_amdgcn_s_memrealtime();
    u32x4 gx = ld16<SC1>(rws, W_G0X + (id.tp * 128 + row) * 16, 0);
    BAR(); BAR();
    while (!__all(sealed(gx, tag)) && !expired(t0)) { __builtin_amdgcn_s_sleep(4); FENCE(); gx = ld16<SC1>(rws, W_G0X + (id.tp * 128 + row) * 16, 0); }
    const float var = __uint_as_float(gx.y), r = __builtin_amdgcn_rsqf(var);
    *(LAS unsigned*)(sm + T_M1 + row * 4) = gx.x; *(LAS float*)(sm + T_R1 + row * 4) = r;
    *(LAS float*)(sm + T_A0 + row * 4) = GC0 * r; *(LAS float*)(sm + T_A1 + row * 4) = GC1 * r * r * r; *(LAS float*)(sm + T_IR + row * 4) = var * r;
    WAITL(0); BAR();
  }
  STAMP(1);
  { const rsrc_t rk = mkrs(P.k, S * D * 2), rv = mkrs(P.v, S * D * 2), rs = mkrs(P.shk, D * 4 * 2), rwo = mkrs(P.wout, FH * D * 2);
    const int t0 = 64 * tb + 8 * rg; const unsigned col = (64 * db + 8 * cc) * 2;
    if (kon) {
#pragma unroll
      for (int i = 0; i < 11; ++i) kk[i] = ld16<0>(rk, t0 + i - 3 >= 0 ? (unsigned)(t0 + i - 3) * (D * 2) + col : 0xF0000000u, 0);
    }
    BAR();
    if (kon) {
#pragma unroll
      for (int i = 0; i < 8; ++i) vv[i] = ld16<0>(rv, t0 * (D * 2) + col, i * (D * 2));
#pragma unroll
      for (int i = 0; i < 4; ++i) sh[i] = ld16<0>(rs, col * 4, i * 16);
    }
    BAR();
    if (won) { const int fb0 = wks < 64 ? 144 * (wks >> 2) + 32 * (wks & 3) : 288 * (wks - 64) + 128, fst = wks < 64 ? 0 : 128;
#pragma unroll
      for (int i = 0; i < 9; ++i) { const int ci = 64 * i + lane, rho = __umul24(ci, 3641) >> 16, ch = ci - rho * 18;
        dma_hidden(rwo, stg + i * 1024, (fb0 + rho + (rho >> 4) * fst) * (D * 2) + wu * 288 + ch * 16); } }
    BAR();
    if (kon) {
#pragma unroll
      for (int i = 0; i < 4; ++i) asm volatile("" : "+v"(sh[i]), "+v"(vv[i]), "+v"(vv[4 + i]), "+v"(kk[i]), "+v"(kk[4 + i]), "+v"(kk[i == 3 ? 0 : 8 + i])); }
    if (OPT_TWSP) { const rsrc_t r = mkrs(P.wsp, S * S * 2);
#pragma unroll
      for (int i = 0; i < 2; ++i) asm volatile("buffer_load_dword %0, %1, %2, 0 offen" : "+v"(sink) : "v"(id.b * 32768 + ((2 * i + xw) * 64 + lane) * 128), "s"(r) : "memory"); }
  }
  BAR();
  u32x4 ko[8];
  auto kvstep = [&](int e) {
    if (kon) {
      float c[4], kf[11], y[8];
#pragma unroll
      for (int j = 0; j < 4; ++j) { const unsigned w = sh[e >> 1][(e & 1) * 2 + (j >> 1)]; c[j] = (j & 1) ? bhi(w) : blo(w); }
#pragma unroll
      for (int i = 0; i < 11; ++i) { const unsigned w = kk[i][e >> 1]; kf[i] = (e & 1) ? bhi(w) : blo(w); }
#pragma unroll
      for (int i = 0; i < 8; ++i) { const unsigned w = vv[i][e >> 1];
        float a = c[3] * kf[i]; asm volatile("" : "+v"(a)); a = fmaf(c[2], kf[i + 1], a); asm volatile("" : "+v"(a)); a = fmaf(c[1], kf[i + 2], a); asm volatile("" : "+v"(a)); a = fmaf(c[0], kf[i + 3], a);
        asm volatile("" : "+v"(a)); y[i] = a * ((e & 1) ? bhi(w) : blo(w)); asm volatile("" : "+v"(y[i])); }
      ko[e] = (u32x4){pk(y[7], y[6]), pk(y[5], y[4]), pk(y[3], y[2]), pk(y[1], y[0])};
      asm volatile("" : "+v"(ko[e]));
    } };
  { const int row = 64 * xw + lane;
    const f32x2 a = lds<f32x2>(sm + T_PART + row * 8) + lds<f32x2>(sm + T_PART + 1024 + row * 8);
    st16<SC1>(seal(a.x, a.y, tag), rws, W_G1S + ((id.tp * 16 + id.ft) * 128 + row) * 16, 0);
    kvstep(0); BAR(); kvstep(1); BAR();
    u32x4 gs[16];
    auto gather = [&]() {
#pragma unroll
      for (int i = 0; i < 16; ++i) gs[i] = ld16<SC1>(rws, W_G1S + (id.tp * 16 * 128 + row) * 16, i * 2048); };
    unsigned bad = 0; float s = 0.f, q = 0.f;
    auto sum = [&](int lo, int hi) {
#pragma unroll
      for (int i = lo; i < hi; ++i) { bad |= (gs[i].x ^ gs[i].z ^ tag.x) | (gs[i].y ^ gs[i].w ^ tag.y); s += __uint_as_float(gs[i].x); q += __uint_as_float(gs[i].y); }
      asm volatile("" : "+v"(bad), "+v"(s), "+v"(q)); };
    gather(); kvstep(2); BAR(); kvstep(3); BAR(); sum(0, 10); BAR();
    sum(10, 16);
    if (__builtin_expect(!__all(bad == 0), 0)) { const u64 t0 = __builtin_amdgcn_s_memrealtime();
      do { FENCE(); gather(); bad = 0; s = 0.f; q = 0.f; sum(0, 16); } while (!__all(bad == 0) && !expired(t0)); }
    const float m = s * (1.0f / FH), var = fmaxf(q * (1.0f / FH) - m * m, 0.f), r = __builtin_amdgcn_rsqf(var + 1e-5f);
    *(LAS float*)(sm + T_R2 + row * 4) = r; *(LAS float*)(sm + T_N2 + row * 4) = -m * r;
    asm volatile("s_waitcnt vmcnt(0) lgkmcnt(0)" : "+v"(sink) :: "memory"); BAR();
  }
  STAMP(2);
  kvstep(4); BAR(); kvstep(5); BAR();
  if (xw == 0) set_flag(rws, W_F2 + (id.ft * 16 + id.tp) * 8, tag, lane);
  kvstep(6); kvstep(7); BAR();
  { u32x4 wf[9];
    if (won) {
      const int q = lane >> 4, qq = (lane & 15) >> 2, p = lane & 3;
      LAS char* base = stg + (4 * q + qq) * 288;
#pragma unroll
      for (int T = 0; T < 9; ++T) { const int cb = T < 4 ? 32 * (T >> 1) + 4 * (T & 1) : T == 4 ? 64 : 80 + 32 * ((T - 5) >> 1) + 4 * ((T - 5) & 1);
        LAS char* a = base + cb * 2 + p * (T == 4 ? 8 : 16);
        wf[T] = __builtin_bit_cast(u32x4, cat(ldtr(a), ldtr(a + 16 * 288))); }
      WAITL(0);
    }
    if (kon) {
#pragma unroll
      for (int e = 0; e < 8; ++e) *(LAS u32x4*)(stg + (8 * cc + e) * 128 + (((7 - rg) ^ cc) * 16)) = ko[e];
#pragma unroll
      for (int i = 0; i < 8; ++i) ko[i] = lds<u32x4>(stg + (8 * i + rg) * 128 + ((cc ^ i) * 16));
    }
    const u64 t0 = __builtin_amdgcn_s_memrealtime();
    while (*(volatile LAS unsigned*)(sm + T_GO) == 0u && !expired(t0)) __builtin_amdgcn_s_sleep(2);
    FENCE();
    if (won) {
#pragma unroll
      for (int T = 0; T < 9; ++T) st16<SC1>(wf[T], rws, W_WOF + lane * 16, ((wu * 72 + wks) * 9 + T) * 1024); }
    if (kon) {
#pragma unroll
      for (int i = 0; i < 8; ++i) st16<SC1>(ko[i], rws, W_KVT + (64 * db + rg) * (S * 2) + (S - 64 * (tb + 1)) * 2 + cc * 16, 8 * i * (S * 2)); }
  }
  BAR(); BAR(); BAR();
  const unsigned h0a = ((const CAS unsigned*)P.ssmk)[id.b * S], h0b = ((const CAS unsigned*)P.ssmk)[(xw ? 2 * id.b + 1 : 512 + (id.b >> 2)) * (S / 2)];
  BAR(); WAITV(0); BAR();
  if (xw == 0) set_flag(rws, W_F5 + id.b * 8, tag, lane);
  STAMP(3);
#pragma unroll 1
  for (int i = 0; i < 22; ++i) BAR();
  { u32x4 f0 = {tag.x, tag.y, tag.x, tag.y}, f1 = f0;
    if (xw == 0) { f0 = ld16<SC1>(rws, W_F5 + lane * 16, 0); f1 = ld16<SC1>(rws, W_F5 + lane * 16, 1024); }
    BAR(); BAR();
    if (!__all(flags_ok(f0, tag) && flags_ok(f1, tag))) wait_flags<2, 8>(rws, W_F5 + lane * 16, tag); }
  BAR();
  STAMP(4);
  { const rsrc_t rh = mkrs(P.ssmk, D * S * 2);
    auto fetch = [&](int ci) { const int chn = ci == 2 ? 512 + (id.b >> 2) : 2 * id.b + ci; LAS char* cb = sm + CV + ci * CV_CH;
      const unsigned h0 = ci == 0 ? h0a : h0b;
      if (lane < 8) { *(LAS unsigned*)(cb + lane * 4) = 0u; *(LAS unsigned*)(cb + CV_O + lane * 4) = lane == 7 ? h0 << 16 : 0u; }
      if (lane < 38) *(LAS u32x4*)(cb + CV_U + 4096 + lane * 16) = (u32x4){0u, 0u, 0u, 0u};
      if (ci == 2) { *(LAS u32x4*)(cb + CV_U + 4704 + lane * 16) = (u32x4){0u, 0u, 0u, 0u}; *(LAS u32x4*)(cb + CV_U + 5728 + lane * 16) = (u32x4){0u, 0u, 0u, 0u}; }
      FENCE();
#pragma unroll
      for (int i = 0; i < 4; ++i) { dma<0>(rh, cb + 32 + i * 1024, lane * 16, chn * (S * 2) + i * 1024);
        dma<0>(rh, cb + CV_O + 32 + i * 1024, lane * 16 + 2, chn * (S * 2) + i * 1024);
        dma<SC1>(rws, cb + CV_U + i * 1024, lane * 16, W_KVT + chn * (S * 2) + i * 1024); } };
    if (xw == 0) fetch(0);
    BAR();
    fetch(2 - xw);
    BAR();
    WAITV(0); WAITL(0); BAR(); BAR();
  }
  BAR();
  if (xw == 0) { set_flag(rws, W_F3 + (id.tp * 16 + id.ft) * 8, tag, lane); wait_flags<1, 1>(rws, W_F3 + id.tp * 128 + (lane & 7) * 16, tag); }
  STAMP(5);
  BAR();
  if (xw == 1 && (OPT_TG3 & 4)) {
#pragma unroll
    for (int i = 0; i < 4; ++i) { const unsigned n = i * 64 + lane, o = n < 240 ? W_HF + ((id.tp * 8 + 4 * (id.ft >> 3) + (n & 3)) * 72 + 18 * ((n >> 2) & 3) + 3 + (n >> 4)) * 1024 + (id.ft & 7) * 128 : 0xF0000000u;
      asm volatile("buffer_load_dword %0, %1, %2, 0 offen sc1" : "+v"(sink) : "v"(o), "s"(rws) : "memory"); } }
  if (xw == 0) {
    const u64 t0 = __builtin_amdgcn_s_memrealtime();
    while (*(volatile LAS unsigned*)(sm + T_CNT) != 4u && !expired(t0)) __builtin_amdgcn_s_sleep(2);
    FENCE();
    set_flag(rws, W_F4 + id.b * 8, tag, lane);
    const int g = id.ft & 7, d0 = 144 * (g >> 1) + 64 * (g & 1);
    { const rsrc_t rx = mkrs(P.x, S * D * 2);
#pragma unroll
      for (int i = 0; i < 10; ++i) { const unsigned ci = 64 * i + lane, r = __umul24(ci, 6554) >> 16; dma<0>(rx, sm + XTILE + i * 1024, (id.tp * 128 + 64 * (id.ft >> 3) + r) * (D * 2) + d0 * 2 + (ci - 10 * r) * 16, 0); } }
    __builtin_amdgcn_s_sleep(24); wait_flags<2, 12>(rws, W_F4 + lane * 16, tag);
#pragma unroll
    for (int i = 0; i < 10; ++i) dma<SC1>(rws, sm + YTILE + i * 1024, W_YT + (d0 + (lane >> 3)) * (S * 2) + (id.tp * 128 + 64 * (id.ft >> 3)) * 2 + (lane & 7) * 16, i * 8 * (S * 2));
    WAITV(0);
    asm volatile("ds_write_b32 %0, %1" :: "v"((unsigned)(size_t)(sm + T_YOK)), "v"(1u) : "memory");
    if (id.b == 0 && lane == 0) st8<SC1>((u32x2){(unsigned)(epoch + 1), (unsigned)((epoch + 1) >> 32)}, rws, W_EPOCH, 0);
  }
  STAMP(6);
  asm volatile("s_waitcnt vmcnt(0)" : "+v"(sink) :: "memory"); BAR();
}

DEV void role_C(const Params& P, LAS char* sm, const Ids id, const int wid STAMP_ARG) {
  int bcount = 0; (void)bcount;
  const int lane = id.lane, wt = wid >> 1, wf = wid & 1, c = lane & 15, q = lane >> 4;
  const rsrc_t rws = mkrs(P.ws, W_END);
  __builtin_amdgcn_s_setprio(1);
  unsigned dm = ((const CAS unsigned*)P.ssmd)[id.b], dsh = ((const CAS unsigned*)P.ssmd)[256 + (id.b >> 3)];
  asm volatile("" : "+s"(dm), "+s"(dsh));
  f32x4 acc[4][4], accx[2], csa[3];
  bf16x8 tf[2][4], ff[2][5], ones;
  LAS char *ta[2][2], *fb[2][2], *fx[2];
#pragma unroll
  for (int s = 0; s < 2; ++s) {
    const int o = (32 * (s ^ wt) + 16 * (q >> 1) + 4 * (q & 1) + (c >> 2)) * 288 + 8 * (c & 3);
    fx[s] = sm + BBASE + o + 256;
#pragma unroll
    for (int h = 0; h < 2; ++h) { ta[s][h] = sm + (64 * wt + 32 * (h ^ wf) + c) * 128 + (((q + 4 * (s ^ wt)) ^ ((c >> 1) & 7)) * 16); fb[s][h] = sm + BBASE + o + wf * 128 + (h ^ wt) * 64; }
  }
#pragma unroll
  for (int e = 0; e < 8; ++e) ones[e] = 0x3f80;
  asm volatile("" : "+v"(ones));
  auto rd = [&](auto mode, int b, int as, int bs, int s) {
    constexpr int MODE = decltype(mode)::value;
#pragma unroll
    for (int j = 0; j < 4; ++j) { LAS char* p = fb[s][j >> 1] + bs * BSLOT;
      ff[b][j] = MODE < 2 ? cat(ldtr(p + (j & 1) * 32), ldtr(p + (j & 1) * 32 + 8 * 288)) : lds<bf16x8>(p + (j & 1) * 1024); }
#pragma unroll
    for (int i = 0; i < 4; ++i) tf[b][i] = lds<bf16x8>(ta[s][i >> 1] + as * ASLOT + (i & 1) * 2048);
    ff[b][4] = MODE < 2 ? cat(ldtr(fx[s] + bs * BSLOT), ldtr(fx[s] + bs * BSLOT + 8 * 288)) : lds<bf16x8>(fx[s] + bs * BSLOT);
  };
  auto mm = [&](auto mode, int b, int lo, int hi) {
    constexpr int MODE = decltype(mode)::value;
#pragma unroll
    for (int n = 0; n < (MODE == 2 ? 18 : 21 - b); ++n) if (n >= lo && n < hi) {
      if (n < 16) { const int i = n >> 2, j = (i & 1) ? 3 - (n & 3) : (n & 3);
        acc[i][j] = MODE == 0 ? MFMA(tf[b][i], ff[b][j], acc[i][j]) : MFMA(ff[b][j], tf[b][i], acc[i][j]); }
      else if (n < 18) accx[n - 16] = MODE == 0 ? MFMA(tf[b][n - 16], ff[b][4], accx[n - 16]) : MFMA(ff[b][4], tf[b][n - 16], accx[n - 16]);
      else { const int j = n == 20 ? 4 : n - 18; csa[n - 18] = MODE == 0 ? MFMA(ones, ff[b][j], csa[n - 18]) : MFMA(ff[b][j], ones, csa[n - 18]); }
    }
  };
  auto stage = [&](auto mode, int as, int bs, bool drain) {
    constexpr int MODE = decltype(mode)::value;
    mm(mode, 1, 0, 4); SB();
    if (drain) WAITV(0);
    WAITL(0); BAR(); SB();
    rd(mode, 0, as, bs, 0); mm(mode, 1, 4, 21);
    if (MODE < 2) { for (int i = 0; i < 14; ++i) { SG(0x100, 1); SG(0x8, 1); } SG(0x8, 2); }
    else { for (int i = 0; i < 4; ++i) { SG(0x100, 1); SG(0x8, 1); SG(0x100, 1); SG(0x8, 2); } SG(0x100, 1); SG(0x8, 2); }
    SB();
    rd(mode, 1, as, bs, 1); mm(mode, 0, 0, 21);
    if (MODE < 2) { for (int i = 0; i < 7; ++i) { SG(0x100, 1); SG(0x8, 1); SG(0x100, 1); SG(0x8, 2); } }
    else { for (int i = 0; i < 9; ++i) { SG(0x100, 1); SG(0x8, 2); } }
    SB();
  };
  auto first = [&](auto mode, int as, int bs) { WAITL(0); BAR(); SB(); rd(mode, 0, as, bs, 0); rd(mode, 1, as, bs, 1); mm(mode, 0, 0, 21); SB(); };
  auto zero = [&]() {
#pragma unroll
    for (int i = 0; i < 4; ++i)
#pragma unroll
      for (int j = 0; j < 4; ++j) acc[i][j] = (f32x4){0.f, 0.f, 0.f, 0.f};
    accx[0] = accx[1] = csa[0] = csa[1] = csa[2] = (f32x4){0.f, 0.f, 0.f, 0.f}; };
  const std::integral_constant<int, 0> M0; const std::integral_constant<int, 1> M1; const std::integral_constant<int, 2> M2;
  const int jg0 = 4 * wf + 2 * wt, jg2 = 4 * wf + 2 - 2 * wt;

  zero();
  first(M0, 0, 0);
  STAMP(0);
#pragma unroll 1
  for (int it = 0; it < 2; ++it) { stage(M0, 1, 1, false); stage(M0, 2, 2, false); stage(M0, 3, 3, false); stage(M0, 0, 0, false); }
  mm(M0, 1, 18, 21); mm(M0, 1, 0, 8); SB();
  if (q == 0) { *(LAS float*)(sm + T_CS2 + (jg0 * 16 + c) * 4) = csa[0][0]; *(LAS float*)(sm + T_CS2 + (jg0 * 16 + 16 + c) * 4) = csa[1][0]; *(LAS float*)(sm + T_CS2 + (128 + 16 * wt + c) * 4) = csa[2][0]; }
  mm(M0, 1, 8, 18); SB();
  WAITL(0); BAR();
  STAMP(1);
  u32x4 z2p[2][4], z2x;
  { float cs[5];
    cs[0] = csa[0][0]; cs[1] = csa[1][0]; cs[2] = lds<float>(sm + T_CS2 + (jg2 * 16 + c) * 4); cs[3] = lds<float>(sm + T_CS2 + (jg2 * 16 + 16 + c) * 4);
    cs[4] = csa[2][0] + lds<float>(sm + T_CS2 + (144 - 16 * wt + c) * 4);
    float red[2][16];
#pragma unroll
    for (int i = 0; i < 4; ++i) {
      const int ro = (64 * wt + 32 * ((i >> 1) ^ wf) + 8 * q + 4 * (i & 1)) * 4;
      const f32x4 m = lds<f32x4>(sm + T_M1 + ro), a0 = lds<f32x4>(sm + T_A0 + ro), a1 = lds<f32x4>(sm + T_A1 + ro), ir = lds<f32x4>(sm + T_IR + ro);
      f32x4 su = {0.f, 0.f, 0.f, 0.f}, sq = {0.f, 0.f, 0.f, 0.f};
#pragma unroll
      for (int j = 0; j < (i < 2 ? 5 : 4); ++j) {
        const f32x4 z = gelu4((j < 4 ? acc[i][j] : accx[i]) - m * cs[j], a0, a1, ir);
        su = j ? su + z : z; sq = j ? sq + z * z : z * z;
        u32x4& dst = j < 4 ? z2p[i >> 1][j] : z2x;
        dst[2 * (i & 1)] = pk(z[0], z[1]); dst[2 * (i & 1) + 1] = pk(z[2], z[3]);
      }
#pragma unroll
      for (int e = 0; e < 4; ++e) { red[0][4 * i + e] = su[e]; red[1][4 * i + e] = sq[e]; }
    }
    float r8[2][8], r4[2][4];
#pragma unroll
    for (int st = 0; st < 2; ++st) {
#pragma unroll
      for (int e = 0; e < 8; e += 4)
        asm volatile("s_nop 1\n\t"
                     "v_add_f32_dpp %0, %4, %4 row_ror:8 row_mask:0xf bank_mask:0x3\n\tv_add_f32_dpp %1, %5, %5 row_ror:8 row_mask:0xf bank_mask:0x3\n\t"
                     "v_add_f32_dpp %2, %6, %6 row_ror:8 row_mask:0xf bank_mask:0x3\n\tv_add_f32_dpp %3, %7, %7 row_ror:8 row_mask:0xf bank_mask:0x3\n\t"
                     "v_add_f32_dpp %0, %8, %8 row_ror:8 row_mask:0xf bank_mask:0xc\n\tv_add_f32_dpp %1, %9, %9 row_ror:8 row_mask:0xf bank_mask:0xc\n\t"
                     "v_add_f32_dpp %2, %10, %10 row_ror:8 row_mask:0xf bank_mask:0xc\n\tv_add_f32_dpp %3, %11, %11 row_ror:8 row_mask:0xf bank_mask:0xc"
                     : "=&v"(r8[st][e]), "=&v"(r8[st][e + 1]), "=&v"(r8[st][e + 2]), "=&v"(r8[st][e + 3])
                     : "v"(red[st][e]), "v"(red[st][e + 1]), "v"(red[st][e + 2]), "v"(red[st][e + 3]), "v"(red[st][8 + e]), "v"(red[st][9 + e]), "v"(red[st][10 + e]), "v"(red[st][11 + e]));
      asm volatile("s_nop 1\n\t"
                   "v_add_f32_dpp %0, %4, %4 row_half_mirror row_mask:0xf bank_mask:0x5\n\tv_add_f32_dpp %1, %5, %5 row_half_mirror row_mask:0xf bank_mask:0x5\n\t"
                   "v_add_f32_dpp %2, %6, %6 row_half_mirror row_mask:0xf bank_mask:0x5\n\tv_add_f32_dpp %3, %7, %7 row_half_mirror row_mask:0xf bank_mask:0x5\n\t"
                   "v_add_f32_dpp %0, %8, %8 row_half_mirror row_mask:0xf bank_mask:0xa\n\tv_add_f32_dpp %1, %9, %9 row_half_mirror row_mask:0xf bank_mask:0xa\n\t"
                   "v_add_f32_dpp %2, %10, %10 row_half_mirror row_mask:0xf bank_mask:0xa\n\tv_add_f32_dpp %3, %11, %11 row_half_mirror row_mask:0xf bank_mask:0xa\n\t"
                   "v_add_f32_dpp %0, %0, %0 quad_perm:[1,0,3,2] row_mask:0xf bank_mask:0xf\n\tv_add_f32_dpp %1, %1, %1 quad_perm:[1,0,3,2] row_mask:0xf bank_mask:0xf\n\t"
                   "v_add_f32_dpp %2, %2, %2 quad_perm:[1,0,3,2] row_mask:0xf bank_mask:0xf\n\tv_add_f32_dpp %3, %3, %3 quad_perm:[1,0,3,2] row_mask:0xf bank_mask:0xf\n\t"
                   "v_add_f32_dpp %0, %0, %0 quad_perm:[2,3,0,1] row_mask:0xf bank_mask:0xf\n\tv_add_f32_dpp %1, %1, %1 quad_perm:[2,3,0,1] row_mask:0xf bank_mask:0xf\n\t"
                   "v_add_f32_dpp %2, %2, %2 quad_perm:[2,3,0,1] row_mask:0xf bank_mask:0xf\n\tv_add_f32_dpp %3, %3, %3 quad_perm:[2,3,0,1] row_mask:0xf bank_mask:0xf"
                   : "=&v"(r4[st][0]), "=&v"(r4[st][1]), "=&v"(r4[st][2]), "=&v"(r4[st][3])
                   : "v"(r8[st][0]), "v"(r8[st][1]), "v"(r8[st][2]), "v"(r8[st][3]), "v"(r8[st][4]), "v"(r8[st][5]), "v"(r8[st][6]), "v"(r8[st][7]));
    }
    if ((c & 3) == 0) { LAS char* pp = sm + T_PART + wf * 1024 + (64 * wt + 32 * ((c >> 3) ^ wf) + 8 * q + 4 * ((c >> 2) & 1)) * 8;
      *(LAS f32x4*)pp = (f32x4){r4[0][0], r4[1][0], r4[0][1], r4[1][1]}; *(LAS f32x4*)(pp + 16) = (f32x4){r4[0][2], r4[1][2], r4[0][3], r4[1][3]}; }
#pragma unroll
    for (int i = 0; i < 2; ++i) asm volatile("" : "+v"(z2p[i][0]), "+v"(z2p[i][1]), "+v"(z2p[i][2]), "+v"(z2p[i][3]));
    asm volatile("" : "+v"(z2x));
  }
  STAMP(2);
  zero();
  first(M1, 0, 1);
  stage(M1, 3, 2, false); stage(M1, 2, 3, false); stage(M1, 1, 0, false);
  stage(M1, 0, 1, false); stage(M1, 3, 2, false); stage(M1, 2, 3, false);
  STAMP(3);
  {
#pragma unroll
    for (int i = 0; i < 2; ++i) asm volatile("" : "+v"(z2p[i][0]), "+v"(z2p[i][1]), "+v"(z2p[i][2]), "+v"(z2p[i][3]));
    asm volatile("" : "+v"(z2x));
#pragma unroll
    for (int ip = 0; ip < 2; ++ip) {
      const int ro = (64 * wt + 32 * (ip ^ wf) + 8 * q) * 4;
      const f32x4 r0 = lds<f32x4>(sm + T_R2 + ro), r1 = lds<f32x4>(sm + T_R2 + ro + 16), n0 = lds<f32x4>(sm + T_N2 + ro), n1 = lds<f32x4>(sm + T_N2 + ro + 16);
      const unsigned so = W_ZF + ((id.ft * 64 + 4 * id.tp + 2 * wt + (ip ^ wf)) * 9) * 1024;
#pragma unroll
      for (int j = 0; j < (ip == 0 ? 5 : 4); ++j) { const u32x4 z = j < 4 ? z2p[ip][j] : z2x;
        const f32x4 a = (f32x4){blo(z[0]), bhi(z[0]), blo(z[1]), bhi(z[1])} * r0 + n0, b = (f32x4){blo(z[2]), bhi(z[2]), blo(z[3]), bhi(z[3])} * r1 + n1;
        st16<SC1>((u32x4){pk(a[0], a[1]), pk(a[2], a[3]), pk(b[0], b[1]), pk(b[2], b[3])}, rws, lane * 16, so + (j < 2 ? jg0 + j : j < 4 ? jg2 + j - 2 : 8) * 1024); }
    }
    SB();
  }
  unsigned braw[4];
  { const rsrc_t rb = mkrs(P.bsp, S * 2);
#pragma unroll
    for (int i = 0; i < 4; ++i) braw[i] = __builtin_amdgcn_raw_buffer_load_b16(rb, (id.tp * 128 + 64 * wt + prow(i ^ (2 * wf), c)) * 2, 0, 0); }
  STAMP(4);
  stage(M1, 1, 0, false); stage(M1, 0, 1, true);
  mm(M1, 1, 18, 21); mm(M1, 1, 0, 8); SB();
  if (c == 0) { *(LAS f32x4*)(sm + T_CS1 + (jg0 * 16 + 4 * q) * 4) = csa[0]; *(LAS f32x4*)(sm + T_CS1 + (jg0 * 16 + 16 + 4 * q) * 4) = csa[1]; *(LAS f32x4*)(sm + T_CS1 + (128 + 16 * wt + 4 * q) * 4) = csa[2]; }
  mm(M1, 1, 8, 18); SB();
  WAITL(0); BAR();
  STAMP(5);
  u32x2 z1p[4][4], z1x[2];
  { f32x4 cs[5];
    cs[0] = csa[0]; cs[1] = csa[1]; cs[2] = lds<f32x4>(sm + T_CS1 + (jg2 * 16 + 4 * q) * 4); cs[3] = lds<f32x4>(sm + T_CS1 + (jg2 * 16 + 16 + 4 * q) * 4);
    cs[4] = csa[2] + lds<f32x4>(sm + T_CS1 + (144 - 16 * wt + 4 * q) * 4);
#pragma unroll
    for (int i = 0; i < 4; ++i) { const int ro = (64 * wt + prow(i ^ (2 * wf), c)) * 4;
      const float m = lds<float>(sm + T_M1 + ro), a0 = lds<float>(sm + T_A0 + ro), a1 = lds<float>(sm + T_A1 + ro), ir = lds<float>(sm + T_IR + ro), bias = blo(braw[i]);
#pragma unroll
      for (int j = 0; j < (i < 2 ? 5 : 4); ++j) {
        f32x4& a = j < 4 ? acc[i][j] : accx[i];
        const f32x4 z = gelu4(a - m * cs[j], a0, a1, ir);
        (j < 4 ? z1p[i][j] : z1x[i]) = (u32x2){pk(z[0], z[1]), pk(z[2], z[3])};
        a = (f32x4){bias, bias, bias, bias};
      } }
#pragma unroll
    for (int s = 0; s < 2; ++s) { fx[s] = sm + BBASE + (s * 9 + 8) * 1024 + lane * 16;
#pragma unroll
      for (int h = 0; h < 2; ++h) { fb[s][h] = sm + BBASE + (s * 9 + (h ? jg2 : jg0)) * 1024 + lane * 16; ta[s][h] = sm + (64 * wt + 32 * (h ^ wf) + c) * 128 + (((q + 4 * s) ^ ((c >> 1) & 7)) * 16); } }
#pragma unroll
    for (int i = 0; i < 4; ++i) asm volatile("" : "+v"(z1p[i][0]), "+v"(z1p[i][1]), "+v"(z1p[i][2]), "+v"(z1p[i][3]), "+v"(acc[i][0]), "+v"(acc[i][1]), "+v"(acc[i][2]), "+v"(acc[i][3]));
    asm volatile("" : "+v"(z1x[0]), "+v"(z1x[1]), "+v"(accx[0]), "+v"(accx[1]));
  }
  STAMP(6);
  const int jq = id.b & 3;
  LAS char *cs_ = sm + CV + 2 * CV_CH, *cb = sm + CV + wt * CV_CH;
  LAS char *am[4], *um = cb + CV_U + (2032 - 16 * c + 8 * q) * 2 + 256 * wf - 3584;
#pragma unroll
  for (int i = 0; i < 4; ++i) { am[i] = cb + ((c & 1) ? 0 : CV_O) + (4 * q + ((c + 1) >> 1)) * 4 + 256 * wf + 1024 * i; asm volatile("" : "+v"(am[i])); }
  asm volatile("" : "+v"(um));
  bf16x8 ca[2][8], cu[2][12];
  auto afrag = [&](LAS char* p) { const unsigned d0 = lds<unsigned>(p), d1 = lds<unsigned>(p + 4), d2 = lds<unsigned>(p + 8), d3 = lds<unsigned>(p + 12);
    return __builtin_bit_cast(bf16x8, (u32x4){d0, d1, d2, d3}); };
  auto lds_mn = [&](int b, int kk) {
#pragma unroll
    for (int e = 0; e < 8; ++e) { cu[b][e] = lds<bf16x8>(um + kk * 64 + 512 * (7 - e)); if (e == 0) ca[b][0] = afrag(am[0] + kk * 64); }
#pragma unroll
    for (int e = 1; e < 8; ++e) ca[b][e] = afrag(am[e >> 1] + kk * 64 + (e & 1) * 512); };
  first(M2, 3, 2);
  STAMP(7);
#pragma unroll 1
  for (int it = 0; it < 7; ++it) { stage(M2, 2, 3, false); stage(M2, 1, 0, false); stage(M2, 0, 1, false); stage(M2, 3, 2, false); }
  stage(M2, 2, 3, false); stage(M2, 1, 0, false); stage(M2, 0, 1, false);
  mm(M2, 1, 0, 4); SB(); WAITL(0); BAR(); SB();
  mm(M2, 1, 4, 18); SB();
  lds_mn(0, 0); SB();
  STAMP(8);
#pragma unroll
  for (int i = 0; i < 4; ++i) asm volatile("" : "+v"(z1p[i][0]), "+v"(z1p[i][1]), "+v"(z1p[i][2]), "+v"(z1p[i][3]));
  asm volatile("" : "+v"(z1x[0]), "+v"(z1x[1]));
#pragma unroll
  for (int i = 0; i < 4; ++i) {
    const unsigned so = W_HF + (id.tp * 8 + 4 * wt + (i ^ (2 * wf))) * 72 * 1024;
    auto gate = [&](u32x2 z, f32x4 a) { const f32x4 h = (f32x4){blo(z.x), bhi(z.x), blo(z.y), bhi(z.y)} * a; return (u32x2){pk(h[0], h[1]), pk(h[2], h[3])}; };
#pragma unroll
    for (int pp = 0; pp < 2; ++pp) { const u32x2 lo = gate(z1p[i][2 * pp], acc[i][2 * pp]), hi = gate(z1p[i][2 * pp + 1], acc[i][2 * pp + 1]);
      st16<SC1>((u32x4){lo.x, lo.y, hi.x, hi.y}, rws, lane * 16, so + (4 * id.ft + 2 * wf + (pp ^ wt)) * 1024); }
    if (i < 2) st8<SC1>(gate(z1x[i], accx[i]), rws, lane * 16 + (id.ft & 1) * 8, so + (64 + (id.ft >> 1)) * 1024);
  }
  const int g = id.ft & 7, gp = g & 1, half = id.ft >> 3, rot = krot(id);
  const unsigned wso = W_WOF + ((((EXP & 4) ? 0 : g >> 1) * 72 + 18 * wid) * 9) * 1024;
  u32x4 wr[WD][5];
  auto ldw = [&](int slot, int i) {
#pragma unroll
    for (int t = 0; t < 5; ++t) wr[slot][t] = ld16<(EXP & 8) ? 0 : SC1>(rws, lane * 16, wso + kwrap(i, rot) * 9216 + (t == 4 ? 4 : t + gp * 5) * 1024); };
  SB();
  SB(); BAR(); SB();
  STAMP(9);
  u32x4 hr[3][4];
  const unsigned hso = W_HF + (((EXP & 2) ? (id.b & 7) * 4 : id.tp * 8 + 4 * half) * 72 + 18 * wid) * 1024;
  auto ldh = [&](int slot, int i) {
#pragma unroll
    for (int t = 0; t < 4; ++t) hr[slot][t] = ld16<(EXP & 8) ? 0 : SC1>(rws, lane * 16, hso + (((unsigned)t ^ (wf ^ (2 * gp))) * 72 + kwrap(i, rot)) * 1024); };
  { const float dvm = wt ? bhi(dm) : blo(dm), dvs = (id.b & 4) ? bhi(dsh) : blo(dsh);
    f32x4 cm[8], s0 = {0.f, 0.f, 0.f, 0.f}, s1 = s0;
#pragma unroll
    for (int j = 0; j < 8; ++j) cm[j] = s0;
    LAS char *as[4], *u1, *u0;
    auto addr_sh = [&]() {
      const int da = (2 - wt) * CV_CH + 128 * wid - 256 * wf;
#pragma unroll
      for (int i = 0; i < 4; ++i) { as[i] = am[i] + da; asm volatile("" : "+v"(as[i])); }
      u1 = um + da + 512 * jq; u0 = um + da + 512 * (7 - jq); asm volatile("" : "+v"(u1), "+v"(u0)); };
    auto lds_sh = [&](int b, int kk) {
#pragma unroll
      for (int e = 0; e < 8; ++e) { ca[b][e] = afrag(as[e >> 1] + kk * 64 + (e & 1) * 512); cu[b][e] = lds<bf16x8>(u1 + kk * 64 + 512 * e);
        if (e < 4) cu[b][8 + e] = lds<bf16x8>(u0 + kk * 64 + 512 * e); } };
    auto mm_sh = [&](int b) {
#pragma unroll
      for (int e = 0; e < 8; ++e) { s1 = MFMA(ca[b][e], cu[b][e], s1); if (e < 4) s0 = MFMA(ca[b][e], cu[b][8 + e], s0); } };
    auto mm_mn = [&](int b) {
#pragma unroll
      for (int e = 0; e < 8; ++e)
#pragma unroll
        for (int j = e; j < 8; ++j) cm[j] = MFMA(ca[b][e], cu[b][j - e], cm[j]); };
#define il(nr, per) do { for (int i_ = 0; i_ < nr; ++i_) { SG(0x100, per); SG(0x8, 1); } SG(0x8, 36); SB(); } while (0)
#define ilv() do { for (int i_ = 0; i_ < 5; ++i_) { SG(0x100, 1); SG(0x8, 1); SG(0x100, 1); SG(0x8, 1); SG(0x100, 1); SG(0x8, 1); SG(0x100, 1); SG(0x8, 1); SG(0x100, 2); SG(0x8, 2); SG(0x20, 1); SG(0x8, 1); } SG(0x8, 36); SB(); } while (0)
    lds_mn(1, 1); mm_mn(0); il(24, 1); STAMP(15);
    lds_mn(0, 2); mm_mn(1); il(24, 1); STAMP(16); addr_sh(); SB();
    lds_sh(1, 0); mm_mn(0); il(28, 1);
    lds_sh(0, 1); mm_sh(1); il(10, 3);
    lds_mn(1, 3); mm_sh(0); il(12, 2);
    if (wid != 0) *(LAS f32x4*)(sm + CV_PS + (wid - 1) * 1024 + lane * 16) = s0;
    if (wid != 1) *(LAS f32x4*)(sm + CV_PS + (3 + (wid ? wid - 1 : 0)) * 1024 + lane * 16) = s1;
    SB(); STAMP(17); WAITV(0); WAITL(0); BAR(); SB();
    STAMP(10);
    f32x4 ys = wf ? s1 : s0;
    if (wid < 2) {
#pragma unroll
      for (int w = 0; w < 3; ++w) ys += lds<f32x4>(sm + CV_PS + (3 * wid + w) * 1024 + lane * 16);
    }
    SB(); ldw(0, 5); ldw(1, 6); if (WD > 2) ldw(2, 7); mm_mn(1); SG(0x8, 4); for (int i_ = 0; i_ < 15; ++i_) { SG(0x8, 2); SG(0x20, 1); } SG(0x8, 36); SB();
    LAS char* pm = sm + (wt ? CV_PM23 : CV + 2 * CV_CH);
#pragma unroll
    for (int j = 0; j < 4; ++j) { f32x4 lo = cm[j], hi = cm[4 + j]; asm volatile("" : "+v"(lo), "+v"(hi));
      *(LAS f32x4*)(pm + wf * 4096 + j * 1024 + lane * 16) = wf ? lo : hi; cm[j] = wf ? hi : lo; }
    SB(); WAITL(0); BAR(); SB();
    STAMP(11);
    ldh(0, 0); ldh(1, 1); ldh(2, 2);
    auto yout = [&](f32x4 a, int chn, LAS char* ub, int j, float dv) {
      const u32x2 u = lds<u32x2>(ub + CV_U + (2044 - 256 * j - 16 * c - 4 * q) * 2);
      const f32x4 y = a + (f32x4){bhi(u.y), blo(u.y), bhi(u.x), blo(u.x)} * dv;
      st8<SC1>((u32x2){pk(y[0], y[1]), pk(y[2], y[3])}, rws, (16 * c + 4 * q) * 2, W_YT + chn * (S * 2) + j * 512); };
#pragma unroll
    for (int j = 0; j < 4; ++j) yout(cm[j] + lds<f32x4>(pm + (wf ^ 1) * 4096 + j * 1024 + lane * 16), 2 * id.b + wt, cb, 4 * wf + j, dvm);
    if (wid < 2) yout(ys, 512 + (id.b >> 2), cs_, wid ? 7 - jq : jq, dvs);
    SB();
  }
  STAMP(19);
  f32x4 o[4][4], ox[2];
#pragma unroll
  for (int i = 0; i < 4; ++i)
#pragma unroll
    for (int t = 0; t < 4; ++t) o[i][t] = (f32x4){0.f, 0.f, 0.f, 0.f};
  ox[0] = ox[1] = (f32x4){0.f, 0.f, 0.f, 0.f};
  const int dp = 144 * (g >> 1) + 80 * gp, ds = 144 * (g >> 1) + 64, trow = 64 * half + prow(wid, c);
  const unsigned xo = (id.tp * 128 + trow) * (D * 2);
  f32x4 rs[5];
  { LAS char* wl = sm + wid * G3W + lane * 16;
    bf16x8 wq[2][5];
#pragma unroll
    for (int t = 0; t < 5; ++t) wq[0][t] = lds<bf16x8>(wl + t * 1024);
#pragma unroll
    for (int i = 0; i < 18; ++i) {
      if (i < 4) {
#pragma unroll
        for (int t = 0; t < 5; ++t) wq[(i + 1) & 1][t] = lds<bf16x8>(wl + ((i + 1) * 5 + t) * 1024); }
#pragma unroll
      for (int r = 0; r < 4; ++r) { const bf16x8 hf = __builtin_bit_cast(bf16x8, hr[i % 3][r]);
#pragma unroll
        for (int t = 0; t < 5; ++t) if (t < 4 || r < 2) { const int tt = (r & 1) && t < 4 ? 3 - t : t;
          const bf16x8 wf_ = i < 5 ? wq[i & 1][tt] : __builtin_bit_cast(bf16x8, wr[(i - 5) % WD][tt]);
          if (tt < 4) o[r][tt] = MFMA(wf_, hf, o[r][tt]); else ox[r] = MFMA(wf_, hf, ox[r]); } }
      SB();
      if (i == 0 || i == 2 || i == 4 || i == 7 || i == 10 || i == 13 || i == 15) STAMP(20 + i);
      if (i + 3 < 18) ldh(i % 3, i + 3);
      if (i >= 5 && i + WD < 18) ldw((i - 5) % WD, i + WD);
      if (i == 2) { WAITV(12); if (lane == 0) __hip_atomic_fetch_add((LAS unsigned*)(sm + T_CNT), 1u, __ATOMIC_RELAXED, __HIP_MEMORY_SCOPE_WORKGROUP); }
      if (i == 16) {
        const u64 t0 = __builtin_amdgcn_s_memrealtime();
        while (*(volatile LAS unsigned*)(sm + T_YOK) == 0u && !expired(t0)) __builtin_amdgcn_s_sleep(1);
        FENCE();
        const float m = lds<float>(sm + T_M1 + trow * 4), r = lds<float>(sm + T_R1 + trow * 4);
        LAS char* yb = sm + YTILE + (c >> 2) * 128 + (32 * wt + 4 * wf + 8 * (c & 3)) * 2;
        auto res = [&](u32x2 xv, bf16x4 yv) { const u32x2 yu = __builtin_bit_cast(u32x2, yv);
          const f32x4 xf = {blo(xv.x), bhi(xv.x), blo(xv.y), bhi(xv.y)}, yf = {blo(yu.x), bhi(yu.x), blo(yu.y), bhi(yu.y)};
          return xf + (xf - m) * r * yf; };
        const int yp = 16 * gp, ysh = 64 * (gp ^ 1);
        LAS char* xb = sm + XTILE + prow(wid, c) * 160;
        const u32x4 x0 = lds<u32x4>(xb + yp * 2 + q * 16), x1 = lds<u32x4>(xb + yp * 2 + 64 + q * 16); const u32x2 x2 = lds<u32x2>(xb + ysh * 2 + q * 8);
        rs[0] = res(x0.xy, ldtr(yb + (yp + 8 * q) * 128)); rs[1] = res(x0.zw, ldtr(yb + (yp + 8 * q + 4) * 128));
        rs[2] = res(x1.xy, ldtr(yb + (yp + 32 + 8 * q) * 128)); rs[3] = res(x1.zw, ldtr(yb + (yp + 36 + 8 * q) * 128));
        rs[4] = res(x2, ldtr(yb + (ysh + 4 * q) * 128));
        asm volatile("" : "+v"(rs[0]), "+v"(rs[1]), "+v"(rs[2]), "+v"(rs[3]), "+v"(rs[4])); }
      SB();
    }
  }
  STAMP(12);
  const bool own0 = wt == gp;
  { LAS char* pw = sm + wid * G3W + lane * 16;
    auto put = [&](int r) { LAS char* p = pw + ((unsigned)r ^ (wf ^ (2 * gp))) * 5120;
#pragma unroll
      for (int t = 0; t < 4; ++t) *(LAS f32x4*)(p + t * 1024) = o[r][t];
      if (r < 2) *(LAS f32x4*)(p + 4096) = ox[r]; };
    put(1); put(3);
    if (own0) { put(2);
#pragma unroll
      for (int t = 0; t < 4; ++t) rs[t] += o[0][t];
      rs[4] += ox[0]; }
    else { put(0);
#pragma unroll
      for (int t = 0; t < 4; ++t) rs[t] += o[2][t]; }
  }
  SB(); WAITL(0); BAR(); SB();
  STAMP(13);
  { f32x4 pt[3][5];
#pragma unroll
    for (int t = 0; t < 5; ++t)
#pragma unroll
      for (int w = 0; w < 3; ++w) pt[w][t] = lds<f32x4>(sm + (wid ^ (w + 1)) * G3W + wid * 5120 + lane * 16 + t * 1024);
    const rsrc_t ro = mkrs(P.out, S * D * 2);
    auto fin = [&](int t) { const f32x4 v = (rs[t] + pt[0][t]) + (pt[1][t] + pt[2][t]); return (u32x2){pk(v[0], v[1]), pk(v[2], v[3])}; };
#pragma unroll
    for (int pp = 0; pp < 2; ++pp) { const u32x2 lo = fin(2 * pp), hi = fin(2 * pp + 1); st16<0>((u32x4){lo.x, lo.y, hi.x, hi.y}, ro, xo + q * 16, dp * 2 + 64 * pp); }
    const u32x2 ls = fin(4);
    if (own0) st8<0>(ls, ro, xo + q * 8, ds * 2);
  }
  STAMP(14);
}
}

extern "C" __global__ __launch_bounds__(512, 2) void h3_gmlp_fused(const Params P) {
  extern __shared__ __attribute__((aligned(16))) char smem_[];
  LAS char* sm = (LAS char*)smem_;
  const int wid = __builtin_amdgcn_readfirstlane(threadIdx.x >> 6);
  Ids id; id.b = blockIdx.x; id.lane = threadIdx.x & 63;
  const int xcd = id.b & 7, loc = id.b >> 3;
  id.tp = (xcd >> 1) * 4 + (loc >> 3); id.ft = (xcd & 1) * 8 + (loc & 7);
  unsigned stampv = 0; { const int lane = id.lane; STAMP(63); }
  if (wid == 0) {
    unsigned lo, hi, bytes, sink;
    asm volatile("s_getpc_b64 vcc\n1:\n\ts_sub_u32 %0, vcc_lo, 1b-h3_gmlp_fused\n\ts_subb_u32 %1, vcc_hi, 0\n\ts_mov_b32 %2, h3_gmlp_end-h3_gmlp_fused" : "=s"(lo), "=s"(hi), "=s"(bytes) :: "vcc", "scc");
    const rsrc_t rc = mkrs((const void*)(((u64)hi << 32) | lo), bytes);
    asm volatile("buffer_load_dword %0, %1, %2, 0 offen\n\ts_waitcnt vmcnt(0)" : "=&v"(sink) : "v"((id.lane < 16 ? id.lane * 32 + loc : 0x100000) * 128), "s"(rc) : "memory");
  }
  if (__builtin_expect(wid == 4 || wid == 5, 1)) role_L(P, sm, id, wid - 4 STAMP_PASS);
  else if (wid >= 6) role_X(P, sm, id, wid - 6 STAMP_PASS);
  else role_C(P, sm, id, wid STAMP_PASS);
  { const int lane = id.lane; STAMP(62); *(unsigned*)(P.ws + (40 << 20) + ((blockIdx.x * 8 + wid) * 64 + lane) * 4) = stampv; }
  if (wid == 5 && blockIdx.x == 0) { const unsigned t_a = __builtin_amdgcn_readlane(stampv, MK_AMP), t_s = __builtin_amdgcn_readlane(stampv, 63);
    const unsigned long long d_ = (unsigned long long)(t_a - t_s) * 10, t0_ = __builtin_amdgcn_s_memrealtime();
    while (__builtin_amdgcn_s_memrealtime() - t0_ < (d_ > 200000 ? 200000 : d_)) __builtin_amdgcn_s_sleep(2); }
}
extern "C" __global__ void h3_gmlp_end() {}

extern "C" void kernel_launch(void* const* d_in, const int* in_sizes, int n_in, void* d_out, int out_size, void* d_ws, size_t ws_size, hipStream_t stream) {
  Params p;
  p.x = (const u16*)d_in[0]; p.k = (const u16*)d_in[1]; p.v = (const u16*)d_in[2]; p.shk = (const u16*)d_in[3]; p.ssmk = (const u16*)d_in[4]; p.ssmd = (const u16*)d_in[5];
  p.win = (const u16*)d_in[7]; p.wsp = (const u16*)d_in[8]; p.bsp = (const u16*)d_in[9]; p.wout = (const u16*)d_in[10];
  p.out = (u16*)d_out; p.ws = (char*)d_ws;
  (void)hipFuncSetAttribute((const void*)h3_gmlp_fused, hipFuncAttributeMaxDynamicSharedMemorySize, LDS_BYTES);
  h3_gmlp_fused<<<256, 512, LDS_BYTES, stream>>>(p);
}
```

```cpp
#define PGW 4096
#define PGM 9
#include <hip/hip_runtime.h>
#include <hip/hip_bf16.h>
#include <type_traits>

namespace {
typedef unsigned short u16;
typedef unsigned long long u64;
typedef short bf16x8 __attribute__((ext_vector_type(8)));
typedef short bf16x4 __attribute__((ext_vector_type(4)));
typedef __bf16 bf2 __attribute__((ext_vector_type(2)));
typedef float f32x2 __attribute__((ext_vector_type(2)));
typedef float f32x4 __attribute__((ext_vector_type(4)));
typedef unsigned u32x2 __attribute__((ext_vector_type(2)));
typedef unsigned u32x4 __attribute__((ext_vector_type(4)));
typedef __amdgpu_buffer_rsrc_t rsrc_t;
#define LAS __attribute__((address_space(3)))
#define CAS __attribute__((address_space(4)))
#define DEV __device__ __forceinline__

constexpr int S = 2048, D = 576, F = 4608, FH = 2304;
constexpr int ASLOT = 16384, BSLOT = 18432, BBASE = 4 * ASLOT;
constexpr int STG = 139264, STG_W = 9216;
constexpr int T_M1 = 157952, T_R1 = T_M1 + 512, T_A0 = T_R1 + 512, T_A1 = T_A0 + 512, T_IR = T_A1 + 512;
constexpr int T_CS2 = T_IR + 512, T_CS1 = T_CS2;
constexpr int T_PART = T_CS1 + 640, T_N2 = T_PART, T_R2 = T_N2 + 512;
constexpr int T_TAG = T_PART + 2048, T_CNT = T_TAG + 16, T_GO = T_CNT + 4, T_YOK = T_CNT + 8;
constexpr int LDS_BYTES = 163840;
static_assert(T_CNT + 16 <= LDS_BYTES, "LDS map");
constexpr int G3W = 25600;
constexpr int CV = 102400, CV_CH = 13056, CV_O = 4160, CV_U = 8320;
constexpr int CV_PS = 143616, CV_PM23 = 149760;
constexpr int YTILE = 122880, XTILE = 133120;
constexpr int W_EPOCH = 0, W_F2 = 4096, W_F3 = 8192, W_F4 = 12288, W_F5 = 16384;
constexpr int W_G0X = 0x8000, W_G1F = 0x10000, W_G1S = 0x60000;
constexpr int W_KVT = 1 << 20, W_YT = 4 << 20, W_WOF = 7 << 20, W_ZF = 10 << 20, W_HF = 20 << 20, W_END = 30 << 20;
constexpr int SC1 = 16;
#define OPT_TWIN 0
#define OPT_TWSP 1
#define OPT_TG3 0
#define EXP 0
#define RH 0
#define RW 0
#define WD 3

#define BAR0() do { asm volatile("" ::: "memory"); __builtin_amdgcn_s_barrier(); asm volatile("" ::: "memory"); } while (0)
#define BAR() BAR0()
#define SB() __builtin_amdgcn_sched_barrier(0)
#define SG(m, n) __builtin_amdgcn_sched_group_barrier(m, n, 0)
#define WAITV(n) asm volatile("s_waitcnt vmcnt(%0)" ::"i"(n) : "memory")
#define WAITL(n) asm volatile("s_waitcnt lgkmcnt(%0)" ::"i"(n) : "memory")
#define FENCE() asm volatile("" ::: "memory")
#define MFMA(a, b, c) __builtin_amdgcn_mfma_f32_16x16x32_bf16(a, b, c, 0, 0, 0)
#define STAMP(i) do {} while (0)
#define LBAR() BAR()
#define STAMP_ARG
#define STAMP_PASS

struct Params {
  const u16 *x, *k, *v, *shk, *ssmk, *ssmd, *win, *wsp, *bsp, *wout;
  u16* out; char* ws;
};

DEV rsrc_t mkrs(const void* p, unsigned bytes) { return __builtin_amdgcn_make_buffer_rsrc((void*)p, 0, bytes, 0x00020000); }
template <int AUX> DEV void dma(rsrc_t rs, LAS char* l, unsigned voff, unsigned soff) { __builtin_amdgcn_raw_ptr_buffer_load_lds(rs, (LAS void*)l, 16, voff, soff, 0, AUX); }
DEV void dma_hidden(rsrc_t rs, LAS char* l, unsigned voff) { unsigned keep;
  asm volatile("s_mov_b32 %0, m0\n\ts_mov_b32 m0, %3\n\ts_nop 0\n\tbuffer_load_dwordx4 %1, %2, 0 offen lds\n\ts_mov_b32 m0, %0" : "=&s"(keep) : "v"(voff), "s"(rs), "s"((unsigned)(size_t)l) : "memory"); }
template <int AUX> DEV u32x4 ld16(rsrc_t rs, unsigned voff, unsigned soff) { return __builtin_amdgcn_raw_buffer_load_b128(rs, voff, soff, AUX); }
template <int AUX> DEV void st16(u32x4 v, rsrc_t rs, unsigned voff, unsigned soff) { __builtin_amdgcn_raw_buffer_store_b128(v, rs, voff, soff, AUX); }
template <int AUX> DEV void st8(u32x2 v, rsrc_t rs, unsigned voff, unsigned soff) { __builtin_amdgcn_raw_buffer_store_b64(v, rs, voff, soff, AUX); }
template <class T> DEV T lds(LAS char* p) { return *(LAS T*)p; }
DEV bf16x4 ldtr(LAS char* p) { return __builtin_amdgcn_ds_read_tr16_b64_v4i16((LAS bf16x4*)p); }
DEV bf16x8 cat(bf16x4 a, bf16x4 b) { return __builtin_shufflevector(a, b, 0, 1, 2, 3, 4, 5, 6, 7); }
DEV float blo(unsigned u) { return __uint_as_float(u << 16); }
DEV float bhi(unsigned u) { return __uint_as_float(u & 0xffff0000u); }
DEV unsigned pk(float a, float b) { return __builtin_bit_cast(unsigned, __builtin_convertvector((f32x2){a, b}, bf2)); }
DEV float dot2(unsigned a, unsigned b, float c) { return __builtin_amdgcn_fdot2_f32_bf16(__builtin_bit_cast(bf2, a), __builtin_bit_cast(bf2, b), c, false); }
template <int CTRL> DEV float dpp(float x) { return __builtin_bit_cast(float, __builtin_amdgcn_mov_dpp(__builtin_bit_cast(int, x), CTRL, 0xf, 0xf, true)); }
constexpr int XOR1 = 0xB1, XOR2 = 0x4E, XOR7 = 0x141, XOR8 = 0x128;
constexpr float GC0 = -2.30220819f, GC1 = -0.10294324f;
template <class T> DEV f32x2 gelu2(f32x2 a, T a0, T a1, T ir) {
  const f32x2 g = a * (a * a * a1 + a0);
  f32x2 e; e.x = __builtin_amdgcn_exp2f(g.x); e.y = __builtin_amdgcn_exp2f(g.y);
  e = e * ir + ir;
  f32x2 r; r.x = __builtin_amdgcn_rcpf(e.x); r.y = __builtin_amdgcn_rcpf(e.y);
  return a * r;
}
DEV f32x4 gelu4(f32x4 a, f32x4 a0, f32x4 a1, f32x4 ir) { const f32x2 p = gelu2(a.xy, a0.xy, a1.xy, ir.xy), q = gelu2(a.zw, a0.zw, a1.zw, ir.zw); return (f32x4){p.x, p.y, q.x, q.y}; }
DEV f32x4 gelu4(f32x4 a, float a0, float a1, float ir) { const f32x2 p = gelu2(a.xy, a0, a1, ir), q = gelu2(a.zw, a0, a1, ir); return (f32x4){p.x, p.y, q.x, q.y}; }
DEV u32x4 seal(float a, float b, u32x2 tag) { const unsigned ua = __float_as_uint(a), ub = __float_as_uint(b); return (u32x4){ua, ub, ua ^ tag.x, ub ^ tag.y}; }
DEV bool sealed(u32x4 g, u32x2 tag) { return (g.x ^ g.z) == tag.x && (g.y ^ g.w) == tag.y; }
DEV bool expired(u64 t0) { return __builtin_amdgcn_s_memrealtime() - t0 > 2000000ull; }
template <int NL, int NAP> DEV void wait_flags(rsrc_t ws, unsigned voff, u32x2 tag) {
  const u64 t0 = __builtin_amdgcn_s_memrealtime();
  for (;;) {
    bool ok = true;
#pragma unroll
    for (int i = 0; i < NL; ++i) { const u32x4 f = ld16<SC1>(ws, voff, i * 1024); ok &= f.x == tag.x && f.y == tag.y && f.z == tag.x && f.w == tag.y; }
    if (__all(ok) || expired(t0)) break;
    __builtin_amdgcn_s_sleep(NAP); FENCE();
  }
  FENCE();
}
DEV bool flags_ok(u32x4 f, u32x2 tag) { return f.x == tag.x && f.y == tag.y && f.z == tag.x && f.w == tag.y; }
DEV int prow(int T, int c) { return 32 * (T >> 1) + 8 * (c >> 2) + 4 * (T & 1) + (c & 3); }
DEV void set_flag(rsrc_t ws, unsigned off, u32x2 tag, int lane) { if (lane == 0) st8<SC1>(tag, ws, off, 0); }

struct Ids { int b, tp, ft, lane; };
DEV int krot(const Ids& id) { return (RH * (id.ft & 7) + ((RW * (id.tp & 3)) >> 1)) % 18; }
DEV int kwrap(int i, int r) { const int k = i + r; return k >= 18 ? k - 18 : k; }

DEV void role_L(const Params& P, LAS char* sm, const Ids id, const int lw STAMP_ARG) {
  int bcount = 0; (void)bcount;
  const int lane = id.lane, rr = lane >> 3, sl = lane & 7;
  const rsrc_t rx = mkrs(P.x, S * D * 2), rwin = mkrs(P.win, D * F * 2);
  const unsigned swz = (sl ^ (4 * lw + (rr >> 1))) * 16, prr = 16 * lw + 8 * (rr >> 2) + (rr & 3);
  const unsigned vx = prr * (D * 2) + swz;
  unsigned vb[9];
  auto issue1a = [&](int slot, int i, int ks) { dma<0>(rx, sm + slot * ASLOT + lw * 1024 + i * 2048, vx, (id.tp * 128 + 32 * (i >> 1) + 4 * (i & 1)) * (D * 2) + ks * 128); };
  auto issue1b = [&](int slot, int i, int ks, int col) { dma<0>(rwin, sm + BBASE + slot * BSLOT + lw * 1024 + i * 2048, vb[i], ks * 64 * (F * 2) + (col + id.ft * 144) * 2); };
#pragma unroll
  for (int i = 0; i < 9; ++i) {
    if (i < 8) issue1a(0, i, 0);
    SB();
    const unsigned ci = 64 * (2 * i + lw) + lane, rho = __umul24(ci, 3641) >> 16;
    vb[i] = __umul24(rho, F * 2 - 288) + 16 * ci + __mul24((int)(rho & 4) - (int)((rho >> 1) & 4), F * 2);
    issue1b(0, i, 0, FH);
    SB();
  }
  auto issueA = [&](int n) {
#pragma unroll
    for (int i = 0; i < 9; ++i) { if (i < 8) issue1a(n & 3, i, n); issue1b(n & 3, i, n, FH); } };
  auto issueB = [&](int n, bool with_x) {
#pragma unroll
    for (int i = 0; i < 9; ++i) { if (with_x && i < 8) issue1a((9 - n) & 3, i, 17 - n); issue1b(n & 3, i, 17 - n, 0); } };
  issueA(1); issueA(2);
  STAMP(60);
  const rsrc_t rwsp = mkrs(P.wsp, S * S * 2), rws = mkrs(P.ws, W_END);
  const unsigned vw = prr * (S * 2) + swz, vl = lane * 16 + lw * 1024;
  auto issue2a = [&](int n) { LAS char* a = sm + ((9 - n) & 3) * ASLOT + lw * 1024; const unsigned sa = id.tp * 128 * (S * 2) + (n - 18) * 128;
#pragma unroll
    for (int i = 0; i < 8; ++i) dma<0>(rwsp, a + i * 2048, vw, sa + (32 * (i >> 1) + 4 * (i & 1)) * (S * 2)); };
  auto issue2b = [&](int n) { LAS char* bb = sm + BBASE + (n & 3) * BSLOT + lw * 1024; const unsigned sb = W_ZF + (id.ft * 576 + (n - 18) * 18) * 1024;
#pragma unroll
    for (int i = 0; i < 9; ++i) dma<SC1>(rws, bb + i * 2048, vl, sb + i * 2048); };
#pragma unroll 1
  for (int n = 0; n < 6; ++n) { WAITV(34); LBAR(); issueA(n + 3); }
  WAITV(34); LBAR(); issueB(9, false);
  WAITV(26); LBAR(); issueB(10, false);
  WAITV(18); LBAR(); issueB(11, false);
  BAR();
  WAITV(18); LBAR(); issueB(12, false);
  WAITV(18); LBAR(); issueB(13, true);
  WAITV(26); LBAR();
#pragma unroll 1
  for (int n = 12; n < 16; ++n) { WAITV(17); LBAR(); issueB(n + 2, true); }
  WAITV(17); LBAR(); issue2a(18);
  WAITV(8); LBAR(); issue2a(19);
  BAR(); issue2a(20);
  { const u32x2 tag = lds<u32x2>(sm + T_TAG); wait_flags<1, 2>(rws, W_F2 + id.ft * 128 + (lane & 7) * 16, tag); }
  STAMP(61);
  issue2b(18); issue2b(19); issue2b(20);
  asm volatile("ds_write_b32 %0, %1" :: "v"((unsigned)(size_t)(sm + T_GO)), "v"(1u) : "memory");
  WAITV(18); LBAR(); issue2a(21); issue2b(21);
  WAITV(26); LBAR(); issue2a(22); issue2b(22);
#pragma unroll 1
  for (int n = 20; n < 47; ++n) { WAITV(34); LBAR(); issue2a(n + 3); issue2b(n + 3); }
  const int g = id.ft & 7;
  const unsigned wb0 = W_WOF + (g >> 1) * 72 * 9 * 1024, wb1 = wb0 + (g & 1) * 5 * 1024, vq = lane * 16;
  const int rot = krot(id);
  auto issue3 = [&](auto lo_, auto hi_) { constexpr int lo = decltype(lo_)::value, hi = decltype(hi_)::value;
#pragma unroll
    for (int p0 = lo; p0 < hi; p0 += 2) {
      auto off = [&](int pc) { return (pc % 5 == 4 ? wb0 : wb1) + (18 * (pc / 25) * 9 + pc % 5) * 1024 + kwrap((pc % 25) / 5, rot) * 9216; };
      dma<SC1>(rws, sm + p0 * 1024 + lw * 1024, vq, lw ? off(p0 + 1) : off(p0)); } };
#define IC(n) std::integral_constant<int, n>{}
  WAITV(34); LBAR(); issue3(IC(48), IC(64));
  WAITV(25); LBAR();
  WAITV(8); LBAR(); issue3(IC(16), IC(48));
  LBAR();
  LBAR(); issue3(IC(0), IC(16)); issue3(IC(64), IC(100));
  unsigned sink = 0;
  auto wtouch = [&]() {
    constexpr int K0 = 5 + WD, NK = 18 - K0;
#pragma unroll
    for (int i = 0; i < (NK * 40 + 127) / 128; ++i) { const unsigned n = (i * 2 + lw) * 64 + lane, blk = n >> 1, kw = __umul24(blk, 13108) >> 16, t = blk - kw * 5;
      const unsigned o = n < NK * 40 ? (t == 4 ? wb0 : wb1) + ((18 * (kw & 3) + K0 + (kw >> 2)) * 9 + t) * 1024 + ((id.tp & 3) * 2 + (n & 1)) * 128 : 0xF0000000u;
      asm volatile("buffer_load_dword %0, %1, %2, 0 offen sc1" : "+v"(sink) : "v"(o), "s"(rws) : "memory"); } };
  if ((OPT_TG3 & 3) == 1) wtouch();
  LBAR();
  if ((OPT_TG3 & 3) == 2) { __builtin_amdgcn_s_sleep(2); wtouch(); }
  WAITV((OPT_TG3 & 3) == 2 ? (( 18 - 5 - WD) * 40 + 127) / 128 : 0); LBAR();
  asm volatile("s_waitcnt vmcnt(0)" : "+v"(sink) :: "memory"); LBAR();
}

DEV void role_X(const Params& P, LAS char* sm, const Ids id, const int xw STAMP_ARG) {
  int bcount = 0; (void)bcount;
  const int lane = id.lane;
  const rsrc_t rws = mkrs(P.ws, W_END);
  unsigned sink = 0;
  const u32x2 ev = __builtin_amdgcn_raw_buffer_load_b64(rws, W_EPOCH, 0, SC1);
  const int srow = id.tp * 128 + id.ft * 8 + 4 * xw + (lane >> 4);
  u32x4 xv[5];
  { const rsrc_t rx = mkrs(P.x, S * D * 2);
#pragma unroll
    for (int i = 0; i < 5; ++i) xv[i] = ld16<0>(rx, i < 4 || (lane & 8) == 0 ? srow * (D * 2) + (lane & 15) * 16 : 0xF0000000u, i * 256); }
  const u64 epoch = ((u64)__builtin_amdgcn_readfirstlane(ev.y) << 32) | __builtin_amdgcn_readfirstlane(ev.x);
  u32x2 tag; { const u64 t = ((epoch + 1) * 0x9E3779B97F4A7C15ull) | 1ull; tag.x = (unsigned)t; tag.y = (unsigned)(t >> 32); }
  { float s = 0.f, q = 0.f; unsigned ones = 0x3f803f80u; asm volatile("" : "+v"(ones));
#pragma unroll
    for (int i = 0; i < 5; ++i)
#pragma unroll
      for (int e = 0; e < 4; ++e) { s = dot2(xv[i][e], ones, s); q = dot2(xv[i][e], xv[i][e], q); }
    s += dpp<XOR1>(s); q += dpp<XOR1>(q); s += dpp<XOR2>(s); q += dpp<XOR2>(q); s += dpp<XOR7>(s); q += dpp<XOR7>(q); s += dpp<XOR8>(s); q += dpp<XOR8>(q);
    const float m = s * (1.0f / D), var = fmaxf(q * (1.0f / D) - m * m, 0.f) + 1e-5f;
    if ((lane & 15) == 0) st16<SC1>(seal(m, var, tag), rws, W_G0X + srow * 16, 0);
    if (xw == 0 && lane == 0) { *(LAS u32x2*)(sm + T_TAG) = tag; *(LAS u32x4*)(sm + T_CNT) = (u32x4){0u, 0u, 0u, 0u}; }
  }
  STAMP(0);
  BAR(); BAR(); BAR(); BAR();
  LAS char* stg = sm + STG + xw * STG_W;
  const int unit = xw ? 256 + id.b : id.b, wks = unit >> 2, wu = unit & 3; const bool won = !xw || id.b < 32;
  const int tile = xw ? id.b : 256 + id.b - 32; const bool kon = xw || (id.b >= 32 && id.b < 64);
  const int tb = tile / 9, db = tile - tb * 9, cc = lane & 7, rg = lane >> 3;
  u32x4 kk[11], vv[8], sh[4];
  { const int row = 64 * xw + lane; const u64 t0 = __builtin_amdgcn_s_memrealtime();
    u32x4 gx = ld16<SC1>(rws, W_G0X + (id.tp * 128 + row) * 16, 0);
    BAR(); BAR();
    while (!__all(sealed(gx, tag)) && !expired(t0)) { __builtin_amdgcn_s_sleep(4); FENCE(); gx = ld16<SC1>(rws, W_G0X + (id.tp * 128 + row) * 16, 0); }
    const float var = __uint_as_float(gx.y), r = __builtin_amdgcn_rsqf(var);
    *(LAS unsigned*)(sm + T_M1 + row * 4) = gx.x; *(LAS float*)(sm + T_R1 + row * 4) = r;
    *(LAS float*)(sm + T_A0 + row * 4) = GC0 * r; *(LAS float*)(sm + T_A1 + row * 4) = GC1 * r * r * r; *(LAS float*)(sm + T_IR + row * 4) = var * r;
    WAITL(0); BAR();
  }
  STAMP(1);
  { const rsrc_t rk = mkrs(P.k, S * D * 2), rv = mkrs(P.v, S * D * 2), rs = mkrs(P.shk, D * 4 * 2), rwo = mkrs(P.wout, FH * D * 2);
    const int t0 = 64 * tb + 8 * rg; const unsigned col = (64 * db + 8 * cc) * 2;
    if (kon) {
#pragma unroll
      for (int i = 0; i < 11; ++i) kk[i] = ld16<0>(rk, t0 + i - 3 >= 0 ? (unsigned)(t0 + i - 3) * (D * 2) + col : 0xF0000000u, 0);
    }
    BAR();
    if (kon) {
#pragma unroll
      for (int i = 0; i < 8; ++i) vv[i] = ld16<0>(rv, t0 * (D * 2) + col, i * (D * 2));
#pragma unroll
      for (int i = 0; i < 4; ++i) sh[i] = ld16<0>(rs, col * 4, i * 16);
    }
    BAR();
    if (won) { const int fb0 = wks < 64 ? 144 * (wks >> 2) + 32 * (wks & 3) : 288 * (wks - 64) + 128, fst = wks < 64 ? 0 : 128;
#pragma unroll
      for (int i = 0; i < 9; ++i) { const int ci = 64 * i + lane, rho = __umul24(ci, 3641) >> 16, ch = ci - rho * 18;
        dma_hidden(rwo, stg + i * 1024, (fb0 + rho + (rho >> 4) * fst) * (D * 2) + wu * 288 + ch * 16); } }
    BAR();
    if (kon) {
#pragma unroll
      for (int i = 0; i < 4; ++i) asm volatile("" : "+v"(sh[i]), "+v"(vv[i]), "+v"(vv[4 + i]), "+v"(kk[i]), "+v"(kk[4 + i]), "+v"(kk[i == 3 ? 0 : 8 + i])); }
    if (PGW) {
      auto warm = [&](rsrc_t r, unsigned base, unsigned bytes, int who) { if (who != xw) return;
        for (unsigned p0 = 0; p0 * PGW < bytes; p0 += 2048) { const unsigned o = (p0 + lane * 32 + (id.b >> 3)) * PGW;
          asm volatile("buffer_load_dword %0, %1, %2, 0 offen sc1" : "+v"(sink) : "v"(o < bytes ? base + o : 0xF0000000u), "s"(r) : "memory"); } };
      if (PGM & 1) warm(rws, W_ZF + (id.ft & 8) * 576 * 1024, 8 * 576 * 1024, 0);
      if (PGM & 2) warm(rws, W_HF + (id.tp & 12) * 576 * 1024, 4 * 576 * 1024, 1);
      if (PGM & 4) { warm(rws, W_KVT, W_ZF - W_KVT - (1 << 19), 1); warm(mkrs(P.out, S * D * 2), 0, S * D * 2, 0); warm(mkrs(P.ssmk, S * D * 2), 0, S * D * 2, 1); }
      if (PGM & 8) warm(mkrs(P.wsp, S * S * 2), (id.tp & 12) * 128 * S * 2, 512 * S * 2, 0);
    }
    if (OPT_TWSP) { const rsrc_t r = mkrs(P.wsp, S * S * 2);
#pragma unroll
      for (int i = 0; i < 2; ++i) asm volatile("buffer_load_dword %0, %1, %2, 0 offen" : "+v"(sink) : "v"(id.b * 32768 + ((2 * i + xw) * 64 + lane) * 128), "s"(r) : "memory"); }
  }
  BAR();
  u32x4 ko[8];
  auto kvstep = [&](int e) {
    if (kon) {
      float c[4], kf[11], y[8];
#pragma unroll
      for (int j = 0; j < 4; ++j) { const unsigned w = sh[e >> 1][(e & 1) * 2 + (j >> 1)]; c[j] = (j & 1) ? bhi(w) : blo(w); }
#pragma unroll
      for (int i = 0; i < 11; ++i) { const unsigned w = kk[i][e >> 1]; kf[i] = (e & 1) ? bhi(w) : blo(w); }
#pragma unroll
      for (int i = 0; i < 8; ++i) { const unsigned w = vv[i][e >> 1];
        float a = c[3] * kf[i]; asm volatile("" : "+v"(a)); a = fmaf(c[2], kf[i + 1], a); asm volatile("" : "+v"(a)); a = fmaf(c[1], kf[i + 2], a); asm volatile("" : "+v"(a)); a = fmaf(c[0], kf[i + 3], a);
        asm volatile("" : "+v"(a)); y[i] = a * ((e & 1) ? bhi(w) : blo(w)); asm volatile("" : "+v"(y[i])); }
      ko[e] = (u32x4){pk(y[7], y[6]), pk(y[5], y[4]), pk(y[3], y[2]), pk(y[1], y[0])};
      asm volatile("" : "+v"(ko[e]));
    } };
  { const int row = 64 * xw + lane;
    const f32x2 a = lds<f32x2>(sm + T_PART + row * 8) + lds<f32x2>(sm + T_PART + 1024 + row * 8);
    st16<SC1>(seal(a.x, a.y, tag), rws, W_G1S + ((id.tp * 16 + id.ft) * 128 + row) * 16, 0);
    kvstep(0); BAR(); kvstep(1); BAR();
    u32x4 gs[16];
    auto gather = [&]() {
#pragma unroll
      for (int i = 0; i < 16; ++i) gs[i] = ld16<SC1>(rws, W_G1S + (id.tp * 16 * 128 + row) * 16, i * 2048); };
    unsigned bad = 0; float s = 0.f, q = 0.f;
    auto sum = [&](int lo, int hi) {
#pragma unroll
      for (int i = lo; i < hi; ++i) { bad |= (gs[i].x ^ gs[i].z ^ tag.x) | (gs[i].y ^ gs[i].w ^ tag.y); s += __uint_as_float(gs[i].x); q += __uint_as_float(gs[i].y); }
      asm volatile("" : "+v"(bad), "+v"(s), "+v"(q)); };
    gather(); kvstep(2); BAR(); kvstep(3); BAR(); sum(0, 10); BAR();
    sum(10, 16);
    if (__builtin_expect(!__all(bad == 0), 0)) { const u64 t0 = __builtin_amdgcn_s_memrealtime();
      do { FENCE(); gather(); bad = 0; s = 0.f; q = 0.f; sum(0, 16); } while (!__all(bad == 0) && !expired(t0)); }
    const float m = s * (1.0f / FH), var = fmaxf(q * (1.0f / FH) - m * m, 0.f), r = __builtin_amdgcn_rsqf(var + 1e-5f);
    *(LAS float*)(sm + T_R2 + row * 4) = r; *(LAS float*)(sm + T_N2 + row * 4) = -m * r;
    asm volatile("s_waitcnt vmcnt(0) lgkmcnt(0)" : "+v"(sink) :: "memory"); BAR();
  }
  STAMP(2);
  kvstep(4); BAR(); kvstep(5); BAR();
  if (xw == 0) set_flag(rws, W_F2 + (id.ft * 16 + id.tp) * 8, tag, lane);
  kvstep(6); kvstep(7); BAR();
  { u32x4 wf[9];
    if (won) {
      const int q = lane >> 4, qq = (lane & 15) >> 2, p = lane & 3;
      LAS char* base = stg + (4 * q + qq) * 288;
#pragma unroll
      for (int T = 0; T < 9; ++T) { const int cb = T < 4 ? 32 * (T >> 1) + 4 * (T & 1) : T == 4 ? 64 : 80 + 32 * ((T - 5) >> 1) + 4 * ((T - 5) & 1);
        LAS char* a = base + cb * 2 + p * (T == 4 ? 8 : 16);
        wf[T] = __builtin_bit_cast(u32x4, cat(ldtr(a), ldtr(a + 16 * 288))); }
      WAITL(0);
    }
    if (kon) {
#pragma unroll
      for (int e = 0; e < 8; ++e) *(LAS u32x4*)(stg + (8 * cc + e) * 128 + (((7 - rg) ^ cc) * 16)) = ko[e];
#pragma unroll
      for (int i = 0; i < 8; ++i) ko[i] = lds<u32x4>(stg + (8 * i + rg) * 128 + ((cc ^ i) * 16));
    }
    const u64 t0 = __builtin_amdgcn_s_memrealtime();
    while (*(volatile LAS unsigned*)(sm + T_GO) == 0u && !expired(t0)) __builtin_amdgcn_s_sleep(2);
    FENCE();
    if (won) {
#pragma unroll
      for (int T = 0; T < 9; ++T) st16<SC1>(wf[T], rws, W_WOF + lane * 16, ((wu * 72 + wks) * 9 + T) * 1024); }
    if (kon) {
#pragma unroll
      for (int i = 0; i < 8; ++i) st16<SC1>(ko[i], rws, W_KVT + (64 * db + rg) * (S * 2) + (S - 64 * (tb + 1)) * 2 + cc * 16, 8 * i * (S * 2)); }
  }
  BAR(); BAR(); BAR();
  const unsigned h0a = ((const CAS unsigned*)P.ssmk)[id.b * S], h0b = ((const CAS unsigned*)P.ssmk)[(xw ? 2 * id.b + 1 : 512 + (id.b >> 2)) * (S / 2)];
  BAR(); WAITV(0); BAR();
  if (xw == 0) set_flag(rws, W_F5 + id.b * 8, tag, lane);
  STAMP(3);
#pragma unroll 1
  for (int i = 0; i < 22; ++i) BAR();
  { u32x4 f0 = {tag.x, tag.y, tag.x, tag.y}, f1 = f0;
    if (xw == 0) { f0 = ld16<SC1>(rws, W_F5 + lane * 16, 0); f1 = ld16<SC1>(rws, W_F5 + lane * 16, 1024); }
    BAR(); BAR();
    if (!__all(flags_ok(f0, tag) && flags_ok(f1, tag))) wait_flags<2, 8>(rws, W_F5 + lane * 16, tag); }
  BAR();
  STAMP(4);
  { const rsrc_t rh = mkrs(P.ssmk, D * S * 2);
    auto fetch = [&](int ci) { const int chn = ci == 2 ? 512 + (id.b >> 2) : 2 * id.b + ci; LAS char* cb = sm + CV + ci * CV_CH;
      const unsigned h0 = ci == 0 ? h0a : h0b;
      if (lane < 8) { *(LAS unsigned*)(cb + lane * 4) = 0u; *(LAS unsigned*)(cb + CV_O + lane * 4) = lane == 7 ? h0 << 16 : 0u; }
      if (lane < 38) *(LAS u32x4*)(cb + CV_U + 4096 + lane * 16) = (u32x4){0u, 0u, 0u, 0u};
      if (ci == 2) { *(LAS u32x4*)(cb + CV_U + 4704 + lane * 16) = (u32x4){0u, 0u, 0u, 0u}; *(LAS u32x4*)(cb + CV_U + 5728 + lane * 16) = (u32x4){0u, 0u, 0u, 0u}; }
      FENCE();
#pragma unroll
      for (int i = 0; i < 4; ++i) { dma<0>(rh, cb + 32 + i * 1024, lane * 16, chn * (S * 2) + i * 1024);
        dma<0>(rh, cb + CV_O + 32 + i * 1024, lane * 16 + 2, chn * (S * 2) + i * 1024);
        dma<SC1>(rws, cb + CV_U + i * 1024, lane * 16, W_KVT + chn * (S * 2) + i * 1024); } };
    if (xw == 0) fetch(0);
    BAR();
    fetch(2 - xw);
    BAR();
    WAITV(0); WAITL(0); BAR(); BAR();
  }
  BAR();
  if (xw == 0) { set_flag(rws, W_F3 + (id.tp * 16 + id.ft) * 8, tag, lane); wait_flags<1, 1>(rws, W_F3 + id.tp * 128 + (lane & 7) * 16, tag); }
  STAMP(5);
  BAR();
  if (xw == 1 && (OPT_TG3 & 4)) {
#pragma unroll
    for (int i = 0; i < 4; ++i) { const unsigned n = i * 64 + lane, o = n < 240 ? W_HF + ((id.tp * 8 + 4 * (id.ft >> 3) + (n & 3)) * 72 + 18 * ((n >> 2) & 3) + 3 + (n >> 4)) * 1024 + (id.ft & 7) * 128 : 0xF0000000u;
      asm volatile("buffer_load_dword %0, %1, %2, 0 offen sc1" : "+v"(sink) : "v"(o), "s"(rws) : "memory"); } }
  if (xw == 0) {
    const u64 t0 = __builtin_amdgcn_s_memrealtime();
    while (*(volatile LAS unsigned*)(sm + T_CNT) != 4u && !expired(t0)) __builtin_amdgcn_s_sleep(2);
    FENCE();
    set_flag(rws, W_F4 + id.b * 8, tag, lane);
    const int g = id.ft & 7, d0 = 144 * (g >> 1) + 64 * (g & 1);
    { const rsrc_t rx = mkrs(P.x, S * D * 2);
#pragma unroll
      for (int i = 0; i < 10; ++i) { const unsigned ci = 64 * i + lane, r = __umul24(ci, 6554) >> 16; dma<0>(rx, sm + XTILE + i * 1024, (id.tp * 128 + 64 * (id.ft >> 3) + r) * (D * 2) + d0 * 2 + (ci - 10 * r) * 16, 0); } }
    __builtin_amdgcn_s_sleep(24); wait_flags<2, 12>(rws, W_F4 + lane * 16, tag);
#pragma unroll
    for (int i = 0; i < 10; ++i) dma<SC1>(rws, sm + YTILE + i * 1024, W_YT + (d0 + (lane >> 3)) * (S * 2) + (id.tp * 128 + 64 * (id.ft >> 3)) * 2 + (lane & 7) * 16, i * 8 * (S * 2));
    WAITV(0);
    asm volatile("ds_write_b32 %0, %1" :: "v"((unsigned)(size_t)(sm + T_YOK)), "v"(1u) : "memory");
    if (id.b == 0 && lane == 0) st8<SC1>((u32x2){(unsigned)(epoch + 1), (unsigned)((epoch + 1) >> 32)}, rws, W_EPOCH, 0);
  }
  STAMP(6);
  asm volatile("s_waitcnt vmcnt(0)" : "+v"(sink) :: "memory"); BAR();
}

DEV void role_C(const Params& P, LAS char* sm, const Ids id, const int wid STAMP_ARG) {
  int bcount = 0; (void)bcount;
  const int lane = id.lane, wt = wid >> 1, wf = wid & 1, c = lane & 15, q = lane >> 4;
  const rsrc_t rws = mkrs(P.ws, W_END);
  __builtin_amdgcn_s_setprio(1);
  unsigned dm = ((const CAS unsigned*)P.ssmd)[id.b], dsh = ((const CAS unsigned*)P.ssmd)[256 + (id.b >> 3)];
  asm volatile("" : "+s"(dm), "+s"(dsh));
  f32x4 acc[4][4], accx[2], csa[3];
  bf16x8 tf[2][4], ff[2][5], ones;
  LAS char *ta[2][2], *fb[2][2], *fx[2];
#pragma unroll
  for (int s = 0; s < 2; ++s) {
    const int o = (32 * (s ^ wt) + 16 * (q >> 1) + 4 * (q & 1) + (c >> 2)) * 288 + 8 * (c & 3);
    fx[s] = sm + BBASE + o + 256;
#pragma unroll
    for (int h = 0; h < 2; ++h) { ta[s][h] = sm + (64 * wt + 32 * (h ^ wf) + c) * 128 + (((q + 4 * (s ^ wt)) ^ ((c >> 1) & 7)) * 16); fb[s][h] = sm + BBASE + o + wf * 128 + (h ^ wt) * 64; }
  }
#pragma unroll
  for (int e = 0; e < 8; ++e) ones[e] = 0x3f80;
  asm volatile("" : "+v"(ones));
  auto rd = [&](auto mode, int b, int as, int bs, int s) {
    constexpr int MODE = decltype(mode)::value;
#pragma unroll
    for (int j = 0; j < 4; ++j) { LAS char* p = fb[s][j >> 1] + bs * BSLOT;
      ff[b][j] = MODE < 2 ? cat(ldtr(p + (j & 1) * 32), ldtr(p + (j & 1) * 32 + 8 * 288)) : lds<bf16x8>(p + (j & 1) * 1024); }
#pragma unroll
    for (int i = 0; i < 4; ++i) tf[b][i] = lds<bf16x8>(ta[s][i >> 1] + as * ASLOT + (i & 1) * 2048);
    ff[b][4] = MODE < 2 ? cat(ldtr(fx[s] + bs * BSLOT), ldtr(fx[s] + bs * BSLOT + 8 * 288)) : lds<bf16x8>(fx[s] + bs * BSLOT);
  };
  auto mm = [&](auto mode, int b, int lo, int hi) {
    constexpr int MODE = decltype(mode)::value;
#pragma unroll
    for (int n = 0; n < (MODE == 2 ? 18 : 21 - b); ++n) if (n >= lo && n < hi) {
      if (n < 16) { const int i = n >> 2, j = (i & 1) ? 3 - (n & 3) : (n & 3);
        acc[i][j] = MODE == 0 ? MFMA(tf[b][i], ff[b][j], acc[i][j]) : MFMA(ff[b][j], tf[b][i], acc[i][j]); }
      else if (n < 18) accx[n - 16] = MODE == 0 ? MFMA(tf[b][n - 16], ff[b][4], accx[n - 16]) : MFMA(ff[b][4], tf[b][n - 16], accx[n - 16]);
      else { const int j = n == 20 ? 4 : n - 18; csa[n - 18] = MODE == 0 ? MFMA(ones, ff[b][j], csa[n - 18]) : MFMA(ff[b][j], ones, csa[n - 18]); }
    }
  };
  auto stage = [&](auto mode, int as, int bs, bool drain) {
    constexpr int MODE = decltype(mode)::value;
    mm(mode, 1, 0, 4); SB();
    if (drain) WAITV(0);
    WAITL(0); BAR(); SB();
    rd(mode, 0, as, bs, 0); mm(mode, 1, 4, 21);
    if (MODE < 2) { for (int i = 0; i < 14; ++i) { SG(0x100, 1); SG(0x8, 1); } SG(0x8, 2); }
    else { for (int i = 0; i < 4; ++i) { SG(0x100, 1); SG(0x8, 1); SG(0x100, 1); SG(0x8, 2); } SG(0x100, 1); SG(0x8, 2); }
    SB();
    rd(mode, 1, as, bs, 1); mm(mode, 0, 0, 21);
    if (MODE < 2) { for (int i = 0; i < 7; ++i) { SG(0x100, 1); SG(0x8, 1); SG(0x100, 1); SG(0x8, 2); } }
    else { for (int i = 0; i < 9; ++i) { SG(0x100, 1); SG(0x8, 2); } }
    SB();
  };
  auto first = [&](auto mode, int as, int bs) { WAITL(0); BAR(); SB(); rd(mode, 0, as, bs, 0); rd(mode, 1, as, bs, 1); mm(mode, 0, 0, 21); SB(); };
  auto zero = [&]() {
#pragma unroll
    for (int i = 0; i < 4; ++i)
#pragma unroll
      for (int j = 0; j < 4; ++j) acc[i][j] = (f32x4){0.f, 0.f, 0.f, 0.f};
    accx[0] = accx[1] = csa[0] = csa[1] = csa[2] = (f32x4){0.f, 0.f, 0.f, 0.f}; };
  const std::integral_constant<int, 0> M0; const std::integral_constant<int, 1> M1; const std::integral_constant<int, 2> M2;
  const int jg0 = 4 * wf + 2 * wt, jg2 = 4 * wf + 2 - 2 * wt;

  zero();
  first(M0, 0, 0);
  STAMP(0);
#pragma unroll 1
  for (int it = 0; it < 2; ++it) { stage(M0, 1, 1, false); stage(M0, 2, 2, false); stage(M0, 3, 3, false); stage(M0, 0, 0, false); }
  mm(M0, 1, 18, 21); mm(M0, 1, 0, 8); SB();
  if (q == 0) { *(LAS float*)(sm + T_CS2 + (jg0 * 16 + c) * 4) = csa[0][0]; *(LAS float*)(sm + T_CS2 + (jg0 * 16 + 16 + c) * 4) = csa[1][0]; *(LAS float*)(sm + T_CS2 + (128 + 16 * wt + c) * 4) = csa[2][0]; }
  mm(M0, 1, 8, 18); SB();
  WAITL(0); BAR();
  STAMP(1);
  u32x4 z2p[2][4], z2x;
  { float cs[5];
    cs[0] = csa[0][0]; cs[1] = csa[1][0]; cs[2] = lds<float>(sm + T_CS2 + (jg2 * 16 + c) * 4); cs[3] = lds<float>(sm + T_CS2 + (jg2 * 16 + 16 + c) * 4);
    cs[4] = csa[2][0] + lds<float>(sm + T_CS2 + (144 - 16 * wt + c) * 4);
    float red[2][16];
#pragma unroll
    for (int i = 0; i < 4; ++i) {
      const int ro = (64 * wt + 32 * ((i >> 1) ^ wf) + 8 * q + 4 * (i & 1)) * 4;
      const f32x4 m = lds<f32x4>(sm + T_M1 + ro), a0 = lds<f32x4>(sm + T_A0 + ro), a1 = lds<f32x4>(sm + T_A1 + ro), ir = lds<f32x4>(sm + T_IR + ro);
      f32x4 su = {0.f, 0.f, 0.f, 0.f}, sq = {0.f, 0.f, 0.f, 0.f};
#pragma unroll
      for (int j = 0; j < (i < 2 ? 5 : 4); ++j) {
        const f32x4 z = gelu4((j < 4 ? acc[i][j] : accx[i]) - m * cs[j], a0, a1, ir);
        su = j ? su + z : z; sq = j ? sq + z * z : z * z;
        u32x4& dst = j < 4 ? z2p[i >> 1][j] : z2x;
        dst[2 * (i & 1)] = pk(z[0], z[1]); dst[2 * (i & 1) + 1] = pk(z[2], z[3]);
      }
#pragma unroll
      for (int e = 0; e < 4; ++e) { red[0][4 * i + e] = su[e]; red[1][4 * i + e] = sq[e]; }
    }
    float r8[2][8], r4[2][4];
#pragma unroll
    for (int st = 0; st < 2; ++st) {
#pragma unroll
      for (int e = 0; e < 8; e += 4)
        asm volatile("s_nop 1\n\t"
                     "v_add_f32_dpp %0, %4, %4 row_ror:8 row_mask:0xf bank_mask:0x3\n\tv_add_f32_dpp %1, %5, %5 row_ror:8 row_mask:0xf bank_mask:0x3\n\t"
                     "v_add_f32_dpp %2, %6, %6 row_ror:8 row_mask:0xf bank_mask:0x3\n\tv_add_f32_dpp %3, %7, %7 row_ror:8 row_mask:0xf bank_mask:0x3\n\t"
                     "v_add_f32_dpp %0, %8, %8 row_ror:8 row_mask:0xf bank_mask:0xc\n\tv_add_f32_dpp %1, %9, %9 row_ror:8 row_mask:0xf bank_mask:0xc\n\t"
                     "v_add_f32_dpp %2, %10, %10 row_ror:8 row_mask:0xf bank_mask:0xc\n\tv_add_f32_dpp %3, %11, %11 row_ror:8 row_mask:0xf bank_mask:0xc"
                     : "=&v"(r8[st][e]), "=&v"(r8[st][e + 1]), "=&v"(r8[st][e + 2]), "=&v"(r8[st][e + 3])
                     : "v"(red[st][e]), "v"(red[st][e + 1]), "v"(red[st][e + 2]), "v"(red[st][e + 3]), "v"(red[st][8 + e]), "v"(red[st][9 + e]), "v"(red[st][10 + e]), "v"(red[st][11 + e]));
      asm volatile("s_nop 1\n\t"
                   "v_add_f32_dpp %0, %4, %4 row_half_mirror row_mask:0xf bank_mask:0x5\n\tv_add_f32_dpp %1, %5, %5 row_half_mirror row_mask:0xf bank_mask:0x5\n\t"
                   "v_add_f32_dpp %2, %6, %6 row_half_mirror row_mask:0xf bank_mask:0x5\n\tv_add_f32_dpp %3, %7, %7 row_half_mirror row_mask:0xf bank_mask:0x5\n\t"
                   "v_add_f32_dpp %0, %8, %8 row_half_mirror row_mask:0xf bank_mask:0xa\n\tv_add_f32_dpp %1, %9, %9 row_half_mirror row_mask:0xf bank_mask:0xa\n\t"
                   "v_add_f32_dpp %2, %10, %10 row_half_mirror row_mask:0xf bank_mask:0xa\n\tv_add_f32_dpp %3, %11, %11 row_half_mirror row_mask:0xf bank_mask:0xa\n\t"
                   "v_add_f32_dpp %0, %0, %0 quad_perm:[1,0,3,2] row_mask:0xf bank_mask:0xf\n\tv_add_f32_dpp %1, %1, %1 quad_perm:[1,0,3,2] row_mask:0xf bank_mask:0xf\n\t"
                   "v_add_f32_dpp %2, %2, %2 quad_perm:[1,0,3,2] row_mask:0xf bank_mask:0xf\n\tv_add_f32_dpp %3, %3, %3 quad_perm:[1,0,3,2] row_mask:0xf bank_mask:0xf\n\t"
                   "v_add_f32_dpp %0, %0, %0 quad_perm:[2,3,0,1] row_mask:0xf bank_mask:0xf\n\tv_add_f32_dpp %1, %1, %1 quad_perm:[2,3,0,1] row_mask:0xf bank_mask:0xf\n\t"
                   "v_add_f32_dpp %2, %2, %2 quad_perm:[2,3,0,1] row_mask:0xf bank_mask:0xf\n\tv_add_f32_dpp %3, %3, %3 quad_perm:[2,3,0,1] row_mask:0xf bank_mask:0xf"
                   : "=&v"(r4[st][0]), "=&v"(r4[st][1]), "=&v"(r4[st][2]), "=&v"(r4[st][3])
                   : "v"(r8[st][0]), "v"(r8[st][1]), "v"(r8[st][2]), "v"(r8[st][3]), "v"(r8[st][4]), "v"(r8[st][5]), "v"(r8[st][6]), "v"(r8[st][7]));
    }
    if ((c & 3) == 0) { LAS char* pp = sm + T_PART + wf * 1024 + (64 * wt + 32 * ((c >> 3) ^ wf) + 8 * q + 4 * ((c >> 2) & 1)) * 8;
      *(LAS f32x4*)pp = (f32x4){r4[0][0], r4[1][0], r4[0][1], r4[1][1]}; *(LAS f32x4*)(pp + 16) = (f32x4){r4[0][2], r4[1][2], r4[0][3], r4[1][3]}; }
#pragma unroll
    for (int i = 0; i < 2; ++i) asm volatile("" : "+v"(z2p[i][0]), "+v"(z2p[i][1]), "+v"(z2p[i][2]), "+v"(z2p[i][3]));
    asm volatile("" : "+v"(z2x));
  }
  STAMP(2);
  zero();
  first(M1, 0, 1);
  stage(M1, 3, 2, false); stage(M1, 2, 3, false); stage(M1, 1, 0, false);
  stage(M1, 0, 1, false); stage(M1, 3, 2, false); stage(M1, 2, 3, false);
  STAMP(3);
  {
#pragma unroll
    for (int i = 0; i < 2; ++i) asm volatile("" : "+v"(z2p[i][0]), "+v"(z2p[i][1]), "+v"(z2p[i][2]), "+v"(z2p[i][3]));
    asm volatile("" : "+v"(z2x));
#pragma unroll
    for (int ip = 0; ip < 2; ++ip) {
      const int ro = (64 * wt + 32 * (ip ^ wf) + 8 * q) * 4;
      const f32x4 r0 = lds<f32x4>(sm + T_R2 + ro), r1 = lds<f32x4>(sm + T_R2 + ro + 16), n0 = lds<f32x4>(sm + T_N2 + ro), n1 = lds<f32x4>(sm + T_N2 + ro + 16);
      const unsigned so = W_ZF + ((id.ft * 64 + 4 * id.tp + 2 * wt + (ip ^ wf)) * 9) * 1024;
#pragma unroll
      for (int j = 0; j < (ip == 0 ? 5 : 4); ++j) { const u32x4 z = j < 4 ? z2p[ip][j] : z2x;
        const f32x4 a = (f32x4){blo(z[0]), bhi(z[0]), blo(z[1]), bhi(z[1])} * r0 + n0, b = (f32x4){blo(z[2]), bhi(z[2]), blo(z[3]), bhi(z[3])} * r1 + n1;
        st16<SC1>((u32x4){pk(a[0], a[1]), pk(a[2], a[3]), pk(b[0], b[1]), pk(b[2], b[3])}, rws, lane * 16, so + (j < 2 ? jg0 + j : j < 4 ? jg2 + j - 2 : 8) * 1024); }
    }
    SB();
  }
  unsigned braw[4];
  { const rsrc_t rb = mkrs(P.bsp, S * 2);
#pragma unroll
    for (int i = 0; i < 4; ++i) braw[i] = __builtin_amdgcn_raw_buffer_load_b16(rb, (id.tp * 128 + 64 * wt + prow(i ^ (2 * wf), c)) * 2, 0, 0); }
  STAMP(4);
  stage(M1, 1, 0, false); stage(M1, 0, 1, true);
  mm(M1, 1, 18, 21); mm(M1, 1, 0, 8); SB();
  if (c == 0) { *(LAS f32x4*)(sm + T_CS1 + (jg0 * 16 + 4 * q) * 4) = csa[0]; *(LAS f32x4*)(sm + T_CS1 + (jg0 * 16 + 16 + 4 * q) * 4) = csa[1]; *(LAS f32x4*)(sm + T_CS1 + (128 + 16 * wt + 4 * q) * 4) = csa[2]; }
  mm(M1, 1, 8, 18); SB();
  WAITL(0); BAR();
  STAMP(5);
  u32x2 z1p[4][4], z1x[2];
  { f32x4 cs[5];
    cs[0] = csa[0]; cs[1] = csa[1]; cs[2] = lds<f32x4>(sm + T_CS1 + (jg2 * 16 + 4 * q) * 4); cs[3] = lds<f32x4>(sm + T_CS1 + (jg2 * 16 + 16 + 4 * q) * 4);
    cs[4] = csa[2] + lds<f32x4>(sm + T_CS1 + (144 - 16 * wt + 4 * q) * 4);
#pragma unroll
    for (int i = 0; i < 4; ++i) { const int ro = (64 * wt + prow(i ^ (2 * wf), c)) * 4;
      const float m = lds<float>(sm + T_M1 + ro), a0 = lds<float>(sm + T_A0 + ro), a1 = lds<float>(sm + T_A1 + ro), ir = lds<float>(sm + T_IR + ro), bias = blo(braw[i]);
#pragma unroll
      for (int j = 0; j < (i < 2 ? 5 : 4); ++j) {
        f32x4& a = j < 4 ? acc[i][j] : accx[i];
        const f32x4 z = gelu4(a - m * cs[j], a0, a1, ir);
        (j < 4 ? z1p[i][j] : z1x[i]) = (u32x2){pk(z[0], z[1]), pk(z[2], z[3])};
        a = (f32x4){bias, bias, bias, bias};
      } }
#pragma unroll
    for (int s = 0; s < 2; ++s) { fx[s] = sm + BBASE + (s * 9 + 8) * 1024 + lane * 16;
#pragma unroll
      for (int h = 0; h < 2; ++h) { fb[s][h] = sm + BBASE + (s * 9 + (h ? jg2 : jg0)) * 1024 + lane * 16; ta[s][h] = sm + (64 * wt + 32 * (h ^ wf) + c) * 128 + (((q + 4 * s) ^ ((c >> 1) & 7)) * 16); } }
#pragma unroll
    for (int i = 0; i < 4; ++i) asm volatile("" : "+v"(z1p[i][0]), "+v"(z1p[i][1]), "+v"(z1p[i][2]), "+v"(z1p[i][3]), "+v"(acc[i][0]), "+v"(acc[i][1]), "+v"(acc[i][2]), "+v"(acc[i][3]));
    asm volatile("" : "+v"(z1x[0]), "+v"(z1x[1]), "+v"(accx[0]), "+v"(accx[1]));
  }
  STAMP(6);
  const int jq = id.b & 3;
  LAS char *cs_ = sm + CV + 2 * CV_CH, *cb = sm + CV + wt * CV_CH;
  LAS char *am[4], *um = cb + CV_U + (2032 - 16 * c + 8 * q) * 2 + 256 * wf - 3584;
#pragma unroll
  for (int i = 0; i < 4; ++i) { am[i] = cb + ((c & 1) ? 0 : CV_O) + (4 * q + ((c + 1) >> 1)) * 4 + 256 * wf + 1024 * i; asm volatile("" : "+v"(am[i])); }
  asm volatile("" : "+v"(um));
  bf16x8 ca[2][8], cu[2][12];
  auto afrag = [&](LAS char* p) { const unsigned d0 = lds<unsigned>(p), d1 = lds<unsigned>(p + 4), d2 = lds<unsigned>(p + 8), d3 = lds<unsigned>(p + 12);
    return __builtin_bit_cast(bf16x8, (u32x4){d0, d1, d2, d3}); };
  auto lds_mn = [&](int b, int kk) {
#pragma unroll
    for (int e = 0; e < 8; ++e) { cu[b][e] = lds<bf16x8>(um + kk * 64 + 512 * (7 - e)); if (e == 0) ca[b][0] = afrag(am[0] + kk * 64); }
#pragma unroll
    for (int e = 1; e < 8; ++e) ca[b][e] = afrag(am[e >> 1] + kk * 64 + (e & 1) * 512); };
  first(M2, 3, 2);
  STAMP(7);
#pragma unroll 1
  for (int it = 0; it < 7; ++it) { stage(M2, 2, 3, false); stage(M2, 1, 0, false); stage(M2, 0, 1, false); stage(M2, 3, 2, false); }
  stage(M2, 2, 3, false); stage(M2, 1, 0, false); stage(M2, 0, 1, false);
  mm(M2, 1, 0, 4); SB(); WAITL(0); BAR(); SB();
  mm(M2, 1, 4, 18); SB();
  lds_mn(0, 0); SB();
  STAMP(8);
#pragma unroll
  for (int i = 0; i < 4; ++i) asm volatile("" : "+v"(z1p[i][0]), "+v"(z1p[i][1]), "+v"(z1p[i][2]), "+v"(z1p[i][3]));
  asm volatile("" : "+v"(z1x[0]), "+v"(z1x[1]));
#pragma unroll
  for (int i = 0; i < 4; ++i) {
    const unsigned so = W_HF + (id.tp * 8 + 4 * wt + (i ^ (2 * wf))) * 72 * 1024;
    auto gate = [&](u32x2 z, f32x4 a) { const f32x4 h = (f32x4){blo(z.x), bhi(z.x), blo(z.y), bhi(z.y)} * a; return (u32x2){pk(h[0], h[1]), pk(h[2], h[3])}; };
#pragma unroll
    for (int pp = 0; pp < 2; ++pp) { const u32x2 lo = gate(z1p[i][2 * pp], acc[i][2 * pp]), hi = gate(z1p[i][2 * pp + 1], acc[i][2 * pp + 1]);
      st16<SC1>((u32x4){lo.x, lo.y, hi.x, hi.y}, rws, lane * 16, so + (4 * id.ft + 2 * wf + (pp ^ wt)) * 1024); }
    if (i < 2) st8<SC1>(gate(z1x[i], accx[i]), rws, lane * 16 + (id.ft & 1) * 8, so + (64 + (id.ft >> 1)) * 1024);
  }
  const int g = id.ft & 7, gp = g & 1, half = id.ft >> 3, rot = krot(id);
  const unsigned wso = W_WOF + ((((EXP & 4) ? 0 : g >> 1) * 72 + 18 * wid) * 9) * 1024;
  u32x4 wr[WD][5];
  auto ldw = [&](int slot, int i) {
#pragma unroll
    for (int t = 0; t < 5; ++t) wr[slot][t] = ld16<(EXP & 8) ? 0 : SC1>(rws, lane * 16, wso + kwrap(i, rot) * 9216 + (t == 4 ? 4 : t + gp * 5) * 1024); };
  SB();
  SB(); BAR(); SB();
  STAMP(9);
  u32x4 hr[3][4];
  const unsigned hso = W_HF + (((EXP & 2) ? (id.b & 7) * 4 : id.tp * 8 + 4 * half) * 72 + 18 * wid) * 1024;
  auto ldh = [&](int slot, int i) {
#pragma unroll
    for (int t = 0; t < 4; ++t) hr[slot][t] = ld16<(EXP & 8) ? 0 : SC1>(rws, lane * 16, hso + (((unsigned)t ^ (wf ^ (2 * gp))) * 72 + kwrap(i, rot)) * 1024); };
  { const float dvm = wt ? bhi(dm) : blo(dm), dvs = (id.b & 4) ? bhi(dsh) : blo(dsh);
    f32x4 cm[8], s0 = {0.f, 0.f, 0.f, 0.f}, s1 = s0;
#pragma unroll
    for (int j = 0; j < 8; ++j) cm[j] = s0;
    LAS char *as[4], *u1, *u0;
    auto addr_sh = [&]() {
      const int da = (2 - wt) * CV_CH + 128 * wid - 256 * wf;
#pragma unroll
      for (int i = 0; i < 4; ++i) { as[i] = am[i] + da; asm volatile("" : "+v"(as[i])); }
      u1 = um + da + 512 * jq; u0 = um + da + 512 * (7 - jq); asm volatile("" : "+v"(u1), "+v"(u0)); };
    auto lds_sh = [&](int b, int kk) {
#pragma unroll
      for (int e = 0; e < 8; ++e) { ca[b][e] = afrag(as[e >> 1] + kk * 64 + (e & 1) * 512); cu[b][e] = lds<bf16x8>(u1 + kk * 64 + 512 * e);
        if (e < 4) cu[b][8 + e] = lds<bf16x8>(u0 + kk * 64 + 512 * e); } };
    auto mm_sh = [&](int b) {
#pragma unroll
      for (int e = 0; e < 8; ++e) { s1 = MFMA(ca[b][e], cu[b][e], s1); if (e < 4) s0 = MFMA(ca[b][e], cu[b][8 + e], s0); } };
    auto mm_mn = [&](int b) {
#pragma unroll
      for (int e = 0; e < 8; ++e)
#pragma unroll
        for (int j = e; j < 8; ++j) cm[j] = MFMA(ca[b][e], cu[b][j - e], cm[j]); };
#define il(nr, per) do { for (int i_ = 0; i_ < nr; ++i_) { SG(0x100, per); SG(0x8, 1); } SG(0x8, 36); SB(); } while (0)
#define ilv() do { for (int i_ = 0; i_ < 5; ++i_) { SG(0x100, 1); SG(0x8, 1); SG(0x100, 1); SG(0x8, 1); SG(0x100, 1); SG(0x8, 1); SG(0x100, 1); SG(0x8, 1); SG(0x100, 2); SG(0x8, 2); SG(0x20, 1); SG(0x8, 1); } SG(0x8, 36); SB(); } while (0)
    lds_mn(1, 1); mm_mn(0); il(24, 1); STAMP(15);
    lds_mn(0, 2); mm_mn(1); il(24, 1); STAMP(16); addr_sh(); SB();
    lds_sh(1, 0); mm_mn(0); il(28, 1);
    lds_sh(0, 1); mm_sh(1); il(10, 3);
    lds_mn(1, 3); mm_sh(0); il(12, 2);
    if (wid != 0) *(LAS f32x4*)(sm + CV_PS + (wid - 1) * 1024 + lane * 16) = s0;
    if (wid != 1) *(LAS f32x4*)(sm + CV_PS + (3 + (wid ? wid - 1 : 0)) * 1024 + lane * 16) = s1;
    SB(); STAMP(17); WAITV(0); WAITL(0); BAR(); SB();
    STAMP(10);
    f32x4 ys = wf ? s1 : s0;
    if (wid < 2) {
#pragma unroll
      for (int w = 0; w < 3; ++w) ys += lds<f32x4>(sm + CV_PS + (3 * wid + w) * 1024 + lane * 16);
    }
    SB(); ldw(0, 5); ldw(1, 6); if (WD > 2) ldw(2, 7); mm_mn(1); SG(0x8, 4); for (int i_ = 0; i_ < 15; ++i_) { SG(0x8, 2); SG(0x20, 1); } SG(0x8, 36); SB();
    LAS char* pm = sm + (wt ? CV_PM23 : CV + 2 * CV_CH);
#pragma unroll
    for (int j = 0; j < 4; ++j) { f32x4 lo = cm[j], hi = cm[4 + j]; asm volatile("" : "+v"(lo), "+v"(hi));
      *(LAS f32x4*)(pm + wf * 4096 + j * 1024 + lane * 16) = wf ? lo : hi; cm[j] = wf ? hi : lo; }
    SB(); WAITL(0); BAR(); SB();
    STAMP(11);
    ldh(0, 0); ldh(1, 1); ldh(2, 2);
    auto yout = [&](f32x4 a, int chn, LAS char* ub, int j, float dv) {
      const u32x2 u = lds<u32x2>(ub + CV_U + (2044 - 256 * j - 16 * c - 4 * q) * 2);
      const f32x4 y = a + (f32x4){bhi(u.y), blo(u.y), bhi(u.x), blo(u.x)} * dv;
      st8<SC1>((u32x2){pk(y[0], y[1]), pk(y[2], y[3])}, rws, (16 * c + 4 * q) * 2, W_YT + chn * (S * 2) + j * 512); };
#pragma unroll
    for (int j = 0; j < 4; ++j) yout(cm[j] + lds<f32x4>(pm + (wf ^ 1) * 4096 + j * 1024 + lane * 16), 2 * id.b + wt, cb, 4 * wf + j, dvm);
    if (wid < 2) yout(ys, 512 + (id.b >> 2), cs_, wid ? 7 - jq : jq, dvs);
    SB();
  }
  STAMP(19);
  f32x4 o[4][4], ox[2];
#pragma unroll
  for (int i = 0; i < 4; ++i)
#pragma unroll
    for (int t = 0; t < 4; ++t) o[i][t] = (f32x4){0.f, 0.f, 0.f, 0.f};
  ox[0] = ox[1] = (f32x4){0.f, 0.f, 0.f, 0.f};
  const int dp = 144 * (g >> 1) + 80 * gp, ds = 144 * (g >> 1) + 64, trow = 64 * half + prow(wid, c);
  const unsigned xo = (id.tp * 128 + trow) * (D * 2);
  f32x4 rs[5];
  { LAS char* wl = sm + wid * G3W + lane * 16;
    bf16x8 wq[2][5];
#pragma unroll
    for (int t = 0; t < 5; ++t) wq[0][t] = lds<bf16x8>(wl + t * 1024);
#pragma unroll
    for (int i = 0; i < 18; ++i) {
      if (i < 4) {
#pragma unroll
        for (int t = 0; t < 5; ++t) wq[(i + 1) & 1][t] = lds<bf16x8>(wl + ((i + 1) * 5 + t) * 1024); }
#pragma unroll
      for (int r = 0; r < 4; ++r) { const bf16x8 hf = __builtin_bit_cast(bf16x8, hr[i % 3][r]);
#pragma unroll
        for (int t = 0; t < 5; ++t) if (t < 4 || r < 2) { const int tt = (r & 1) && t < 4 ? 3 - t : t;
          const bf16x8 wf_ = i < 5 ? wq[i & 1][tt] : __builtin_bit_cast(bf16x8, wr[(i - 5) % WD][tt]);
          if (tt < 4) o[r][tt] = MFMA(wf_, hf, o[r][tt]); else ox[r] = MFMA(wf_, hf, ox[r]); } }
      SB();
      if (i == 0 || i == 2 || i == 4 || i == 7 || i == 10 || i == 13 || i == 15) STAMP(20 + i);
      if (i + 3 < 18) ldh(i % 3, i + 3);
      if (i >= 5 && i + WD < 18) ldw((i - 5) % WD, i + WD);
      if (i == 2) { WAITV(12); if (lane == 0) __hip_atomic_fetch_add((LAS unsigned*)(sm + T_CNT), 1u, __ATOMIC_RELAXED, __HIP_MEMORY_SCOPE_WORKGROUP); }
      if (i == 16) {
        const u64 t0 = __builtin_amdgcn_s_memrealtime();
        while (*(volatile LAS unsigned*)(sm + T_YOK) == 0u && !expired(t0)) __builtin_amdgcn_s_sleep(1);
        FENCE();
        const float m = lds<float>(sm + T_M1 + trow * 4), r = lds<float>(sm + T_R1 + trow * 4);
        LAS char* yb = sm + YTILE + (c >> 2) * 128 + (32 * wt + 4 * wf + 8 * (c & 3)) * 2;
        auto res = [&](u32x2 xv, bf16x4 yv) { const u32x2 yu = __builtin_bit_cast(u32x2, yv);
          const f32x4 xf = {blo(xv.x), bhi(xv.x), blo(xv.y), bhi(xv.y)}, yf = {blo(yu.x), bhi(yu.x), blo(yu.y), bhi(yu.y)};
          return xf + (xf - m) * r * yf; };
        const int yp = 16 * gp, ysh = 64 * (gp ^ 1);
        LAS char* xb = sm + XTILE + prow(wid, c) * 160;
        const u32x4 x0 = lds<u32x4>(xb + yp * 2 + q * 16), x1 = lds<u32x4>(xb + yp * 2 + 64 + q * 16); const u32x2 x2 = lds<u32x2>(xb + ysh * 2 + q * 8);
        rs[0] = res(x0.xy, ldtr(yb + (yp + 8 * q) * 128)); rs[1] = res(x0.zw, ldtr(yb + (yp + 8 * q + 4) * 128));
        rs[2] = res(x1.xy, ldtr(yb + (yp + 32 + 8 * q) * 128)); rs[3] = res(x1.zw, ldtr(yb + (yp + 36 + 8 * q) * 128));
        rs[4] = res(x2, ldtr(yb + (ysh + 4 * q) * 128));
        asm volatile("" : "+v"(rs[0]), "+v"(rs[1]), "+v"(rs[2]), "+v"(rs[3]), "+v"(rs[4])); }
      SB();
    }
  }
  STAMP(12);
  const bool own0 = wt == gp;
  { LAS char* pw = sm + wid * G3W + lane * 16;
    auto put = [&](int r) { LAS char* p = pw + ((unsigned)r ^ (wf ^ (2 * gp))) * 5120;
#pragma unroll
      for (int t = 0; t < 4; ++t) *(LAS f32x4*)(p + t * 1024) = o[r][t];
      if (r < 2) *(LAS f32x4*)(p + 4096) = ox[r]; };
    put(1); put(3);
    if (own0) { put(2);
#pragma unroll
      for (int t = 0; t < 4; ++t) rs[t] += o[0][t];
      rs[4] += ox[0]; }
    else { put(0);
#pragma unroll
      for (int t = 0; t < 4; ++t) rs[t] += o[2][t]; }
  }
  SB(); WAITL(0); BAR(); SB();
  STAMP(13);
  { f32x4 pt[3][5];
#pragma unroll
    for (int t = 0; t < 5; ++t)
#pragma unroll
      for (int w = 0; w < 3; ++w) pt[w][t] = lds<f32x4>(sm + (wid ^ (w + 1)) * G3W + wid * 5120 + lane * 16 + t * 1024);
    const rsrc_t ro = mkrs(P.out, S * D * 2);
    auto fin = [&](int t) { const f32x4 v = (rs[t] + pt[0][t]) + (pt[1][t] + pt[2][t]); return (u32x2){pk(v[0], v[1]), pk(v[2], v[3])}; };
#pragma unroll
    for (int pp = 0; pp < 2; ++pp) { const u32x2 lo = fin(2 * pp), hi = fin(2 * pp + 1); st16<0>((u32x4){lo.x, lo.y, hi.x, hi.y}, ro, xo + q * 16, dp * 2 + 64 * pp); }
    const u32x2 ls = fin(4);
    if (own0) st8<0>(ls, ro, xo + q * 8, ds * 2);
  }
  STAMP(14);
}
}

extern "C" __global__ __launch_bounds__(512, 2) void h3_gmlp_fused(const Params P) {
  extern __shared__ __attribute__((aligned(16))) char smem_[];
  LAS char* sm = (LAS char*)smem_;
  const int wid = __builtin_amdgcn_readfirstlane(threadIdx.x >> 6);
  Ids id; id.b = blockIdx.x; id.lane = threadIdx.x & 63;
  const int xcd = id.b & 7, loc = id.b >> 3;
  id.tp = (xcd >> 1) * 4 + (loc >> 3); id.ft = (xcd & 1) * 8 + (loc & 7);
  if (wid == 0) {
    unsigned lo, hi, bytes, sink;
    asm volatile("s_getpc_b64 vcc\n1:\n\ts_sub_u32 %0, vcc_lo, 1b-h3_gmlp_fused\n\ts_subb_u32 %1, vcc_hi, 0\n\ts_mov_b32 %2, h3_gmlp_end-h3_gmlp_fused" : "=s"(lo), "=s"(hi), "=s"(bytes) :: "vcc", "scc");
    const rsrc_t rc = mkrs((const void*)(((u64)hi << 32) | lo), bytes);
    asm volatile("buffer_load_dword %0, %1, %2, 0 offen\n\ts_waitcnt vmcnt(0)" : "=&v"(sink) : "v"((id.lane < 16 ? id.lane * 32 + loc : 0x100000) * 128), "s"(rc) : "memory");
  }
  if (__builtin_expect(wid == 4 || wid == 5, 1)) role_L(P, sm, id, wid - 4 STAMP_PASS);
  else if (wid >= 6) role_X(P, sm, id, wid - 6 STAMP_PASS);
  else role_C(P, sm, id, wid STAMP_PASS);
}
extern "C" __global__ void h3_gmlp_end() {}

extern "C" void kernel_launch(void* const* d_in, const int* in_sizes, int n_in, void* d_out, int out_size, void* d_ws, size_t ws_size, hipStream_t stream) {
  Params p;
  p.x = (const u16*)d_in[0]; p.k = (const u16*)d_in[1]; p.v = (const u16*)d_in[2]; p.shk = (const u16*)d_in[3]; p.ssmk = (const u16*)d_in[4]; p.ssmd = (const u16*)d_in[5];
  p.win = (const u16*)d_in[7]; p.wsp = (const u16*)d_in[8]; p.bsp = (const u16*)d_in[9]; p.wout = (const u16*)d_in[10];
  p.out = (u16*)d_out; p.ws = (char*)d_ws;
  (void)hipFuncSetAttribute((const void*)h3_gmlp_fused, hipFuncAttributeMaxDynamicSharedMemorySize, LDS_BYTES);
  h3_gmlp_fused<<<256, 512, LDS_BYTES, stream>>>(p);
}
```

```cpp
#define MK_STAMP
#define MK_CYC
#define MK_AMP 8
#define MK_AMP0 0
#define MK_NUM 1
#define MK_DEN 2
#include <hip/hip_runtime.h>
#include <hip/hip_bf16.h>
#include <type_traits>

namespace {
typedef unsigned short u16;
typedef unsigned long long u64;
typedef short bf16x8 __attribute__((ext_vector_type(8)));
typedef short bf16x4 __attribute__((ext_vector_type(4)));
typedef __bf16 bf2 __attribute__((ext_vector_type(2)));
typedef float f32x2 __attribute__((ext_vector_type(2)));
typedef float f32x4 __attribute__((ext_vector_type(4)));
typedef unsigned u32x2 __attribute__((ext_vector_type(2)));
typedef unsigned u32x4 __attribute__((ext_vector_type(4)));
typedef __amdgpu_buffer_rsrc_t rsrc_t;
#define LAS __attribute__((address_space(3)))
#define CAS __attribute__((address_space(4)))
#define DEV __device__ __forceinline__

constexpr int S = 2048, D = 576, F = 4608, FH = 2304;
constexpr int ASLOT = 16384, BSLOT = 18432, BBASE = 4 * ASLOT;
constexpr int STG = 139264, STG_W = 9216;
constexpr int T_M1 = 157952, T_R1 = T_M1 + 512, T_A0 = T_R1 + 512, T_A1 = T_A0 + 512, T_IR = T_A1 + 512;
constexpr int T_CS2 = T_IR + 512, T_CS1 = T_CS2;
constexpr int T_PART = T_CS1 + 640, T_N2 = T_PART, T_R2 = T_N2 + 512;
constexpr int T_TAG = T_PART + 2048, T_CNT = T_TAG + 16, T_GO = T_CNT + 4, T_YOK = T_CNT + 8;
constexpr int LDS_BYTES = 163840;
static_assert(T_CNT + 16 <= LDS_BYTES, "LDS map");
constexpr int G3W = 25600;
constexpr int CV = 102400, CV_CH = 13056, CV_O = 4160, CV_U = 8320;
constexpr int CV_PS = 143616, CV_PM23 = 149760;
constexpr int YTILE = 122880, XTILE = 133120;
constexpr int W_EPOCH = 0, W_F2 = 4096, W_F3 = 8192, W_F4 = 12288, W_F5 = 16384;
constexpr int W_G0X = 0x8000, W_G1F = 0x10000, W_G1S = 0x60000;
constexpr int W_KVT = 1 << 20, W_YT = 4 << 20, W_WOF = 7 << 20, W_ZF = 10 << 20, W_HF = 20 << 20, W_END = 30 << 20;
constexpr int SC1 = 16;
#define OPT_TWIN 0
#define OPT_TWSP 1
#define OPT_TG3 0
#define EXP 0
#define RH 0
#define RW 0
#define WD 3

#define BAR0() do { asm volatile("" ::: "memory"); __builtin_amdgcn_s_barrier(); asm volatile("" ::: "memory"); } while (0)
#define BAR() BAR0()
#define SB() __builtin_amdgcn_sched_barrier(0)
#define SG(m, n) __builtin_amdgcn_sched_group_barrier(m, n, 0)
#define WAITV(n) asm volatile("s_waitcnt vmcnt(%0)" ::"i"(n) : "memory")
#define WAITL(n) asm volatile("s_waitcnt lgkmcnt(%0)" ::"i"(n) : "memory")
#define FENCE() asm volatile("" ::: "memory")
#define MFMA(a, b, c) __builtin_amdgcn_mfma_f32_16x16x32_bf16(a, b, c, 0, 0, 0)
#define CLK __builtin_amdgcn_s_memtime
#define STAMP(i) do { const unsigned t_ = (unsigned)CLK(); stampv = lane == (i) ? t_ : stampv; asm volatile("" : "+v"(stampv)); } while (0)
#define LBAR() do { BAR(); const int bi_ = bcount - (lw ? 0 : 40); ++bcount; if (bi_ >= 0 && bi_ < 60) STAMP(bi_); } while (0)
#define STAMP_ARG , unsigned& stampv
#define STAMP_PASS , stampv

struct Params {
  const u16 *x, *k, *v, *shk, *ssmk, *ssmd, *win, *wsp, *bsp, *wout;
  u16* out; char* ws;
};

DEV rsrc_t mkrs(const void* p, unsigned bytes) { return __builtin_amdgcn_make_buffer_rsrc((void*)p, 0, bytes, 0x00020000); }
template <int AUX> DEV void dma(rsrc_t rs, LAS char* l, unsigned voff, unsigned soff) { __builtin_amdgcn_raw_ptr_buffer_load_lds(rs, (LAS void*)l, 16, voff, soff, 0, AUX); }
DEV void dma_hidden(rsrc_t rs, LAS char* l, unsigned voff) { unsigned keep;
  asm volatile("s_mov_b32 %0, m0\n\ts_mov_b32 m0, %3\n\ts_nop 0\n\tbuffer_load_dwordx4 %1, %2, 0 offen lds\n\ts_mov_b32 m0, %0" : "=&s"(keep) : "v"(voff), "s"(rs), "s"((unsigned)(size_t)l) : "memory"); }
template <int AUX> DEV u32x4 ld16(rsrc_t rs, unsigned voff, unsigned soff) { return __builtin_amdgcn_raw_buffer_load_b128(rs, voff, soff, AUX); }
template <int AUX> DEV void st16(u32x4 v, rsrc_t rs, unsigned voff, unsigned soff) { __builtin_amdgcn_raw_buffer_store_b128(v, rs, voff, soff, AUX); }
template <int AUX> DEV void st8(u32x2 v, rsrc_t rs, unsigned voff, unsigned soff) { __builtin_amdgcn_raw_buffer_store_b64(v, rs, voff, soff, AUX); }
template <class T> DEV T lds(LAS char* p) { return *(LAS T*)p; }
DEV bf16x4 ldtr(LAS char* p) { return __builtin_amdgcn_ds_read_tr16_b64_v4i16((LAS bf16x4*)p); }
DEV bf16x8 cat(bf16x4 a, bf16x4 b) { return __builtin_shufflevector(a, b, 0, 1, 2, 3, 4, 5, 6, 7); }
DEV float blo(unsigned u) { return __uint_as_float(u << 16); }
DEV float bhi(unsigned u) { return __uint_as_float(u & 0xffff0000u); }
DEV unsigned pk(float a, float b) { return __builtin_bit_cast(unsigned, __builtin_convertvector((f32x2){a, b}, bf2)); }
DEV float dot2(unsigned a, unsigned b, float c) { return __builtin_amdgcn_fdot2_f32_bf16(__builtin_bit_cast(bf2, a), __builtin_bit_cast(bf2, b), c, false); }
template <int CTRL> DEV float dpp(float x) { return __builtin_bit_cast(float, __builtin_amdgcn_mov_dpp(__builtin_bit_cast(int, x), CTRL, 0xf, 0xf, true)); }
constexpr int XOR1 = 0xB1, XOR2 = 0x4E, XOR7 = 0x141, XOR8 = 0x128;
constexpr float GC0 = -2.30220819f, GC1 = -0.10294324f;
template <class T> DEV f32x2 gelu2(f32x2 a, T a0, T a1, T ir) {
  const f32x2 g = a * (a * a * a1 + a0);
  f32x2 e; e.x = __builtin_amdgcn_exp2f(g.x); e.y = __builtin_amdgcn_exp2f(g.y);
  e = e * ir + ir;
  f32x2 r; r.x = __builtin_amdgcn_rcpf(e.x); r.y = __builtin_amdgcn_rcpf(e.y);
  return a * r;
}
DEV f32x4 gelu4(f32x4 a, f32x4 a0, f32x4 a1, f32x4 ir) { const f32x2 p = gelu2(a.xy, a0.xy, a1.xy, ir.xy), q = gelu2(a.zw, a0.zw, a1.zw, ir.zw); return (f32x4){p.x, p.y, q.x, q.y}; }
DEV f32x4 gelu4(f32x4 a, float a0, float a1, float ir) { const f32x2 p = gelu2(a.xy, a0, a1, ir), q = gelu2(a.zw, a0, a1, ir); return (f32x4){p.x, p.y, q.x, q.y}; }
DEV u32x4 seal(float a, float b, u32x2 tag) { const unsigned ua = __float_as_uint(a), ub = __float_as_uint(b); return (u32x4){ua, ub, ua ^ tag.x, ub ^ tag.y}; }
DEV bool sealed(u32x4 g, u32x2 tag) { return (g.x ^ g.z) == tag.x && (g.y ^ g.w) == tag.y; }
DEV bool expired(u64 t0) { return __builtin_amdgcn_s_memrealtime() - t0 > 2000000ull; }
template <int NL, int NAP> DEV void wait_flags(rsrc_t ws, unsigned voff, u32x2 tag) {
  const u64 t0 = __builtin_amdgcn_s_memrealtime();
  for (;;) {
    bool ok = true;
#pragma unroll
    for (int i = 0; i < NL; ++i) { const u32x4 f = ld16<SC1>(ws, voff, i * 1024); ok &= f.x == tag.x && f.y == tag.y && f.z == tag.x && f.w == tag.y; }
    if (__all(ok) || expired(t0)) break;
    __builtin_amdgcn_s_sleep(NAP); FENCE();
  }
  FENCE();
}
DEV bool flags_ok(u32x4 f, u32x2 tag) { return f.x == tag.x && f.y == tag.y && f.z == tag.x && f.w == tag.y; }
DEV int prow(int T, int c) { return 32 * (T >> 1) + 8 * (c >> 2) + 4 * (T & 1) + (c & 3); }
DEV void set_flag(rsrc_t ws, unsigned off, u32x2 tag, int lane) { if (lane == 0) st8<SC1>(tag, ws, off, 0); }

struct Ids { int b, tp, ft, lane; };
DEV int krot(const Ids& id) { return (RH * (id.ft & 7) + ((RW * (id.tp & 3)) >> 1)) % 18; }
DEV int kwrap(int i, int r) { const int k = i + r; return k >= 18 ? k - 18 : k; }

DEV void role_L(const Params& P, LAS char* sm, const Ids id, const int lw STAMP_ARG) {
  int bcount = 0; (void)bcount;
  const int lane = id.lane, rr = lane >> 3, sl = lane & 7;
  const rsrc_t rx = mkrs(P.x, S * D * 2), rwin = mkrs(P.win, D * F * 2);
  const unsigned swz = (sl ^ (4 * lw + (rr >> 1))) * 16, prr = 16 * lw + 8 * (rr >> 2) + (rr & 3);
  const unsigned vx = prr * (D * 2) + swz;
  unsigned vb[9];
  auto issue1a = [&](int slot, int i, int ks) { dma<0>(rx, sm + slot * ASLOT + lw * 1024 + i * 2048, vx, (id.tp * 128 + 32 * (i >> 1) + 4 * (i & 1)) * (D * 2) + ks * 128); };
  auto issue1b = [&](int slot, int i, int ks, int col) { dma<0>(rwin, sm + BBASE + slot * BSLOT + lw * 1024 + i * 2048, vb[i], ks * 64 * (F * 2) + (col + id.ft * 144) * 2); };
#pragma unroll
  for (int i = 0; i < 9; ++i) {
    if (i < 8) issue1a(0, i, 0);
    SB();
    const unsigned ci = 64 * (2 * i + lw) + lane, rho = __umul24(ci, 3641) >> 16;
    vb[i] = __umul24(rho, F * 2 - 288) + 16 * ci + __mul24((int)(rho & 4) - (int)((rho >> 1) & 4), F * 2);
    issue1b(0, i, 0, FH);
    SB();
  }
  auto issueA = [&](int n) {
#pragma unroll
    for (int i = 0; i < 9; ++i) { if (i < 8) issue1a(n & 3, i, n); issue1b(n & 3, i, n, FH); } };
  auto issueB = [&](int n, bool with_x) {
#pragma unroll
    for (int i = 0; i < 9; ++i) { if (with_x && i < 8) issue1a((9 - n) & 3, i, 17 - n); issue1b(n & 3, i, 17 - n, 0); } };
  issueA(1); issueA(2);
  STAMP(60);
  const rsrc_t rwsp = mkrs(P.wsp, S * S * 2), rws = mkrs(P.ws, W_END);
  const unsigned vw = prr * (S * 2) + swz, vl = lane * 16 + lw * 1024;
  auto issue2a = [&](int n) { LAS char* a = sm + ((9 - n) & 3) * ASLOT + lw * 1024; const unsigned sa = id.tp * 128 * (S * 2) + (n - 18) * 128;
#pragma unroll
    for (int i = 0; i < 8; ++i) dma<0>(rwsp, a + i * 2048, vw, sa + (32 * (i >> 1) + 4 * (i & 1)) * (S * 2)); };
  auto issue2b = [&](int n) { LAS char* bb = sm + BBASE + (n & 3) * BSLOT + lw * 1024; const unsigned sb = W_ZF + (id.ft * 576 + (n - 18) * 18) * 1024;
#pragma unroll
    for (int i = 0; i < 9; ++i) dma<SC1>(rws, bb + i * 2048, vl, sb + i * 2048); };
#pragma unroll 1
  for (int n = 0; n < 6; ++n) { WAITV(34); LBAR(); issueA(n + 3); }
  WAITV(34); LBAR(); issueB(9, false);
  WAITV(26); LBAR(); issueB(10, false);
  WAITV(18); LBAR(); issueB(11, false);
  BAR();
  WAITV(18); LBAR(); issueB(12, false);
  WAITV(18); LBAR(); issueB(13, true);
  WAITV(26); LBAR();
#pragma unroll 1
  for (int n = 12; n < 16; ++n) { WAITV(17); LBAR(); issueB(n + 2, true); }
  WAITV(17); LBAR(); issue2a(18);
  WAITV(8); LBAR(); issue2a(19);
  BAR(); issue2a(20);
  { const u32x2 tag = lds<u32x2>(sm + T_TAG); wait_flags<1, 2>(rws, W_F2 + id.ft * 128 + (lane & 7) * 16, tag); }
  STAMP(61);
  issue2b(18); issue2b(19); issue2b(20);
  asm volatile("ds_write_b32 %0, %1" :: "v"((unsigned)(size_t)(sm + T_GO)), "v"(1u) : "memory");
  WAITV(18); LBAR(); issue2a(21); issue2b(21);
  WAITV(26); LBAR(); issue2a(22); issue2b(22);
#pragma unroll 1
  for (int n = 20; n < 47; ++n) { WAITV(34); LBAR(); issue2a(n + 3); issue2b(n + 3); }
  const int g = id.ft & 7;
  const unsigned wb0 = W_WOF + (g >> 1) * 72 * 9 * 1024, wb1 = wb0 + (g & 1) * 5 * 1024, vq = lane * 16;
  const int rot = krot(id);
  auto issue3 = [&](auto lo_, auto hi_) { constexpr int lo = decltype(lo_)::value, hi = decltype(hi_)::value;
#pragma unroll
    for (int p0 = lo; p0 < hi; p0 += 2) {
      auto off = [&](int pc) { return (pc % 5 == 4 ? wb0 : wb1) + (18 * (pc / 25) * 9 + pc % 5) * 1024 + kwrap((pc % 25) / 5, rot) * 9216; };
      dma<SC1>(rws, sm + p0 * 1024 + lw * 1024, vq, lw ? off(p0 + 1) : off(p0)); } };
#define IC(n) std::integral_constant<int, n>{}
  WAITV(34); LBAR(); issue3(IC(48), IC(64));
  WAITV(25); LBAR();
  WAITV(8); LBAR(); issue3(IC(16), IC(48));
  LBAR();
  LBAR(); issue3(IC(0), IC(16)); issue3(IC(64), IC(100));
  unsigned sink = 0;
  auto wtouch = [&]() {
    constexpr int K0 = 5 + WD, NK = 18 - K0;
#pragma unroll
    for (int i = 0; i < (NK * 40 + 127) / 128; ++i) { const unsigned n = (i * 2 + lw) * 64 + lane, blk = n >> 1, kw = __umul24(blk, 13108) >> 16, t = blk - kw * 5;
      const unsigned o = n < NK * 40 ? (t == 4 ? wb0 : wb1) + ((18 * (kw & 3) + K0 + (kw >> 2)) * 9 + t) * 1024 + ((id.tp & 3) * 2 + (n & 1)) * 128 : 0xF0000000u;
      asm volatile("buffer_load_dword %0, %1, %2, 0 offen sc1" : "+v"(sink) : "v"(o), "s"(rws) : "memory"); } };
  if ((OPT_TG3 & 3) == 1) wtouch();
  LBAR();
  if ((OPT_TG3 & 3) == 2) { __builtin_amdgcn_s_sleep(2); wtouch(); }
  WAITV((OPT_TG3 & 3) == 2 ? (( 18 - 5 - WD) * 40 + 127) / 128 : 0); LBAR();
  asm volatile("s_waitcnt vmcnt(0)" : "+v"(sink) :: "memory"); LBAR();
}

DEV void role_X(const Params& P, LAS char* sm, const Ids id, const int xw STAMP_ARG) {
  int bcount = 0; (void)bcount;
  const int lane = id.lane;
  const rsrc_t rws = mkrs(P.ws, W_END);
  unsigned sink = 0;
  const u32x2 ev = __builtin_amdgcn_raw_buffer_load_b64(rws, W_EPOCH, 0, SC1);
  const int srow = id.tp * 128 + id.ft * 8 + 4 * xw + (lane >> 4);
  u32x4 xv[5];
  { const rsrc_t rx = mkrs(P.x, S * D * 2);
#pragma unroll
    for (int i = 0; i < 5; ++i) xv[i] = ld16<0>(rx, i < 4 || (lane & 8) == 0 ? srow * (D * 2) + (lane & 15) * 16 : 0xF0000000u, i * 256); }
  const u64 epoch = ((u64)__builtin_amdgcn_readfirstlane(ev.y) << 32) | __builtin_amdgcn_readfirstlane(ev.x);
  u32x2 tag; { const u64 t = ((epoch + 1) * 0x9E3779B97F4A7C15ull) | 1ull; tag.x = (unsigned)t; tag.y = (unsigned)(t >> 32); }
  { float s = 0.f, q = 0.f; unsigned ones = 0x3f803f80u; asm volatile("" : "+v"(ones));
#pragma unroll
    for (int i = 0; i < 5; ++i)
#pragma unroll
      for (int e = 0; e < 4; ++e) { s = dot2(xv[i][e], ones, s); q = dot2(xv[i][e], xv[i][e], q); }
    s += dpp<XOR1>(s); q += dpp<XOR1>(q); s += dpp<XOR2>(s); q += dpp<XOR2>(q); s += dpp<XOR7>(s); q += dpp<XOR7>(q); s += dpp<XOR8>(s); q += dpp<XOR8>(q);
    const float m = s * (1.0f / D), var = fmaxf(q * (1.0f / D) - m * m, 0.f) + 1e-5f;
    if ((lane & 15) == 0) st16<SC1>(seal(m, var, tag), rws, W_G0X + srow * 16, 0);
    if (xw == 0 && lane == 0) { *(LAS u32x2*)(sm + T_TAG) = tag; *(LAS u32x4*)(sm + T_CNT) = (u32x4){0u, 0u, 0u, 0u}; }
  }
  STAMP(0);
  BAR(); BAR(); BAR(); BAR();
  LAS char* stg = sm + STG + xw * STG_W;
  const int unit = xw ? 256 + id.b : id.b, wks = unit >> 2, wu = unit & 3; const bool won = !xw || id.b < 32;
  const int tile = xw ? id.b : 256 + id.b - 32; const bool kon = xw || (id.b >= 32 && id.b < 64);
  const int tb = tile / 9, db = tile - tb * 9, cc = lane & 7, rg = lane >> 3;
  u32x4 kk[11], vv[8], sh[4];
  { const int row = 64 * xw + lane; const u64 t0 = __builtin_amdgcn_s_memrealtime();
    u32x4 gx = ld16<SC1>(rws, W_G0X + (id.tp * 128 + row) * 16, 0);
    BAR(); BAR();
    while (!__all(sealed(gx, tag)) && !expired(t0)) { __builtin_amdgcn_s_sleep(4); FENCE(); gx = ld16<SC1>(rws, W_G0X + (id.tp * 128 + row) * 16, 0); }
    const float var = __uint_as_float(gx.y), r = __builtin_amdgcn_rsqf(var);
    *(LAS unsigned*)(sm + T_M1 + row * 4) = gx.x; *(LAS float*)(sm + T_R1 + row * 4) = r;
    *(LAS float*)(sm + T_A0 + row * 4) = GC0 * r; *(LAS float*)(sm + T_A1 + row * 4) = GC1 * r * r * r; *(LAS float*)(sm + T_IR + row * 4) = var * r;
    WAITL(0); BAR();
  }
  STAMP(1);
  { const rsrc_t rk = mkrs(P.k, S * D * 2), rv = mkrs(P.v, S * D * 2), rs = mkrs(P.shk, D * 4 * 2), rwo = mkrs(P.wout, FH * D * 2);
    const int t0 = 64 * tb + 8 * rg; const unsigned col = (64 * db + 8 * cc) * 2;
    if (kon) {
#pragma unroll
      for (int i = 0; i < 11; ++i) kk[i] = ld16<0>(rk, t0 + i - 3 >= 0 ? (unsigned)(t0 + i - 3) * (D * 2) + col : 0xF0000000u, 0);
    }
    BAR();
    if (kon) {
#pragma unroll
      for (int i = 0; i < 8; ++i) vv[i] = ld16<0>(rv, t0 * (D * 2) + col, i * (D * 2));
#pragma unroll
      for (int i = 0; i < 4; ++i) sh[i] = ld16<0>(rs, col * 4, i * 16);
    }
    BAR();
    if (won) { const int fb0 = wks < 64 ? 144 * (wks >> 2) + 32 * (wks & 3) : 288 * (wks - 64) + 128, fst = wks < 64 ? 0 : 128;
#pragma unroll
      for (int i = 0; i < 9; ++i) { const int ci = 64 * i + lane, rho = __umul24(ci, 3641) >> 16, ch = ci - rho * 18;
        dma_hidden(rwo, stg + i * 1024, (fb0 + rho + (rho >> 4) * fst) * (D * 2) + wu * 288 + ch * 16); } }
    BAR();
    if (kon) {
#pragma unroll
      for (int i = 0; i < 4; ++i) asm volatile("" : "+v"(sh[i]), "+v"(vv[i]), "+v"(vv[4 + i]), "+v"(kk[i]), "+v"(kk[4 + i]), "+v"(kk[i == 3 ? 0 : 8 + i])); }
    if (OPT_TWSP) { const rsrc_t r = mkrs(P.wsp, S * S * 2);
#pragma unroll
      for (int i = 0; i < 2; ++i) asm volatile("buffer_load_dword %0, %1, %2, 0 offen" : "+v"(sink) : "v"(id.b * 32768 + ((2 * i + xw) * 64 + lane) * 128), "s"(r) : "memory"); }
  }
  BAR();
  u32x4 ko[8];
  auto kvstep = [&](int e) {
    if (kon) {
      float c[4], kf[11], y[8];
#pragma unroll
      for (int j = 0; j < 4; ++j) { const unsigned w = sh[e >> 1][(e & 1) * 2 + (j >> 1)]; c[j] = (j & 1) ? bhi(w) : blo(w); }
#pragma unroll
      for (int i = 0; i < 11; ++i) { const unsigned w = kk[i][e >> 1]; kf[i] = (e & 1) ? bhi(w) : blo(w); }
#pragma unroll
      for (int i = 0; i < 8; ++i) { const unsigned w = vv[i][e >> 1];
        float a = c[3] * kf[i]; asm volatile("" : "+v"(a)); a = fmaf(c[2], kf[i + 1], a); asm volatile("" : "+v"(a)); a = fmaf(c[1], kf[i + 2], a); asm volatile("" : "+v"(a)); a = fmaf(c[0], kf[i + 3], a);
        asm volatile("" : "+v"(a)); y[i] = a * ((e & 1) ? bhi(w) : blo(w)); asm volatile("" : "+v"(y[i])); }
      ko[e] = (u32x4){pk(y[7], y[6]), pk(y[5], y[4]), pk(y[3], y[2]), pk(y[1], y[0])};
      asm volatile("" : "+v"(ko[e]));
    } };
  { const int row = 64 * xw + lane;
    const f32x2 a = lds<f32x2>(sm + T_PART + row * 8) + lds<f32x2>(sm + T_PART + 1024 + row * 8);
    st16<SC1>(seal(a.x, a.y, tag), rws, W_G1S + ((id.tp * 16 + id.ft) * 128 + row) * 16, 0);
    kvstep(0); BAR(); kvstep(1); BAR();
    u32x4 gs[16];
    auto gather = [&]() {
#pragma unroll
      for (int i = 0; i < 16; ++i) gs[i] = ld16<SC1>(rws, W_G1S + (id.tp * 16 * 128 + row) * 16, i * 2048); };
    unsigned bad = 0; float s = 0.f, q = 0.f;
    auto sum = [&](int lo, int hi) {
#pragma unroll
      for (int i = lo; i < hi; ++i) { bad |= (gs[i].x ^ gs[i].z ^ tag.x) | (gs[i].y ^ gs[i].w ^ tag.y); s += __uint_as_float(gs[i].x); q += __uint_as_float(gs[i].y); }
      asm volatile("" : "+v"(bad), "+v"(s), "+v"(q)); };
    gather(); kvstep(2); BAR(); kvstep(3); BAR(); sum(0, 10); BAR();
    sum(10, 16);
    if (__builtin_expect(!__all(bad == 0), 0)) { const u64 t0 = __builtin_amdgcn_s_memrealtime();
      do { FENCE(); gather(); bad = 0; s = 0.f; q = 0.f; sum(0, 16); } while (!__all(bad == 0) && !expired(t0)); }
    const float m = s * (1.0f / FH), var = fmaxf(q * (1.0f / FH) - m * m, 0.f), r = __builtin_amdgcn_rsqf(var + 1e-5f);
    *(LAS float*)(sm + T_R2 + row * 4) = r; *(LAS float*)(sm + T_N2 + row * 4) = -m * r;
    asm volatile("s_waitcnt vmcnt(0) lgkmcnt(0)" : "+v"(sink) :: "memory"); BAR();
  }
  STAMP(2);
  kvstep(4); BAR(); kvstep(5); BAR();
  if (xw == 0) set_flag(rws, W_F2 + (id.ft * 16 + id.tp) * 8, tag, lane);
  kvstep(6); kvstep(7); BAR();
  { u32x4 wf[9];
    if (won) {
      const int q = lane >> 4, qq = (lane & 15) >> 2, p = lane & 3;
      LAS char* base = stg + (4 * q + qq) * 288;
#pragma unroll
      for (int T = 0; T < 9; ++T) { const int cb = T < 4 ? 32 * (T >> 1) + 4 * (T & 1) : T == 4 ? 64 : 80 + 32 * ((T - 5) >> 1) + 4 * ((T - 5) & 1);
        LAS char* a = base + cb * 2 + p * (T == 4 ? 8 : 16);
        wf[T] = __builtin_bit_cast(u32x4, cat(ldtr(a), ldtr(a + 16 * 288))); }
      WAITL(0);
    }
    if (kon) {
#pragma unroll
      for (int e = 0; e < 8; ++e) *(LAS u32x4*)(stg + (8 * cc + e) * 128 + (((7 - rg) ^ cc) * 16)) = ko[e];
#pragma unroll
      for (int i = 0; i < 8; ++i) ko[i] = lds<u32x4>(stg + (8 * i + rg) * 128 + ((cc ^ i) * 16));
    }
    const u64 t0 = __builtin_amdgcn_s_memrealtime();
    while (*(volatile LAS unsigned*)(sm + T_GO) == 0u && !expired(t0)) __builtin_amdgcn_s_sleep(2);
    FENCE();
    if (won) {
#pragma unroll
      for (int T = 0; T < 9; ++T) st16<SC1>(wf[T], rws, W_WOF + lane * 16, ((wu * 72 + wks) * 9 + T) * 1024); }
    if (kon) {
#pragma unroll
      for (int i = 0; i < 8; ++i) st16<SC1>(ko[i], rws, W_KVT + (64 * db + rg) * (S * 2) + (S - 64 * (tb + 1)) * 2 + cc * 16, 8 * i * (S * 2)); }
  }
  BAR(); BAR(); BAR();
  const unsigned h0a = ((const CAS unsigned*)P.ssmk)[id.b * S], h0b = ((const CAS unsigned*)P.ssmk)[(xw ? 2 * id.b + 1 : 512 + (id.b >> 2)) * (S / 2)];
  BAR(); WAITV(0); BAR();
  if (xw == 0) set_flag(rws, W_F5 + id.b * 8, tag, lane);
  STAMP(3);
#pragma unroll 1
  for (int i = 0; i < 22; ++i) BAR();
  { u32x4 f0 = {tag.x, tag.y, tag.x, tag.y}, f1 = f0;
    if (xw == 0) { f0 = ld16<SC1>(rws, W_F5 + lane * 16, 0); f1 = ld16<SC1>(rws, W_F5 + lane * 16, 1024); }
    BAR(); BAR();
    if (!__all(flags_ok(f0, tag) && flags_ok(f1, tag))) wait_flags<2, 8>(rws, W_F5 + lane * 16, tag); }
  BAR();
  STAMP(4);
  { const rsrc_t rh = mkrs(P.ssmk, D * S * 2);
    auto fetch = [&](int ci) { const int chn = ci == 2 ? 512 + (id.b >> 2) : 2 * id.b + ci; LAS char* cb = sm + CV + ci * CV_CH;
      const unsigned h0 = ci == 0 ? h0a : h0b;
      if (lane < 8) { *(LAS unsigned*)(cb + lane * 4) = 0u; *(LAS unsigned*)(cb + CV_O + lane * 4) = lane == 7 ? h0 << 16 : 0u; }
      if (lane < 38) *(LAS u32x4*)(cb + CV_U + 4096 + lane * 16) = (u32x4){0u, 0u, 0u, 0u};
      if (ci == 2) { *(LAS u32x4*)(cb + CV_U + 4704 + lane * 16) = (u32x4){0u, 0u, 0u, 0u}; *(LAS u32x4*)(cb + CV_U + 5728 + lane * 16) = (u32x4){0u, 0u, 0u, 0u}; }
      FENCE();
#pragma unroll
      for (int i = 0; i < 4; ++i) { dma<0>(rh, cb + 32 + i * 1024, lane * 16, chn * (S * 2) + i * 1024);
        dma<0>(rh, cb + CV_O + 32 + i * 1024, lane * 16 + 2, chn * (S * 2) + i * 1024);
        dma<SC1>(rws, cb + CV_U + i * 1024, lane * 16, W_KVT + chn * (S * 2) + i * 1024); } };
    if (xw == 0) fetch(0);
    BAR();
    fetch(2 - xw);
    BAR();
    WAITV(0); WAITL(0); BAR(); BAR();
  }
  BAR();
  if (xw == 0) { set_flag(rws, W_F3 + (id.tp * 16 + id.ft) * 8, tag, lane); wait_flags<1, 1>(rws, W_F3 + id.tp * 128 + (lane & 7) * 16, tag); }
  STAMP(5);
  BAR();
  if (xw == 1 && (OPT_TG3 & 4)) {
#pragma unroll
    for (int i = 0; i < 4; ++i) { const unsigned n = i * 64 + lane, o = n < 240 ? W_HF + ((id.tp * 8 + 4 * (id.ft >> 3) + (n & 3)) * 72 + 18 * ((n >> 2) & 3) + 3 + (n >> 4)) * 1024 + (id.ft & 7) * 128 : 0xF0000000u;
      asm volatile("buffer_load_dword %0, %1, %2, 0 offen sc1" : "+v"(sink) : "v"(o), "s"(rws) : "memory"); } }
  if (xw == 0) {
    const u64 t0 = __builtin_amdgcn_s_memrealtime();
    while (*(volatile LAS unsigned*)(sm + T_CNT) != 4u && !expired(t0)) __builtin_amdgcn_s_sleep(2);
    FENCE();
    set_flag(rws, W_F4 + id.b * 8, tag, lane);
    const int g = id.ft & 7, d0 = 144 * (g >> 1) + 64 * (g & 1);
    { const rsrc_t rx = mkrs(P.x, S * D * 2);
#pragma unroll
      for (int i = 0; i < 10; ++i) { const unsigned ci = 64 * i + lane, r = __umul24(ci, 6554) >> 16; dma<0>(rx, sm + XTILE + i * 1024, (id.tp * 128 + 64 * (id.ft >> 3) + r) * (D * 2) + d0 * 2 + (ci - 10 * r) * 16, 0); } }
    __builtin_amdgcn_s_sleep(24); wait_flags<2, 12>(rws, W_F4 + lane * 16, tag);
#pragma unroll
    for (int i = 0; i < 10; ++i) dma<SC1>(rws, sm + YTILE + i * 1024, W_YT + (d0 + (lane >> 3)) * (S * 2) + (id.tp * 128 + 64 * (id.ft >> 3)) * 2 + (lane & 7) * 16, i * 8 * (S * 2));
    WAITV(0);
    asm volatile("ds_write_b32 %0, %1" :: "v"((unsigned)(size_t)(sm + T_YOK)), "v"(1u) : "memory");
    if (id.b == 0 && lane == 0) st8<SC1>((u32x2){(unsigned)(epoch + 1), (unsigned)((epoch + 1) >> 32)}, rws, W_EPOCH, 0);
  }
  STAMP(6);
  asm volatile("s_waitcnt vmcnt(0)" : "+v"(sink) :: "memory"); BAR();
}

DEV void role_C(const Params& P, LAS char* sm, const Ids id, const int wid STAMP_ARG) {
  int bcount = 0; (void)bcount;
  const int lane = id.lane, wt = wid >> 1, wf = wid & 1, c = lane & 15, q = lane >> 4;
  const rsrc_t rws = mkrs(P.ws, W_END);
  __builtin_amdgcn_s_setprio(1);
  unsigned dm = ((const CAS unsigned*)P.ssmd)[id.b], dsh = ((const CAS unsigned*)P.ssmd)[256 + (id.b >> 3)];
  asm volatile("" : "+s"(dm), "+s"(dsh));
  f32x4 acc[4][4], accx[2], csa[3];
  bf16x8 tf[2][4], ff[2][5], ones;
  LAS char *ta[2][2], *fb[2][2], *fx[2];
#pragma unroll
  for (int s = 0; s < 2; ++s) {
    const int o = (32 * (s ^ wt) + 16 * (q >> 1) + 4 * (q & 1) + (c >> 2)) * 288 + 8 * (c & 3);
    fx[s] = sm + BBASE + o + 256;
#pragma unroll
    for (int h = 0; h < 2; ++h) { ta[s][h] = sm + (64 * wt + 32 * (h ^ wf) + c) * 128 + (((q + 4 * (s ^ wt)) ^ ((c >> 1) & 7)) * 16); fb[s][h] = sm + BBASE + o + wf * 128 + (h ^ wt) * 64; }
  }
#pragma unroll
  for (int e = 0; e < 8; ++e) ones[e] = 0x3f80;
  asm volatile("" : "+v"(ones));
  auto rd = [&](auto mode, int b, int as, int bs, int s) {
    constexpr int MODE = decltype(mode)::value;
#pragma unroll
    for (int j = 0; j < 4; ++j) { LAS char* p = fb[s][j >> 1] + bs * BSLOT;
      ff[b][j] = MODE < 2 ? cat(ldtr(p + (j & 1) * 32), ldtr(p + (j & 1) * 32 + 8 * 288)) : lds<bf16x8>(p + (j & 1) * 1024); }
#pragma unroll
    for (int i = 0; i < 4; ++i) tf[b][i] = lds<bf16x8>(ta[s][i >> 1] + as * ASLOT + (i & 1) * 2048);
    ff[b][4] = MODE < 2 ? cat(ldtr(fx[s] + bs * BSLOT), ldtr(fx[s] + bs * BSLOT + 8 * 288)) : lds<bf16x8>(fx[s] + bs * BSLOT);
  };
  auto mm = [&](auto mode, int b, int lo, int hi) {
    constexpr int MODE = decltype(mode)::value;
#pragma unroll
    for (int n = 0; n < (MODE == 2 ? 18 : 21 - b); ++n) if (n >= lo && n < hi) {
      if (n < 16) { const int i = n >> 2, j = (i & 1) ? 3 - (n & 3) : (n & 3);
        acc[i][j] = MODE == 0 ? MFMA(tf[b][i], ff[b][j], acc[i][j]) : MFMA(ff[b][j], tf[b][i], acc[i][j]); }
      else if (n < 18) accx[n - 16] = MODE == 0 ? MFMA(tf[b][n - 16], ff[b][4], accx[n - 16]) : MFMA(ff[b][4], tf[b][n - 16], accx[n - 16]);
      else { const int j = n == 20 ? 4 : n - 18; csa[n - 18] = MODE == 0 ? MFMA(ones, ff[b][j], csa[n - 18]) : MFMA(ff[b][j], ones, csa[n - 18]); }
    }
  };
  auto stage = [&](auto mode, int as, int bs, bool drain) {
    constexpr int MODE = decltype(mode)::value;
    mm(mode, 1, 0, 4); SB();
    if (drain) WAITV(0);
    WAITL(0); BAR(); SB();
    rd(mode, 0, as, bs, 0); mm(mode, 1, 4, 21);
    if (MODE < 2) { for (int i = 0; i < 14; ++i) { SG(0x100, 1); SG(0x8, 1); } SG(0x8, 2); }
    else { for (int i = 0; i < 4; ++i) { SG(0x100, 1); SG(0x8, 1); SG(0x100, 1); SG(0x8, 2); } SG(0x100, 1); SG(0x8, 2); }
    SB();
    rd(mode, 1, as, bs, 1); mm(mode, 0, 0, 21);
    if (MODE < 2) { for (int i = 0; i < 7; ++i) { SG(0x100, 1); SG(0x8, 1); SG(0x100, 1); SG(0x8, 2); } }
    else { for (int i = 0; i < 9; ++i) { SG(0x100, 1); SG(0x8, 2); } }
    SB();
  };
  auto first = [&](auto mode, int as, int bs) { WAITL(0); BAR(); SB(); rd(mode, 0, as, bs, 0); rd(mode, 1, as, bs, 1); mm(mode, 0, 0, 21); SB(); };
  auto zero = [&]() {
#pragma unroll
    for (int i = 0; i < 4; ++i)
#pragma unroll
      for (int j = 0; j < 4; ++j) acc[i][j] = (f32x4){0.f, 0.f, 0.f, 0.f};
    accx[0] = accx[1] = csa[0] = csa[1] = csa[2] = (f32x4){0.f, 0.f, 0.f, 0.f}; };
  const std::integral_constant<int, 0> M0; const std::integral_constant<int, 1> M1; const std::integral_constant<int, 2> M2;
  const int jg0 = 4 * wf + 2 * wt, jg2 = 4 * wf + 2 - 2 * wt;

  zero();
  first(M0, 0, 0);
  STAMP(0);
#pragma unroll 1
  for (int it = 0; it < 2; ++it) { stage(M0, 1, 1, false); stage(M0, 2, 2, false); stage(M0, 3, 3, false); stage(M0, 0, 0, false); }
  mm(M0, 1, 18, 21); mm(M0, 1, 0, 8); SB();
  if (q == 0) { *(LAS float*)(sm + T_CS2 + (jg0 * 16 + c) * 4) = csa[0][0]; *(LAS float*)(sm + T_CS2 + (jg0 * 16 + 16 + c) * 4) = csa[1][0]; *(LAS float*)(sm + T_CS2 + (128 + 16 * wt + c) * 4) = csa[2][0]; }
  mm(M0, 1, 8, 18); SB();
  WAITL(0); BAR();
  STAMP(1);
  u32x4 z2p[2][4], z2x;
  { float cs[5];
    cs[0] = csa[0][0]; cs[1] = csa[1][0]; cs[2] = lds<float>(sm + T_CS2 + (jg2 * 16 + c) * 4); cs[3] = lds<float>(sm + T_CS2 + (jg2 * 16 + 16 + c) * 4);
    cs[4] = csa[2][0] + lds<float>(sm + T_CS2 + (144 - 16 * wt + c) * 4);
    float red[2][16];
#pragma unroll
    for (int i = 0; i < 4; ++i) {
      const int ro = (64 * wt + 32 * ((i >> 1) ^ wf) + 8 * q + 4 * (i & 1)) * 4;
      const f32x4 m = lds<f32x4>(sm + T_M1 + ro), a0 = lds<f32x4>(sm + T_A0 + ro), a1 = lds<f32x4>(sm + T_A1 + ro), ir = lds<f32x4>(sm + T_IR + ro);
      f32x4 su = {0.f, 0.f, 0.f, 0.f}, sq = {0.f, 0.f, 0.f, 0.f};
#pragma unroll
      for (int j = 0; j < (i < 2 ? 5 : 4); ++j) {
        const f32x4 z = gelu4((j < 4 ? acc[i][j] : accx[i]) - m * cs[j], a0, a1, ir);
        su = j ? su + z : z; sq = j ? sq + z * z : z * z;
        u32x4& dst = j < 4 ? z2p[i >> 1][j] : z2x;
        dst[2 * (i & 1)] = pk(z[0], z[1]); dst[2 * (i & 1) + 1] = pk(z[2], z[3]);
      }
#pragma unroll
      for (int e = 0; e < 4; ++e) { red[0][4 * i + e] = su[e]; red[1][4 * i + e] = sq[e]; }
    }
    float r8[2][8], r4[2][4];
#pragma unroll
    for (int st = 0; st < 2; ++st) {
#pragma unroll
      for (int e = 0; e < 8; e += 4)
        asm volatile("s_nop 1\n\t"
                     "v_add_f32_dpp %0, %4, %4 row_ror:8 row_mask:0xf bank_mask:0x3\n\tv_add_f32_dpp %1, %5, %5 row_ror:8 row_mask:0xf bank_mask:0x3\n\t"
                     "v_add_f32_dpp %2, %6, %6 row_ror:8 row_mask:0xf bank_mask:0x3\n\tv_add_f32_dpp %3, %7, %7 row_ror:8 row_mask:0xf bank_mask:0x3\n\t"
                     "v_add_f32_dpp %0, %8, %8 row_ror:8 row_mask:0xf bank_mask:0xc\n\tv_add_f32_dpp %1, %9, %9 row_ror:8 row_mask:0xf bank_mask:0xc\n\t"
                     "v_add_f32_dpp %2, %10, %10 row_ror:8 row_mask:0xf bank_mask:0xc\n\tv_add_f32_dpp %3, %11, %11 row_ror:8 row_mask:0xf bank_mask:0xc"
                     : "=&v"(r8[st][e]), "=&v"(r8[st][e + 1]), "=&v"(r8[st][e + 2]), "=&v"(r8[st][e + 3])
                     : "v"(red[st][e]), "v"(red[st][e + 1]), "v"(red[st][e + 2]), "v"(red[st][e + 3]), "v"(red[st][8 + e]), "v"(red[st][9 + e]), "v"(red[st][10 + e]), "v"(red[st][11 + e]));
      asm volatile("s_nop 1\n\t"
                   "v_add_f32_dpp %0, %4, %4 row_half_mirror row_mask:0xf bank_mask:0x5\n\tv_add_f32_dpp %1, %5, %5 row_half_mirror row_mask:0xf bank_mask:0x5\n\t"
                   "v_add_f32_dpp %2, %6, %6 row_half_mirror row_mask:0xf bank_mask:0x5\n\tv_add_f32_dpp %3, %7, %7 row_half_mirror row_mask:0xf bank_mask:0x5\n\t"
                   "v_add_f32_dpp %0, %8, %8 row_half_mirror row_mask:0xf bank_mask:0xa\n\tv_add_f32_dpp %1, %9, %9 row_half_mirror row_mask:0xf bank_mask:0xa\n\t"
                   "v_add_f32_dpp %2, %10, %10 row_half_mirror row_mask:0xf bank_mask:0xa\n\tv_add_f32_dpp %3, %11, %11 row_half_mirror row_mask:0xf bank_mask:0xa\n\t"
                   "v_add_f32_dpp %0, %0, %0 quad_perm:[1,0,3,2] row_mask:0xf bank_mask:0xf\n\tv_add_f32_dpp %1, %1, %1 quad_perm:[1,0,3,2] row_mask:0xf bank_mask:0xf\n\t"
                   "v_add_f32_dpp %2, %2, %2 quad_perm:[1,0,3,2] row_mask:0xf bank_mask:0xf\n\tv_add_f32_dpp %3, %3, %3 quad_perm:[1,0,3,2] row_mask:0xf bank_mask:0xf\n\t"
                   "v_add_f32_dpp %0, %0, %0 quad_perm:[2,3,0,1] row_mask:0xf bank_mask:0xf\n\tv_add_f32_dpp %1, %1, %1 quad_perm:[2,3,0,1] row_mask:0xf bank_mask:0xf\n\t"
                   "v_add_f32_dpp %2, %2, %2 quad_perm:[2,3,0,1] row_mask:0xf bank_mask:0xf\n\tv_add_f32_dpp %3, %3, %3 quad_perm:[2,3,0,1] row_mask:0xf bank_mask:0xf"
                   : "=&v"(r4[st][0]), "=&v"(r4[st][1]), "=&v"(r4[st][2]), "=&v"(r4[st][3])
                   : "v"(r8[st][0]), "v"(r8[st][1]), "v"(r8[st][2]), "v"(r8[st][3]), "v"(r8[st][4]), "v"(r8[st][5]), "v"(r8[st][6]), "v"(r8[st][7]));
    }
    if ((c & 3) == 0) { LAS char* pp = sm + T_PART + wf * 1024 + (64 * wt + 32 * ((c >> 3) ^ wf) + 8 * q + 4 * ((c >> 2) & 1)) * 8;
      *(LAS f32x4*)pp = (f32x4){r4[0][0], r4[1][0], r4[0][1], r4[1][1]}; *(LAS f32x4*)(pp + 16) = (f32x4){r4[0][2], r4[1][2], r4[0][3], r4[1][3]}; }
#pragma unroll
    for (int i = 0; i < 2; ++i) asm volatile("" : "+v"(z2p[i][0]), "+v"(z2p[i][1]), "+v"(z2p[i][2]), "+v"(z2p[i][3]));
    asm volatile("" : "+v"(z2x));
  }
  STAMP(2);
  zero();
  first(M1, 0, 1);
  stage(M1, 3, 2, false); stage(M1, 2, 3, false); stage(M1, 1, 0, false);
  stage(M1, 0, 1, false); stage(M1, 3, 2, false); stage(M1, 2, 3, false);
  STAMP(3);
  {
#pragma unroll
    for (int i = 0; i < 2; ++i) asm volatile("" : "+v"(z2p[i][0]), "+v"(z2p[i][1]), "+v"(z2p[i][2]), "+v"(z2p[i][3]));
    asm volatile("" : "+v"(z2x));
#pragma unroll
    for (int ip = 0; ip < 2; ++ip) {
      const int ro = (64 * wt + 32 * (ip ^ wf) + 8 * q) * 4;
      const f32x4 r0 = lds<f32x4>(sm + T_R2 + ro), r1 = lds<f32x4>(sm + T_R2 + ro + 16), n0 = lds<f32x4>(sm + T_N2 + ro), n1 = lds<f32x4>(sm + T_N2 + ro + 16);
      const unsigned so = W_ZF + ((id.ft * 64 + 4 * id.tp + 2 * wt + (ip ^ wf)) * 9) * 1024;
#pragma unroll
      for (int j = 0; j < (ip == 0 ? 5 : 4); ++j) { const u32x4 z = j < 4 ? z2p[ip][j] : z2x;
        const f32x4 a = (f32x4){blo(z[0]), bhi(z[0]), blo(z[1]), bhi(z[1])} * r0 + n0, b = (f32x4){blo(z[2]), bhi(z[2]), blo(z[3]), bhi(z[3])} * r1 + n1;
        st16<SC1>((u32x4){pk(a[0], a[1]), pk(a[2], a[3]), pk(b[0], b[1]), pk(b[2], b[3])}, rws, lane * 16, so + (j < 2 ? jg0 + j : j < 4 ? jg2 + j - 2 : 8) * 1024); }
    }
    SB();
  }
  unsigned braw[4];
  { const rsrc_t rb = mkrs(P.bsp, S * 2);
#pragma unroll
    for (int i = 0; i < 4; ++i) braw[i] = __builtin_amdgcn_raw_buffer_load_b16(rb, (id.tp * 128 + 64 * wt + prow(i ^ (2 * wf), c)) * 2, 0, 0); }
  STAMP(4);
  stage(M1, 1, 0, false); stage(M1, 0, 1, true);
  mm(M1, 1, 18, 21); mm(M1, 1, 0, 8); SB();
  if (c == 0) { *(LAS f32x4*)(sm + T_CS1 + (jg0 * 16 + 4 * q) * 4) = csa[0]; *(LAS f32x4*)(sm + T_CS1 + (jg0 * 16 + 16 + 4 * q) * 4) = csa[1]; *(LAS f32x4*)(sm + T_CS1 + (128 + 16 * wt + 4 * q) * 4) = csa[2]; }
  mm(M1, 1, 8, 18); SB();
  WAITL(0); BAR();
  STAMP(5);
  u32x2 z1p[4][4], z1x[2];
  { f32x4 cs[5];
    cs[0] = csa[0]; cs[1] = csa[1]; cs[2] = lds<f32x4>(sm + T_CS1 + (jg2 * 16 + 4 * q) * 4); cs[3] = lds<f32x4>(sm + T_CS1 + (jg2 * 16 + 16 + 4 * q) * 4);
    cs[4] = csa[2] + lds<f32x4>(sm + T_CS1 + (144 - 16 * wt + 4 * q) * 4);
#pragma unroll
    for (int i = 0; i < 4; ++i) { const int ro = (64 * wt + prow(i ^ (2 * wf), c)) * 4;
      const float m = lds<float>(sm + T_M1 + ro), a0 = lds<float>(sm + T_A0 + ro), a1 = lds<float>(sm + T_A1 + ro), ir = lds<float>(sm + T_IR + ro), bias = blo(braw[i]);
#pragma unroll
      for (int j = 0; j < (i < 2 ? 5 : 4); ++j) {
        f32x4& a = j < 4 ? acc[i][j] : accx[i];
        const f32x4 z = gelu4(a - m * cs[j], a0, a1, ir);
        (j < 4 ? z1p[i][j] : z1x[i]) = (u32x2){pk(z[0], z[1]), pk(z[2], z[3])};
        a = (f32x4){bias, bias, bias, bias};
      } }
#pragma unroll
    for (int s = 0; s < 2; ++s) { fx[s] = sm + BBASE + (s * 9 + 8) * 1024 + lane * 16;
#pragma unroll
      for (int h = 0; h < 2; ++h) { fb[s][h] = sm + BBASE + (s * 9 + (h ? jg2 : jg0)) * 1024 + lane * 16; ta[s][h] = sm + (64 * wt + 32 * (h ^ wf) + c) * 128 + (((q + 4 * s) ^ ((c >> 1) & 7)) * 16); } }
#pragma unroll
    for (int i = 0; i < 4; ++i) asm volatile("" : "+v"(z1p[i][0]), "+v"(z1p[i][1]), "+v"(z1p[i][2]), "+v"(z1p[i][3]), "+v"(acc[i][0]), "+v"(acc[i][1]), "+v"(acc[i][2]), "+v"(acc[i][3]));
    asm volatile("" : "+v"(z1x[0]), "+v"(z1x[1]), "+v"(accx[0]), "+v"(accx[1]));
  }
  STAMP(6);
  const int jq = id.b & 3;
  LAS char *cs_ = sm + CV + 2 * CV_CH, *cb = sm + CV + wt * CV_CH;
  LAS char *am[4], *um = cb + CV_U + (2032 - 16 * c + 8 * q) * 2 + 256 * wf - 3584;
#pragma unroll
  for (int i = 0; i < 4; ++i) { am[i] = cb + ((c & 1) ? 0 : CV_O) + (4 * q + ((c + 1) >> 1)) * 4 + 256 * wf + 1024 * i; asm volatile("" : "+v"(am[i])); }
  asm volatile("" : "+v"(um));
  bf16x8 ca[2][8], cu[2][12];
  auto afrag = [&](LAS char* p) { const unsigned d0 = lds<unsigned>(p), d1 = lds<unsigned>(p + 4), d2 = lds<unsigned>(p + 8), d3 = lds<unsigned>(p + 12);
    return __builtin_bit_cast(bf16x8, (u32x4){d0, d1, d2, d3}); };
  auto lds_mn = [&](int b, int kk) {
#pragma unroll
    for (int e = 0; e < 8; ++e) { cu[b][e] = lds<bf16x8>(um + kk * 64 + 512 * (7 - e)); if (e == 0) ca[b][0] = afrag(am[0] + kk * 64); }
#pragma unroll
    for (int e = 1; e < 8; ++e) ca[b][e] = afrag(am[e >> 1] + kk * 64 + (e & 1) * 512); };
  first(M2, 3, 2);
  STAMP(7);
#pragma unroll 1
  for (int it = 0; it < 7; ++it) { stage(M2, 2, 3, false); stage(M2, 1, 0, false); stage(M2, 0, 1, false); stage(M2, 3, 2, false); }
  stage(M2, 2, 3, false); stage(M2, 1, 0, false); stage(M2, 0, 1, false);
  mm(M2, 1, 0, 4); SB(); WAITL(0); BAR(); SB();
  mm(M2, 1, 4, 18); SB();
  lds_mn(0, 0); SB();
  STAMP(8);
#pragma unroll
  for (int i = 0; i < 4; ++i) asm volatile("" : "+v"(z1p[i][0]), "+v"(z1p[i][1]), "+v"(z1p[i][2]), "+v"(z1p[i][3]));
  asm volatile("" : "+v"(z1x[0]), "+v"(z1x[1]));
#pragma unroll
  for (int i = 0; i < 4; ++i) {
    const unsigned so = W_HF + (id.tp * 8 + 4 * wt + (i ^ (2 * wf))) * 72 * 1024;
    auto gate = [&](u32x2 z, f32x4 a) { const f32x4 h = (f32x4){blo(z.x), bhi(z.x), blo(z.y), bhi(z.y)} * a; return (u32x2){pk(h[0], h[1]), pk(h[2], h[3])}; };
#pragma unroll
    for (int pp = 0; pp < 2; ++pp) { const u32x2 lo = gate(z1p[i][2 * pp], acc[i][2 * pp]), hi = gate(z1p[i][2 * pp + 1], acc[i][2 * pp + 1]);
      st16<SC1>((u32x4){lo.x, lo.y, hi.x, hi.y}, rws, lane * 16, so + (4 * id.ft + 2 * wf + (pp ^ wt)) * 1024); }
    if (i < 2) st8<SC1>(gate(z1x[i], accx[i]), rws, lane * 16 + (id.ft & 1) * 8, so + (64 + (id.ft >> 1)) * 1024);
  }
  const int g = id.ft & 7, gp = g & 1, half = id.ft >> 3, rot = krot(id);
  const unsigned wso = W_WOF + ((((EXP & 4) ? 0 : g >> 1) * 72 + 18 * wid) * 9) * 1024;
  u32x4 wr[WD][5];
  auto ldw = [&](int slot, int i) {
#pragma unroll
    for (int t = 0; t < 5; ++t) wr[slot][t] = ld16<(EXP & 8) ? 0 : SC1>(rws, lane * 16, wso + kwrap(i, rot) * 9216 + (t == 4 ? 4 : t + gp * 5) * 1024); };
  SB();
  SB(); BAR(); SB();
  STAMP(9);
  u32x4 hr[3][4];
  const unsigned hso = W_HF + (((EXP & 2) ? (id.b & 7) * 4 : id.tp * 8 + 4 * half) * 72 + 18 * wid) * 1024;
  auto ldh = [&](int slot, int i) {
#pragma unroll
    for (int t = 0; t < 4; ++t) hr[slot][t] = ld16<(EXP & 8) ? 0 : SC1>(rws, lane * 16, hso + (((unsigned)t ^ (wf ^ (2 * gp))) * 72 + kwrap(i, rot)) * 1024); };
  { const float dvm = wt ? bhi(dm) : blo(dm), dvs = (id.b & 4) ? bhi(dsh) : blo(dsh);
    f32x4 cm[8], s0 = {0.f, 0.f, 0.f, 0.f}, s1 = s0;
#pragma unroll
    for (int j = 0; j < 8; ++j) cm[j] = s0;
    LAS char *as[4], *u1, *u0;
    auto addr_sh = [&]() {
      const int da = (2 - wt) * CV_CH + 128 * wid - 256 * wf;
#pragma unroll
      for (int i = 0; i < 4; ++i) { as[i] = am[i] + da; asm volatile("" : "+v"(as[i])); }
      u1 = um + da + 512 * jq; u0 = um + da + 512 * (7 - jq); asm volatile("" : "+v"(u1), "+v"(u0)); };
    auto lds_sh = [&](int b, int kk) {
#pragma unroll
      for (int e = 0; e < 8; ++e) { ca[b][e] = afrag(as[e >> 1] + kk * 64 + (e & 1) * 512); cu[b][e] = lds<bf16x8>(u1 + kk * 64 + 512 * e);
        if (e < 4) cu[b][8 + e] = lds<bf16x8>(u0 + kk * 64 + 512 * e); } };
    auto mm_sh = [&](int b) {
#pragma unroll
      for (int e = 0; e < 8; ++e) { s1 = MFMA(ca[b][e], cu[b][e], s1); if (e < 4) s0 = MFMA(ca[b][e], cu[b][8 + e], s0); } };
    auto mm_mn = [&](int b) {
#pragma unroll
      for (int e = 0; e < 8; ++e)
#pragma unroll
        for (int j = e; j < 8; ++j) cm[j] = MFMA(ca[b][e], cu[b][j - e], cm[j]); };
#define il(nr, per) do { for (int i_ = 0; i_ < nr; ++i_) { SG(0x100, per); SG(0x8, 1); } SG(0x8, 36); SB(); } while (0)
#define ilv() do { for (int i_ = 0; i_ < 5; ++i_) { SG(0x100, 1); SG(0x8, 1); SG(0x100, 1); SG(0x8, 1); SG(0x100, 1); SG(0x8, 1); SG(0x100, 1); SG(0x8, 1); SG(0x100, 2); SG(0x8, 2); SG(0x20, 1); SG(0x8, 1); } SG(0x8, 36); SB(); } while (0)
    lds_mn(1, 1); mm_mn(0); il(24, 1); STAMP(15);
    lds_mn(0, 2); mm_mn(1); il(24, 1); STAMP(16); addr_sh(); SB();
    lds_sh(1, 0); mm_mn(0); il(28, 1);
    lds_sh(0, 1); mm_sh(1); il(10, 3);
    lds_mn(1, 3); mm_sh(0); il(12, 2);
    if (wid != 0) *(LAS f32x4*)(sm + CV_PS + (wid - 1) * 1024 + lane * 16) = s0;
    if (wid != 1) *(LAS f32x4*)(sm + CV_PS + (3 + (wid ? wid - 1 : 0)) * 1024 + lane * 16) = s1;
    SB(); STAMP(17); WAITV(0); WAITL(0); BAR(); SB();
    STAMP(10);
    f32x4 ys = wf ? s1 : s0;
    if (wid < 2) {
#pragma unroll
      for (int w = 0; w < 3; ++w) ys += lds<f32x4>(sm + CV_PS + (3 * wid + w) * 1024 + lane * 16);
    }
    SB(); ldw(0, 5); ldw(1, 6); if (WD > 2) ldw(2, 7); mm_mn(1); SG(0x8, 4); for (int i_ = 0; i_ < 15; ++i_) { SG(0x8, 2); SG(0x20, 1); } SG(0x8, 36); SB();
    LAS char* pm = sm + (wt ? CV_PM23 : CV + 2 * CV_CH);
#pragma unroll
    for (int j = 0; j < 4; ++j) { f32x4 lo = cm[j], hi = cm[4 + j]; asm volatile("" : "+v"(lo), "+v"(hi));
      *(LAS f32x4*)(pm + wf * 4096 + j * 1024 + lane * 16) = wf ? lo : hi; cm[j] = wf ? hi : lo; }
    SB(); WAITL(0); BAR(); SB();
    STAMP(11);
    ldh(0, 0); ldh(1, 1); ldh(2, 2);
    auto yout = [&](f32x4 a, int chn, LAS char* ub, int j, float dv) {
      const u32x2 u = lds<u32x2>(ub + CV_U + (2044 - 256 * j - 16 * c - 4 * q) * 2);
      const f32x4 y = a + (f32x4){bhi(u.y), blo(u.y), bhi(u.x), blo(u.x)} * dv;
      st8<SC1>((u32x2){pk(y[0], y[1]), pk(y[2], y[3])}, rws, (16 * c + 4 * q) * 2, W_YT + chn * (S * 2) + j * 512); };
#pragma unroll
    for (int j = 0; j < 4; ++j) yout(cm[j] + lds<f32x4>(pm + (wf ^ 1) * 4096 + j * 1024 + lane * 16), 2 * id.b + wt, cb, 4 * wf + j, dvm);
    if (wid < 2) yout(ys, 512 + (id.b >> 2), cs_, wid ? 7 - jq : jq, dvs);
    SB();
  }
  STAMP(19);
  f32x4 o[4][4], ox[2];
#pragma unroll
  for (int i = 0; i < 4; ++i)
#pragma unroll
    for (int t = 0; t < 4; ++t) o[i][t] = (f32x4){0.f, 0.f, 0.f, 0.f};
  ox[0] = ox[1] = (f32x4){0.f, 0.f, 0.f, 0.f};
  const int dp = 144 * (g >> 1) + 80 * gp, ds = 144 * (g >> 1) + 64, trow = 64 * half + prow(wid, c);
  const unsigned xo = (id.tp * 128 + trow) * (D * 2);
  f32x4 rs[5];
  { LAS char* wl = sm + wid * G3W + lane * 16;
    bf16x8 wq[2][5];
#pragma unroll
    for (int t = 0; t < 5; ++t) wq[0][t] = lds<bf16x8>(wl + t * 1024);
#pragma unroll
    for (int i = 0; i < 18; ++i) {
      if (i < 4) {
#pragma unroll
        for (int t = 0; t < 5; ++t) wq[(i + 1) & 1][t] = lds<bf16x8>(wl + ((i + 1) * 5 + t) * 1024); }
#pragma unroll
      for (int r = 0; r < 4; ++r) { const bf16x8 hf = __builtin_bit_cast(bf16x8, hr[i % 3][r]);
#pragma unroll
        for (int t = 0; t < 5; ++t) if (t < 4 || r < 2) { const int tt = (r & 1) && t < 4 ? 3 - t : t;
          const bf16x8 wf_ = i < 5 ? wq[i & 1][tt] : __builtin_bit_cast(bf16x8, wr[(i - 5) % WD][tt]);
          if (tt < 4) o[r][tt] = MFMA(wf_, hf, o[r][tt]); else ox[r] = MFMA(wf_, hf, ox[r]); } }
      SB();
      if (i == 0 || i == 2 || i == 4 || i == 7 || i == 10 || i == 13 || i == 15) STAMP(20 + i);
      if (i + 3 < 18) ldh(i % 3, i + 3);
      if (i >= 5 && i + WD < 18) ldw((i - 5) % WD, i + WD);
      if (i == 2) { WAITV(12); if (lane == 0) __hip_atomic_fetch_add((LAS unsigned*)(sm + T_CNT), 1u, __ATOMIC_RELAXED, __HIP_MEMORY_SCOPE_WORKGROUP); }
      if (i == 16) {
        const u64 t0 = __builtin_amdgcn_s_memrealtime();
        while (*(volatile LAS unsigned*)(sm + T_YOK) == 0u && !expired(t0)) __builtin_amdgcn_s_sleep(1);
        FENCE();
        const float m = lds<float>(sm + T_M1 + trow * 4), r = lds<float>(sm + T_R1 + trow * 4);
        LAS char* yb = sm + YTILE + (c >> 2) * 128 + (32 * wt + 4 * wf + 8 * (c & 3)) * 2;
        auto res = [&](u32x2 xv, bf16x4 yv) { const u32x2 yu = __builtin_bit_cast(u32x2, yv);
          const f32x4 xf = {blo(xv.x), bhi(xv.x), blo(xv.y), bhi(xv.y)}, yf = {blo(yu.x), bhi(yu.x), blo(yu.y), bhi(yu.y)};
          return xf + (xf - m) * r * yf; };
        const int yp = 16 * gp, ysh = 64 * (gp ^ 1);
        LAS char* xb = sm + XTILE + prow(wid, c) * 160;
        const u32x4 x0 = lds<u32x4>(xb + yp * 2 + q * 16), x1 = lds<u32x4>(xb + yp * 2 + 64 + q * 16); const u32x2 x2 = lds<u32x2>(xb + ysh * 2 + q * 8);
        rs[0] = res(x0.xy, ldtr(yb + (yp + 8 * q) * 128)); rs[1] = res(x0.zw, ldtr(yb + (yp + 8 * q + 4) * 128));
        rs[2] = res(x1.xy, ldtr(yb + (yp + 32 + 8 * q) * 128)); rs[3] = res(x1.zw, ldtr(yb + (yp + 36 + 8 * q) * 128));
        rs[4] = res(x2, ldtr(yb + (ysh + 4 * q) * 128));
        asm volatile("" : "+v"(rs[0]), "+v"(rs[1]), "+v"(rs[2]), "+v"(rs[3]), "+v"(rs[4])); }
      SB();
    }
  }
  STAMP(12);
  const bool own0 = wt == gp;
  { LAS char* pw = sm + wid * G3W + lane * 16;
    auto put = [&](int r) { LAS char* p = pw + ((unsigned)r ^ (wf ^ (2 * gp))) * 5120;
#pragma unroll
      for (int t = 0; t < 4; ++t) *(LAS f32x4*)(p + t * 1024) = o[r][t];
      if (r < 2) *(LAS f32x4*)(p + 4096) = ox[r]; };
    put(1); put(3);
    if (own0) { put(2);
#pragma unroll
      for (int t = 0; t < 4; ++t) rs[t] += o[0][t];
      rs[4] += ox[0]; }
    else { put(0);
#pragma unroll
      for (int t = 0; t < 4; ++t) rs[t] += o[2][t]; }
  }
  SB(); WAITL(0); BAR(); SB();
  STAMP(13);
  { f32x4 pt[3][5];
#pragma unroll
    for (int t = 0; t < 5; ++t)
#pragma unroll
      for (int w = 0; w < 3; ++w) pt[w][t] = lds<f32x4>(sm + (wid ^ (w + 1)) * G3W + wid * 5120 + lane * 16 + t * 1024);
    const rsrc_t ro = mkrs(P.out, S * D * 2);
    auto fin = [&](int t) { const f32x4 v = (rs[t] + pt[0][t]) + (pt[1][t] + pt[2][t]); return (u32x2){pk(v[0], v[1]), pk(v[2], v[3])}; };
#pragma unroll
    for (int pp = 0; pp < 2; ++pp) { const u32x2 lo = fin(2 * pp), hi = fin(2 * pp + 1); st16<0>((u32x4){lo.x, lo.y, hi.x, hi.y}, ro, xo + q * 16, dp * 2 + 64 * pp); }
    const u32x2 ls = fin(4);
    if (own0) st8<0>(ls, ro, xo + q * 8, ds * 2);
  }
  STAMP(14);
}
}

extern "C" __global__ __launch_bounds__(512, 2) void h3_gmlp_fused(const Params P) {
  extern __shared__ __attribute__((aligned(16))) char smem_[];
  LAS char* sm = (LAS char*)smem_;
  const int wid = __builtin_amdgcn_readfirstlane(threadIdx.x >> 6);
  Ids id; id.b = blockIdx.x; id.lane = threadIdx.x & 63;
  const int xcd = id.b & 7, loc = id.b >> 3;
  id.tp = (xcd >> 1) * 4 + (loc >> 3); id.ft = (xcd & 1) * 8 + (loc & 7);
  unsigned stampv = 0; { const int lane = id.lane; STAMP(63); }
  if (wid == 0) {
    unsigned lo, hi, bytes, sink;
    asm volatile("s_getpc_b64 vcc\n1:\n\ts_sub_u32 %0, vcc_lo, 1b-h3_gmlp_fused\n\ts_subb_u32 %1, vcc_hi, 0\n\ts_mov_b32 %2, h3_gmlp_end-h3_gmlp_fused" : "=s"(lo), "=s"(hi), "=s"(bytes) :: "vcc", "scc");
    const rsrc_t rc = mkrs((const void*)(((u64)hi << 32) | lo), bytes);
    asm volatile("buffer_load_dword %0, %1, %2, 0 offen\n\ts_waitcnt vmcnt(0)" : "=&v"(sink) : "v"((id.lane < 16 ? id.lane * 32 + loc : 0x100000) * 128), "s"(rc) : "memory");
  }
  if (__builtin_expect(wid == 4 || wid == 5, 1)) role_L(P, sm, id, wid - 4 STAMP_PASS);
  else if (wid >= 6) role_X(P, sm, id, wid - 6 STAMP_PASS);
  else role_C(P, sm, id, wid STAMP_PASS);
  { const int lane = id.lane; STAMP(62); *(unsigned*)(P.ws + (40 << 20) + ((blockIdx.x * 8 + wid) * 64 + lane) * 4) = stampv; }
  if (wid == 5 && blockIdx.x == 0) { const unsigned t_a = __builtin_amdgcn_readlane(stampv, MK_AMP), t_s = __builtin_amdgcn_readlane(stampv, MK_AMP0);
    const unsigned long long d_ = (unsigned long long)(t_a - t_s) * MK_NUM / MK_DEN, t0_ = __builtin_amdgcn_s_memrealtime();
    while (__builtin_amdgcn_s_memrealtime() - t0_ < (d_ > 200000 ? 200000 : d_)) __builtin_amdgcn_s_sleep(2); }
}
extern "C" __global__ void h3_gmlp_end() {}

extern "C" void kernel_launch(void* const* d_in, const int* in_sizes, int n_in, void* d_out, int out_size, void* d_ws, size_t ws_size, hipStream_t stream) {
  Params p;
  p.x = (const u16*)d_in[0]; p.k = (const u16*)d_in[1]; p.v = (const u16*)d_in[2]; p.shk = (const u16*)d_in[3]; p.ssmk = (const u16*)d_in[4]; p.ssmd = (const u16*)d_in[5];
  p.win = (const u16*)d_in[7]; p.wsp = (const u16*)d_in[8]; p.bsp = (const u16*)d_in[9]; p.wout = (const u16*)d_in[10];
  p.out = (u16*)d_out; p.ws = (char*)d_ws;
  (void)hipFuncSetAttribute((const void*)h3_gmlp_fused, hipFuncAttributeMaxDynamicSharedMemorySize, LDS_BYTES);
  h3_gmlp_fused<<<256, 512, LDS_BYTES, stream>>>(p);
}
```

```cpp
#define KB 8
#include <hip/hip_runtime.h>
#include <hip/hip_bf16.h>
#include <type_traits>

namespace {
typedef unsigned short u16;
typedef unsigned long long u64;
typedef short bf16x8 __attribute__((ext_vector_type(8)));
typedef short bf16x4 __attribute__((ext_vector_type(4)));
typedef __bf16 bf2 __attribute__((ext_vector_type(2)));
typedef float f32x2 __attribute__((ext_vector_type(2)));
typedef float f32x4 __attribute__((ext_vector_type(4)));
typedef unsigned u32x2 __attribute__((ext_vector_type(2)));
typedef unsigned u32x4 __attribute__((ext_vector_type(4)));
typedef __amdgpu_buffer_rsrc_t rsrc_t;
#define LAS __attribute__((address_space(3)))
#define CAS __attribute__((address_space(4)))
#define DEV __device__ __forceinline__

constexpr int S = 2048, D = 576, F = 4608, FH = 2304;
constexpr int ASLOT = 16384, BSLOT = 18432, BBASE = 4 * ASLOT;
constexpr int STG = 139264, STG_W = 9216;
constexpr int T_M1 = 157952, T_R1 = T_M1 + 512, T_A0 = T_R1 + 512, T_A1 = T_A0 + 512, T_IR = T_A1 + 512;
constexpr int T_CS2 = T_IR + 512, T_CS1 = T_CS2;
constexpr int T_PART = T_CS1 + 640, T_N2 = T_PART, T_R2 = T_N2 + 512;
constexpr int T_TAG = T_PART + 2048, T_CNT = T_TAG + 16, T_GO = T_CNT + 4, T_YOK = T_CNT + 8;
constexpr int LDS_BYTES = 163840;
static_assert(T_CNT + 16 <= LDS_BYTES, "LDS map");
constexpr int G3W = 25600;
constexpr int CV = 102400, CV_CH = 13056, CV_O = 4160, CV_U = 8320;
constexpr int CV_PS = 143616, CV_PM23 = 149760;
constexpr int YTILE = 122880, XTILE = 133120;
constexpr int W_EPOCH = 0, W_F2 = 4096, W_F3 = 8192, W_F4 = 12288, W_F5 = 16384;
constexpr int W_G0X = 0x8000, W_G1F = 0x10000, W_G1S = 0x60000;
constexpr int W_KVT = 1 << 20, W_YT = 4 << 20, W_WOF = 7 << 20, W_ZF = 10 << 20, W_HF = 20 << 20, W_END = 30 << 20;
constexpr int SC1 = 16;
#define OPT_TWIN 0
#define OPT_TWSP 1
#define OPT_TG3 0
#define EXP 0
#define RH 0
#define RW 0
#define PR 0
#define XM 0
#define WD 3

#define BAR0() do { asm volatile("" ::: "memory"); __builtin_amdgcn_s_barrier(); asm volatile("" ::: "memory"); } while (0)
#define BAR() BAR0()
#define SB() __builtin_amdgcn_sched_barrier(0)
#define SG(m, n) __builtin_amdgcn_sched_group_barrier(m, n, 0)
#define WAITV(n) asm volatile("s_waitcnt vmcnt(%0)" ::"i"(n) : "memory")
#define WAITL(n) asm volatile("s_waitcnt lgkmcnt(%0)" ::"i"(n) : "memory")
#define FENCE() asm volatile("" ::: "memory")
#define MFMA(a, b, c) __builtin_amdgcn_mfma_f32_16x16x32_bf16(a, b, c, 0, 0, 0)
#define STAMP(i) do {} while (0)
#define LBAR() BAR()
#define STAMP_ARG
#define STAMP_PASS

struct Params {
  const u16 *x, *k, *v, *shk, *ssmk, *ssmd, *win, *wsp, *bsp, *wout;
  u16* out; char* ws;
};

DEV rsrc_t mkrs(const void* p, unsigned bytes) { return __builtin_amdgcn_make_buffer_rsrc((void*)p, 0, bytes, 0x00020000); }
template <int AUX> DEV void dma(rsrc_t rs, LAS char* l, unsigned voff, unsigned soff) { __builtin_amdgcn_raw_ptr_buffer_load_lds(rs, (LAS void*)l, 16, voff, soff, 0, AUX); }
DEV void dma_hidden(rsrc_t rs, LAS char* l, unsigned voff) { unsigned keep;
  asm volatile("s_mov_b32 %0, m0\n\ts_mov_b32 m0, %3\n\ts_nop 0\n\tbuffer_load_dwordx4 %1, %2, 0 offen lds\n\ts_mov_b32 m0, %0" : "=&s"(keep) : "v"(voff), "s"(rs), "s"((unsigned)(size_t)l) : "memory"); }
template <int AUX> DEV u32x4 ld16(rsrc_t rs, unsigned voff, unsigned soff) { return __builtin_amdgcn_raw_buffer_load_b128(rs, voff, soff, AUX); }
template <int AUX> DEV void st16(u32x4 v, rsrc_t rs, unsigned voff, unsigned soff) { __builtin_amdgcn_raw_buffer_store_b128(v, rs, voff, soff, AUX); }
template <int AUX> DEV void st8(u32x2 v, rsrc_t rs, unsigned voff, unsigned soff) { __builtin_amdgcn_raw_buffer_store_b64(v, rs, voff, soff, AUX); }
template <class T> DEV T lds(LAS char* p) { return *(LAS T*)p; }
DEV bf16x4 ldtr(LAS char* p) { return __builtin_amdgcn_ds_read_tr16_b64_v4i16((LAS bf16x4*)p); }
DEV bf16x8 cat(bf16x4 a, bf16x4 b) { return __builtin_shufflevector(a, b, 0, 1, 2, 3, 4, 5, 6, 7); }
DEV float blo(unsigned u) { return __uint_as_float(u << 16); }
DEV float bhi(unsigned u) { return __uint_as_float(u & 0xffff0000u); }
DEV unsigned pk(float a, float b) { return __builtin_bit_cast(unsigned, __builtin_convertvector((f32x2){a, b}, bf2)); }
DEV float dot2(unsigned a, unsigned b, float c) { return __builtin_amdgcn_fdot2_f32_bf16(__builtin_bit_cast(bf2, a), __builtin_bit_cast(bf2, b), c, false); }
template <int CTRL> DEV float dpp(float x) { return __builtin_bit_cast(float, __builtin_amdgcn_mov_dpp(__builtin_bit_cast(int, x), CTRL, 0xf, 0xf, true)); }
constexpr int XOR1 = 0xB1, XOR2 = 0x4E, XOR7 = 0x141, XOR8 = 0x128;
constexpr float GC0 = -2.30220819f, GC1 = -0.10294324f;
template <class T> DEV f32x2 gelu2(f32x2 a, T a0, T a1, T ir) {
  const f32x2 g = a * (a * a * a1 + a0);
  f32x2 e; e.x = __builtin_amdgcn_exp2f(g.x); e.y = __builtin_amdgcn_exp2f(g.y);
  e = e * ir + ir;
  f32x2 r; r.x = __builtin_amdgcn_rcpf(e.x); r.y = __builtin_amdgcn_rcpf(e.y);
  return a * r;
}
DEV f32x4 gelu4(f32x4 a, f32x4 a0, f32x4 a1, f32x4 ir) { const f32x2 p = gelu2(a.xy, a0.xy, a1.xy, ir.xy), q = gelu2(a.zw, a0.zw, a1.zw, ir.zw); return (f32x4){p.x, p.y, q.x, q.y}; }
DEV f32x4 gelu4(f32x4 a, float a0, float a1, float ir) { const f32x2 p = gelu2(a.xy, a0, a1, ir), q = gelu2(a.zw, a0, a1, ir); return (f32x4){p.x, p.y, q.x, q.y}; }
DEV u32x4 seal(float a, float b, u32x2 tag) { const unsigned ua = __float_as_uint(a), ub = __float_as_uint(b); return (u32x4){ua, ub, ua ^ tag.x, ub ^ tag.y}; }
DEV bool sealed(u32x4 g, u32x2 tag) { return (g.x ^ g.z) == tag.x && (g.y ^ g.w) == tag.y; }
DEV bool expired(u64 t0) { return __builtin_amdgcn_s_memrealtime() - t0 > 2000000ull; }
template <int NL, int NAP> DEV void wait_flags(rsrc_t ws, unsigned voff, u32x2 tag) {
  const u64 t0 = __builtin_amdgcn_s_memrealtime();
  for (;;) {
    bool ok = true;
#pragma unroll
    for (int i = 0; i < NL; ++i) { const u32x4 f = ld16<SC1>(ws, voff, i * 1024); ok &= f.x == tag.x && f.y == tag.y && f.z == tag.x && f.w == tag.y; }
    if (__all(ok) || expired(t0)) break;
    __builtin_amdgcn_s_sleep(NAP); FENCE();
  }
  FENCE();
}
DEV bool flags_ok(u32x4 f, u32x2 tag) { return f.x == tag.x && f.y == tag.y && f.z == tag.x && f.w == tag.y; }
DEV int prow(int T, int c) { return 32 * (T >> 1) + 8 * (c >> 2) + 4 * (T & 1) + (c & 3); }
DEV void set_flag(rsrc_t ws, unsigned off, u32x2 tag, int lane) { if (lane == 0) st8<SC1>(tag, ws, off, 0); }

struct Ids { int b, tp, ft, lane; };
DEV int krot(const Ids& id) { return (RH * (id.ft & 7) + ((RW * (id.tp & 3)) >> 1)) % 18; }
DEV int kwrap(int i, int r) { const int k = i + r; return k >= 18 ? k - 18 : k; }

DEV void role_L(const Params& P, LAS char* sm, const Ids id, const int lw STAMP_ARG) {
  int bcount = 0; (void)bcount;
  const int lane = id.lane, rr = lane >> 3, sl = lane & 7;
  const rsrc_t rx = mkrs(P.x, S * D * 2), rwin = mkrs(P.win, D * F * 2);
  const unsigned swz = (sl ^ (4 * lw + (rr >> 1))) * 16, prr = 16 * lw + 8 * (rr >> 2) + (rr & 3);
  const unsigned vx = prr * (D * 2) + swz;
  unsigned vb[9];
  auto issue1a = [&](int slot, int i, int ks) { dma<0>(rx, sm + slot * ASLOT + lw * 1024 + i * 2048, vx, (id.tp * 128 + 32 * (i >> 1) + 4 * (i & 1)) * (D * 2) + ks * 128); };
  auto issue1b = [&](int slot, int i, int ks, int col) { dma<0>(rwin, sm + BBASE + slot * BSLOT + lw * 1024 + i * 2048, vb[i], ks * 64 * (F * 2) + (col + id.ft * 144) * 2); };
#pragma unroll
  for (int i = 0; i < 9; ++i) {
    if (i < 8) issue1a(0, i, 0);
    SB();
    const unsigned ci = 64 * (2 * i + lw) + lane, rho = __umul24(ci, 3641) >> 16;
    vb[i] = __umul24(rho, F * 2 - 288) + 16 * ci + __mul24((int)(rho & 4) - (int)((rho >> 1) & 4), F * 2);
    issue1b(0, i, 0, FH);
    SB();
  }
  auto issueA = [&](int n) {
#pragma unroll
    for (int i = 0; i < 9; ++i) { if (i < 8) issue1a(n & 3, i, n); issue1b(n & 3, i, n, FH); } };
  auto issueB = [&](int n, bool with_x) {
#pragma unroll
    for (int i = 0; i < 9; ++i) { if (with_x && i < 8) issue1a((9 - n) & 3, i, 17 - n); issue1b(n & 3, i, 17 - n, 0); } };
  issueA(1); issueA(2);
  STAMP(60);
  const rsrc_t rwsp = mkrs(P.wsp, S * S * 2), rws = mkrs(P.ws, W_END);
  const unsigned vw = prr * (S * 2) + swz, vl = lane * 16 + lw * 1024;
  const int kc = (id.tp + id.ft) & (KB - 1), ra = PR ? (id.ft & 7) : 0, rb = PR ? 2 * (id.tp & 3) : 0;
  auto kof = [&](int n) { const int k = n - 18; return (k & ~(KB - 1)) | ((k + kc) & (KB - 1)); };
  auto issue2a = [&](int n) { LAS char* a = sm + ((9 - n) & 3) * ASLOT + lw * 1024; const unsigned sa = id.tp * 128 * (S * 2) + kof(n) * 128;
#pragma unroll
    for (int i0 = 0; i0 < 8; ++i0) { const int i = (i0 + ra) & 7; dma<0>(rwsp, a + i * 2048, vw, sa + (32 * (i >> 1) + 4 * (i & 1)) * (S * 2)); } };
  auto issue2b = [&](int n) { LAS char* bb = sm + BBASE + (n & 3) * BSLOT + lw * 1024; const unsigned sb = W_ZF + (id.ft * 576 + kof(n) * 18) * 1024;
#pragma unroll
    for (int i0 = 0; i0 < 9; ++i0) { const int i = i0 + rb >= 9 ? i0 + rb - 9 : i0 + rb; dma<SC1>(rws, bb + i * 2048, vl, sb + i * 2048); } };
#pragma unroll 1
  for (int n = 0; n < 6; ++n) { WAITV(34); LBAR(); issueA(n + 3); }
  WAITV(34); LBAR(); issueB(9, false);
  WAITV(26); LBAR(); issueB(10, false);
  WAITV(18); LBAR(); issueB(11, false);
  BAR();
  WAITV(18); LBAR(); issueB(12, false);
  WAITV(18); LBAR(); issueB(13, true);
  WAITV(26); LBAR();
#pragma unroll 1
  for (int n = 12; n < 16; ++n) { WAITV(17); LBAR(); issueB(n + 2, true); }
  WAITV(17); LBAR(); issue2a(18);
  WAITV(8); LBAR(); issue2a(19);
  BAR(); issue2a(20);
  { const u32x2 tag = lds<u32x2>(sm + T_TAG); wait_flags<1, 2>(rws, W_F2 + id.ft * 128 + (lane & 7) * 16, tag); }
  STAMP(61);
  issue2b(18); issue2b(19); issue2b(20);
  asm volatile("ds_write_b32 %0, %1" :: "v"((unsigned)(size_t)(sm + T_GO)), "v"(1u) : "memory");
  WAITV(18); LBAR(); issue2a(21); issue2b(21);
  WAITV(26); LBAR(); issue2a(22); issue2b(22);
#pragma unroll 1
  for (int n = 20; n < 47; ++n) { WAITV(34); LBAR(); issue2a(n + 3); issue2b(n + 3); }
  const int g = id.ft & 7;
  const unsigned wb0 = W_WOF + (g >> 1) * 72 * 9 * 1024, wb1 = wb0 + (g & 1) * 5 * 1024, vq = lane * 16;
  const int rot = krot(id);
  auto issue3 = [&](auto lo_, auto hi_) { constexpr int lo = decltype(lo_)::value, hi = decltype(hi_)::value;
#pragma unroll
    for (int p0 = lo; p0 < hi; p0 += 2) {
      auto off = [&](int pc) { return (pc % 5 == 4 ? wb0 : wb1) + (18 * (pc / 25) * 9 + pc % 5) * 1024 + kwrap((pc % 25) / 5, rot) * 9216; };
      dma<SC1>(rws, sm + p0 * 1024 + lw * 1024, vq, lw ? off(p0 + 1) : off(p0)); } };
#define IC(n) std::integral_constant<int, n>{}
  WAITV(34); LBAR(); issue3(IC(48), IC(64));
  WAITV(25); LBAR();
  WAITV(8); LBAR(); issue3(IC(16), IC(48));
  LBAR();
  LBAR(); issue3(IC(0), IC(16)); issue3(IC(64), IC(100));
  unsigned sink = 0;
  auto wtouch = [&]() {
    constexpr int K0 = 5 + WD, NK = 18 - K0;
#pragma unroll
    for (int i = 0; i < (NK * 40 + 127) / 128; ++i) { const unsigned n = (i * 2 + lw) * 64 + lane, blk = n >> 1, kw = __umul24(blk, 13108) >> 16, t = blk - kw * 5;
      const unsigned o = n < NK * 40 ? (t == 4 ? wb0 : wb1) + ((18 * (kw & 3) + K0 + (kw >> 2)) * 9 + t) * 1024 + ((id.tp & 3) * 2 + (n & 1)) * 128 : 0xF0000000u;
      asm volatile("buffer_load_dword %0, %1, %2, 0 offen sc1" : "+v"(sink) : "v"(o), "s"(rws) : "memory"); } };
  if ((OPT_TG3 & 3) == 1) wtouch();
  LBAR();
  if ((OPT_TG3 & 3) == 2) { __builtin_amdgcn_s_sleep(2); wtouch(); }
  WAITV((OPT_TG3 & 3) == 2 ? (( 18 - 5 - WD) * 40 + 127) / 128 : 0); LBAR();
  asm volatile("s_waitcnt vmcnt(0)" : "+v"(sink) :: "memory"); LBAR();
}

DEV void role_X(const Params& P, LAS char* sm, const Ids id, const int xw STAMP_ARG) {
  int bcount = 0; (void)bcount;
  const int lane = id.lane;
  const rsrc_t rws = mkrs(P.ws, W_END);
  unsigned sink = 0;
  const u32x2 ev = __builtin_amdgcn_raw_buffer_load_b64(rws, W_EPOCH, 0, SC1);
  const int srow = id.tp * 128 + id.ft * 8 + 4 * xw + (lane >> 4);
  u32x4 xv[5];
  { const rsrc_t rx = mkrs(P.x, S * D * 2);
#pragma unroll
    for (int i = 0; i < 5; ++i) xv[i] = ld16<0>(rx, i < 4 || (lane & 8) == 0 ? srow * (D * 2) + (lane & 15) * 16 : 0xF0000000u, i * 256); }
  const u64 epoch = ((u64)__builtin_amdgcn_readfirstlane(ev.y) << 32) | __builtin_amdgcn_readfirstlane(ev.x);
  u32x2 tag; { const u64 t = ((epoch + 1) * 0x9E3779B97F4A7C15ull) | 1ull; tag.x = (unsigned)t; tag.y = (unsigned)(t >> 32); }
  { float s = 0.f, q = 0.f; unsigned ones = 0x3f803f80u; asm volatile("" : "+v"(ones));
#pragma unroll
    for (int i = 0; i < 5; ++i)
#pragma unroll
      for (int e = 0; e < 4; ++e) { s = dot2(xv[i][e], ones, s); q = dot2(xv[i][e], xv[i][e], q); }
    s += dpp<XOR1>(s); q += dpp<XOR1>(q); s += dpp<XOR2>(s); q += dpp<XOR2>(q); s += dpp<XOR7>(s); q += dpp<XOR7>(q); s += dpp<XOR8>(s); q += dpp<XOR8>(q);
    const float m = s * (1.0f / D), var = fmaxf(q * (1.0f / D) - m * m, 0.f) + 1e-5f;
    if ((lane & 15) == 0) st16<SC1>(seal(m, var, tag), rws, W_G0X + srow * 16, 0);
    if (xw == 0 && lane == 0) { *(LAS u32x2*)(sm + T_TAG) = tag; *(LAS u32x4*)(sm + T_CNT) = (u32x4){0u, 0u, 0u, 0u}; }
  }
  STAMP(0);
  BAR(); BAR(); BAR(); BAR();
  LAS char* stg = sm + STG + xw * STG_W;
  const int unit = xw ? 256 + id.b : id.b, wks = unit >> 2, wu = unit & 3; const bool won = !xw || id.b < 32;
  const int tile = xw ? id.b : 256 + id.b - 32; const bool kon = xw || (id.b >= 32 && id.b < 64);
  const int tb = tile / 9, db = tile - tb * 9, cc = lane & 7, rg = lane >> 3;
  u32x4 kk[11], vv[8], sh[4];
  { const int row = 64 * xw + lane; const u64 t0 = __builtin_amdgcn_s_memrealtime();
    u32x4 gx = ld16<SC1>(rws, W_G0X + (id.tp * 128 + row) * 16, 0);
    BAR(); BAR();
    while (!__all(sealed(gx, tag)) && !expired(t0)) { __builtin_amdgcn_s_sleep(4); FENCE(); gx = ld16<SC1>(rws, W_G0X + (id.tp * 128 + row) * 16, 0); }
    const float var = __uint_as_float(gx.y), r = __builtin_amdgcn_rsqf(var);
    *(LAS unsigned*)(sm + T_M1 + row * 4) = gx.x; *(LAS float*)(sm + T_R1 + row * 4) = r;
    *(LAS float*)(sm + T_A0 + row * 4) = GC0 * r; *(LAS float*)(sm + T_A1 + row * 4) = GC1 * r * r * r; *(LAS float*)(sm + T_IR + row * 4) = var * r;
    WAITL(0); BAR();
  }
  STAMP(1);
  { const rsrc_t rk = mkrs(P.k, S * D * 2), rv = mkrs(P.v, S * D * 2), rs = mkrs(P.shk, D * 4 * 2), rwo = mkrs(P.wout, FH * D * 2);
    const int t0 = 64 * tb + 8 * rg; const unsigned col = (64 * db + 8 * cc) * 2;
    if (kon) {
#pragma unroll
      for (int i = 0; i < 11; ++i) kk[i] = ld16<0>(rk, t0 + i - 3 >= 0 ? (unsigned)(t0 + i - 3) * (D * 2) + col : 0xF0000000u, 0);
    }
    BAR();
    if (kon) {
#pragma unroll
      for (int i = 0; i < 8; ++i) vv[i] = ld16<0>(rv, t0 * (D * 2) + col, i * (D * 2));
#pragma unroll
      for (int i = 0; i < 4; ++i) sh[i] = ld16<0>(rs, col * 4, i * 16);
    }
    BAR();
    if (won) { const int fb0 = wks < 64 ? 144 * (wks >> 2) + 32 * (wks & 3) : 288 * (wks - 64) + 128, fst = wks < 64 ? 0 : 128;
#pragma unroll
      for (int i = 0; i < 9; ++i) { const int ci = 64 * i + lane, rho = __umul24(ci, 3641) >> 16, ch = ci - rho * 18;
        dma_hidden(rwo, stg + i * 1024, (fb0 + rho + (rho >> 4) * fst) * (D * 2) + wu * 288 + ch * 16); } }
    BAR();
    if (kon) {
#pragma unroll
      for (int i = 0; i < 4; ++i) asm volatile("" : "+v"(sh[i]), "+v"(vv[i]), "+v"(vv[4 + i]), "+v"(kk[i]), "+v"(kk[4 + i]), "+v"(kk[i == 3 ? 0 : 8 + i])); }
    if (OPT_TWSP) { const rsrc_t r = mkrs(P.wsp, S * S * 2);
#pragma unroll
      for (int i = 0; i < 2; ++i) asm volatile("buffer_load_dword %0, %1, %2, 0 offen" : "+v"(sink) : "v"(id.b * 32768 + ((2 * i + xw) * 64 + lane) * 128), "s"(r) : "memory"); }
  }
  BAR();
  u32x4 ko[8];
  auto kvstep = [&](int e) {
    if (kon) {
      float c[4], kf[11], y[8];
#pragma unroll
      for (int j = 0; j < 4; ++j) { const unsigned w = sh[e >> 1][(e & 1) * 2 + (j >> 1)]; c[j] = (j & 1) ? bhi(w) : blo(w); }
#pragma unroll
      for (int i = 0; i < 11; ++i) { const unsigned w = kk[i][e >> 1]; kf[i] = (e & 1) ? bhi(w) : blo(w); }
#pragma unroll
      for (int i = 0; i < 8; ++i) { const unsigned w = vv[i][e >> 1];
        float a = c[3] * kf[i]; asm volatile("" : "+v"(a)); a = fmaf(c[2], kf[i + 1], a); asm volatile("" : "+v"(a)); a = fmaf(c[1], kf[i + 2], a); asm volatile("" : "+v"(a)); a = fmaf(c[0], kf[i + 3], a);
        asm volatile("" : "+v"(a)); y[i] = a * ((e & 1) ? bhi(w) : blo(w)); asm volatile("" : "+v"(y[i])); }
      ko[e] = (u32x4){pk(y[7], y[6]), pk(y[5], y[4]), pk(y[3], y[2]), pk(y[1], y[0])};
      asm volatile("" : "+v"(ko[e]));
    } };
  { const int row = 64 * xw + lane;
    const f32x2 a = lds<f32x2>(sm + T_PART + row * 8) + lds<f32x2>(sm + T_PART + 1024 + row * 8);
    st16<SC1>(seal(a.x, a.y, tag), rws, W_G1S + ((id.tp * 16 + id.ft) * 128 + row) * 16, 0);
    kvstep(0); BAR(); kvstep(1); BAR();
    u32x4 gs[16];
    auto gather = [&]() {
#pragma unroll
      for (int i = 0; i < 16; ++i) gs[i] = ld16<SC1>(rws, W_G1S + (id.tp * 16 * 128 + row) * 16, i * 2048); };
    unsigned bad = 0; float s = 0.f, q = 0.f;
    auto sum = [&](int lo, int hi) {
#pragma unroll
      for (int i = lo; i < hi; ++i) { bad |= (gs[i].x ^ gs[i].z ^ tag.x) | (gs[i].y ^ gs[i].w ^ tag.y); s += __uint_as_float(gs[i].x); q += __uint_as_float(gs[i].y); }
      asm volatile("" : "+v"(bad), "+v"(s), "+v"(q)); };
    gather(); kvstep(2); BAR(); kvstep(3); BAR(); sum(0, 10); BAR();
    sum(10, 16);
    if (__builtin_expect(!__all(bad == 0), 0)) { const u64 t0 = __builtin_amdgcn_s_memrealtime();
      do { FENCE(); gather(); bad = 0; s = 0.f; q = 0.f; sum(0, 16); } while (!__all(bad == 0) && !expired(t0)); }
    const float m = s * (1.0f / FH), var = fmaxf(q * (1.0f / FH) - m * m, 0.f), r = __builtin_amdgcn_rsqf(var + 1e-5f);
    *(LAS float*)(sm + T_R2 + row * 4) = r; *(LAS float*)(sm + T_N2 + row * 4) = -m * r;
    asm volatile("s_waitcnt vmcnt(0) lgkmcnt(0)" : "+v"(sink) :: "memory"); BAR();
  }
  STAMP(2);
  kvstep(4); BAR(); kvstep(5); BAR();
  if (xw == 0) set_flag(rws, W_F2 + (id.ft * 16 + id.tp) * 8, tag, lane);
  kvstep(6); kvstep(7); BAR();
  { u32x4 wf[9];
    if (won) {
      const int q = lane >> 4, qq = (lane & 15) >> 2, p = lane & 3;
      LAS char* base = stg + (4 * q + qq) * 288;
#pragma unroll
      for (int T = 0; T < 9; ++T) { const int cb = T < 4 ? 32 * (T >> 1) + 4 * (T & 1) : T == 4 ? 64 : 80 + 32 * ((T - 5) >> 1) + 4 * ((T - 5) & 1);
        LAS char* a = base + cb * 2 + p * (T == 4 ? 8 : 16);
        wf[T] = __builtin_bit_cast(u32x4, cat(ldtr(a), ldtr(a + 16 * 288))); }
      WAITL(0);
    }
    if (kon) {
#pragma unroll
      for (int e = 0; e < 8; ++e) *(LAS u32x4*)(stg + (8 * cc + e) * 128 + (((7 - rg) ^ cc) * 16)) = ko[e];
#pragma unroll
      for (int i = 0; i < 8; ++i) ko[i] = lds<u32x4>(stg + (8 * i + rg) * 128 + ((cc ^ i) * 16));
    }
    const u64 t0 = __builtin_amdgcn_s_memrealtime();
    while (*(volatile LAS unsigned*)(sm + T_GO) == 0u && !expired(t0)) __builtin_amdgcn_s_sleep(2);
    FENCE();
    if (won) {
#pragma unroll
      for (int T = 0; T < 9; ++T) st16<SC1>(wf[T], rws, W_WOF + lane * 16, ((wu * 72 + wks) * 9 + T) * 1024); }
    if (kon) {
#pragma unroll
      for (int i = 0; i < 8; ++i) st16<SC1>(ko[i], rws, W_KVT + (64 * db + rg) * (S * 2) + (S - 64 * (tb + 1)) * 2 + cc * 16, 8 * i * (S * 2)); }
  }
  BAR(); BAR(); BAR();
  const unsigned h0a = ((const CAS unsigned*)P.ssmk)[id.b * S], h0b = ((const CAS unsigned*)P.ssmk)[(xw ? 2 * id.b + 1 : 512 + (id.b >> 2)) * (S / 2)];
  BAR(); WAITV(0); BAR();
  if (xw == 0) set_flag(rws, W_F5 + id.b * 8, tag, lane);
  STAMP(3);
#pragma unroll 1
  for (int i = 0; i < 22; ++i) BAR();
  { u32x4 f0 = {tag.x, tag.y, tag.x, tag.y}, f1 = f0;
    if (xw == 0) { f0 = ld16<SC1>(rws, W_F5 + lane * 16, 0); f1 = ld16<SC1>(rws, W_F5 + lane * 16, 1024); }
    BAR(); BAR();
    if (!__all(flags_ok(f0, tag) && flags_ok(f1, tag))) wait_flags<2, 8>(rws, W_F5 + lane * 16, tag); }
  BAR();
  STAMP(4);
  { const rsrc_t rh = mkrs(P.ssmk, D * S * 2);
    auto fetch = [&](int ci) { const int chn = ci == 2 ? 512 + (id.b >> 2) : 2 * id.b + ci; LAS char* cb = sm + CV + ci * CV_CH;
      const unsigned h0 = ci == 0 ? h0a : h0b;
      if (lane < 8) { *(LAS unsigned*)(cb + lane * 4) = 0u; *(LAS unsigned*)(cb + CV_O + lane * 4) = lane == 7 ? h0 << 16 : 0u; }
      if (lane < 38) *(LAS u32x4*)(cb + CV_U + 4096 + lane * 16) = (u32x4){0u, 0u, 0u, 0u};
      if (ci == 2) { *(LAS u32x4*)(cb + CV_U + 4704 + lane * 16) = (u32x4){0u, 0u, 0u, 0u}; *(LAS u32x4*)(cb + CV_U + 5728 + lane * 16) = (u32x4){0u, 0u, 0u, 0u}; }
      FENCE();
#pragma unroll
      for (int i = 0; i < 4; ++i) { dma<0>(rh, cb + 32 + i * 1024, lane * 16, chn * (S * 2) + i * 1024);
        dma<0>(rh, cb + CV_O + 32 + i * 1024, lane * 16 + 2, chn * (S * 2) + i * 1024);
        dma<SC1>(rws, cb + CV_U + i * 1024, lane * 16, W_KVT + chn * (S * 2) + i * 1024); } };
    if (xw == 0) fetch(0);
    BAR();
    fetch(2 - xw);
    BAR();
    WAITV(0); WAITL(0); BAR(); BAR();
  }
  BAR();
  if (xw == 0) { set_flag(rws, W_F3 + (id.tp * 16 + id.ft) * 8, tag, lane); wait_flags<1, 1>(rws, W_F3 + id.tp * 128 + (lane & 7) * 16, tag); }
  STAMP(5);
  BAR();
  if (xw == 1 && (OPT_TG3 & 4)) {
#pragma unroll
    for (int i = 0; i < 4; ++i) { const unsigned n = i * 64 + lane, o = n < 240 ? W_HF + ((id.tp * 8 + 4 * (id.ft >> 3) + (n & 3)) * 72 + 18 * ((n >> 2) & 3) + 3 + (n >> 4)) * 1024 + (id.ft & 7) * 128 : 0xF0000000u;
      asm volatile("buffer_load_dword %0, %1, %2, 0 offen sc1" : "+v"(sink) : "v"(o), "s"(rws) : "memory"); } }
  if (xw == 0) {
    const u64 t0 = __builtin_amdgcn_s_memrealtime();
    while (*(volatile LAS unsigned*)(sm + T_CNT) != 4u && !expired(t0)) __builtin_amdgcn_s_sleep(2);
    FENCE();
    set_flag(rws, W_F4 + id.b * 8, tag, lane);
    const int g = id.ft & 7, d0 = 144 * (g >> 1) + 64 * (g & 1);
    { const rsrc_t rx = mkrs(P.x, S * D * 2);
#pragma unroll
      for (int i = 0; i < 10; ++i) { const unsigned ci = 64 * i + lane, r = __umul24(ci, 6554) >> 16; dma<0>(rx, sm + XTILE + i * 1024, (id.tp * 128 + 64 * (id.ft >> 3) + r) * (D * 2) + d0 * 2 + (ci - 10 * r) * 16, 0); } }
    __builtin_amdgcn_s_sleep(24); wait_flags<2, 12>(rws, W_F4 + lane * 16, tag);
#pragma unroll
    for (int i = 0; i < 10; ++i) dma<SC1>(rws, sm + YTILE + i * 1024, W_YT + (d0 + (lane >> 3)) * (S * 2) + (id.tp * 128 + 64 * (id.ft >> 3)) * 2 + (lane & 7) * 16, i * 8 * (S * 2));
    WAITV(0);
    asm volatile("ds_write_b32 %0, %1" :: "v"((unsigned)(size_t)(sm + T_YOK)), "v"(1u) : "memory");
    if (id.b == 0 && lane == 0) st8<SC1>((u32x2){(unsigned)(epoch + 1), (unsigned)((epoch + 1) >> 32)}, rws, W_EPOCH, 0);
  }
  STAMP(6);
  asm volatile("s_waitcnt vmcnt(0)" : "+v"(sink) :: "memory"); BAR();
}

DEV void role_C(const Params& P, LAS char* sm, const Ids id, const int wid STAMP_ARG) {
  int bcount = 0; (void)bcount;
  const int lane = id.lane, wt = wid >> 1, wf = wid & 1, c = lane & 15, q = lane >> 4;
  const rsrc_t rws = mkrs(P.ws, W_END);
  __builtin_amdgcn_s_setprio(1);
  unsigned dm = ((const CAS unsigned*)P.ssmd)[id.b], dsh = ((const CAS unsigned*)P.ssmd)[256 + (id.b >> 3)];
  asm volatile("" : "+s"(dm), "+s"(dsh));
  f32x4 acc[4][4], accx[2], csa[3];
  bf16x8 tf[2][4], ff[2][5], ones;
  LAS char *ta[2][2], *fb[2][2], *fx[2];
#pragma unroll
  for (int s = 0; s < 2; ++s) {
    const int o = (32 * (s ^ wt) + 16 * (q >> 1) + 4 * (q & 1) + (c >> 2)) * 288 + 8 * (c & 3);
    fx[s] = sm + BBASE + o + 256;
#pragma unroll
    for (int h = 0; h < 2; ++h) { ta[s][h] = sm + (64 * wt + 32 * (h ^ wf) + c) * 128 + (((q + 4 * (s ^ wt)) ^ ((c >> 1) & 7)) * 16); fb[s][h] = sm + BBASE + o + wf * 128 + (h ^ wt) * 64; }
  }
#pragma unroll
  for (int e = 0; e < 8; ++e) ones[e] = 0x3f80;
  asm volatile("" : "+v"(ones));
  auto rd = [&](auto mode, int b, int as, int bs, int s) {
    constexpr int MODE = decltype(mode)::value;
#pragma unroll
    for (int j = 0; j < 4; ++j) { LAS char* p = fb[s][j >> 1] + bs * BSLOT;
      ff[b][j] = MODE < 2 ? cat(ldtr(p + (j & 1) * 32), ldtr(p + (j & 1) * 32 + 8 * 288)) : lds<bf16x8>(p + (j & 1) * 1024); }
#pragma unroll
    for (int i = 0; i < 4; ++i) tf[b][i] = lds<bf16x8>(ta[s][i >> 1] + as * ASLOT + (i & 1) * 2048);
    ff[b][4] = MODE < 2 ? cat(ldtr(fx[s] + bs * BSLOT), ldtr(fx[s] + bs * BSLOT + 8 * 288)) : lds<bf16x8>(fx[s] + bs * BSLOT);
  };
  auto mm = [&](auto mode, int b, int lo, int hi) {
    constexpr int MODE = decltype(mode)::value;
#pragma unroll
    for (int n = 0; n < (MODE == 2 ? 18 : 21 - b); ++n) if (n >= lo && n < hi) {
      if (n < 16) { const int i = n >> 2, j = (i & 1) ? 3 - (n & 3) : (n & 3);
        acc[i][j] = MODE == 0 ? MFMA(tf[b][i], ff[b][j], acc[i][j]) : MFMA(ff[b][j], tf[b][i], acc[i][j]); }
      else if (n < 18) accx[n - 16] = MODE == 0 ? MFMA(tf[b][n - 16], ff[b][4], accx[n - 16]) : MFMA(ff[b][4], tf[b][n - 16], accx[n - 16]);
      else { const int j = n == 20 ? 4 : n - 18; csa[n - 18] = MODE == 0 ? MFMA(ones, ff[b][j], csa[n - 18]) : MFMA(ff[b][j], ones, csa[n - 18]); }
    }
  };
  auto stage = [&](auto mode, int as, int bs, bool drain) {
    constexpr int MODE = decltype(mode)::value;
    mm(mode, 1, 0, 4); SB();
    if (drain) WAITV(0);
    WAITL(0); BAR(); SB();
    rd(mode, 0, as, bs, 0); mm(mode, 1, 4, 21);
    if (MODE < 2) { for (int i = 0; i < 14; ++i) { SG(0x100, 1); SG(0x8, 1); } SG(0x8, 2); }
    else { for (int i = 0; i < 4; ++i) { SG(0x100, 1); SG(0x8, 1); SG(0x100, 1); SG(0x8, 2); } SG(0x100, 1); SG(0x8, 2); }
    SB();
    rd(mode, 1, as, bs, 1); mm(mode, 0, 0, 21);
    if (MODE < 2) { for (int i = 0; i < 7; ++i) { SG(0x100, 1); SG(0x8, 1); SG(0x100, 1); SG(0x8, 2); } }
    else { for (int i = 0; i < 9; ++i) { SG(0x100, 1); SG(0x8, 2); } }
    SB();
  };
  auto first = [&](auto mode, int as, int bs) { WAITL(0); BAR(); SB(); rd(mode, 0, as, bs, 0); rd(mode, 1, as, bs, 1); mm(mode, 0, 0, 21); SB(); };
  auto zero = [&]() {
#pragma unroll
    for (int i = 0; i < 4; ++i)
#pragma unroll
      for (int j = 0; j < 4; ++j) acc[i][j] = (f32x4){0.f, 0.f, 0.f, 0.f};
    accx[0] = accx[1] = csa[0] = csa[1] = csa[2] = (f32x4){0.f, 0.f, 0.f, 0.f}; };
  const std::integral_constant<int, 0> M0; const std::integral_constant<int, 1> M1; const std::integral_constant<int, 2> M2;
  const int jg0 = 4 * wf + 2 * wt, jg2 = 4 * wf + 2 - 2 * wt;

  zero();
  first(M0, 0, 0);
  STAMP(0);
#pragma unroll 1
  for (int it = 0; it < 2; ++it) { stage(M0, 1, 1, false); stage(M0, 2, 2, false); stage(M0, 3, 3, false); stage(M0, 0, 0, false); }
  mm(M0, 1, 18, 21); mm(M0, 1, 0, 8); SB();
  if (q == 0) { *(LAS float*)(sm + T_CS2 + (jg0 * 16 + c) * 4) = csa[0][0]; *(LAS float*)(sm + T_CS2 + (jg0 * 16 + 16 + c) * 4) = csa[1][0]; *(LAS float*)(sm + T_CS2 + (128 + 16 * wt + c) * 4) = csa[2][0]; }
  mm(M0, 1, 8, 18); SB();
  WAITL(0); BAR();
  STAMP(1);
  u32x4 z2p[2][4], z2x;
  { float cs[5];
    cs[0] = csa[0][0]; cs[1] = csa[1][0]; cs[2] = lds<float>(sm + T_CS2 + (jg2 * 16 + c) * 4); cs[3] = lds<float>(sm + T_CS2 + (jg2 * 16 + 16 + c) * 4);
    cs[4] = csa[2][0] + lds<float>(sm + T_CS2 + (144 - 16 * wt + c) * 4);
    float red[2][16];
#pragma unroll
    for (int i = 0; i < 4; ++i) {
      const int ro = (64 * wt + 32 * ((i >> 1) ^ wf) + 8 * q + 4 * (i & 1)) * 4;
      const f32x4 m = lds<f32x4>(sm + T_M1 + ro), a0 = lds<f32x4>(sm + T_A0 + ro), a1 = lds<f32x4>(sm + T_A1 + ro), ir = lds<f32x4>(sm + T_IR + ro);
      f32x4 su = {0.f, 0.f, 0.f, 0.f}, sq = {0.f, 0.f, 0.f, 0.f};
#pragma unroll
      for (int j = 0; j < (i < 2 ? 5 : 4); ++j) {
        const f32x4 z = gelu4((j < 4 ? acc[i][j] : accx[i]) - m * cs[j], a0, a1, ir);
        su = j ? su + z : z; sq = j ? sq + z * z : z * z;
        u32x4& dst = j < 4 ? z2p[i >> 1][j] : z2x;
        dst[2 * (i & 1)] = pk(z[0], z[1]); dst[2 * (i & 1) + 1] = pk(z[2], z[3]);
      }
#pragma unroll
      for (int e = 0; e < 4; ++e) { red[0][4 * i + e] = su[e]; red[1][4 * i + e] = sq[e]; }
    }
    float r8[2][8], r4[2][4];
#pragma unroll
    for (int st = 0; st < 2; ++st) {
#pragma unroll
      for (int e = 0; e < 8; e += 4)
        asm volatile("s_nop 1\n\t"
                     "v_add_f32_dpp %0, %4, %4 row_ror:8 row_mask:0xf bank_mask:0x3\n\tv_add_f32_dpp %1, %5, %5 row_ror:8 row_mask:0xf bank_mask:0x3\n\t"
                     "v_add_f32_dpp %2, %6, %6 row_ror:8 row_mask:0xf bank_mask:0x3\n\tv_add_f32_dpp %3, %7, %7 row_ror:8 row_mask:0xf bank_mask:0x3\n\t"
                     "v_add_f32_dpp %0, %8, %8 row_ror:8 row_mask:0xf bank_mask:0xc\n\tv_add_f32_dpp %1, %9, %9 row_ror:8 row_mask:0xf bank_mask:0xc\n\t"
                     "v_add_f32_dpp %2, %10, %10 row_ror:8 row_mask:0xf bank_mask:0xc\n\tv_add_f32_dpp %3, %11, %11 row_ror:8 row_mask:0xf bank_mask:0xc"
                     : "=&v"(r8[st][e]), "=&v"(r8[st][e + 1]), "=&v"(r8[st][e + 2]), "=&v"(r8[st][e + 3])
                     : "v"(red[st][e]), "v"(red[st][e + 1]), "v"(red[st][e + 2]), "v"(red[st][e + 3]), "v"(red[st][8 + e]), "v"(red[st][9 + e]), "v"(red[st][10 + e]), "v"(red[st][11 + e]));
      asm volatile("s_nop 1\n\t"
                   "v_add_f32_dpp %0, %4, %4 row_half_mirror row_mask:0xf bank_mask:0x5\n\tv_add_f32_dpp %1, %5, %5 row_half_mirror row_mask:0xf bank_mask:0x5\n\t"
                   "v_add_f32_dpp %2, %6, %6 row_half_mirror row_mask:0xf bank_mask:0x5\n\tv_add_f32_dpp %3, %7, %7 row_half_mirror row_mask:0xf bank_mask:0x5\n\t"
                   "v_add_f32_dpp %0, %8, %8 row_half_mirror row_mask:0xf bank_mask:0xa\n\tv_add_f32_dpp %1, %9, %9 row_half_mirror row_mask:0xf bank_mask:0xa\n\t"
                   "v_add_f32_dpp %2, %10, %10 row_half_mirror row_mask:0xf bank_mask:0xa\n\tv_add_f32_dpp %3, %11, %11 row_half_mirror row_mask:0xf bank_mask:0xa\n\t"
                   "v_add_f32_dpp %0, %0, %0 quad_perm:[1,0,3,2] row_mask:0xf bank_mask:0xf\n\tv_add_f32_dpp %1, %1, %1 quad_perm:[1,0,3,2] row_mask:0xf bank_mask:0xf\n\t"
                   "v_add_f32_dpp %2, %2, %2 quad_perm:[1,0,3,2] row_mask:0xf bank_mask:0xf\n\tv_add_f32_dpp %3, %3, %3 quad_perm:[1,0,3,2] row_mask:0xf bank_mask:0xf\n\t"
                   "v_add_f32_dpp %0, %0, %0 quad_perm:[2,3,0,1] row_mask:0xf bank_mask:0xf\n\tv_add_f32_dpp %1, %1, %1 quad_perm:[2,3,0,1] row_mask:0xf bank_mask:0xf\n\t"
                   "v_add_f32_dpp %2, %2, %2 quad_perm:[2,3,0,1] row_mask:0xf bank_mask:0xf\n\tv_add_f32_dpp %3, %3, %3 quad_perm:[2,3,0,1] row_mask:0xf bank_mask:0xf"
                   : "=&v"(r4[st][0]), "=&v"(r4[st][1]), "=&v"(r4[st][2]), "=&v"(r4[st][3])
                   : "v"(r8[st][0]), "v"(r8[st][1]), "v"(r8[st][2]), "v"(r8[st][3]), "v"(r8[st][4]), "v"(r8[st][5]), "v"(r8[st][6]), "v"(r8[st][7]));
    }
    if ((c & 3) == 0) { LAS char* pp = sm + T_PART + wf * 1024 + (64 * wt + 32 * ((c >> 3) ^ wf) + 8 * q + 4 * ((c >> 2) & 1)) * 8;
      *(LAS f32x4*)pp = (f32x4){r4[0][0], r4[1][0], r4[0][1], r4[1][1]}; *(LAS f32x4*)(pp + 16) = (f32x4){r4[0][2], r4[1][2], r4[0][3], r4[1][3]}; }
#pragma unroll
    for (int i = 0; i < 2; ++i) asm volatile("" : "+v"(z2p[i][0]), "+v"(z2p[i][1]), "+v"(z2p[i][2]), "+v"(z2p[i][3]));
    asm volatile("" : "+v"(z2x));
  }
  STAMP(2);
  zero();
  first(M1, 0, 1);
  stage(M1, 3, 2, false); stage(M1, 2, 3, false); stage(M1, 1, 0, false);
  stage(M1, 0, 1, false); stage(M1, 3, 2, false); stage(M1, 2, 3, false);
  STAMP(3);
  {
#pragma unroll
    for (int i = 0; i < 2; ++i) asm volatile("" : "+v"(z2p[i][0]), "+v"(z2p[i][1]), "+v"(z2p[i][2]), "+v"(z2p[i][3]));
    asm volatile("" : "+v"(z2x));
#pragma unroll
    for (int ip = 0; ip < 2; ++ip) {
      const int ro = (64 * wt + 32 * (ip ^ wf) + 8 * q) * 4;
      const f32x4 r0 = lds<f32x4>(sm + T_R2 + ro), r1 = lds<f32x4>(sm + T_R2 + ro + 16), n0 = lds<f32x4>(sm + T_N2 + ro), n1 = lds<f32x4>(sm + T_N2 + ro + 16);
      const unsigned so = W_ZF + ((id.ft * 64 + 4 * id.tp + 2 * wt + (ip ^ wf)) * 9) * 1024;
#pragma unroll
      for (int j = 0; j < (ip == 0 ? 5 : 4); ++j) { const u32x4 z = j < 4 ? z2p[ip][j] : z2x;
        const f32x4 a = (f32x4){blo(z[0]), bhi(z[0]), blo(z[1]), bhi(z[1])} * r0 + n0, b = (f32x4){blo(z[2]), bhi(z[2]), blo(z[3]), bhi(z[3])} * r1 + n1;
        st16<SC1>((u32x4){pk(a[0], a[1]), pk(a[2], a[3]), pk(b[0], b[1]), pk(b[2], b[3])}, rws, lane * 16, so + (j < 2 ? jg0 + j : j < 4 ? jg2 + j - 2 : 8) * 1024); }
    }
    SB();
  }
  unsigned braw[4];
  { const rsrc_t rb = mkrs(P.bsp, S * 2);
#pragma unroll
    for (int i = 0; i < 4; ++i) braw[i] = __builtin_amdgcn_raw_buffer_load_b16(rb, (id.tp * 128 + 64 * wt + prow(i ^ (2 * wf), c)) * 2, 0, 0); }
  STAMP(4);
  stage(M1, 1, 0, false); stage(M1, 0, 1, true);
  mm(M1, 1, 18, 21); mm(M1, 1, 0, 8); SB();
  if (c == 0) { *(LAS f32x4*)(sm + T_CS1 + (jg0 * 16 + 4 * q) * 4) = csa[0]; *(LAS f32x4*)(sm + T_CS1 + (jg0 * 16 + 16 + 4 * q) * 4) = csa[1]; *(LAS f32x4*)(sm + T_CS1 + (128 + 16 * wt + 4 * q) * 4) = csa[2]; }
  mm(M1, 1, 8, 18); SB();
  WAITL(0); BAR();
  STAMP(5);
  u32x2 z1p[4][4], z1x[2];
  { f32x4 cs[5];
    cs[0] = csa[0]; cs[1] = csa[1]; cs[2] = lds<f32x4>(sm + T_CS1 + (jg2 * 16 + 4 * q) * 4); cs[3] = lds<f32x4>(sm + T_CS1 + (jg2 * 16 + 16 + 4 * q) * 4);
    cs[4] = csa[2] + lds<f32x4>(sm + T_CS1 + (144 - 16 * wt + 4 * q) * 4);
#pragma unroll
    for (int i = 0; i < 4; ++i) { const int ro = (64 * wt + prow(i ^ (2 * wf), c)) * 4;
      const float m = lds<float>(sm + T_M1 + ro), a0 = lds<float>(sm + T_A0 + ro), a1 = lds<float>(sm + T_A1 + ro), ir = lds<float>(sm + T_IR + ro), bias = blo(braw[i]);
#pragma unroll
      for (int j = 0; j < (i < 2 ? 5 : 4); ++j) {
        f32x4& a = j < 4 ? acc[i][j] : accx[i];
        const f32x4 z = gelu4(a - m * cs[j], a0, a1, ir);
        (j < 4 ? z1p[i][j] : z1x[i]) = (u32x2){pk(z[0], z[1]), pk(z[2], z[3])};
        a = (f32x4){bias, bias, bias, bias};
      } }
#pragma unroll
    for (int s = 0; s < 2; ++s) { fx[s] = sm + BBASE + (s * 9 + 8) * 1024 + lane * 16;
#pragma unroll
      for (int h = 0; h < 2; ++h) { fb[s][h] = sm + BBASE + (s * 9 + (h ? jg2 : jg0)) * 1024 + lane * 16; ta[s][h] = sm + (64 * wt + 32 * (h ^ wf) + c) * 128 + (((q + 4 * s) ^ ((c >> 1) & 7)) * 16); } }
#pragma unroll
    for (int i = 0; i < 4; ++i) asm volatile("" : "+v"(z1p[i][0]), "+v"(z1p[i][1]), "+v"(z1p[i][2]), "+v"(z1p[i][3]), "+v"(acc[i][0]), "+v"(acc[i][1]), "+v"(acc[i][2]), "+v"(acc[i][3]));
    asm volatile("" : "+v"(z1x[0]), "+v"(z1x[1]), "+v"(accx[0]), "+v"(accx[1]));
  }
  STAMP(6);
  const int jq = id.b & 3;
  LAS char *cs_ = sm + CV + 2 * CV_CH, *cb = sm + CV + wt * CV_CH;
  LAS char *am[4], *um = cb + CV_U + (2032 - 16 * c + 8 * q) * 2 + 256 * wf - 3584;
#pragma unroll
  for (int i = 0; i < 4; ++i) { am[i] = cb + ((c & 1) ? 0 : CV_O) + (4 * q + ((c + 1) >> 1)) * 4 + 256 * wf + 1024 * i; asm volatile("" : "+v"(am[i])); }
  asm volatile("" : "+v"(um));
  bf16x8 ca[2][8], cu[2][12];
  auto afrag = [&](LAS char* p) { const unsigned d0 = lds<unsigned>(p), d1 = lds<unsigned>(p + 4), d2 = lds<unsigned>(p + 8), d3 = lds<unsigned>(p + 12);
    return __builtin_bit_cast(bf16x8, (u32x4){d0, d1, d2, d3}); };
  auto lds_mn = [&](int b, int kk) {
#pragma unroll
    for (int e = 0; e < 8; ++e) { cu[b][e] = lds<bf16x8>(um + kk * 64 + 512 * (7 - e)); if (e == 0) ca[b][0] = afrag(am[0] + kk * 64); }
#pragma unroll
    for (int e = 1; e < 8; ++e) ca[b][e] = afrag(am[e >> 1] + kk * 64 + (e & 1) * 512); };
  first(M2, 3, 2);
  STAMP(7);
#pragma unroll 1
  for (int it = 0; it < 7; ++it) { stage(M2, 2, 3, false); stage(M2, 1, 0, false); stage(M2, 0, 1, false); stage(M2, 3, 2, false); }
  stage(M2, 2, 3, false); stage(M2, 1, 0, false); stage(M2, 0, 1, false);
  mm(M2, 1, 0, 4); SB(); WAITL(0); BAR(); SB();
  mm(M2, 1, 4, 18); SB();
  lds_mn(0, 0); SB();
  STAMP(8);
#pragma unroll
  for (int i = 0; i < 4; ++i) asm volatile("" : "+v"(z1p[i][0]), "+v"(z1p[i][1]), "+v"(z1p[i][2]), "+v"(z1p[i][3]));
  asm volatile("" : "+v"(z1x[0]), "+v"(z1x[1]));
#pragma unroll
  for (int i = 0; i < 4; ++i) {
    const unsigned so = W_HF + (id.tp * 8 + 4 * wt + (i ^ (2 * wf))) * 72 * 1024;
    auto gate = [&](u32x2 z, f32x4 a) { const f32x4 h = (f32x4){blo(z.x), bhi(z.x), blo(z.y), bhi(z.y)} * a; return (u32x2){pk(h[0], h[1]), pk(h[2], h[3])}; };
#pragma unroll
    for (int pp = 0; pp < 2; ++pp) { const u32x2 lo = gate(z1p[i][2 * pp], acc[i][2 * pp]), hi = gate(z1p[i][2 * pp + 1], acc[i][2 * pp + 1]);
      st16<SC1>((u32x4){lo.x, lo.y, hi.x, hi.y}, rws, lane * 16, so + (4 * id.ft + 2 * wf + (pp ^ wt)) * 1024); }
    if (i < 2) st8<SC1>(gate(z1x[i], accx[i]), rws, lane * 16 + (id.ft & 1) * 8, so + (64 + (id.ft >> 1)) * 1024);
  }
  const int g = id.ft & 7, gp = g & 1, half = id.ft >> 3, rot = krot(id);
  const unsigned wso = W_WOF + ((((EXP & 4) ? 0 : g >> 1) * 72 + 18 * wid) * 9) * 1024;
  u32x4 wr[WD][5];
  auto ldw = [&](int slot, int i) {
#pragma unroll
    for (int t = 0; t < 5; ++t) wr[slot][t] = ld16<(EXP & 8) ? 0 : SC1>(rws, lane * 16, wso + kwrap(i, rot) * 9216 + (t == 4 ? 4 : t + gp * 5) * 1024); };
  SB();
  SB(); BAR(); SB();
  STAMP(9);
  u32x4 hr[3][4];
  const unsigned hso = W_HF + (((EXP & 2) ? (id.b & 7) * 4 : id.tp * 8 + 4 * half) * 72 + 18 * wid) * 1024;
  auto ldh = [&](int slot, int i) {
#pragma unroll
    for (int t = 0; t < 4; ++t) hr[slot][t] = ld16<(EXP & 8) ? 0 : SC1>(rws, lane * 16, hso + (((unsigned)t ^ (wf ^ (2 * gp))) * 72 + kwrap(i, rot)) * 1024); };
  { const float dvm = wt ? bhi(dm) : blo(dm), dvs = (id.b & 4) ? bhi(dsh) : blo(dsh);
    f32x4 cm[8], s0 = {0.f, 0.f, 0.f, 0.f}, s1 = s0;
#pragma unroll
    for (int j = 0; j < 8; ++j) cm[j] = s0;
    LAS char *as[4], *u1, *u0;
    auto addr_sh = [&]() {
      const int da = (2 - wt) * CV_CH + 128 * wid - 256 * wf;
#pragma unroll
      for (int i = 0; i < 4; ++i) { as[i] = am[i] + da; asm volatile("" : "+v"(as[i])); }
      u1 = um + da + 512 * jq; u0 = um + da + 512 * (7 - jq); asm volatile("" : "+v"(u1), "+v"(u0)); };
    auto lds_sh = [&](int b, int kk) {
#pragma unroll
      for (int e = 0; e < 8; ++e) { ca[b][e] = afrag(as[e >> 1] + kk * 64 + (e & 1) * 512); cu[b][e] = lds<bf16x8>(u1 + kk * 64 + 512 * e);
        if (e < 4) cu[b][8 + e] = lds<bf16x8>(u0 + kk * 64 + 512 * e); } };
    auto mm_sh = [&](int b) {
#pragma unroll
      for (int e = 0; e < 8; ++e) { s1 = MFMA(ca[b][e], cu[b][e], s1); if (e < 4) s0 = MFMA(ca[b][e], cu[b][8 + e], s0); } };
    auto mm_mn = [&](int b) {
#pragma unroll
      for (int e = 0; e < 8; ++e)
#pragma unroll
        for (int j = e; j < 8; ++j) cm[j] = MFMA(ca[b][e], cu[b][j - e], cm[j]); };
#define il(nr, per) do { for (int i_ = 0; i_ < nr; ++i_) { SG(0x100, per); SG(0x8, 1); } SG(0x8, 36); SB(); } while (0)
#define ilv() do { for (int i_ = 0; i_ < 5; ++i_) { SG(0x100, 1); SG(0x8, 1); SG(0x100, 1); SG(0x8, 1); SG(0x100, 1); SG(0x8, 1); SG(0x100, 1); SG(0x8, 1); SG(0x100, 2); SG(0x8, 2); SG(0x20, 1); SG(0x8, 1); } SG(0x8, 36); SB(); } while (0)
    lds_mn(1, 1); mm_mn(0); il(24, 1); STAMP(15);
    lds_mn(0, 2); mm_mn(1); il(24, 1); STAMP(16); addr_sh(); SB();
    lds_sh(1, 0); mm_mn(0); il(28, 1);
    lds_sh(0, 1); mm_sh(1); il(10, 3);
    lds_mn(1, 3); mm_sh(0); il(12, 2);
    if (wid != 0) *(LAS f32x4*)(sm + CV_PS + (wid - 1) * 1024 + lane * 16) = s0;
    if (wid != 1) *(LAS f32x4*)(sm + CV_PS + (3 + (wid ? wid - 1 : 0)) * 1024 + lane * 16) = s1;
    SB(); STAMP(17); WAITV(0); WAITL(0); BAR(); SB();
    STAMP(10);
    f32x4 ys = wf ? s1 : s0;
    if (wid < 2) {
#pragma unroll
      for (int w = 0; w < 3; ++w) ys += lds<f32x4>(sm + CV_PS + (3 * wid + w) * 1024 + lane * 16);
    }
    SB(); ldw(0, 5); ldw(1, 6); if (WD > 2) ldw(2, 7); mm_mn(1); SG(0x8, 4); for (int i_ = 0; i_ < 15; ++i_) { SG(0x8, 2); SG(0x20, 1); } SG(0x8, 36); SB();
    LAS char* pm = sm + (wt ? CV_PM23 : CV + 2 * CV_CH);
#pragma unroll
    for (int j = 0; j < 4; ++j) { f32x4 lo = cm[j], hi = cm[4 + j]; asm volatile("" : "+v"(lo), "+v"(hi));
      *(LAS f32x4*)(pm + wf * 4096 + j * 1024 + lane * 16) = wf ? lo : hi; cm[j] = wf ? hi : lo; }
    SB(); WAITL(0); BAR(); SB();
    STAMP(11);
    ldh(0, 0); ldh(1, 1); ldh(2, 2);
    auto yout = [&](f32x4 a, int chn, LAS char* ub, int j, float dv) {
      const u32x2 u = lds<u32x2>(ub + CV_U + (2044 - 256 * j - 16 * c - 4 * q) * 2);
      const f32x4 y = a + (f32x4){bhi(u.y), blo(u.y), bhi(u.x), blo(u.x)} * dv;
      st8<SC1>((u32x2){pk(y[0], y[1]), pk(y[2], y[3])}, rws, (16 * c + 4 * q) * 2, W_YT + chn * (S * 2) + j * 512); };
#pragma unroll
    for (int j = 0; j < 4; ++j) yout(cm[j] + lds<f32x4>(pm + (wf ^ 1) * 4096 + j * 1024 + lane * 16), 2 * id.b + wt, cb, 4 * wf + j, dvm);
    if (wid < 2) yout(ys, 512 + (id.b >> 2), cs_, wid ? 7 - jq : jq, dvs);
    SB();
  }
  STAMP(19);
  f32x4 o[4][4], ox[2];
#pragma unroll
  for (int i = 0; i < 4; ++i)
#pragma unroll
    for (int t = 0; t < 4; ++t) o[i][t] = (f32x4){0.f, 0.f, 0.f, 0.f};
  ox[0] = ox[1] = (f32x4){0.f, 0.f, 0.f, 0.f};
  const int dp = 144 * (g >> 1) + 80 * gp, ds = 144 * (g >> 1) + 64, trow = 64 * half + prow(wid, c);
  const unsigned xo = (id.tp * 128 + trow) * (D * 2);
  f32x4 rs[5];
  { LAS char* wl = sm + wid * G3W + lane * 16;
    bf16x8 wq[2][5];
#pragma unroll
    for (int t = 0; t < 5; ++t) wq[0][t] = lds<bf16x8>(wl + t * 1024);
#pragma unroll
    for (int i = 0; i < 18; ++i) {
      if (i < 4) {
#pragma unroll
        for (int t = 0; t < 5; ++t) wq[(i + 1) & 1][t] = lds<bf16x8>(wl + ((i + 1) * 5 + t) * 1024); }
#pragma unroll
      for (int r = 0; r < 4; ++r) { const bf16x8 hf = __builtin_bit_cast(bf16x8, hr[i % 3][r]);
#pragma unroll
        for (int t = 0; t < 5; ++t) if (t < 4 || r < 2) { const int tt = (r & 1) && t < 4 ? 3 - t : t;
          const bf16x8 wf_ = i < 5 ? wq[i & 1][tt] : __builtin_bit_cast(bf16x8, wr[(i - 5) % WD][tt]);
          if (tt < 4) o[r][tt] = MFMA(wf_, hf, o[r][tt]); else ox[r] = MFMA(wf_, hf, ox[r]); } }
      SB();
      if (i == 0 || i == 2 || i == 4 || i == 7 || i == 10 || i == 13 || i == 15) STAMP(20 + i);
      if (i + 3 < 18) ldh(i % 3, i + 3);
      if (i >= 5 && i + WD < 18) ldw((i - 5) % WD, i + WD);
      if (i == 2) { WAITV(12); if (lane == 0) __hip_atomic_fetch_add((LAS unsigned*)(sm + T_CNT), 1u, __ATOMIC_RELAXED, __HIP_MEMORY_SCOPE_WORKGROUP); }
      if (i == 16) {
        const u64 t0 = __builtin_amdgcn_s_memrealtime();
        while (*(volatile LAS unsigned*)(sm + T_YOK) == 0u && !expired(t0)) __builtin_amdgcn_s_sleep(1);
        FENCE();
        const float m = lds<float>(sm + T_M1 + trow * 4), r = lds<float>(sm + T_R1 + trow * 4);
        LAS char* yb = sm + YTILE + (c >> 2) * 128 + (32 * wt + 4 * wf + 8 * (c & 3)) * 2;
        auto res = [&](u32x2 xv, bf16x4 yv) { const u32x2 yu = __builtin_bit_cast(u32x2, yv);
          const f32x4 xf = {blo(xv.x), bhi(xv.x), blo(xv.y), bhi(xv.y)}, yf = {blo(yu.x), bhi(yu.x), blo(yu.y), bhi(yu.y)};
          return xf + (xf - m) * r * yf; };
        const int yp = 16 * gp, ysh = 64 * (gp ^ 1);
        LAS char* xb = sm + XTILE + prow(wid, c) * 160;
        const u32x4 x0 = lds<u32x4>(xb + yp * 2 + q * 16), x1 = lds<u32x4>(xb + yp * 2 + 64 + q * 16); const u32x2 x2 = lds<u32x2>(xb + ysh * 2 + q * 8);
        rs[0] = res(x0.xy, ldtr(yb + (yp + 8 * q) * 128)); rs[1] = res(x0.zw, ldtr(yb + (yp + 8 * q + 4) * 128));
        rs[2] = res(x1.xy, ldtr(yb + (yp + 32 + 8 * q) * 128)); rs[3] = res(x1.zw, ldtr(yb + (yp + 36 + 8 * q) * 128));
        rs[4] = res(x2, ldtr(yb + (ysh + 4 * q) * 128));
        asm volatile("" : "+v"(rs[0]), "+v"(rs[1]), "+v"(rs[2]), "+v"(rs[3]), "+v"(rs[4])); }
      SB();
    }
  }
  STAMP(12);
  const bool own0 = wt == gp;
  { LAS char* pw = sm + wid * G3W + lane * 16;
    auto put = [&](int r) { LAS char* p = pw + ((unsigned)r ^ (wf ^ (2 * gp))) * 5120;
#pragma unroll
      for (int t = 0; t < 4; ++t) *(LAS f32x4*)(p + t * 1024) = o[r][t];
      if (r < 2) *(LAS f32x4*)(p + 4096) = ox[r]; };
    put(1); put(3);
    if (own0) { put(2);
#pragma unroll
      for (int t = 0; t < 4; ++t) rs[t] += o[0][t];
      rs[4] += ox[0]; }
    else { put(0);
#pragma unroll
      for (int t = 0; t < 4; ++t) rs[t] += o[2][t]; }
  }
  SB(); WAITL(0); BAR(); SB();
  STAMP(13);
  { f32x4 pt[3][5];
#pragma unroll
    for (int t = 0; t < 5; ++t)
#pragma unroll
      for (int w = 0; w < 3; ++w) pt[w][t] = lds<f32x4>(sm + (wid ^ (w + 1)) * G3W + wid * 5120 + lane * 16 + t * 1024);
    const rsrc_t ro = mkrs(P.out, S * D * 2);
    auto fin = [&](int t) { const f32x4 v = (rs[t] + pt[0][t]) + (pt[1][t] + pt[2][t]); return (u32x2){pk(v[0], v[1]), pk(v[2], v[3])}; };
#pragma unroll
    for (int pp = 0; pp < 2; ++pp) { const u32x2 lo = fin(2 * pp), hi = fin(2 * pp + 1); st16<0>((u32x4){lo.x, lo.y, hi.x, hi.y}, ro, xo + q * 16, dp * 2 + 64 * pp); }
    const u32x2 ls = fin(4);
    if (own0) st8<0>(ls, ro, xo + q * 8, ds * 2);
  }
  STAMP(14);
}
}

extern "C" __global__ __launch_bounds__(512, 2) void h3_gmlp_fused(const Params P) {
  extern __shared__ __attribute__((aligned(16))) char smem_[];
  LAS char* sm = (LAS char*)smem_;
  const int wid = __builtin_amdgcn_readfirstlane(threadIdx.x >> 6);
  Ids id; id.b = blockIdx.x; id.lane = threadIdx.x & 63;
  const int xcd = id.b & 7, loc = id.b >> 3;
  id.tp = (xcd >> 1) * 4 + (loc >> 3); id.ft = (xcd & 1) * 8 + (loc & 7);
  if (wid == 0) {
    unsigned lo, hi, bytes, sink;
    asm volatile("s_getpc_b64 vcc\n1:\n\ts_sub_u32 %0, vcc_lo, 1b-h3_gmlp_fused\n\ts_subb_u32 %1, vcc_hi, 0\n\ts_mov_b32 %2, h3_gmlp_end-h3_gmlp_fused" : "=s"(lo), "=s"(hi), "=s"(bytes) :: "vcc", "scc");
    const rsrc_t rc = mkrs((const void*)(((u64)hi << 32) | lo), bytes);
    asm volatile("buffer_load_dword %0, %1, %2, 0 offen\n\ts_waitcnt vmcnt(0)" : "=&v"(sink) : "v"((id.lane < 16 ? id.lane * 32 + loc : 0x100000) * 128), "s"(rc) : "memory");
  }
  if (__builtin_expect(wid == 4 || wid == 5, 1)) role_L(P, sm, id, wid - 4 STAMP_PASS);
  else if (wid >= 6) role_X(P, sm, id, wid - 6 STAMP_PASS);
  else role_C(P, sm, id, wid STAMP_PASS);
}
extern "C" __global__ void h3_gmlp_end() {}

extern "C" void kernel_launch(void* const* d_in, const int* in_sizes, int n_in, void* d_out, int out_size, void* d_ws, size_t ws_size, hipStream_t stream) {
  Params p;
  p.x = (const u16*)d_in[0]; p.k = (const u16*)d_in[1]; p.v = (const u16*)d_in[2]; p.shk = (const u16*)d_in[3]; p.ssmk = (const u16*)d_in[4]; p.ssmd = (const u16*)d_in[5];
  p.win = (const u16*)d_in[7]; p.wsp = (const u16*)d_in[8]; p.bsp = (const u16*)d_in[9]; p.wout = (const u16*)d_in[10];
  p.out = (u16*)d_out; p.ws = (char*)d_ws;
  (void)hipFuncSetAttribute((const void*)h3_gmlp_fused, hipFuncAttributeMaxDynamicSharedMemorySize, LDS_BYTES);
  h3_gmlp_fused<<<256, 512, LDS_BYTES, stream>>>(p);
}
```

```cpp
#define RT 7
#include <hip/hip_runtime.h>
#include <hip/hip_bf16.h>
#include <type_traits>

namespace {
typedef unsigned short u16;
typedef unsigned long long u64;
typedef short bf16x8 __attribute__((ext_vector_type(8)));
typedef short bf16x4 __attribute__((ext_vector_type(4)));
typedef __bf16 bf2 __attribute__((ext_vector_type(2)));
typedef float f32x2 __attribute__((ext_vector_type(2)));
typedef float f32x4 __attribute__((ext_vector_type(4)));
typedef unsigned u32x2 __attribute__((ext_vector_type(2)));
typedef unsigned u32x4 __attribute__((ext_vector_type(4)));
typedef __amdgpu_buffer_rsrc_t rsrc_t;
#define LAS __attribute__((address_space(3)))
#define CAS __attribute__((address_space(4)))
#define DEV __device__ __forceinline__

constexpr int S = 2048, D = 576, F = 4608, FH = 2304;
constexpr int ASLOT = 16384, BSLOT = 18432, BBASE = 4 * ASLOT;
constexpr int STG = 139264, STG_W = 9216;
constexpr int T_M1 = 157952, T_R1 = T_M1 + 512, T_A0 = T_R1 + 512, T_A1 = T_A0 + 512, T_IR = T_A1 + 512;
constexpr int T_CS2 = T_IR + 512, T_CS1 = T_CS2;
constexpr int T_PART = T_CS1 + 640, T_N2 = T_PART, T_R2 = T_N2 + 512;
constexpr int T_TAG = T_PART + 2048, T_CNT = T_TAG + 16, T_GO = T_CNT + 4, T_YOK = T_CNT + 8;
constexpr int LDS_BYTES = 163840;
static_assert(T_CNT + 16 <= LDS_BYTES, "LDS map");
constexpr int G3W = 25600;
constexpr int CV = 102400, CV_CH = 13056, CV_O = 4160, CV_U = 8320;
constexpr int CV_PS = 143616, CV_PM23 = 149760;
constexpr int YTILE = 122880, XTILE = 133120;
constexpr int W_EPOCH = 0, W_F2 = 4096, W_F3 = 8192, W_F4 = 12288, W_F5 = 16384;
constexpr int W_G0X = 0x8000, W_G1F = 0x10000, W_G1S = 0x60000;
constexpr int W_KVT = 1 << 20, W_YT = 4 << 20, W_WOF = 7 << 20, W_ZF = 10 << 20, W_HF = 20 << 20, W_END = 30 << 20;
constexpr int SC1 = 16;
#define OPT_TWIN 0
#define OPT_TWSP 1
#define OPT_TG3 0
#define EXP 0
#define RH 0
#define RW 0
#define KB 1
#define PR 0
#define XM 0
#define WD 3

#define BAR0() do { asm volatile("" ::: "memory"); __builtin_amdgcn_s_barrier(); asm volatile("" ::: "memory"); } while (0)
#define BAR() BAR0()
#define SB() __builtin_amdgcn_sched_barrier(0)
#define SG(m, n) __builtin_amdgcn_sched_group_barrier(m, n, 0)
#define WAITV(n) asm volatile("s_waitcnt vmcnt(%0)" ::"i"(n) : "memory")
#define WAITL(n) asm volatile("s_waitcnt lgkmcnt(%0)" ::"i"(n) : "memory")
#define FENCE() asm volatile("" ::: "memory")
#define MFMA(a, b, c) __builtin_amdgcn_mfma_f32_16x16x32_bf16(a, b, c, 0, 0, 0)
#define STAMP(i) do {} while (0)
#define LBAR() BAR()
#define STAMP_ARG
#define STAMP_PASS

struct Params {
  const u16 *x, *k, *v, *shk, *ssmk, *ssmd, *win, *wsp, *bsp, *wout;
  u16* out; char* ws;
};

DEV rsrc_t mkrs(const void* p, unsigned bytes) { return __builtin_amdgcn_make_buffer_rsrc((void*)p, 0, bytes, 0x00020000); }
template <int AUX> DEV void dma(rsrc_t rs, LAS char* l, unsigned voff, unsigned soff) { __builtin_amdgcn_raw_ptr_buffer_load_lds(rs, (LAS void*)l, 16, voff, soff, 0, AUX); }
DEV void dma_hidden(rsrc_t rs, LAS char* l, unsigned voff) { unsigned keep;
  asm volatile("s_mov_b32 %0, m0\n\ts_mov_b32 m0, %3\n\ts_nop 0\n\tbuffer_load_dwordx4 %1, %2, 0 offen lds\n\ts_mov_b32 m0, %0" : "=&s"(keep) : "v"(voff), "s"(rs), "s"((unsigned)(size_t)l) : "memory"); }
template <int AUX> DEV u32x4 ld16(rsrc_t rs, unsigned voff, unsigned soff) { return __builtin_amdgcn_raw_buffer_load_b128(rs, voff, soff, AUX); }
template <int AUX> DEV void st16(u32x4 v, rsrc_t rs, unsigned voff, unsigned soff) { __builtin_amdgcn_raw_buffer_store_b128(v, rs, voff, soff, AUX); }
template <int AUX> DEV void st8(u32x2 v, rsrc_t rs, unsigned voff, unsigned soff) { __builtin_amdgcn_raw_buffer_store_b64(v, rs, voff, soff, AUX); }
template <class T> DEV T lds(LAS char* p) { return *(LAS T*)p; }
DEV bf16x4 ldtr(LAS char* p) { return __builtin_amdgcn_ds_read_tr16_b64_v4i16((LAS bf16x4*)p); }
DEV bf16x8 cat(bf16x4 a, bf16x4 b) { return __builtin_shufflevector(a, b, 0, 1, 2, 3, 4, 5, 6, 7); }
DEV float blo(unsigned u) { return __uint_as_float(u << 16); }
DEV float bhi(unsigned u) { return __uint_as_float(u & 0xffff0000u); }
DEV unsigned pk(float a, float b) { return __builtin_bit_cast(unsigned, __builtin_convertvector((f32x2){a, b}, bf2)); }
DEV float dot2(unsigned a, unsigned b, float c) { return __builtin_amdgcn_fdot2_f32_bf16(__builtin_bit_cast(bf2, a), __builtin_bit_cast(bf2, b), c, false); }
template <int CTRL> DEV float dpp(float x) { return __builtin_bit_cast(float, __builtin_amdgcn_mov_dpp(__builtin_bit_cast(int, x), CTRL, 0xf, 0xf, true)); }
constexpr int XOR1 = 0xB1, XOR2 = 0x4E, XOR7 = 0x141, XOR8 = 0x128;
constexpr float GC0 = -2.30220819f, GC1 = -0.10294324f;
template <class T> DEV f32x2 gelu2(f32x2 a, T a0, T a1, T ir) {
  const f32x2 g = a * (a * a * a1 + a0);
  f32x2 e; e.x = __builtin_amdgcn_exp2f(g.x); e.y = __builtin_amdgcn_exp2f(g.y);
  e = e * ir + ir;
  f32x2 r; r.x = __builtin_amdgcn_rcpf(e.x); r.y = __builtin_amdgcn_rcpf(e.y);
  return a * r;
}
DEV f32x4 gelu4(f32x4 a, f32x4 a0, f32x4 a1, f32x4 ir) { const f32x2 p = gelu2(a.xy, a0.xy, a1.xy, ir.xy), q = gelu2(a.zw, a0.zw, a1.zw, ir.zw); return (f32x4){p.x, p.y, q.x, q.y}; }
DEV f32x4 gelu4(f32x4 a, float a0, float a1, float ir) { const f32x2 p = gelu2(a.xy, a0, a1, ir), q = gelu2(a.zw, a0, a1, ir); return (f32x4){p.x, p.y, q.x, q.y}; }
DEV u32x4 seal(float a, float b, u32x2 tag) { const unsigned ua = __float_as_uint(a), ub = __float_as_uint(b); return (u32x4){ua, ub, ua ^ tag.x, ub ^ tag.y}; }
DEV bool sealed(u32x4 g, u32x2 tag) { return (g.x ^ g.z) == tag.x && (g.y ^ g.w) == tag.y; }
DEV bool expired(u64 t0) { return __builtin_amdgcn_s_memrealtime() - t0 > 2000000ull; }
template <int NL, int NAP> DEV void wait_flags(rsrc_t ws, unsigned voff, u32x2 tag) {
  const u64 t0 = __builtin_amdgcn_s_memrealtime();
  for (;;) {
    bool ok = true;
#pragma unroll
    for (int i = 0; i < NL; ++i) { const u32x4 f = ld16<SC1>(ws, voff, i * 1024); ok &= f.x == tag.x && f.y == tag.y && f.z == tag.x && f.w == tag.y; }
    if (__all(ok) || expired(t0)) break;
    __builtin_amdgcn_s_sleep(NAP); FENCE();
  }
  FENCE();
}
DEV bool flags_ok(u32x4 f, u32x2 tag) { return f.x == tag.x && f.y == tag.y && f.z == tag.x && f.w == tag.y; }
DEV int prow(int T, int c) { return 32 * (T >> 1) + 8 * (c >> 2) + 4 * (T & 1) + (c & 3); }
DEV void set_flag(rsrc_t ws, unsigned off, u32x2 tag, int lane) { if (lane == 0) st8<SC1>(tag, ws, off, 0); }

struct Ids { int b, tp, ft, lane; };
DEV void code_touch(const Ids& id, unsigned& sink) {
  unsigned lo, hi, bytes;
  asm volatile("s_getpc_b64 vcc\n1:\n\ts_sub_u32 %0, vcc_lo, 1b-h3_gmlp_fused\n\ts_subb_u32 %1, vcc_hi, 0\n\ts_mov_b32 %2, h3_gmlp_end-h3_gmlp_fused" : "=s"(lo), "=s"(hi), "=s"(bytes) :: "vcc", "scc");
  const rsrc_t rc = mkrs((const void*)(((u64)hi << 32) | lo), bytes);
  asm volatile("buffer_load_dword %0, %1, %2, 0 offen" : "+v"(sink) : "v"((id.lane < 20 ? id.lane * 32 + (id.b >> 3) : 0x100000) * 128), "s"(rc) : "memory");
}
DEV int krot(const Ids& id) { return (RH * (id.ft & 7) + ((RW * (id.tp & 3)) >> 1)) % 18; }
DEV int kwrap(int i, int r) { const int k = i + r; return k >= 18 ? k - 18 : k; }

DEV void role_L(const Params& P, LAS char* sm, const Ids id, const int lw STAMP_ARG) {
  int bcount = 0; (void)bcount;
  const int lane = id.lane, rr = lane >> 3, sl = lane & 7;
  const rsrc_t rx = mkrs(P.x, S * D * 2), rwin = mkrs(P.win, D * F * 2);
  const unsigned swz = (sl ^ (4 * lw + (rr >> 1))) * 16, prr = 16 * lw + 8 * (rr >> 2) + (rr & 3);
  const unsigned vx = prr * (D * 2) + swz;
  unsigned vb[9];
  auto issue1a = [&](int slot, int i, int ks) { dma<0>(rx, sm + slot * ASLOT + lw * 1024 + i * 2048, vx, (id.tp * 128 + 32 * (i >> 1) + 4 * (i & 1)) * (D * 2) + ks * 128); };
  auto issue1b = [&](int slot, int i, int ks, int col) { dma<0>(rwin, sm + BBASE + slot * BSLOT + lw * 1024 + i * 2048, vb[i], ks * 64 * (F * 2) + (col + id.ft * 144) * 2); };
#pragma unroll
  for (int i = 0; i < 9; ++i) {
    if (i < 8) issue1a(0, i, 0);
    SB();
    const unsigned ci = 64 * (2 * i + lw) + lane, rho = __umul24(ci, 3641) >> 16;
    vb[i] = __umul24(rho, F * 2 - 288) + 16 * ci + __mul24((int)(rho & 4) - (int)((rho >> 1) & 4), F * 2);
    issue1b(0, i, 0, FH);
    SB();
  }
  auto issueA = [&](int n) {
#pragma unroll
    for (int i = 0; i < 9; ++i) { if (i < 8) issue1a(n & 3, i, n); issue1b(n & 3, i, n, FH); } };
  auto issueB = [&](int n, bool with_x) {
#pragma unroll
    for (int i = 0; i < 9; ++i) { if (with_x && i < 8) issue1a((9 - n) & 3, i, 17 - n); issue1b(n & 3, i, 17 - n, 0); } };
  issueA(1); issueA(2);
  STAMP(60);
  const rsrc_t rwsp = mkrs(P.wsp, S * S * 2), rws = mkrs(P.ws, W_END);
  const unsigned vw = prr * (S * 2) + swz, vl = lane * 16 + lw * 1024;
  const int kc = (id.tp + id.ft) & (KB - 1), ra = PR ? (id.ft & 7) : 0, rb = PR ? 2 * (id.tp & 3) : 0;
  auto kof = [&](int n) { const int k = n - 18; return (k & ~(KB - 1)) | ((k + kc) & (KB - 1)); };
  auto issue2a = [&](int n) { LAS char* a = sm + ((9 - n) & 3) * ASLOT + lw * 1024; const unsigned sa = id.tp * 128 * (S * 2) + kof(n) * 128;
#pragma unroll
    for (int i0 = 0; i0 < 8; ++i0) { const int i = (i0 + ra) & 7; dma<0>(rwsp, a + i * 2048, vw, sa + (32 * (i >> 1) + 4 * (i & 1)) * (S * 2)); } };
  auto issue2b = [&](int n) { LAS char* bb = sm + BBASE + (n & 3) * BSLOT + lw * 1024; const unsigned sb = W_ZF + (id.ft * 576 + kof(n) * 18) * 1024;
#pragma unroll
    for (int i0 = 0; i0 < 9; ++i0) { const int i = i0 + rb >= 9 ? i0 + rb - 9 : i0 + rb; dma<SC1>(rws, bb + i * 2048, vl, sb + i * 2048); } };
#pragma unroll 1
  for (int n = 0; n < 6; ++n) { WAITV(34); LBAR(); issueA(n + 3); }
  WAITV(34); LBAR(); issueB(9, false);
  WAITV(26); LBAR(); issueB(10, false);
  WAITV(18); LBAR(); issueB(11, false);
  BAR();
  WAITV(18); LBAR(); issueB(12, false);
  WAITV(18); LBAR(); issueB(13, true);
  WAITV(26); LBAR();
#pragma unroll 1
  for (int n = 12; n < 16; ++n) { WAITV(17); LBAR(); issueB(n + 2, true); }
  WAITV(17); LBAR(); issue2a(18);
  WAITV(8); LBAR(); issue2a(19);
  BAR(); issue2a(20);
  { const u32x2 tag = lds<u32x2>(sm + T_TAG); wait_flags<1, 2>(rws, W_F2 + id.ft * 128 + (lane & 7) * 16, tag); }
  STAMP(61);
  issue2b(18); issue2b(19); issue2b(20);
  asm volatile("ds_write_b32 %0, %1" :: "v"((unsigned)(size_t)(sm + T_GO)), "v"(1u) : "memory");
  WAITV(18); LBAR(); issue2a(21); issue2b(21);
  WAITV(26); LBAR(); issue2a(22); issue2b(22);
#pragma unroll 1
  for (int n = 20; n < 47; ++n) { WAITV(34); LBAR(); issue2a(n + 3); issue2b(n + 3); }
  const int g = id.ft & 7;
  const unsigned wb0 = W_WOF + (g >> 1) * 72 * 9 * 1024, wb1 = wb0 + (g & 1) * 5 * 1024, vq = lane * 16;
  const int rot = krot(id);
  auto issue3 = [&](auto lo_, auto hi_) { constexpr int lo = decltype(lo_)::value, hi = decltype(hi_)::value;
#pragma unroll
    for (int p0 = lo; p0 < hi; p0 += 2) {
      auto off = [&](int pc) { return (pc % 5 == 4 ? wb0 : wb1) + (18 * (pc / 25) * 9 + pc % 5) * 1024 + kwrap((pc % 25) / 5, rot) * 9216; };
      dma<SC1>(rws, sm + p0 * 1024 + lw * 1024, vq, lw ? off(p0 + 1) : off(p0)); } };
#define IC(n) std::integral_constant<int, n>{}
  WAITV(34); LBAR(); issue3(IC(48), IC(64));
  WAITV(25); LBAR();
  WAITV(8); LBAR(); issue3(IC(16), IC(48));
  LBAR();
  LBAR(); issue3(IC(0), IC(16)); issue3(IC(64), IC(100));
  unsigned sink = 0;
  auto wtouch = [&]() {
    constexpr int K0 = 5 + WD, NK = 18 - K0;
#pragma unroll
    for (int i = 0; i < (NK * 40 + 127) / 128; ++i) { const unsigned n = (i * 2 + lw) * 64 + lane, blk = n >> 1, kw = __umul24(blk, 13108) >> 16, t = blk - kw * 5;
      const unsigned o = n < NK * 40 ? (t == 4 ? wb0 : wb1) + ((18 * (kw & 3) + K0 + (kw >> 2)) * 9 + t) * 1024 + ((id.tp & 3) * 2 + (n & 1)) * 128 : 0xF0000000u;
      asm volatile("buffer_load_dword %0, %1, %2, 0 offen sc1" : "+v"(sink) : "v"(o), "s"(rws) : "memory"); } };
  if ((OPT_TG3 & 3) == 1) wtouch();
  LBAR();
  if ((OPT_TG3 & 3) == 2) { __builtin_amdgcn_s_sleep(2); wtouch(); }
  WAITV((OPT_TG3 & 3) == 2 ? (( 18 - 5 - WD) * 40 + 127) / 128 : 0); LBAR();
  asm volatile("s_waitcnt vmcnt(0)" : "+v"(sink) :: "memory"); LBAR();
}

DEV void role_X(const Params& P, LAS char* sm, const Ids id, const int xw STAMP_ARG) {
  int bcount = 0; (void)bcount;
  const int lane = id.lane;
  const rsrc_t rws = mkrs(P.ws, W_END);
  unsigned sink = 0;
  const u32x2 ev = __builtin_amdgcn_raw_buffer_load_b64(rws, W_EPOCH, 0, SC1);
  const int srow = id.tp * 128 + id.ft * 8 + 4 * xw + (lane >> 4);
  u32x4 xv[5];
  { const rsrc_t rx = mkrs(P.x, S * D * 2);
#pragma unroll
    for (int i = 0; i < 5; ++i) xv[i] = ld16<0>(rx, i < 4 || (lane & 8) == 0 ? srow * (D * 2) + (lane & 15) * 16 : 0xF0000000u, i * 256); }
  const u64 epoch = ((u64)__builtin_amdgcn_readfirstlane(ev.y) << 32) | __builtin_amdgcn_readfirstlane(ev.x);
  u32x2 tag; { const u64 t = ((epoch + 1) * 0x9E3779B97F4A7C15ull) | 1ull; tag.x = (unsigned)t; tag.y = (unsigned)(t >> 32); }
  { float s = 0.f, q = 0.f; unsigned ones = 0x3f803f80u; asm volatile("" : "+v"(ones));
#pragma unroll
    for (int i = 0; i < 5; ++i)
#pragma unroll
      for (int e = 0; e < 4; ++e) { s = dot2(xv[i][e], ones, s); q = dot2(xv[i][e], xv[i][e], q); }
    s += dpp<XOR1>(s); q += dpp<XOR1>(q); s += dpp<XOR2>(s); q += dpp<XOR2>(q); s += dpp<XOR7>(s); q += dpp<XOR7>(q); s += dpp<XOR8>(s); q += dpp<XOR8>(q);
    const float m = s * (1.0f / D), var = fmaxf(q * (1.0f / D) - m * m, 0.f) + 1e-5f;
    if ((lane & 15) == 0) st16<SC1>(seal(m, var, tag), rws, W_G0X + srow * 16, 0);
    if (xw == 0 && lane == 0) { *(LAS u32x2*)(sm + T_TAG) = tag; *(LAS u32x4*)(sm + T_CNT) = (u32x4){0u, 0u, 0u, 0u}; }
  }
  STAMP(0);
  BAR(); BAR(); BAR(); BAR();
  LAS char* stg = sm + STG + xw * STG_W;
  const int unit = xw ? 256 + id.b : id.b, wks = unit >> 2, wu = unit & 3; const bool won = !xw || id.b < 32;
  const int tile = xw ? id.b : 256 + id.b - 32; const bool kon = xw || (id.b >= 32 && id.b < 64);
  const int tb = tile / 9, db = tile - tb * 9, cc = lane & 7, rg = lane >> 3;
  u32x4 kk[11], vv[8], sh[4];
  { const int row = 64 * xw + lane; const u64 t0 = __builtin_amdgcn_s_memrealtime();
    u32x4 gx = ld16<SC1>(rws, W_G0X + (id.tp * 128 + row) * 16, 0);
    BAR(); BAR();
    while (!__all(sealed(gx, tag)) && !expired(t0)) { __builtin_amdgcn_s_sleep(4); FENCE(); gx = ld16<SC1>(rws, W_G0X + (id.tp * 128 + row) * 16, 0); }
    const float var = __uint_as_float(gx.y), r = __builtin_amdgcn_rsqf(var);
    *(LAS unsigned*)(sm + T_M1 + row * 4) = gx.x; *(LAS float*)(sm + T_R1 + row * 4) = r;
    *(LAS float*)(sm + T_A0 + row * 4) = GC0 * r; *(LAS float*)(sm + T_A1 + row * 4) = GC1 * r * r * r; *(LAS float*)(sm + T_IR + row * 4) = var * r;
    if ((RT & 1) && xw == 0) code_touch(id, sink);
    WAITL(0); BAR();
  }
  STAMP(1);
  { const rsrc_t rk = mkrs(P.k, S * D * 2), rv = mkrs(P.v, S * D * 2), rs = mkrs(P.shk, D * 4 * 2), rwo = mkrs(P.wout, FH * D * 2);
    const int t0 = 64 * tb + 8 * rg; const unsigned col = (64 * db + 8 * cc) * 2;
    if (kon) {
#pragma unroll
      for (int i = 0; i < 11; ++i) kk[i] = ld16<0>(rk, t0 + i - 3 >= 0 ? (unsigned)(t0 + i - 3) * (D * 2) + col : 0xF0000000u, 0);
    }
    BAR();
    if (kon) {
#pragma unroll
      for (int i = 0; i < 8; ++i) vv[i] = ld16<0>(rv, t0 * (D * 2) + col, i * (D * 2));
#pragma unroll
      for (int i = 0; i < 4; ++i) sh[i] = ld16<0>(rs, col * 4, i * 16);
    }
    BAR();
    if (won) { const int fb0 = wks < 64 ? 144 * (wks >> 2) + 32 * (wks & 3) : 288 * (wks - 64) + 128, fst = wks < 64 ? 0 : 128;
#pragma unroll
      for (int i = 0; i < 9; ++i) { const int ci = 64 * i + lane, rho = __umul24(ci, 3641) >> 16, ch = ci - rho * 18;
        dma_hidden(rwo, stg + i * 1024, (fb0 + rho + (rho >> 4) * fst) * (D * 2) + wu * 288 + ch * 16); } }
    BAR();
    if (kon) {
#pragma unroll
      for (int i = 0; i < 4; ++i) asm volatile("" : "+v"(sh[i]), "+v"(vv[i]), "+v"(vv[4 + i]), "+v"(kk[i]), "+v"(kk[4 + i]), "+v"(kk[i == 3 ? 0 : 8 + i])); }
    if (OPT_TWSP) { const rsrc_t r = mkrs(P.wsp, S * S * 2);
#pragma unroll
      for (int i = 0; i < 2; ++i) asm volatile("buffer_load_dword %0, %1, %2, 0 offen" : "+v"(sink) : "v"(id.b * 32768 + ((2 * i + xw) * 64 + lane) * 128), "s"(r) : "memory"); }
  }
  BAR();
  u32x4 ko[8];
  auto kvstep = [&](int e) {
    if (kon) {
      float c[4], kf[11], y[8];
#pragma unroll
      for (int j = 0; j < 4; ++j) { const unsigned w = sh[e >> 1][(e & 1) * 2 + (j >> 1)]; c[j] = (j & 1) ? bhi(w) : blo(w); }
#pragma unroll
      for (int i = 0; i < 11; ++i) { const unsigned w = kk[i][e >> 1]; kf[i] = (e & 1) ? bhi(w) : blo(w); }
#pragma unroll
      for (int i = 0; i < 8; ++i) { const unsigned w = vv[i][e >> 1];
        float a = c[3] * kf[i]; asm volatile("" : "+v"(a)); a = fmaf(c[2], kf[i + 1], a); asm volatile("" : "+v"(a)); a = fmaf(c[1], kf[i + 2], a); asm volatile("" : "+v"(a)); a = fmaf(c[0], kf[i + 3], a);
        asm volatile("" : "+v"(a)); y[i] = a * ((e & 1) ? bhi(w) : blo(w)); asm volatile("" : "+v"(y[i])); }
      ko[e] = (u32x4){pk(y[7], y[6]), pk(y[5], y[4]), pk(y[3], y[2]), pk(y[1], y[0])};
      asm volatile("" : "+v"(ko[e]));
    } };
  { const int row = 64 * xw + lane;
    const f32x2 a = lds<f32x2>(sm + T_PART + row * 8) + lds<f32x2>(sm + T_PART + 1024 + row * 8);
    st16<SC1>(seal(a.x, a.y, tag), rws, W_G1S + ((id.tp * 16 + id.ft) * 128 + row) * 16, 0);
    kvstep(0); BAR(); kvstep(1); BAR();
    u32x4 gs[16];
    auto gather = [&]() {
#pragma unroll
      for (int i = 0; i < 16; ++i) gs[i] = ld16<SC1>(rws, W_G1S + (id.tp * 16 * 128 + row) * 16, i * 2048); };
    unsigned bad = 0; float s = 0.f, q = 0.f;
    auto sum = [&](int lo, int hi) {
#pragma unroll
      for (int i = lo; i < hi; ++i) { bad |= (gs[i].x ^ gs[i].z ^ tag.x) | (gs[i].y ^ gs[i].w ^ tag.y); s += __uint_as_float(gs[i].x); q += __uint_as_float(gs[i].y); }
      asm volatile("" : "+v"(bad), "+v"(s), "+v"(q)); };
    gather(); kvstep(2); BAR(); kvstep(3); BAR(); sum(0, 10); BAR();
    sum(10, 16);
    if (__builtin_expect(!__all(bad == 0), 0)) { const u64 t0 = __builtin_amdgcn_s_memrealtime();
      do { FENCE(); gather(); bad = 0; s = 0.f; q = 0.f; sum(0, 16); } while (!__all(bad == 0) && !expired(t0)); }
    const float m = s * (1.0f / FH), var = fmaxf(q * (1.0f / FH) - m * m, 0.f), r = __builtin_amdgcn_rsqf(var + 1e-5f);
    *(LAS float*)(sm + T_R2 + row * 4) = r; *(LAS float*)(sm + T_N2 + row * 4) = -m * r;
    asm volatile("s_waitcnt vmcnt(0) lgkmcnt(0)" : "+v"(sink) :: "memory"); BAR();
  }
  STAMP(2);
  if ((RT & 4) && xw == 0) code_touch(id, sink);
  kvstep(4); BAR(); kvstep(5); BAR();
  if (xw == 0) set_flag(rws, W_F2 + (id.ft * 16 + id.tp) * 8, tag, lane);
  kvstep(6); kvstep(7); BAR();
  { u32x4 wf[9];
    if (won) {
      const int q = lane >> 4, qq = (lane & 15) >> 2, p = lane & 3;
      LAS char* base = stg + (4 * q + qq) * 288;
#pragma unroll
      for (int T = 0; T < 9; ++T) { const int cb = T < 4 ? 32 * (T >> 1) + 4 * (T & 1) : T == 4 ? 64 : 80 + 32 * ((T - 5) >> 1) + 4 * ((T - 5) & 1);
        LAS char* a = base + cb * 2 + p * (T == 4 ? 8 : 16);
        wf[T] = __builtin_bit_cast(u32x4, cat(ldtr(a), ldtr(a + 16 * 288))); }
      WAITL(0);
    }
    if (kon) {
#pragma unroll
      for (int e = 0; e < 8; ++e) *(LAS u32x4*)(stg + (8 * cc + e) * 128 + (((7 - rg) ^ cc) * 16)) = ko[e];
#pragma unroll
      for (int i = 0; i < 8; ++i) ko[i] = lds<u32x4>(stg + (8 * i + rg) * 128 + ((cc ^ i) * 16));
    }
    const u64 t0 = __builtin_amdgcn_s_memrealtime();
    while (*(volatile LAS unsigned*)(sm + T_GO) == 0u && !expired(t0)) __builtin_amdgcn_s_sleep(2);
    FENCE();
    if (won) {
#pragma unroll
      for (int T = 0; T < 9; ++T) st16<SC1>(wf[T], rws, W_WOF + lane * 16, ((wu * 72 + wks) * 9 + T) * 1024); }
    if (kon) {
#pragma unroll
      for (int i = 0; i < 8; ++i) st16<SC1>(ko[i], rws, W_KVT + (64 * db + rg) * (S * 2) + (S - 64 * (tb + 1)) * 2 + cc * 16, 8 * i * (S * 2)); }
  }
  BAR(); BAR(); BAR();
  const unsigned h0a = ((const CAS unsigned*)P.ssmk)[id.b * S], h0b = ((const CAS unsigned*)P.ssmk)[(xw ? 2 * id.b + 1 : 512 + (id.b >> 2)) * (S / 2)];
  BAR(); WAITV(0); BAR();
  if (xw == 0) set_flag(rws, W_F5 + id.b * 8, tag, lane);
  STAMP(3);
#pragma unroll 1
  for (int i = 0; i < 22; ++i) BAR();
  if ((RT & 2) && xw == 1) code_touch(id, sink);
  { u32x4 f0 = {tag.x, tag.y, tag.x, tag.y}, f1 = f0;
    if (xw == 0) { f0 = ld16<SC1>(rws, W_F5 + lane * 16, 0); f1 = ld16<SC1>(rws, W_F5 + lane * 16, 1024); }
    BAR(); BAR();
    if (!__all(flags_ok(f0, tag) && flags_ok(f1, tag))) wait_flags<2, 8>(rws, W_F5 + lane * 16, tag); }
  BAR();
  STAMP(4);
  { const rsrc_t rh = mkrs(P.ssmk, D * S * 2);
    auto fetch = [&](int ci) { const int chn = ci == 2 ? 512 + (id.b >> 2) : 2 * id.b + ci; LAS char* cb = sm + CV + ci * CV_CH;
      const unsigned h0 = ci == 0 ? h0a : h0b;
      if (lane < 8) { *(LAS unsigned*)(cb + lane * 4) = 0u; *(LAS unsigned*)(cb + CV_O + lane * 4) = lane == 7 ? h0 << 16 : 0u; }
      if (lane < 38) *(LAS u32x4*)(cb + CV_U + 4096 + lane * 16) = (u32x4){0u, 0u, 0u, 0u};
      if (ci == 2) { *(LAS u32x4*)(cb + CV_U + 4704 + lane * 16) = (u32x4){0u, 0u, 0u, 0u}; *(LAS u32x4*)(cb + CV_U + 5728 + lane * 16) = (u32x4){0u, 0u, 0u, 0u}; }
      FENCE();
#pragma unroll
      for (int i = 0; i < 4; ++i) { dma<0>(rh, cb + 32 + i * 1024, lane * 16, chn * (S * 2) + i * 1024);
        dma<0>(rh, cb + CV_O + 32 + i * 1024, lane * 16 + 2, chn * (S * 2) + i * 1024);
        dma<SC1>(rws, cb + CV_U + i * 1024, lane * 16, W_KVT + chn * (S * 2) + i * 1024); } };
    if (xw == 0) fetch(0);
    BAR();
    fetch(2 - xw);
    BAR();
    WAITV(0); WAITL(0); BAR(); BAR();
  }
  BAR();
  if (xw == 0) { set_flag(rws, W_F3 + (id.tp * 16 + id.ft) * 8, tag, lane); wait_flags<1, 1>(rws, W_F3 + id.tp * 128 + (lane & 7) * 16, tag); }
  STAMP(5);
  BAR();
  if (xw == 1 && (OPT_TG3 & 4)) {
#pragma unroll
    for (int i = 0; i < 4; ++i) { const unsigned n = i * 64 + lane, o = n < 240 ? W_HF + ((id.tp * 8 + 4 * (id.ft >> 3) + (n & 3)) * 72 + 18 * ((n >> 2) & 3) + 3 + (n >> 4)) * 1024 + (id.ft & 7) * 128 : 0xF0000000u;
      asm volatile("buffer_load_dword %0, %1, %2, 0 offen sc1" : "+v"(sink) : "v"(o), "s"(rws) : "memory"); } }
  if (xw == 0) {
    const u64 t0 = __builtin_amdgcn_s_memrealtime();
    while (*(volatile LAS unsigned*)(sm + T_CNT) != 4u && !expired(t0)) __builtin_amdgcn_s_sleep(2);
    FENCE();
    set_flag(rws, W_F4 + id.b * 8, tag, lane);
    const int g = id.ft & 7, d0 = 144 * (g >> 1) + 64 * (g & 1);
    { const rsrc_t rx = mkrs(P.x, S * D * 2);
#pragma unroll
      for (int i = 0; i < 10; ++i) { const unsigned ci = 64 * i + lane, r = __umul24(ci, 6554) >> 16; dma<0>(rx, sm + XTILE + i * 1024, (id.tp * 128 + 64 * (id.ft >> 3) + r) * (D * 2) + d0 * 2 + (ci - 10 * r) * 16, 0); } }
    __builtin_amdgcn_s_sleep(24); wait_flags<2, 12>(rws, W_F4 + lane * 16, tag);
#pragma unroll
    for (int i = 0; i < 10; ++i) dma<SC1>(rws, sm + YTILE + i * 1024, W_YT + (d0 + (lane >> 3)) * (S * 2) + (id.tp * 128 + 64 * (id.ft >> 3)) * 2 + (lane & 7) * 16, i * 8 * (S * 2));
    WAITV(0);
    asm volatile("ds_write_b32 %0, %1" :: "v"((unsigned)(size_t)(sm + T_YOK)), "v"(1u) : "memory");
    if (id.b == 0 && lane == 0) st8<SC1>((u32x2){(unsigned)(epoch + 1), (unsigned)((epoch + 1) >> 32)}, rws, W_EPOCH, 0);
  }
  STAMP(6);
  asm volatile("s_waitcnt vmcnt(0)" : "+v"(sink) :: "memory"); BAR();
}

DEV void role_C(const Params& P, LAS char* sm, const Ids id, const int wid STAMP_ARG) {
  int bcount = 0; (void)bcount;
  const int lane = id.lane, wt = wid >> 1, wf = wid & 1, c = lane & 15, q = lane >> 4;
  const rsrc_t rws = mkrs(P.ws, W_END);
  __builtin_amdgcn_s_setprio(1);
  unsigned dm = ((const CAS unsigned*)P.ssmd)[id.b], dsh = ((const CAS unsigned*)P.ssmd)[256 + (id.b >> 3)];
  asm volatile("" : "+s"(dm), "+s"(dsh));
  f32x4 acc[4][4], accx[2], csa[3];
  bf16x8 tf[2][4], ff[2][5], ones;
  LAS char *ta[2][2], *fb[2][2], *fx[2];
#pragma unroll
  for (int s = 0; s < 2; ++s) {
    const int o = (32 * (s ^ wt) + 16 * (q >> 1) + 4 * (q & 1) + (c >> 2)) * 288 + 8 * (c & 3);
    fx[s] = sm + BBASE + o + 256;
#pragma unroll
    for (int h = 0; h < 2; ++h) { ta[s][h] = sm + (64 * wt + 32 * (h ^ wf) + c) * 128 + (((q + 4 * (s ^ wt)) ^ ((c >> 1) & 7)) * 16); fb[s][h] = sm + BBASE + o + wf * 128 + (h ^ wt) * 64; }
  }
#pragma unroll
  for (int e = 0; e < 8; ++e) ones[e] = 0x3f80;
  asm volatile("" : "+v"(ones));
  auto rd = [&](auto mode, int b, int as, int bs, int s) {
    constexpr int MODE = decltype(mode)::value;
#pragma unroll
    for (int j = 0; j < 4; ++j) { LAS char* p = fb[s][j >> 1] + bs * BSLOT;
      ff[b][j] = MODE < 2 ? cat(ldtr(p + (j & 1) * 32), ldtr(p + (j & 1) * 32 + 8 * 288)) : lds<bf16x8>(p + (j & 1) * 1024); }
#pragma unroll
    for (int i = 0; i < 4; ++i) tf[b][i] = lds<bf16x8>(ta[s][i >> 1] + as * ASLOT + (i & 1) * 2048);
    ff[b][4] = MODE < 2 ? cat(ldtr(fx[s] + bs * BSLOT), ldtr(fx[s] + bs * BSLOT + 8 * 288)) : lds<bf16x8>(fx[s] + bs * BSLOT);
  };
  auto mm = [&](auto mode, int b, int lo, int hi) {
    constexpr int MODE = decltype(mode)::value;
#pragma unroll
    for (int n = 0; n < (MODE == 2 ? 18 : 21 - b); ++n) if (n >= lo && n < hi) {
      if (n < 16) { const int i = n >> 2, j = (i & 1) ? 3 - (n & 3) : (n & 3);
        acc[i][j] = MODE == 0 ? MFMA(tf[b][i], ff[b][j], acc[i][j]) : MFMA(ff[b][j], tf[b][i], acc[i][j]); }
      else if (n < 18) accx[n - 16] = MODE == 0 ? MFMA(tf[b][n - 16], ff[b][4], accx[n - 16]) : MFMA(ff[b][4], tf[b][n - 16], accx[n - 16]);
      else { const int j = n == 20 ? 4 : n - 18; csa[n - 18] = MODE == 0 ? MFMA(ones, ff[b][j], csa[n - 18]) : MFMA(ff[b][j], ones, csa[n - 18]); }
    }
  };
  auto stage = [&](auto mode, int as, int bs, bool drain) {
    constexpr int MODE = decltype(mode)::value;
    mm(mode, 1, 0, 4); SB();
    if (drain) WAITV(0);
    WAITL(0); BAR(); SB();
    rd(mode, 0, as, bs, 0); mm(mode, 1, 4, 21);
    if (MODE < 2) { for (int i = 0; i < 14; ++i) { SG(0x100, 1); SG(0x8, 1); } SG(0x8, 2); }
    else { for (int i = 0; i < 4; ++i) { SG(0x100, 1); SG(0x8, 1); SG(0x100, 1); SG(0x8, 2); } SG(0x100, 1); SG(0x8, 2); }
    SB();
    rd(mode, 1, as, bs, 1); mm(mode, 0, 0, 21);
    if (MODE < 2) { for (int i = 0; i < 7; ++i) { SG(0x100, 1); SG(0x8, 1); SG(0x100, 1); SG(0x8, 2); } }
    else { for (int i = 0; i < 9; ++i) { SG(0x100, 1); SG(0x8, 2); } }
    SB();
  };
  auto first = [&](auto mode, int as, int bs) { WAITL(0); BAR(); SB(); rd(mode, 0, as, bs, 0); rd(mode, 1, as, bs, 1); mm(mode, 0, 0, 21); SB(); };
  auto zero = [&]() {
#pragma unroll
    for (int i = 0; i < 4; ++i)
#pragma unroll
      for (int j = 0; j < 4; ++j) acc[i][j] = (f32x4){0.f, 0.f, 0.f, 0.f};
    accx[0] = accx[1] = csa[0] = csa[1] = csa[2] = (f32x4){0.f, 0.f, 0.f, 0.f}; };
  const std::integral_constant<int, 0> M0; const std::integral_constant<int, 1> M1; const std::integral_constant<int, 2> M2;
  const int jg0 = 4 * wf + 2 * wt, jg2 = 4 * wf + 2 - 2 * wt;

  zero();
  first(M0, 0, 0);
  STAMP(0);
#pragma unroll 1
  for (int it = 0; it < 2; ++it) { stage(M0, 1, 1, false); stage(M0, 2, 2, false); stage(M0, 3, 3, false); stage(M0, 0, 0, false); }
  mm(M0, 1, 18, 21); mm(M0, 1, 0, 8); SB();
  if (q == 0) { *(LAS float*)(sm + T_CS2 + (jg0 * 16 + c) * 4) = csa[0][0]; *(LAS float*)(sm + T_CS2 + (jg0 * 16 + 16 + c) * 4) = csa[1][0]; *(LAS float*)(sm + T_CS2 + (128 + 16 * wt + c) * 4) = csa[2][0]; }
  mm(M0, 1, 8, 18); SB();
  WAITL(0); BAR();
  STAMP(1);
  u32x4 z2p[2][4], z2x;
  { float cs[5];
    cs[0] = csa[0][0]; cs[1] = csa[1][0]; cs[2] = lds<float>(sm + T_CS2 + (jg2 * 16 + c) * 4); cs[3] = lds<float>(sm + T_CS2 + (jg2 * 16 + 16 + c) * 4);
    cs[4] = csa[2][0] + lds<float>(sm + T_CS2 + (144 - 16 * wt + c) * 4);
    float red[2][16];
#pragma unroll
    for (int i = 0; i < 4; ++i) {
      const int ro = (64 * wt + 32 * ((i >> 1) ^ wf) + 8 * q + 4 * (i & 1)) * 4;
      const f32x4 m = lds<f32x4>(sm + T_M1 + ro), a0 = lds<f32x4>(sm + T_A0 + ro), a1 = lds<f32x4>(sm + T_A1 + ro), ir = lds<f32x4>(sm + T_IR + ro);
      f32x4 su = {0.f, 0.f, 0.f, 0.f}, sq = {0.f, 0.f, 0.f, 0.f};
#pragma unroll
      for (int j = 0; j < (i < 2 ? 5 : 4); ++j) {
        const f32x4 z = gelu4((j < 4 ? acc[i][j] : accx[i]) - m * cs[j], a0, a1, ir);
        su = j ? su + z : z; sq = j ? sq + z * z : z * z;
        u32x4& dst = j < 4 ? z2p[i >> 1][j] : z2x;
        dst[2 * (i & 1)] = pk(z[0], z[1]); dst[2 * (i & 1) + 1] = pk(z[2], z[3]);
      }
#pragma unroll
      for (int e = 0; e < 4; ++e) { red[0][4 * i + e] = su[e]; red[1][4 * i + e] = sq[e]; }
    }
    float r8[2][8], r4[2][4];
#pragma unroll
    for (int st = 0; st < 2; ++st) {
#pragma unroll
      for (int e = 0; e < 8; e += 4)
        asm volatile("s_nop 1\n\t"
                     "v_add_f32_dpp %0, %4, %4 row_ror:8 row_mask:0xf bank_mask:0x3\n\tv_add_f32_dpp %1, %5, %5 row_ror:8 row_mask:0xf bank_mask:0x3\n\t"
                     "v_add_f32_dpp %2, %6, %6 row_ror:8 row_mask:0xf bank_mask:0x3\n\tv_add_f32_dpp %3, %7, %7 row_ror:8 row_mask:0xf bank_mask:0x3\n\t"
                     "v_add_f32_dpp %0, %8, %8 row_ror:8 row_mask:0xf bank_mask:0xc\n\tv_add_f32_dpp %1, %9, %9 row_ror:8 row_mask:0xf bank_mask:0xc\n\t"
                     "v_add_f32_dpp %2, %10, %10 row_ror:8 row_mask:0xf bank_mask:0xc\n\tv_add_f32_dpp %3, %11, %11 row_ror:8 row_mask:0xf bank_mask:0xc"
                     : "=&v"(r8[st][e]), "=&v"(r8[st][e + 1]), "=&v"(r8[st][e + 2]), "=&v"(r8[st][e + 3])
                     : "v"(red[st][e]), "v"(red[st][e + 1]), "v"(red[st][e + 2]), "v"(red[st][e + 3]), "v"(red[st][8 + e]), "v"(red[st][9 + e]), "v"(red[st][10 + e]), "v"(red[st][11 + e]));
      asm volatile("s_nop 1\n\t"
                   "v_add_f32_dpp %0, %4, %4 row_half_mirror row_mask:0xf bank_mask:0x5\n\tv_add_f32_dpp %1, %5, %5 row_half_mirror row_mask:0xf bank_mask:0x5\n\t"
                   "v_add_f32_dpp %2, %6, %6 row_half_mirror row_mask:0xf bank_mask:0x5\n\tv_add_f32_dpp %3, %7, %7 row_half_mirror row_mask:0xf bank_mask:0x5\n\t"
                   "v_add_f32_dpp %0, %8, %8 row_half_mirror row_mask:0xf bank_mask:0xa\n\tv_add_f32_dpp %1, %9, %9 row_half_mirror row_mask:0xf bank_mask:0xa\n\t"
                   "v_add_f32_dpp %2, %10, %10 row_half_mirror row_mask:0xf bank_mask:0xa\n\tv_add_f32_dpp %3, %11, %11 row_half_mirror row_mask:0xf bank_mask:0xa\n\t"
                   "v_add_f32_dpp %0, %0, %0 quad_perm:[1,0,3,2] row_mask:0xf bank_mask:0xf\n\tv_add_f32_dpp %1, %1, %1 quad_perm:[1,0,3,2] row_mask:0xf bank_mask:0xf\n\t"
                   "v_add_f32_dpp %2, %2, %2 quad_perm:[1,0,3,2] row_mask:0xf bank_mask:0xf\n\tv_add_f32_dpp %3, %3, %3 quad_perm:[1,0,3,2] row_mask:0xf bank_mask:0xf\n\t"
                   "v_add_f32_dpp %0, %0, %0 quad_perm:[2,3,0,1] row_mask:0xf bank_mask:0xf\n\tv_add_f32_dpp %1, %1, %1 quad_perm:[2,3,0,1] row_mask:0xf bank_mask:0xf\n\t"
                   "v_add_f32_dpp %2, %2, %2 quad_perm:[2,3,0,1] row_mask:0xf bank_mask:0xf\n\tv_add_f32_dpp %3, %3, %3 quad_perm:[2,3,0,1] row_mask:0xf bank_mask:0xf"
                   : "=&v"(r4[st][0]), "=&v"(r4[st][1]), "=&v"(r4[st][2]), "=&v"(r4[st][3])
                   : "v"(r8[st][0]), "v"(r8[st][1]), "v"(r8[st][2]), "v"(r8[st][3]), "v"(r8[st][4]), "v"(r8[st][5]), "v"(r8[st][6]), "v"(r8[st][7]));
    }
    if ((c & 3) == 0) { LAS char* pp = sm + T_PART + wf * 1024 + (64 * wt + 32 * ((c >> 3) ^ wf) + 8 * q + 4 * ((c >> 2) & 1)) * 8;
      *(LAS f32x4*)pp = (f32x4){r4[0][0], r4[1][0], r4[0][1], r4[1][1]}; *(LAS f32x4*)(pp + 16) = (f32x4){r4[0][2], r4[1][2], r4[0][3], r4[1][3]}; }
#pragma unroll
    for (int i = 0; i < 2; ++i) asm volatile("" : "+v"(z2p[i][0]), "+v"(z2p[i][1]), "+v"(z2p[i][2]), "+v"(z2p[i][3]));
    asm volatile("" : "+v"(z2x));
  }
  STAMP(2);
  zero();
  first(M1, 0, 1);
  stage(M1, 3, 2, false); stage(M1, 2, 3, false); stage(M1, 1, 0, false);
  stage(M1, 0, 1, false); stage(M1, 3, 2, false); stage(M1, 2, 3, false);
  STAMP(3);
  {
#pragma unroll
    for (int i = 0; i < 2; ++i) asm volatile("" : "+v"(z2p[i][0]), "+v"(z2p[i][1]), "+v"(z2p[i][2]), "+v"(z2p[i][3]));
    asm volatile("" : "+v"(z2x));
#pragma unroll
    for (int ip = 0; ip < 2; ++ip) {
      const int ro = (64 * wt + 32 * (ip ^ wf) + 8 * q) * 4;
      const f32x4 r0 = lds<f32x4>(sm + T_R2 + ro), r1 = lds<f32x4>(sm + T_R2 + ro + 16), n0 = lds<f32x4>(sm + T_N2 + ro), n1 = lds<f32x4>(sm + T_N2 + ro + 16);
      const unsigned so = W_ZF + ((id.ft * 64 + 4 * id.tp + 2 * wt + (ip ^ wf)) * 9) * 1024;
#pragma unroll
      for (int j = 0; j < (ip == 0 ? 5 : 4); ++j) { const u32x4 z = j < 4 ? z2p[ip][j] : z2x;
        const f32x4 a = (f32x4){blo(z[0]), bhi(z[0]), blo(z[1]), bhi(z[1])} * r0 + n0, b = (f32x4){blo(z[2]), bhi(z[2]), blo(z[3]), bhi(z[3])} * r1 + n1;
        st16<SC1>((u32x4){pk(a[0], a[1]), pk(a[2], a[3]), pk(b[0], b[1]), pk(b[2], b[3])}, rws, lane * 16, so + (j < 2 ? jg0 + j : j < 4 ? jg2 + j - 2 : 8) * 1024); }
    }
    SB();
  }
  unsigned braw[4];
  { const rsrc_t rb = mkrs(P.bsp, S * 2);
#pragma unroll
    for (int i = 0; i < 4; ++i) braw[i] = __builtin_amdgcn_raw_buffer_load_b16(rb, (id.tp * 128 + 64 * wt + prow(i ^ (2 * wf), c)) * 2, 0, 0); }
  STAMP(4);
  stage(M1, 1, 0, false); stage(M1, 0, 1, true);
  mm(M1, 1, 18, 21); mm(M1, 1, 0, 8); SB();
  if (c == 0) { *(LAS f32x4*)(sm + T_CS1 + (jg0 * 16 + 4 * q) * 4) = csa[0]; *(LAS f32x4*)(sm + T_CS1 + (jg0 * 16 + 16 + 4 * q) * 4) = csa[1]; *(LAS f32x4*)(sm + T_CS1 + (128 + 16 * wt + 4 * q) * 4) = csa[2]; }
  mm(M1, 1, 8, 18); SB();
  WAITL(0); BAR();
  STAMP(5);
  u32x2 z1p[4][4], z1x[2];
  { f32x4 cs[5];
    cs[0] = csa[0]; cs[1] = csa[1]; cs[2] = lds<f32x4>(sm + T_CS1 + (jg2 * 16 + 4 * q) * 4); cs[3] = lds<f32x4>(sm + T_CS1 + (jg2 * 16 + 16 + 4 * q) * 4);
    cs[4] = csa[2] + lds<f32x4>(sm + T_CS1 + (144 - 16 * wt + 4 * q) * 4);
#pragma unroll
    for (int i = 0; i < 4; ++i) { const int ro = (64 * wt + prow(i ^ (2 * wf), c)) * 4;
      const float m = lds<float>(sm + T_M1 + ro), a0 = lds<float>(sm + T_A0 + ro), a1 = lds<float>(sm + T_A1 + ro), ir = lds<float>(sm + T_IR + ro), bias = blo(braw[i]);
#pragma unroll
      for (int j = 0; j < (i < 2 ? 5 : 4); ++j) {
        f32x4& a = j < 4 ? acc[i][j] : accx[i];
        const f32x4 z = gelu4(a - m * cs[j], a0, a1, ir);
        (j < 4 ? z1p[i][j] : z1x[i]) = (u32x2){pk(z[0], z[1]), pk(z[2], z[3])};
        a = (f32x4){bias, bias, bias, bias};
      } }
#pragma unroll
    for (int s = 0; s < 2; ++s) { fx[s] = sm + BBASE + (s * 9 + 8) * 1024 + lane * 16;
#pragma unroll
      for (int h = 0; h < 2; ++h) { fb[s][h] = sm + BBASE + (s * 9 + (h ? jg2 : jg0)) * 1024 + lane * 16; ta[s][h] = sm + (64 * wt + 32 * (h ^ wf) + c) * 128 + (((q + 4 * s) ^ ((c >> 1) & 7)) * 16); } }
#pragma unroll
    for (int i = 0; i < 4; ++i) asm volatile("" : "+v"(z1p[i][0]), "+v"(z1p[i][1]), "+v"(z1p[i][2]), "+v"(z1p[i][3]), "+v"(acc[i][0]), "+v"(acc[i][1]), "+v"(acc[i][2]), "+v"(acc[i][3]));
    asm volatile("" : "+v"(z1x[0]), "+v"(z1x[1]), "+v"(accx[0]), "+v"(accx[1]));
  }
  STAMP(6);
  const int jq = id.b & 3;
  LAS char *cs_ = sm + CV + 2 * CV_CH, *cb = sm + CV + wt * CV_CH;
  LAS char *am[4], *um = cb + CV_U + (2032 - 16 * c + 8 * q) * 2 + 256 * wf - 3584;
#pragma unroll
  for (int i = 0; i < 4; ++i) { am[i] = cb + ((c & 1) ? 0 : CV_O) + (4 * q + ((c + 1) >> 1)) * 4 + 256 * wf + 1024 * i; asm volatile("" : "+v"(am[i])); }
  asm volatile("" : "+v"(um));
  bf16x8 ca[2][8], cu[2][12];
  auto afrag = [&](LAS char* p) { const unsigned d0 = lds<unsigned>(p), d1 = lds<unsigned>(p + 4), d2 = lds<unsigned>(p + 8), d3 = lds<unsigned>(p + 12);
    return __builtin_bit_cast(bf16x8, (u32x4){d0, d1, d2, d3}); };
  auto lds_mn = [&](int b, int kk) {
#pragma unroll
    for (int e = 0; e < 8; ++e) { cu[b][e] = lds<bf16x8>(um + kk * 64 + 512 * (7 - e)); if (e == 0) ca[b][0] = afrag(am[0] + kk * 64); }
#pragma unroll
    for (int e = 1; e < 8; ++e) ca[b][e] = afrag(am[e >> 1] + kk * 64 + (e & 1) * 512); };
  first(M2, 3, 2);
  STAMP(7);
#pragma unroll 1
  for (int it = 0; it < 7; ++it) { stage(M2, 2, 3, false); stage(M2, 1, 0, false); stage(M2, 0, 1, false); stage(M2, 3, 2, false); }
  stage(M2, 2, 3, false); stage(M2, 1, 0, false); stage(M2, 0, 1, false);
  mm(M2, 1, 0, 4); SB(); WAITL(0); BAR(); SB();
  mm(M2, 1, 4, 18); SB();
  lds_mn(0, 0); SB();
  STAMP(8);
#pragma unroll
  for (int i = 0; i < 4; ++i) asm volatile("" : "+v"(z1p[i][0]), "+v"(z1p[i][1]), "+v"(z1p[i][2]), "+v"(z1p[i][3]));
  asm volatile("" : "+v"(z1x[0]), "+v"(z1x[1]));
#pragma unroll
  for (int i = 0; i < 4; ++i) {
    const unsigned so = W_HF + (id.tp * 8 + 4 * wt + (i ^ (2 * wf))) * 72 * 1024;
    auto gate = [&](u32x2 z, f32x4 a) { const f32x4 h = (f32x4){blo(z.x), bhi(z.x), blo(z.y), bhi(z.y)} * a; return (u32x2){pk(h[0], h[1]), pk(h[2], h[3])}; };
#pragma unroll
    for (int pp = 0; pp < 2; ++pp) { const u32x2 lo = gate(z1p[i][2 * pp], acc[i][2 * pp]), hi = gate(z1p[i][2 * pp + 1], acc[i][2 * pp + 1]);
      st16<SC1>((u32x4){lo.x, lo.y, hi.x, hi.y}, rws, lane * 16, so + (4 * id.ft + 2 * wf + (pp ^ wt)) * 1024); }
    if (i < 2) st8<SC1>(gate(z1x[i], accx[i]), rws, lane * 16 + (id.ft & 1) * 8, so + (64 + (id.ft >> 1)) * 1024);
  }
  const int g = id.ft & 7, gp = g & 1, half = id.ft >> 3, rot = krot(id);
  const unsigned wso = W_WOF + ((((EXP & 4) ? 0 : g >> 1) * 72 + 18 * wid) * 9) * 1024;
  u32x4 wr[WD][5];
  auto ldw = [&](int slot, int i) {
#pragma unroll
    for (int t = 0; t < 5; ++t) wr[slot][t] = ld16<(EXP & 8) ? 0 : SC1>(rws, lane * 16, wso + kwrap(i, rot) * 9216 + (t == 4 ? 4 : t + gp * 5) * 1024); };
  SB();
  SB(); BAR(); SB();
  STAMP(9);
  u32x4 hr[3][4];
  const unsigned hso = W_HF + (((EXP & 2) ? (id.b & 7) * 4 : id.tp * 8 + 4 * half) * 72 + 18 * wid) * 1024;
  auto ldh = [&](int slot, int i) {
#pragma unroll
    for (int t = 0; t < 4; ++t) hr[slot][t] = ld16<(EXP & 8) ? 0 : SC1>(rws, lane * 16, hso + (((unsigned)t ^ (wf ^ (2 * gp))) * 72 + kwrap(i, rot)) * 1024); };
  { const float dvm = wt ? bhi(dm) : blo(dm), dvs = (id.b & 4) ? bhi(dsh) : blo(dsh);
    f32x4 cm[8], s0 = {0.f, 0.f, 0.f, 0.f}, s1 = s0;
#pragma unroll
    for (int j = 0; j < 8; ++j) cm[j] = s0;
    LAS char *as[4], *u1, *u0;
    auto addr_sh = [&]() {
      const int da = (2 - wt) * CV_CH + 128 * wid - 256 * wf;
#pragma unroll
      for (int i = 0; i < 4; ++i) { as[i] = am[i] + da; asm volatile("" : "+v"(as[i])); }
      u1 = um + da + 512 * jq; u0 = um + da + 512 * (7 - jq); asm volatile("" : "+v"(u1), "+v"(u0)); };
    auto lds_sh = [&](int b, int kk) {
#pragma unroll
      for (int e = 0; e < 8; ++e) { ca[b][e] = afrag(as[e >> 1] + kk * 64 + (e & 1) * 512); cu[b][e] = lds<bf16x8>(u1 + kk * 64 + 512 * e);
        if (e < 4) cu[b][8 + e] = lds<bf16x8>(u0 + kk * 64 + 512 * e); } };
    auto mm_sh = [&](int b) {
#pragma unroll
      for (int e = 0; e < 8; ++e) { s1 = MFMA(ca[b][e], cu[b][e], s1); if (e < 4) s0 = MFMA(ca[b][e], cu[b][8 + e], s0); } };
    auto mm_mn = [&](int b) {
#pragma unroll
      for (int e = 0; e < 8; ++e)
#pragma unroll
        for (int j = e; j < 8; ++j) cm[j] = MFMA(ca[b][e], cu[b][j - e], cm[j]); };
#define il(nr, per) do { for (int i_ = 0; i_ < nr; ++i_) { SG(0x100, per); SG(0x8, 1); } SG(0x8, 36); SB(); } while (0)
#define ilv() do { for (int i_ = 0; i_ < 5; ++i_) { SG(0x100, 1); SG(0x8, 1); SG(0x100, 1); SG(0x8, 1); SG(0x100, 1); SG(0x8, 1); SG(0x100, 1); SG(0x8, 1); SG(0x100, 2); SG(0x8, 2); SG(0x20, 1); SG(0x8, 1); } SG(0x8, 36); SB(); } while (0)
    lds_mn(1, 1); mm_mn(0); il(24, 1); STAMP(15);
    lds_mn(0, 2); mm_mn(1); il(24, 1); STAMP(16); addr_sh(); SB();
    lds_sh(1, 0); mm_mn(0); il(28, 1);
    lds_sh(0, 1); mm_sh(1); il(10, 3);
    lds_mn(1, 3); mm_sh(0); il(12, 2);
    if (wid != 0) *(LAS f32x4*)(sm + CV_PS + (wid - 1) * 1024 + lane * 16) = s0;
    if (wid != 1) *(LAS f32x4*)(sm + CV_PS + (3 + (wid ? wid - 1 : 0)) * 1024 + lane * 16) = s1;
    SB(); STAMP(17); WAITV(0); WAITL(0); BAR(); SB();
    STAMP(10);
    f32x4 ys = wf ? s1 : s0;
    if (wid < 2) {
#pragma unroll
      for (int w = 0; w < 3; ++w) ys += lds<f32x4>(sm + CV_PS + (3 * wid + w) * 1024 + lane * 16);
    }
    SB(); ldw(0, 5); ldw(1, 6); if (WD > 2) ldw(2, 7); mm_mn(1); SG(0x8, 4); for (int i_ = 0; i_ < 15; ++i_) { SG(0x8, 2); SG(0x20, 1); } SG(0x8, 36); SB();
    LAS char* pm = sm + (wt ? CV_PM23 : CV + 2 * CV_CH);
#pragma unroll
    for (int j = 0; j < 4; ++j) { f32x4 lo = cm[j], hi = cm[4 + j]; asm volatile("" : "+v"(lo), "+v"(hi));
      *(LAS f32x4*)(pm + wf * 4096 + j * 1024 + lane * 16) = wf ? lo : hi; cm[j] = wf ? hi : lo; }
    SB(); WAITL(0); BAR(); SB();
    STAMP(11);
    ldh(0, 0); ldh(1, 1); ldh(2, 2);
    auto yout = [&](f32x4 a, int chn, LAS char* ub, int j, float dv) {
      const u32x2 u = lds<u32x2>(ub + CV_U + (2044 - 256 * j - 16 * c - 4 * q) * 2);
      const f32x4 y = a + (f32x4){bhi(u.y), blo(u.y), bhi(u.x), blo(u.x)} * dv;
      st8<SC1>((u32x2){pk(y[0], y[1]), pk(y[2], y[3])}, rws, (16 * c + 4 * q) * 2, W_YT + chn * (S * 2) + j * 512); };
#pragma unroll
    for (int j = 0; j < 4; ++j) yout(cm[j] + lds<f32x4>(pm + (wf ^ 1) * 4096 + j * 1024 + lane * 16), 2 * id.b + wt, cb, 4 * wf + j, dvm);
    if (wid < 2) yout(ys, 512 + (id.b >> 2), cs_, wid ? 7 - jq : jq, dvs);
    SB();
  }
  STAMP(19);
  f32x4 o[4][4], ox[2];
#pragma unroll
  for (int i = 0; i < 4; ++i)
#pragma unroll
    for (int t = 0; t < 4; ++t) o[i][t] = (f32x4){0.f, 0.f, 0.f, 0.f};
  ox[0] = ox[1] = (f32x4){0.f, 0.f, 0.f, 0.f};
  const int dp = 144 * (g >> 1) + 80 * gp, ds = 144 * (g >> 1) + 64, trow = 64 * half + prow(wid, c);
  const unsigned xo = (id.tp * 128 + trow) * (D * 2);
  f32x4 rs[5];
  { LAS char* wl = sm + wid * G3W + lane * 16;
    bf16x8 wq[2][5];
#pragma unroll
    for (int t = 0; t < 5; ++t) wq[0][t] = lds<bf16x8>(wl + t * 1024);
#pragma unroll
    for (int i = 0; i < 18; ++i) {
      if (i < 4) {
#pragma unroll
        for (int t = 0; t < 5; ++t) wq[(i + 1) & 1][t] = lds<bf16x8>(wl + ((i + 1) * 5 + t) * 1024); }
#pragma unroll
      for (int r = 0; r < 4; ++r) { const bf16x8 hf = __builtin_bit_cast(bf16x8, hr[i % 3][r]);
#pragma unroll
        for (int t = 0; t < 5; ++t) if (t < 4 || r < 2) { const int tt = (r & 1) && t < 4 ? 3 - t : t;
          const bf16x8 wf_ = i < 5 ? wq[i & 1][tt] : __builtin_bit_cast(bf16x8, wr[(i - 5) % WD][tt]);
          if (tt < 4) o[r][tt] = MFMA(wf_, hf, o[r][tt]); else ox[r] = MFMA(wf_, hf, ox[r]); } }
      SB();
      if (i == 0 || i == 2 || i == 4 || i == 7 || i == 10 || i == 13 || i == 15) STAMP(20 + i);
      if (i + 3 < 18) ldh(i % 3, i + 3);
      if (i >= 5 && i + WD < 18) ldw((i - 5) % WD, i + WD);
      if (i == 2) { WAITV(12); if (lane == 0) __hip_atomic_fetch_add((LAS unsigned*)(sm + T_CNT), 1u, __ATOMIC_RELAXED, __HIP_MEMORY_SCOPE_WORKGROUP); }
      if (i == 16) {
        const u64 t0 = __builtin_amdgcn_s_memrealtime();
        while (*(volatile LAS unsigned*)(sm + T_YOK) == 0u && !expired(t0)) __builtin_amdgcn_s_sleep(1);
        FENCE();
        const float m = lds<float>(sm + T_M1 + trow * 4), r = lds<float>(sm + T_R1 + trow * 4);
        LAS char* yb = sm + YTILE + (c >> 2) * 128 + (32 * wt + 4 * wf + 8 * (c & 3)) * 2;
        auto res = [&](u32x2 xv, bf16x4 yv) { const u32x2 yu = __builtin_bit_cast(u32x2, yv);
          const f32x4 xf = {blo(xv.x), bhi(xv.x), blo(xv.y), bhi(xv.y)}, yf = {blo(yu.x), bhi(yu.x), blo(yu.y), bhi(yu.y)};
          return xf + (xf - m) * r * yf; };
        const int yp = 16 * gp, ysh = 64 * (gp ^ 1);
        LAS char* xb = sm + XTILE + prow(wid, c) * 160;
        const u32x4 x0 = lds<u32x4>(xb + yp * 2 + q * 16), x1 = lds<u32x4>(xb + yp * 2 + 64 + q * 16); const u32x2 x2 = lds<u32x2>(xb + ysh * 2 + q * 8);
        rs[0] = res(x0.xy, ldtr(yb + (yp + 8 * q) * 128)); rs[1] = res(x0.zw, ldtr(yb + (yp + 8 * q + 4) * 128));
        rs[2] = res(x1.xy, ldtr(yb + (yp + 32 + 8 * q) * 128)); rs[3] = res(x1.zw, ldtr(yb + (yp + 36 + 8 * q) * 128));
        rs[4] = res(x2, ldtr(yb + (ysh + 4 * q) * 128));
        asm volatile("" : "+v"(rs[0]), "+v"(rs[1]), "+v"(rs[2]), "+v"(rs[3]), "+v"(rs[4])); }
      SB();
    }
  }
  STAMP(12);
  const bool own0 = wt == gp;
  { LAS char* pw = sm + wid * G3W + lane * 16;
    auto put = [&](int r) { LAS char* p = pw + ((unsigned)r ^ (wf ^ (2 * gp))) * 5120;
#pragma unroll
      for (int t = 0; t < 4; ++t) *(LAS f32x4*)(p + t * 1024) = o[r][t];
      if (r < 2) *(LAS f32x4*)(p + 4096) = ox[r]; };
    put(1); put(3);
    if (own0) { put(2);
#pragma unroll
      for (int t = 0; t < 4; ++t) rs[t] += o[0][t];
      rs[4] += ox[0]; }
    else { put(0);
#pragma unroll
      for (int t = 0; t < 4; ++t) rs[t] += o[2][t]; }
  }
  SB(); WAITL(0); BAR(); SB();
  STAMP(13);
  { f32x4 pt[3][5];
#pragma unroll
    for (int t = 0; t < 5; ++t)
#pragma unroll
      for (int w = 0; w < 3; ++w) pt[w][t] = lds<f32x4>(sm + (wid ^ (w + 1)) * G3W + wid * 5120 + lane * 16 + t * 1024);
    const rsrc_t ro = mkrs(P.out, S * D * 2);
    auto fin = [&](int t) { const f32x4 v = (rs[t] + pt[0][t]) + (pt[1][t] + pt[2][t]); return (u32x2){pk(v[0], v[1]), pk(v[2], v[3])}; };
#pragma unroll
    for (int pp = 0; pp < 2; ++pp) { const u32x2 lo = fin(2 * pp), hi = fin(2 * pp + 1); st16<0>((u32x4){lo.x, lo.y, hi.x, hi.y}, ro, xo + q * 16, dp * 2 + 64 * pp); }
    const u32x2 ls = fin(4);
    if (own0) st8<0>(ls, ro, xo + q * 8, ds * 2);
  }
  STAMP(14);
}
}

extern "C" __global__ __launch_bounds__(512, 2) void h3_gmlp_fused(const Params P) {
  extern __shared__ __attribute__((aligned(16))) char smem_[];
  LAS char* sm = (LAS char*)smem_;
  const int wid = __builtin_amdgcn_readfirstlane(threadIdx.x >> 6);
  Ids id; id.b = blockIdx.x; id.lane = threadIdx.x & 63;
  const int xcd = id.b & 7, loc = id.b >> 3;
  id.tp = (xcd >> 1) * 4 + (loc >> 3); id.ft = (xcd & 1) * 8 + (loc & 7);
  if (wid == 0) { unsigned sink = 0; code_touch(id, sink); asm volatile("s_waitcnt vmcnt(0)" : "+v"(sink) :: "memory"); }
  if (__builtin_expect(wid == 4 || wid == 5, 1)) role_L(P, sm, id, wid - 4 STAMP_PASS);
  else if (wid >= 6) role_X(P, sm, id, wid - 6 STAMP_PASS);
  else role_C(P, sm, id, wid STAMP_PASS);
}
extern "C" __global__ void h3_gmlp_end() {}

extern "C" void kernel_launch(void* const* d_in, const int* in_sizes, int n_in, void* d_out, int out_size, void* d_ws, size_t ws_size, hipStream_t stream) {
  Params p;
  p.x = (const u16*)d_in[0]; p.k = (const u16*)d_in[1]; p.v = (const u16*)d_in[2]; p.shk = (const u16*)d_in[3]; p.ssmk = (const u16*)d_in[4]; p.ssmd = (const u16*)d_in[5];
  p.win = (const u16*)d_in[7]; p.wsp = (const u16*)d_in[8]; p.bsp = (const u16*)d_in[9]; p.wout = (const u16*)d_in[10];
  p.out = (u16*)d_out; p.ws = (char*)d_ws;
  (void)hipFuncSetAttribute((const void*)h3_gmlp_fused, hipFuncAttributeMaxDynamicSharedMemorySize, LDS_BYTES);
  h3_gmlp_fused<<<256, 512, LDS_BYTES, stream>>>(p);
}
```

```cpp
#define OPT_TWIN 16
#define TWZ 2
#include <hip/hip_runtime.h>
#include <hip/hip_bf16.h>
#include <type_traits>

namespace {
typedef unsigned short u16;
typedef unsigned long long u64;
typedef short bf16x8 __attribute__((ext_vector_type(8)));
typedef short bf16x4 __attribute__((ext_vector_type(4)));
typedef __bf16 bf2 __attribute__((ext_vector_type(2)));
typedef float f32x2 __attribute__((ext_vector_type(2)));
typedef float f32x4 __attribute__((ext_vector_type(4)));
typedef unsigned u32x2 __attribute__((ext_vector_type(2)));
typedef unsigned u32x4 __attribute__((ext_vector_type(4)));
typedef __amdgpu_buffer_rsrc_t rsrc_t;
#define LAS __attribute__((address_space(3)))
#define CAS __attribute__((address_space(4)))
#define DEV __device__ __forceinline__

constexpr int S = 2048, D = 576, F = 4608, FH = 2304;
constexpr int ASLOT = 16384, BSLOT = 18432, BBASE = 4 * ASLOT;
constexpr int STG = 139264, STG_W = 9216;
constexpr int T_M1 = 157952, T_R1 = T_M1 + 512, T_A0 = T_R1 + 512, T_A1 = T_A0 + 512, T_IR = T_A1 + 512;
constexpr int T_CS2 = T_IR + 512, T_CS1 = T_CS2;
constexpr int T_PART = T_CS1 + 640, T_N2 = T_PART, T_R2 = T_N2 + 512;
constexpr int T_TAG = T_PART + 2048, T_CNT = T_TAG + 16, T_GO = T_CNT + 4, T_YOK = T_CNT + 8;
constexpr int LDS_BYTES = 163840;
static_assert(T_CNT + 16 <= LDS_BYTES, "LDS map");
constexpr int G3W = 25600;
constexpr int CV = 102400, CV_CH = 13056, CV_O = 4160, CV_U = 8320;
constexpr int CV_PS = 143616, CV_PM23 = 149760;
constexpr int YTILE = 122880, XTILE = 133120;
constexpr int W_EPOCH = 0, W_F2 = 4096, W_F3 = 8192, W_F4 = 12288, W_F5 = 16384;
constexpr int W_G0X = 0x8000, W_G1F = 0x10000, W_G1S = 0x60000;
constexpr int W_KVT = 1 << 20, W_YT = 4 << 20, W_WOF = 7 << 20, W_ZF = 10 << 20, W_HF = 20 << 20, W_END = 30 << 20;
constexpr int SC1 = 16;
#define OPT_TWSP 1
#define OPT_TG3 0
#define EXP 0
#define RH 0
#define RW 0
#define KB 1
#define PR 0
#define XM 0
#define RT 0
#define WD 3

#define BAR0() do { asm volatile("" ::: "memory"); __builtin_amdgcn_s_barrier(); asm volatile("" ::: "memory"); } while (0)
#define BAR() BAR0()
#define SB() __builtin_amdgcn_sched_barrier(0)
#define SG(m, n) __builtin_amdgcn_sched_group_barrier(m, n, 0)
#define WAITV(n) asm volatile("s_waitcnt vmcnt(%0)" ::"i"(n) : "memory")
#define WAITL(n) asm volatile("s_waitcnt lgkmcnt(%0)" ::"i"(n) : "memory")
#define FENCE() asm volatile("" ::: "memory")
#define MFMA(a, b, c) __builtin_amdgcn_mfma_f32_16x16x32_bf16(a, b, c, 0, 0, 0)
#define STAMP(i) do {} while (0)
#define LBAR() BAR()
#define STAMP_ARG
#define STAMP_PASS

struct Params {
  const u16 *x, *k, *v, *shk, *ssmk, *ssmd, *win, *wsp, *bsp, *wout;
  u16* out; char* ws;
};

DEV rsrc_t mkrs(const void* p, unsigned bytes) { return __builtin_amdgcn_make_buffer_rsrc((void*)p, 0, bytes, 0x00020000); }
template <int AUX> DEV void dma(rsrc_t rs, LAS char* l, unsigned voff, unsigned soff) { __builtin_amdgcn_raw_ptr_buffer_load_lds(rs, (LAS void*)l, 16, voff, soff, 0, AUX); }
DEV void dma_hidden(rsrc_t rs, LAS char* l, unsigned voff) { unsigned keep;
  asm volatile("s_mov_b32 %0, m0\n\ts_mov_b32 m0, %3\n\ts_nop 0\n\tbuffer_load_dwordx4 %1, %2, 0 offen lds\n\ts_mov_b32 m0, %0" : "=&s"(keep) : "v"(voff), "s"(rs), "s"((unsigned)(size_t)l) : "memory"); }
template <int AUX> DEV u32x4 ld16(rsrc_t rs, unsigned voff, unsigned soff) { return __builtin_amdgcn_raw_buffer_load_b128(rs, voff, soff, AUX); }
template <int AUX> DEV void st16(u32x4 v, rsrc_t rs, unsigned voff, unsigned soff) { __builtin_amdgcn_raw_buffer_store_b128(v, rs, voff, soff, AUX); }
template <int AUX> DEV void st8(u32x2 v, rsrc_t rs, unsigned voff, unsigned soff) { __builtin_amdgcn_raw_buffer_store_b64(v, rs, voff, soff, AUX); }
template <class T> DEV T lds(LAS char* p) { return *(LAS T*)p; }
DEV bf16x4 ldtr(LAS char* p) { return __builtin_amdgcn_ds_read_tr16_b64_v4i16((LAS bf16x4*)p); }
DEV bf16x8 cat(bf16x4 a, bf16x4 b) { return __builtin_shufflevector(a, b, 0, 1, 2, 3, 4, 5, 6, 7); }
DEV float blo(unsigned u) { return __uint_as_float(u << 16); }
DEV float bhi(unsigned u) { return __uint_as_float(u & 0xffff0000u); }
DEV unsigned pk(float a, float b) { return __builtin_bit_cast(unsigned, __builtin_convertvector((f32x2){a, b}, bf2)); }
DEV float dot2(unsigned a, unsigned b, float c) { return __builtin_amdgcn_fdot2_f32_bf16(__builtin_bit_cast(bf2, a), __builtin_bit_cast(bf2, b), c, false); }
template <int CTRL> DEV float dpp(float x) { return __builtin_bit_cast(float, __builtin_amdgcn_mov_dpp(__builtin_bit_cast(int, x), CTRL, 0xf, 0xf, true)); }
constexpr int XOR1 = 0xB1, XOR2 = 0x4E, XOR7 = 0x141, XOR8 = 0x128;
constexpr float GC0 = -2.30220819f, GC1 = -0.10294324f;
template <class T> DEV f32x2 gelu2(f32x2 a, T a0, T a1, T ir) {
  const f32x2 g = a * (a * a * a1 + a0);
  f32x2 e; e.x = __builtin_amdgcn_exp2f(g.x); e.y = __builtin_amdgcn_exp2f(g.y);
  e = e * ir + ir;
  f32x2 r; r.x = __builtin_amdgcn_rcpf(e.x); r.y = __builtin_amdgcn_rcpf(e.y);
  return a * r;
}
DEV f32x4 gelu4(f32x4 a, f32x4 a0, f32x4 a1, f32x4 ir) { const f32x2 p = gelu2(a.xy, a0.xy, a1.xy, ir.xy), q = gelu2(a.zw, a0.zw, a1.zw, ir.zw); return (f32x4){p.x, p.y, q.x, q.y}; }
DEV f32x4 gelu4(f32x4 a, float a0, float a1, float ir) { const f32x2 p = gelu2(a.xy, a0, a1, ir), q = gelu2(a.zw, a0, a1, ir); return (f32x4){p.x, p.y, q.x, q.y}; }
DEV u32x4 seal(float a, float b, u32x2 tag) { const unsigned ua = __float_as_uint(a), ub = __float_as_uint(b); return (u32x4){ua, ub, ua ^ tag.x, ub ^ tag.y}; }
DEV bool sealed(u32x4 g, u32x2 tag) { return (g.x ^ g.z) == tag.x && (g.y ^ g.w) == tag.y; }
DEV bool expired(u64 t0) { return __builtin_amdgcn_s_memrealtime() - t0 > 2000000ull; }
template <int NL, int NAP> DEV void wait_flags(rsrc_t ws, unsigned voff, u32x2 tag) {
  const u64 t0 = __builtin_amdgcn_s_memrealtime();
  for (;;) {
    bool ok = true;
#pragma unroll
    for (int i = 0; i < NL; ++i) { const u32x4 f = ld16<SC1>(ws, voff, i * 1024); ok &= f.x == tag.x && f.y == tag.y && f.z == tag.x && f.w == tag.y; }
    if (__all(ok) || expired(t0)) break;
    __builtin_amdgcn_s_sleep(NAP); FENCE();
  }
  FENCE();
}
DEV bool flags_ok(u32x4 f, u32x2 tag) { return f.x == tag.x && f.y == tag.y && f.z == tag.x && f.w == tag.y; }
DEV int prow(int T, int c) { return 32 * (T >> 1) + 8 * (c >> 2) + 4 * (T & 1) + (c & 3); }
DEV void set_flag(rsrc_t ws, unsigned off, u32x2 tag, int lane) { if (lane == 0) st8<SC1>(tag, ws, off, 0); }

struct Ids { int b, tp, ft, lane; };
DEV void code_touch(const Ids& id, unsigned& sink) {
  unsigned lo, hi, bytes;
  asm volatile("s_getpc_b64 vcc\n1:\n\ts_sub_u32 %0, vcc_lo, 1b-h3_gmlp_fused\n\ts_subb_u32 %1, vcc_hi, 0\n\ts_mov_b32 %2, h3_gmlp_end-h3_gmlp_fused" : "=s"(lo), "=s"(hi), "=s"(bytes) :: "vcc", "scc");
  const rsrc_t rc = mkrs((const void*)(((u64)hi << 32) | lo), bytes);
  asm volatile("buffer_load_dword %0, %1, %2, 0 offen" : "+v"(sink) : "v"((id.lane < 20 ? id.lane * 32 + (id.b >> 3) : 0x100000) * 128), "s"(rc) : "memory");
}
DEV int krot(const Ids& id) { return (RH * (id.ft & 7) + ((RW * (id.tp & 3)) >> 1)) % 18; }
DEV int kwrap(int i, int r) { const int k = i + r; return k >= 18 ? k - 18 : k; }

DEV void role_L(const Params& P, LAS char* sm, const Ids id, const int lw STAMP_ARG) {
  int bcount = 0; (void)bcount;
  const int lane = id.lane, rr = lane >> 3, sl = lane & 7;
  const rsrc_t rx = mkrs(P.x, S * D * 2), rwin = mkrs(P.win, D * F * 2);
  const unsigned swz = (sl ^ (4 * lw + (rr >> 1))) * 16, prr = 16 * lw + 8 * (rr >> 2) + (rr & 3);
  const unsigned vx = prr * (D * 2) + swz;
  unsigned vb[9];
  auto issue1a = [&](int slot, int i, int ks) { dma<0>(rx, sm + slot * ASLOT + lw * 1024 + i * 2048, vx, (id.tp * 128 + 32 * (i >> 1) + 4 * (i & 1)) * (D * 2) + ks * 128); };
  auto issue1b = [&](int slot, int i, int ks, int col) { dma<0>(rwin, sm + BBASE + slot * BSLOT + lw * 1024 + i * 2048, vb[i], ks * 64 * (F * 2) + (col + id.ft * 144) * 2); };
#pragma unroll
  for (int i = 0; i < 9; ++i) {
    if (i < 8) issue1a(0, i, 0);
    SB();
    const unsigned ci = 64 * (2 * i + lw) + lane, rho = __umul24(ci, 3641) >> 16;
    vb[i] = __umul24(rho, F * 2 - 288) + 16 * ci + __mul24((int)(rho & 4) - (int)((rho >> 1) & 4), F * 2);
    issue1b(0, i, 0, FH);
    SB();
  }
  auto issueA = [&](int n) {
#pragma unroll
    for (int i = 0; i < 9; ++i) { if (i < 8) issue1a(n & 3, i, n); issue1b(n & 3, i, n, FH); } };
  auto issueB = [&](int n, bool with_x) {
#pragma unroll
    for (int i = 0; i < 9; ++i) { if (with_x && i < 8) issue1a((9 - n) & 3, i, 17 - n); issue1b(n & 3, i, 17 - n, 0); } };
  issueA(1); issueA(2);
  STAMP(60);
  const rsrc_t rwsp = mkrs(P.wsp, S * S * 2), rws = mkrs(P.ws, W_END);
  const unsigned vw = prr * (S * 2) + swz, vl = lane * 16 + lw * 1024;
  const int kc = (id.tp + id.ft) & (KB - 1), ra = PR ? (id.ft & 7) : 0, rb = PR ? 2 * (id.tp & 3) : 0;
  auto kof = [&](int n) { const int k = n - 18; return (k & ~(KB - 1)) | ((k + kc) & (KB - 1)); };
  auto issue2a = [&](int n) { LAS char* a = sm + ((9 - n) & 3) * ASLOT + lw * 1024; const unsigned sa = id.tp * 128 * (S * 2) + kof(n) * 128;
#pragma unroll
    for (int i0 = 0; i0 < 8; ++i0) { const int i = (i0 + ra) & 7; dma<0>(rwsp, a + i * 2048, vw, sa + (32 * (i >> 1) + 4 * (i & 1)) * (S * 2)); } };
  auto issue2b = [&](int n) { LAS char* bb = sm + BBASE + (n & 3) * BSLOT + lw * 1024; const unsigned sb = W_ZF + (id.ft * 576 + kof(n) * 18) * 1024;
#pragma unroll
    for (int i0 = 0; i0 < 9; ++i0) { const int i = i0 + rb >= 9 ? i0 + rb - 9 : i0 + rb; dma<SC1>(rws, bb + i * 2048, vl, sb + i * 2048); } };
#pragma unroll 1
  for (int n = 0; n < 6; ++n) { WAITV(34); LBAR(); issueA(n + 3); }
  WAITV(34); LBAR(); issueB(9, false);
  WAITV(26); LBAR(); issueB(10, false);
  WAITV(18); LBAR(); issueB(11, false);
  BAR();
  WAITV(18); LBAR(); issueB(12, false);
  WAITV(18); LBAR(); issueB(13, true);
  WAITV(26); LBAR();
#pragma unroll 1
  for (int n = 12; n < 16; ++n) { WAITV(17); LBAR(); issueB(n + 2, true); }
  WAITV(17); LBAR(); issue2a(18);
  WAITV(8); LBAR(); issue2a(19);
  BAR(); issue2a(20);
  { const u32x2 tag = lds<u32x2>(sm + T_TAG); wait_flags<1, 2>(rws, W_F2 + id.ft * 128 + (lane & 7) * 16, tag); }
  STAMP(61);
  issue2b(18); issue2b(19); issue2b(20);
  asm volatile("ds_write_b32 %0, %1" :: "v"((unsigned)(size_t)(sm + T_GO)), "v"(1u) : "memory");
  WAITV(18); LBAR(); issue2a(21); issue2b(21);
  WAITV(26); LBAR(); issue2a(22); issue2b(22);
#pragma unroll 1
  for (int n = 20; n < 47; ++n) { WAITV(34); LBAR(); issue2a(n + 3); issue2b(n + 3); }
  const int g = id.ft & 7;
  const unsigned wb0 = W_WOF + (g >> 1) * 72 * 9 * 1024, wb1 = wb0 + (g & 1) * 5 * 1024, vq = lane * 16;
  const int rot = krot(id);
  auto issue3 = [&](auto lo_, auto hi_) { constexpr int lo = decltype(lo_)::value, hi = decltype(hi_)::value;
#pragma unroll
    for (int p0 = lo; p0 < hi; p0 += 2) {
      auto off = [&](int pc) { return (pc % 5 == 4 ? wb0 : wb1) + (18 * (pc / 25) * 9 + pc % 5) * 1024 + kwrap((pc % 25) / 5, rot) * 9216; };
      dma<SC1>(rws, sm + p0 * 1024 + lw * 1024, vq, lw ? off(p0 + 1) : off(p0)); } };
#define IC(n) std::integral_constant<int, n>{}
  WAITV(34); LBAR(); issue3(IC(48), IC(64));
  WAITV(25); LBAR();
  WAITV(8); LBAR(); issue3(IC(16), IC(48));
  LBAR();
  LBAR(); issue3(IC(0), IC(16)); issue3(IC(64), IC(100));
  unsigned sink = 0;
  auto wtouch = [&]() {
    constexpr int K0 = 5 + WD, NK = 18 - K0;
#pragma unroll
    for (int i = 0; i < (NK * 40 + 127) / 128; ++i) { const unsigned n = (i * 2 + lw) * 64 + lane, blk = n >> 1, kw = __umul24(blk, 13108) >> 16, t = blk - kw * 5;
      const unsigned o = n < NK * 40 ? (t == 4 ? wb0 : wb1) + ((18 * (kw & 3) + K0 + (kw >> 2)) * 9 + t) * 1024 + ((id.tp & 3) * 2 + (n & 1)) * 128 : 0xF0000000u;
      asm volatile("buffer_load_dword %0, %1, %2, 0 offen sc1" : "+v"(sink) : "v"(o), "s"(rws) : "memory"); } };
  if ((OPT_TG3 & 3) == 1) wtouch();
  LBAR();
  if ((OPT_TG3 & 3) == 2) { __builtin_amdgcn_s_sleep(2); wtouch(); }
  WAITV((OPT_TG3 & 3) == 2 ? (( 18 - 5 - WD) * 40 + 127) / 128 : 0); LBAR();
  asm volatile("s_waitcnt vmcnt(0)" : "+v"(sink) :: "memory"); LBAR();
}

DEV void role_X(const Params& P, LAS char* sm, const Ids id, const int xw STAMP_ARG) {
  int bcount = 0; (void)bcount;
  const int lane = id.lane;
  const rsrc_t rws = mkrs(P.ws, W_END);
  unsigned sink = 0;
  const u32x2 ev = __builtin_amdgcn_raw_buffer_load_b64(rws, W_EPOCH, 0, SC1);
  const int srow = id.tp * 128 + id.ft * 8 + 4 * xw + (lane >> 4);
  u32x4 xv[5];
  { const rsrc_t rx = mkrs(P.x, S * D * 2);
#pragma unroll
    for (int i = 0; i < 5; ++i) xv[i] = ld16<0>(rx, i < 4 || (lane & 8) == 0 ? srow * (D * 2) + (lane & 15) * 16 : 0xF0000000u, i * 256); }
  const u64 epoch = ((u64)__builtin_amdgcn_readfirstlane(ev.y) << 32) | __builtin_amdgcn_readfirstlane(ev.x);
  u32x2 tag; { const u64 t = ((epoch + 1) * 0x9E3779B97F4A7C15ull) | 1ull; tag.x = (unsigned)t; tag.y = (unsigned)(t >> 32); }
  { float s = 0.f, q = 0.f; unsigned ones = 0x3f803f80u; asm volatile("" : "+v"(ones));
#pragma unroll
    for (int i = 0; i < 5; ++i)
#pragma unroll
      for (int e = 0; e < 4; ++e) { s = dot2(xv[i][e], ones, s); q = dot2(xv[i][e], xv[i][e], q); }
    s += dpp<XOR1>(s); q += dpp<XOR1>(q); s += dpp<XOR2>(s); q += dpp<XOR2>(q); s += dpp<XOR7>(s); q += dpp<XOR7>(q); s += dpp<XOR8>(s); q += dpp<XOR8>(q);
    const float m = s * (1.0f / D), var = fmaxf(q * (1.0f / D) - m * m, 0.f) + 1e-5f;
    if ((lane & 15) == 0) st16<SC1>(seal(m, var, tag), rws, W_G0X + srow * 16, 0);
    if (xw == 0 && lane == 0) { *(LAS u32x2*)(sm + T_TAG) = tag; *(LAS u32x4*)(sm + T_CNT) = (u32x4){0u, 0u, 0u, 0u}; }
  }
  STAMP(0);
  BAR(); BAR();
  if (TWZ == 2) { const rsrc_t r = mkrs(P.win, D * F * 2); const unsigned n = xw * 64 + lane, m = id.b * 81 + n, row = m / 36;
    asm volatile("buffer_load_dword %0, %1, %2, 0 offen" : "+v"(sink) : "v"(n < 81 ? row * (F * 2) + (m - row * 36) * 128 : 0xF0000000u), "s"(r) : "memory"); }
  BAR(); BAR();
  LAS char* stg = sm + STG + xw * STG_W;
  const int unit = xw ? 256 + id.b : id.b, wks = unit >> 2, wu = unit & 3; const bool won = !xw || id.b < 32;
  const int tile = xw ? id.b : 256 + id.b - 32; const bool kon = xw || (id.b >= 32 && id.b < 64);
  const int tb = tile / 9, db = tile - tb * 9, cc = lane & 7, rg = lane >> 3;
  u32x4 kk[11], vv[8], sh[4];
  { const int row = 64 * xw + lane; const u64 t0 = __builtin_amdgcn_s_memrealtime();
    u32x4 gx = ld16<SC1>(rws, W_G0X + (id.tp * 128 + row) * 16, 0);
    BAR(); BAR();
    while (!__all(sealed(gx, tag)) && !expired(t0)) { __builtin_amdgcn_s_sleep(4); FENCE(); gx = ld16<SC1>(rws, W_G0X + (id.tp * 128 + row) * 16, 0); }
    const float var = __uint_as_float(gx.y), r = __builtin_amdgcn_rsqf(var);
    *(LAS unsigned*)(sm + T_M1 + row * 4) = gx.x; *(LAS float*)(sm + T_R1 + row * 4) = r;
    *(LAS float*)(sm + T_A0 + row * 4) = GC0 * r; *(LAS float*)(sm + T_A1 + row * 4) = GC1 * r * r * r; *(LAS float*)(sm + T_IR + row * 4) = var * r;
    if ((RT & 1) && xw == 0) code_touch(id, sink);
    WAITL(0); BAR();
  }
  STAMP(1);
  { const rsrc_t rk = mkrs(P.k, S * D * 2), rv = mkrs(P.v, S * D * 2), rs = mkrs(P.shk, D * 4 * 2), rwo = mkrs(P.wout, FH * D * 2);
    const int t0 = 64 * tb + 8 * rg; const unsigned col = (64 * db + 8 * cc) * 2;
    if (kon) {
#pragma unroll
      for (int i = 0; i < 11; ++i) kk[i] = ld16<0>(rk, t0 + i - 3 >= 0 ? (unsigned)(t0 + i - 3) * (D * 2) + col : 0xF0000000u, 0);
    }
    BAR();
    if (kon) {
#pragma unroll
      for (int i = 0; i < 8; ++i) vv[i] = ld16<0>(rv, t0 * (D * 2) + col, i * (D * 2));
#pragma unroll
      for (int i = 0; i < 4; ++i) sh[i] = ld16<0>(rs, col * 4, i * 16);
    }
    BAR();
    if (won) { const int fb0 = wks < 64 ? 144 * (wks >> 2) + 32 * (wks & 3) : 288 * (wks - 64) + 128, fst = wks < 64 ? 0 : 128;
#pragma unroll
      for (int i = 0; i < 9; ++i) { const int ci = 64 * i + lane, rho = __umul24(ci, 3641) >> 16, ch = ci - rho * 18;
        dma_hidden(rwo, stg + i * 1024, (fb0 + rho + (rho >> 4) * fst) * (D * 2) + wu * 288 + ch * 16); } }
    BAR();
    if (kon) {
#pragma unroll
      for (int i = 0; i < 4; ++i) asm volatile("" : "+v"(sh[i]), "+v"(vv[i]), "+v"(vv[4 + i]), "+v"(kk[i]), "+v"(kk[4 + i]), "+v"(kk[i == 3 ? 0 : 8 + i])); }
    if (OPT_TWSP) { const rsrc_t r = mkrs(P.wsp, S * S * 2);
#pragma unroll
      for (int i = 0; i < 2; ++i) asm volatile("buffer_load_dword %0, %1, %2, 0 offen" : "+v"(sink) : "v"(id.b * 32768 + ((2 * i + xw) * 64 + lane) * 128), "s"(r) : "memory"); }
  }
  BAR();
  u32x4 ko[8];
  auto kvstep = [&](int e) {
    if (kon) {
      float c[4], kf[11], y[8];
#pragma unroll
      for (int j = 0; j < 4; ++j) { const unsigned w = sh[e >> 1][(e & 1) * 2 + (j >> 1)]; c[j] = (j & 1) ? bhi(w) : blo(w); }
#pragma unroll
      for (int i = 0; i < 11; ++i) { const unsigned w = kk[i][e >> 1]; kf[i] = (e & 1) ? bhi(w) : blo(w); }
#pragma unroll
      for (int i = 0; i < 8; ++i) { const unsigned w = vv[i][e >> 1];
        float a = c[3] * kf[i]; asm volatile("" : "+v"(a)); a = fmaf(c[2], kf[i + 1], a); asm volatile("" : "+v"(a)); a = fmaf(c[1], kf[i + 2], a); asm volatile("" : "+v"(a)); a = fmaf(c[0], kf[i + 3], a);
        asm volatile("" : "+v"(a)); y[i] = a * ((e & 1) ? bhi(w) : blo(w)); asm volatile("" : "+v"(y[i])); }
      ko[e] = (u32x4){pk(y[7], y[6]), pk(y[5], y[4]), pk(y[3], y[2]), pk(y[1], y[0])};
      asm volatile("" : "+v"(ko[e]));
    } };
  { const int row = 64 * xw + lane;
    const f32x2 a = lds<f32x2>(sm + T_PART + row * 8) + lds<f32x2>(sm + T_PART + 1024 + row * 8);
    st16<SC1>(seal(a.x, a.y, tag), rws, W_G1S + ((id.tp * 16 + id.ft) * 128 + row) * 16, 0);
    kvstep(0); BAR(); kvstep(1); BAR();
    u32x4 gs[16];
    auto gather = [&]() {
#pragma unroll
      for (int i = 0; i < 16; ++i) gs[i] = ld16<SC1>(rws, W_G1S + (id.tp * 16 * 128 + row) * 16, i * 2048); };
    unsigned bad = 0; float s = 0.f, q = 0.f;
    auto sum = [&](int lo, int hi) {
#pragma unroll
      for (int i = lo; i < hi; ++i) { bad |= (gs[i].x ^ gs[i].z ^ tag.x) | (gs[i].y ^ gs[i].w ^ tag.y); s += __uint_as_float(gs[i].x); q += __uint_as_float(gs[i].y); }
      asm volatile("" : "+v"(bad), "+v"(s), "+v"(q)); };
    gather(); kvstep(2); BAR(); kvstep(3); BAR(); sum(0, 10); BAR();
    sum(10, 16);
    if (__builtin_expect(!__all(bad == 0), 0)) { const u64 t0 = __builtin_amdgcn_s_memrealtime();
      do { FENCE(); gather(); bad = 0; s = 0.f; q = 0.f; sum(0, 16); } while (!__all(bad == 0) && !expired(t0)); }
    const float m = s * (1.0f / FH), var = fmaxf(q * (1.0f / FH) - m * m, 0.f), r = __builtin_amdgcn_rsqf(var + 1e-5f);
    *(LAS float*)(sm + T_R2 + row * 4) = r; *(LAS float*)(sm + T_N2 + row * 4) = -m * r;
    asm volatile("s_waitcnt vmcnt(0) lgkmcnt(0)" : "+v"(sink) :: "memory"); BAR();
  }
  STAMP(2);
  if ((RT & 4) && xw == 0) code_touch(id, sink);
  kvstep(4); BAR(); kvstep(5); BAR();
  if (xw == 0) set_flag(rws, W_F2 + (id.ft * 16 + id.tp) * 8, tag, lane);
  kvstep(6); kvstep(7); BAR();
  { u32x4 wf[9];
    if (won) {
      const int q = lane >> 4, qq = (lane & 15) >> 2, p = lane & 3;
      LAS char* base = stg + (4 * q + qq) * 288;
#pragma unroll
      for (int T = 0; T < 9; ++T) { const int cb = T < 4 ? 32 * (T >> 1) + 4 * (T & 1) : T == 4 ? 64 : 80 + 32 * ((T - 5) >> 1) + 4 * ((T - 5) & 1);
        LAS char* a = base + cb * 2 + p * (T == 4 ? 8 : 16);
        wf[T] = __builtin_bit_cast(u32x4, cat(ldtr(a), ldtr(a + 16 * 288))); }
      WAITL(0);
    }
    if (kon) {
#pragma unroll
      for (int e = 0; e < 8; ++e) *(LAS u32x4*)(stg + (8 * cc + e) * 128 + (((7 - rg) ^ cc) * 16)) = ko[e];
#pragma unroll
      for (int i = 0; i < 8; ++i) ko[i] = lds<u32x4>(stg + (8 * i + rg) * 128 + ((cc ^ i) * 16));
    }
    const u64 t0 = __builtin_amdgcn_s_memrealtime();
    while (*(volatile LAS unsigned*)(sm + T_GO) == 0u && !expired(t0)) __builtin_amdgcn_s_sleep(2);
    FENCE();
    if (won) {
#pragma unroll
      for (int T = 0; T < 9; ++T) st16<SC1>(wf[T], rws, W_WOF + lane * 16, ((wu * 72 + wks) * 9 + T) * 1024); }
    if (kon) {
#pragma unroll
      for (int i = 0; i < 8; ++i) st16<SC1>(ko[i], rws, W_KVT + (64 * db + rg) * (S * 2) + (S - 64 * (tb + 1)) * 2 + cc * 16, 8 * i * (S * 2)); }
  }
  BAR(); BAR(); BAR();
  const unsigned h0a = ((const CAS unsigned*)P.ssmk)[id.b * S], h0b = ((const CAS unsigned*)P.ssmk)[(xw ? 2 * id.b + 1 : 512 + (id.b >> 2)) * (S / 2)];
  BAR(); WAITV(0); BAR();
  if (xw == 0) set_flag(rws, W_F5 + id.b * 8, tag, lane);
  STAMP(3);
#pragma unroll 1
  for (int i = 0; i < 22; ++i) BAR();
  if ((RT & 2) && xw == 1) code_touch(id, sink);
  { u32x4 f0 = {tag.x, tag.y, tag.x, tag.y}, f1 = f0;
    if (xw == 0) { f0 = ld16<SC1>(rws, W_F5 + lane * 16, 0); f1 = ld16<SC1>(rws, W_F5 + lane * 16, 1024); }
    BAR(); BAR();
    if (!__all(flags_ok(f0, tag) && flags_ok(f1, tag))) wait_flags<2, 8>(rws, W_F5 + lane * 16, tag); }
  BAR();
  STAMP(4);
  { const rsrc_t rh = mkrs(P.ssmk, D * S * 2);
    auto fetch = [&](int ci) { const int chn = ci == 2 ? 512 + (id.b >> 2) : 2 * id.b + ci; LAS char* cb = sm + CV + ci * CV_CH;
      const unsigned h0 = ci == 0 ? h0a : h0b;
      if (lane < 8) { *(LAS unsigned*)(cb + lane * 4) = 0u; *(LAS unsigned*)(cb + CV_O + lane * 4) = lane == 7 ? h0 << 16 : 0u; }
      if (lane < 38) *(LAS u32x4*)(cb + CV_U + 4096 + lane * 16) = (u32x4){0u, 0u, 0u, 0u};
      if (ci == 2) { *(LAS u32x4*)(cb + CV_U + 4704 + lane * 16) = (u32x4){0u, 0u, 0u, 0u}; *(LAS u32x4*)(cb + CV_U + 5728 + lane * 16) = (u32x4){0u, 0u, 0u, 0u}; }
      FENCE();
#pragma unroll
      for (int i = 0; i < 4; ++i) { dma<0>(rh, cb + 32 + i * 1024, lane * 16, chn * (S * 2) + i * 1024);
        dma<0>(rh, cb + CV_O + 32 + i * 1024, lane * 16 + 2, chn * (S * 2) + i * 1024);
        dma<SC1>(rws, cb + CV_U + i * 1024, lane * 16, W_KVT + chn * (S * 2) + i * 1024); } };
    if (xw == 0) fetch(0);
    BAR();
    fetch(2 - xw);
    BAR();
    WAITV(0); WAITL(0); BAR(); BAR();
  }
  BAR();
  if (xw == 0) { set_flag(rws, W_F3 + (id.tp * 16 + id.ft) * 8, tag, lane); wait_flags<1, 1>(rws, W_F3 + id.tp * 128 + (lane & 7) * 16, tag); }
  STAMP(5);
  BAR();
  if (xw == 1 && (OPT_TG3 & 4)) {
#pragma unroll
    for (int i = 0; i < 4; ++i) { const unsigned n = i * 64 + lane, o = n < 240 ? W_HF + ((id.tp * 8 + 4 * (id.ft >> 3) + (n & 3)) * 72 + 18 * ((n >> 2) & 3) + 3 + (n >> 4)) * 1024 + (id.ft & 7) * 128 : 0xF0000000u;
      asm volatile("buffer_load_dword %0, %1, %2, 0 offen sc1" : "+v"(sink) : "v"(o), "s"(rws) : "memory"); } }
  if (xw == 0) {
    const u64 t0 = __builtin_amdgcn_s_memrealtime();
    while (*(volatile LAS unsigned*)(sm + T_CNT) != 4u && !expired(t0)) __builtin_amdgcn_s_sleep(2);
    FENCE();
    set_flag(rws, W_F4 + id.b * 8, tag, lane);
    const int g = id.ft & 7, d0 = 144 * (g >> 1) + 64 * (g & 1);
    { const rsrc_t rx = mkrs(P.x, S * D * 2);
#pragma unroll
      for (int i = 0; i < 10; ++i) { const unsigned ci = 64 * i + lane, r = __umul24(ci, 6554) >> 16; dma<0>(rx, sm + XTILE + i * 1024, (id.tp * 128 + 64 * (id.ft >> 3) + r) * (D * 2) + d0 * 2 + (ci - 10 * r) * 16, 0); } }
    __builtin_amdgcn_s_sleep(24); wait_flags<2, 12>(rws, W_F4 + lane * 16, tag);
#pragma unroll
    for (int i = 0; i < 10; ++i) dma<SC1>(rws, sm + YTILE + i * 1024, W_YT + (d0 + (lane >> 3)) * (S * 2) + (id.tp * 128 + 64 * (id.ft >> 3)) * 2 + (lane & 7) * 16, i * 8 * (S * 2));
    WAITV(0);
    asm volatile("ds_write_b32 %0, %1" :: "v"((unsigned)(size_t)(sm + T_YOK)), "v"(1u) : "memory");
    if (id.b == 0 && lane == 0) st8<SC1>((u32x2){(unsigned)(epoch + 1), (unsigned)((epoch + 1) >> 32)}, rws, W_EPOCH, 0);
  }
  STAMP(6);
  asm volatile("s_waitcnt vmcnt(0)" : "+v"(sink) :: "memory"); BAR();
}

DEV void role_C(const Params& P, LAS char* sm, const Ids id, const int wid STAMP_ARG) {
  int bcount = 0; (void)bcount;
  const int lane = id.lane, wt = wid >> 1, wf = wid & 1, c = lane & 15, q = lane >> 4;
  const rsrc_t rws = mkrs(P.ws, W_END);
  __builtin_amdgcn_s_setprio(1);
  unsigned dm = ((const CAS unsigned*)P.ssmd)[id.b], dsh = ((const CAS unsigned*)P.ssmd)[256 + (id.b >> 3)];
  asm volatile("" : "+s"(dm), "+s"(dsh));
  f32x4 acc[4][4], accx[2], csa[3];
  bf16x8 tf[2][4], ff[2][5], ones;
  LAS char *ta[2][2], *fb[2][2], *fx[2];
#pragma unroll
  for (int s = 0; s < 2; ++s) {
    const int o = (32 * (s ^ wt) + 16 * (q >> 1) + 4 * (q & 1) + (c >> 2)) * 288 + 8 * (c & 3);
    fx[s] = sm + BBASE + o + 256;
#pragma unroll
    for (int h = 0; h < 2; ++h) { ta[s][h] = sm + (64 * wt + 32 * (h ^ wf) + c) * 128 + (((q + 4 * (s ^ wt)) ^ ((c >> 1) & 7)) * 16); fb[s][h] = sm + BBASE + o + wf * 128 + (h ^ wt) * 64; }
  }
#pragma unroll
  for (int e = 0; e < 8; ++e) ones[e] = 0x3f80;
  asm volatile("" : "+v"(ones));
  auto rd = [&](auto mode, int b, int as, int bs, int s) {
    constexpr int MODE = decltype(mode)::value;
#pragma unroll
    for (int j = 0; j < 4; ++j) { LAS char* p = fb[s][j >> 1] + bs * BSLOT;
      ff[b][j] = MODE < 2 ? cat(ldtr(p + (j & 1) * 32), ldtr(p + (j & 1) * 32 + 8 * 288)) : lds<bf16x8>(p + (j & 1) * 1024); }
#pragma unroll
    for (int i = 0; i < 4; ++i) tf[b][i] = lds<bf16x8>(ta[s][i >> 1] + as * ASLOT + (i & 1) * 2048);
    ff[b][4] = MODE < 2 ? cat(ldtr(fx[s] + bs * BSLOT), ldtr(fx[s] + bs * BSLOT + 8 * 288)) : lds<bf16x8>(fx[s] + bs * BSLOT);
  };
  auto mm = [&](auto mode, int b, int lo, int hi) {
    constexpr int MODE = decltype(mode)::value;
#pragma unroll
    for (int n = 0; n < (MODE == 2 ? 18 : 21 - b); ++n) if (n >= lo && n < hi) {
      if (n < 16) { const int i = n >> 2, j = (i & 1) ? 3 - (n & 3) : (n & 3);
        acc[i][j] = MODE == 0 ? MFMA(tf[b][i], ff[b][j], acc[i][j]) : MFMA(ff[b][j], tf[b][i], acc[i][j]); }
      else if (n < 18) accx[n - 16] = MODE == 0 ? MFMA(tf[b][n - 16], ff[b][4], accx[n - 16]) : MFMA(ff[b][4], tf[b][n - 16], accx[n - 16]);
      else { const int j = n == 20 ? 4 : n - 18; csa[n - 18] = MODE == 0 ? MFMA(ones, ff[b][j], csa[n - 18]) : MFMA(ff[b][j], ones, csa[n - 18]); }
    }
  };
  auto stage = [&](auto mode, int as, int bs, bool drain) {
    constexpr int MODE = decltype(mode)::value;
    mm(mode, 1, 0, 4); SB();
    if (drain) WAITV(0);
    WAITL(0); BAR(); SB();
    rd(mode, 0, as, bs, 0); mm(mode, 1, 4, 21);
    if (MODE < 2) { for (int i = 0; i < 14; ++i) { SG(0x100, 1); SG(0x8, 1); } SG(0x8, 2); }
    else { for (int i = 0; i < 4; ++i) { SG(0x100, 1); SG(0x8, 1); SG(0x100, 1); SG(0x8, 2); } SG(0x100, 1); SG(0x8, 2); }
    SB();
    rd(mode, 1, as, bs, 1); mm(mode, 0, 0, 21);
    if (MODE < 2) { for (int i = 0; i < 7; ++i) { SG(0x100, 1); SG(0x8, 1); SG(0x100, 1); SG(0x8, 2); } }
    else { for (int i = 0; i < 9; ++i) { SG(0x100, 1); SG(0x8, 2); } }
    SB();
  };
  auto first = [&](auto mode, int as, int bs) { WAITL(0); BAR(); SB(); rd(mode, 0, as, bs, 0); rd(mode, 1, as, bs, 1); mm(mode, 0, 0, 21); SB(); };
  auto zero = [&]() {
#pragma unroll
    for (int i = 0; i < 4; ++i)
#pragma unroll
      for (int j = 0; j < 4; ++j) acc[i][j] = (f32x4){0.f, 0.f, 0.f, 0.f};
    accx[0] = accx[1] = csa[0] = csa[1] = csa[2] = (f32x4){0.f, 0.f, 0.f, 0.f}; };
  const std::integral_constant<int, 0> M0; const std::integral_constant<int, 1> M1; const std::integral_constant<int, 2> M2;
  const int jg0 = 4 * wf + 2 * wt, jg2 = 4 * wf + 2 - 2 * wt;

  zero();
  first(M0, 0, 0);
  STAMP(0);
#pragma unroll 1
  for (int it = 0; it < 2; ++it) { stage(M0, 1, 1, false); stage(M0, 2, 2, false); stage(M0, 3, 3, false); stage(M0, 0, 0, false); }
  mm(M0, 1, 18, 21); mm(M0, 1, 0, 8); SB();
  if (q == 0) { *(LAS float*)(sm + T_CS2 + (jg0 * 16 + c) * 4) = csa[0][0]; *(LAS float*)(sm + T_CS2 + (jg0 * 16 + 16 + c) * 4) = csa[1][0]; *(LAS float*)(sm + T_CS2 + (128 + 16 * wt + c) * 4) = csa[2][0]; }
  mm(M0, 1, 8, 18); SB();
  WAITL(0); BAR();
  STAMP(1);
  u32x4 z2p[2][4], z2x;
  { float cs[5];
    cs[0] = csa[0][0]; cs[1] = csa[1][0]; cs[2] = lds<float>(sm + T_CS2 + (jg2 * 16 + c) * 4); cs[3] = lds<float>(sm + T_CS2 + (jg2 * 16 + 16 + c) * 4);
    cs[4] = csa[2][0] + lds<float>(sm + T_CS2 + (144 - 16 * wt + c) * 4);
    float red[2][16];
#pragma unroll
    for (int i = 0; i < 4; ++i) {
      const int ro = (64 * wt + 32 * ((i >> 1) ^ wf) + 8 * q + 4 * (i & 1)) * 4;
      const f32x4 m = lds<f32x4>(sm + T_M1 + ro), a0 = lds<f32x4>(sm + T_A0 + ro), a1 = lds<f32x4>(sm + T_A1 + ro), ir = lds<f32x4>(sm + T_IR + ro);
      f32x4 su = {0.f, 0.f, 0.f, 0.f}, sq = {0.f, 0.f, 0.f, 0.f};
#pragma unroll
      for (int j = 0; j < (i < 2 ? 5 : 4); ++j) {
        const f32x4 z = gelu4((j < 4 ? acc[i][j] : accx[i]) - m * cs[j], a0, a1, ir);
        su = j ? su + z : z; sq = j ? sq + z * z : z * z;
        u32x4& dst = j < 4 ? z2p[i >> 1][j] : z2x;
        dst[2 * (i & 1)] = pk(z[0], z[1]); dst[2 * (i & 1) + 1] = pk(z[2], z[3]);
      }
#pragma unroll
      for (int e = 0; e < 4; ++e) { red[0][4 * i + e] = su[e]; red[1][4 * i + e] = sq[e]; }
    }
    float r8[2][8], r4[2][4];
#pragma unroll
    for (int st = 0; st < 2; ++st) {
#pragma unroll
      for (int e = 0; e < 8; e += 4)
        asm volatile("s_nop 1\n\t"
                     "v_add_f32_dpp %0, %4, %4 row_ror:8 row_mask:0xf bank_mask:0x3\n\tv_add_f32_dpp %1, %5, %5 row_ror:8 row_mask:0xf bank_mask:0x3\n\t"
                     "v_add_f32_dpp %2, %6, %6 row_ror:8 row_mask:0xf bank_mask:0x3\n\tv_add_f32_dpp %3, %7, %7 row_ror:8 row_mask:0xf bank_mask:0x3\n\t"
                     "v_add_f32_dpp %0, %8, %8 row_ror:8 row_mask:0xf bank_mask:0xc\n\tv_add_f32_dpp %1, %9, %9 row_ror:8 row_mask:0xf bank_mask:0xc\n\t"
                     "v_add_f32_dpp %2, %10, %10 row_ror:8 row_mask:0xf bank_mask:0xc\n\tv_add_f32_dpp %3, %11, %11 row_ror:8 row_mask:0xf bank_mask:0xc"
                     : "=&v"(r8[st][e]), "=&v"(r8[st][e + 1]), "=&v"(r8[st][e + 2]), "=&v"(r8[st][e + 3])
                     : "v"(red[st][e]), "v"(red[st][e + 1]), "v"(red[st][e + 2]), "v"(red[st][e + 3]), "v"(red[st][8 + e]), "v"(red[st][9 + e]), "v"(red[st][10 + e]), "v"(red[st][11 + e]));
      asm volatile("s_nop 1\n\t"
                   "v_add_f32_dpp %0, %4, %4 row_half_mirror row_mask:0xf bank_mask:0x5\n\tv_add_f32_dpp %1, %5, %5 row_half_mirror row_mask:0xf bank_mask:0x5\n\t"
                   "v_add_f32_dpp %2, %6, %6 row_half_mirror row_mask:0xf bank_mask:0x5\n\tv_add_f32_dpp %3, %7, %7 row_half_mirror row_mask:0xf bank_mask:0x5\n\t"
                   "v_add_f32_dpp %0, %8, %8 row_half_mirror row_mask:0xf bank_mask:0xa\n\tv_add_f32_dpp %1, %9, %9 row_half_mirror row_mask:0xf bank_mask:0xa\n\t"
                   "v_add_f32_dpp %2, %10, %10 row_half_mirror row_mask:0xf bank_mask:0xa\n\tv_add_f32_dpp %3, %11, %11 row_half_mirror row_mask:0xf bank_mask:0xa\n\t"
                   "v_add_f32_dpp %0, %0, %0 quad_perm:[1,0,3,2] row_mask:0xf bank_mask:0xf\n\tv_add_f32_dpp %1, %1, %1 quad_perm:[1,0,3,2] row_mask:0xf bank_mask:0xf\n\t"
                   "v_add_f32_dpp %2, %2, %2 quad_perm:[1,0,3,2] row_mask:0xf bank_mask:0xf\n\tv_add_f32_dpp %3, %3, %3 quad_perm:[1,0,3,2] row_mask:0xf bank_mask:0xf\n\t"
                   "v_add_f32_dpp %0, %0, %0 quad_perm:[2,3,0,1] row_mask:0xf bank_mask:0xf\n\tv_add_f32_dpp %1, %1, %1 quad_perm:[2,3,0,1] row_mask:0xf bank_mask:0xf\n\t"
                   "v_add_f32_dpp %2, %2, %2 quad_perm:[2,3,0,1] row_mask:0xf bank_mask:0xf\n\tv_add_f32_dpp %3, %3, %3 quad_perm:[2,3,0,1] row_mask:0xf bank_mask:0xf"
                   : "=&v"(r4[st][0]), "=&v"(r4[st][1]), "=&v"(r4[st][2]), "=&v"(r4[st][3])
                   : "v"(r8[st][0]), "v"(r8[st][1]), "v"(r8[st][2]), "v"(r8[st][3]), "v"(r8[st][4]), "v"(r8[st][5]), "v"(r8[st][6]), "v"(r8[st][7]));
    }
    if ((c & 3) == 0) { LAS char* pp = sm + T_PART + wf * 1024 + (64 * wt + 32 * ((c >> 3) ^ wf) + 8 * q + 4 * ((c >> 2) & 1)) * 8;
      *(LAS f32x4*)pp = (f32x4){r4[0][0], r4[1][0], r4[0][1], r4[1][1]}; *(LAS f32x4*)(pp + 16) = (f32x4){r4[0][2], r4[1][2], r4[0][3], r4[1][3]}; }
#pragma unroll
    for (int i = 0; i < 2; ++i) asm volatile("" : "+v"(z2p[i][0]), "+v"(z2p[i][1]), "+v"(z2p[i][2]), "+v"(z2p[i][3]));
    asm volatile("" : "+v"(z2x));
  }
  STAMP(2);
  zero();
  first(M1, 0, 1);
  stage(M1, 3, 2, false); stage(M1, 2, 3, false); stage(M1, 1, 0, false);
  stage(M1, 0, 1, false); stage(M1, 3, 2, false); stage(M1, 2, 3, false);
  STAMP(3);
  {
#pragma unroll
    for (int i = 0; i < 2; ++i) asm volatile("" : "+v"(z2p[i][0]), "+v"(z2p[i][1]), "+v"(z2p[i][2]), "+v"(z2p[i][3]));
    asm volatile("" : "+v"(z2x));
#pragma unroll
    for (int ip = 0; ip < 2; ++ip) {
      const int ro = (64 * wt + 32 * (ip ^ wf) + 8 * q) * 4;
      const f32x4 r0 = lds<f32x4>(sm + T_R2 + ro), r1 = lds<f32x4>(sm + T_R2 + ro + 16), n0 = lds<f32x4>(sm + T_N2 + ro), n1 = lds<f32x4>(sm + T_N2 + ro + 16);
      const unsigned so = W_ZF + ((id.ft * 64 + 4 * id.tp + 2 * wt + (ip ^ wf)) * 9) * 1024;
#pragma unroll
      for (int j = 0; j < (ip == 0 ? 5 : 4); ++j) { const u32x4 z = j < 4 ? z2p[ip][j] : z2x;
        const f32x4 a = (f32x4){blo(z[0]), bhi(z[0]), blo(z[1]), bhi(z[1])} * r0 + n0, b = (f32x4){blo(z[2]), bhi(z[2]), blo(z[3]), bhi(z[3])} * r1 + n1;
        st16<SC1>((u32x4){pk(a[0], a[1]), pk(a[2], a[3]), pk(b[0], b[1]), pk(b[2], b[3])}, rws, lane * 16, so + (j < 2 ? jg0 + j : j < 4 ? jg2 + j - 2 : 8) * 1024); }
    }
    SB();
  }
  unsigned braw[4];
  { const rsrc_t rb = mkrs(P.bsp, S * 2);
#pragma unroll
    for (int i = 0; i < 4; ++i) braw[i] = __builtin_amdgcn_raw_buffer_load_b16(rb, (id.tp * 128 + 64 * wt + prow(i ^ (2 * wf), c)) * 2, 0, 0); }
  STAMP(4);
  stage(M1, 1, 0, false); stage(M1, 0, 1, true);
  mm(M1, 1, 18, 21); mm(M1, 1, 0, 8); SB();
  if (c == 0) { *(LAS f32x4*)(sm + T_CS1 + (jg0 * 16 + 4 * q) * 4) = csa[0]; *(LAS f32x4*)(sm + T_CS1 + (jg0 * 16 + 16 + 4 * q) * 4) = csa[1]; *(LAS f32x4*)(sm + T_CS1 + (128 + 16 * wt + 4 * q) * 4) = csa[2]; }
  mm(M1, 1, 8, 18); SB();
  WAITL(0); BAR();
  STAMP(5);
  u32x2 z1p[4][4], z1x[2];
  { f32x4 cs[5];
    cs[0] = csa[0]; cs[1] = csa[1]; cs[2] = lds<f32x4>(sm + T_CS1 + (jg2 * 16 + 4 * q) * 4); cs[3] = lds<f32x4>(sm + T_CS1 + (jg2 * 16 + 16 + 4 * q) * 4);
    cs[4] = csa[2] + lds<f32x4>(sm + T_CS1 + (144 - 16 * wt + 4 * q) * 4);
#pragma unroll
    for (int i = 0; i < 4; ++i) { const int ro = (64 * wt + prow(i ^ (2 * wf), c)) * 4;
      const float m = lds<float>(sm + T_M1 + ro), a0 = lds<float>(sm + T_A0 + ro), a1 = lds<float>(sm + T_A1 + ro), ir = lds<float>(sm + T_IR + ro), bias = blo(braw[i]);
#pragma unroll
      for (int j = 0; j < (i < 2 ? 5 : 4); ++j) {
        f32x4& a = j < 4 ? acc[i][j] : accx[i];
        const f32x4 z = gelu4(a - m * cs[j], a0, a1, ir);
        (j < 4 ? z1p[i][j] : z1x[i]) = (u32x2){pk(z[0], z[1]), pk(z[2], z[3])};
        a = (f32x4){bias, bias, bias, bias};
      } }
#pragma unroll
    for (int s = 0; s < 2; ++s) { fx[s] = sm + BBASE + (s * 9 + 8) * 1024 + lane * 16;
#pragma unroll
      for (int h = 0; h < 2; ++h) { fb[s][h] = sm + BBASE + (s * 9 + (h ? jg2 : jg0)) * 1024 + lane * 16; ta[s][h] = sm + (64 * wt + 32 * (h ^ wf) + c) * 128 + (((q + 4 * s) ^ ((c >> 1) & 7)) * 16); } }
#pragma unroll
    for (int i = 0; i < 4; ++i) asm volatile("" : "+v"(z1p[i][0]), "+v"(z1p[i][1]), "+v"(z1p[i][2]), "+v"(z1p[i][3]), "+v"(acc[i][0]), "+v"(acc[i][1]), "+v"(acc[i][2]), "+v"(acc[i][3]));
    asm volatile("" : "+v"(z1x[0]), "+v"(z1x[1]), "+v"(accx[0]), "+v"(accx[1]));
  }
  STAMP(6);
  const int jq = id.b & 3;
  LAS char *cs_ = sm + CV + 2 * CV_CH, *cb = sm + CV + wt * CV_CH;
  LAS char *am[4], *um = cb + CV_U + (2032 - 16 * c + 8 * q) * 2 + 256 * wf - 3584;
#pragma unroll
  for (int i = 0; i < 4; ++i) { am[i] = cb + ((c & 1) ? 0 : CV_O) + (4 * q + ((c + 1) >> 1)) * 4 + 256 * wf + 1024 * i; asm volatile("" : "+v"(am[i])); }
  asm volatile("" : "+v"(um));
  bf16x8 ca[2][8], cu[2][12];
  auto afrag = [&](LAS char* p) { const unsigned d0 = lds<unsigned>(p), d1 = lds<unsigned>(p + 4), d2 = lds<unsigned>(p + 8), d3 = lds<unsigned>(p + 12);
    return __builtin_bit_cast(bf16x8, (u32x4){d0, d1, d2, d3}); };
  auto lds_mn = [&](int b, int kk) {
#pragma unroll
    for (int e = 0; e < 8; ++e) { cu[b][e] = lds<bf16x8>(um + kk * 64 + 512 * (7 - e)); if (e == 0) ca[b][0] = afrag(am[0] + kk * 64); }
#pragma unroll
    for (int e = 1; e < 8; ++e) ca[b][e] = afrag(am[e >> 1] + kk * 64 + (e & 1) * 512); };
  first(M2, 3, 2);
  STAMP(7);
#pragma unroll 1
  for (int it = 0; it < 7; ++it) { stage(M2, 2, 3, false); stage(M2, 1, 0, false); stage(M2, 0, 1, false); stage(M2, 3, 2, false); }
  stage(M2, 2, 3, false); stage(M2, 1, 0, false); stage(M2, 0, 1, false);
  mm(M2, 1, 0, 4); SB(); WAITL(0); BAR(); SB();
  mm(M2, 1, 4, 18); SB();
  lds_mn(0, 0); SB();
  STAMP(8);
#pragma unroll
  for (int i = 0; i < 4; ++i) asm volatile("" : "+v"(z1p[i][0]), "+v"(z1p[i][1]), "+v"(z1p[i][2]), "+v"(z1p[i][3]));
  asm volatile("" : "+v"(z1x[0]), "+v"(z1x[1]));
#pragma unroll
  for (int i = 0; i < 4; ++i) {
    const unsigned so = W_HF + (id.tp * 8 + 4 * wt + (i ^ (2 * wf))) * 72 * 1024;
    auto gate = [&](u32x2 z, f32x4 a) { const f32x4 h = (f32x4){blo(z.x), bhi(z.x), blo(z.y), bhi(z.y)} * a; return (u32x2){pk(h[0], h[1]), pk(h[2], h[3])}; };
#pragma unroll
    for (int pp = 0; pp < 2; ++pp) { const u32x2 lo = gate(z1p[i][2 * pp], acc[i][2 * pp]), hi = gate(z1p[i][2 * pp + 1], acc[i][2 * pp + 1]);
      st16<SC1>((u32x4){lo.x, lo.y, hi.x, hi.y}, rws, lane * 16, so + (4 * id.ft + 2 * wf + (pp ^ wt)) * 1024); }
    if (i < 2) st8<SC1>(gate(z1x[i], accx[i]), rws, lane * 16 + (id.ft & 1) * 8, so + (64 + (id.ft >> 1)) * 1024);
  }
  const int g = id.ft & 7, gp = g & 1, half = id.ft >> 3, rot = krot(id);
  const unsigned wso = W_WOF + ((((EXP & 4) ? 0 : g >> 1) * 72 + 18 * wid) * 9) * 1024;
  u32x4 wr[WD][5];
  auto ldw = [&](int slot, int i) {
#pragma unroll
    for (int t = 0; t < 5; ++t) wr[slot][t] = ld16<(EXP & 8) ? 0 : SC1>(rws, lane * 16, wso + kwrap(i, rot) * 9216 + (t == 4 ? 4 : t + gp * 5) * 1024); };
  SB();
  SB(); BAR(); SB();
  STAMP(9);
  u32x4 hr[3][4];
  const unsigned hso = W_HF + (((EXP & 2) ? (id.b & 7) * 4 : id.tp * 8 + 4 * half) * 72 + 18 * wid) * 1024;
  auto ldh = [&](int slot, int i) {
#pragma unroll
    for (int t = 0; t < 4; ++t) hr[slot][t] = ld16<(EXP & 8) ? 0 : SC1>(rws, lane * 16, hso + (((unsigned)t ^ (wf ^ (2 * gp))) * 72 + kwrap(i, rot)) * 1024); };
  { const float dvm = wt ? bhi(dm) : blo(dm), dvs = (id.b & 4) ? bhi(dsh) : blo(dsh);
    f32x4 cm[8], s0 = {0.f, 0.f, 0.f, 0.f}, s1 = s0;
#pragma unroll
    for (int j = 0; j < 8; ++j) cm[j] = s0;
    LAS char *as[4], *u1, *u0;
    auto addr_sh = [&]() {
      const int da = (2 - wt) * CV_CH + 128 * wid - 256 * wf;
#pragma unroll
      for (int i = 0; i < 4; ++i) { as[i] = am[i] + da; asm volatile("" : "+v"(as[i])); }
      u1 = um + da + 512 * jq; u0 = um + da + 512 * (7 - jq); asm volatile("" : "+v"(u1), "+v"(u0)); };
    auto lds_sh = [&](int b, int kk) {
#pragma unroll
      for (int e = 0; e < 8; ++e) { ca[b][e] = afrag(as[e >> 1] + kk * 64 + (e & 1) * 512); cu[b][e] = lds<bf16x8>(u1 + kk * 64 + 512 * e);
        if (e < 4) cu[b][8 + e] = lds<bf16x8>(u0 + kk * 64 + 512 * e); } };
    auto mm_sh = [&](int b) {
#pragma unroll
      for (int e = 0; e < 8; ++e) { s1 = MFMA(ca[b][e], cu[b][e], s1); if (e < 4) s0 = MFMA(ca[b][e], cu[b][8 + e], s0); } };
    auto mm_mn = [&](int b) {
#pragma unroll
      for (int e = 0; e < 8; ++e)
#pragma unroll
        for (int j = e; j < 8; ++j) cm[j] = MFMA(ca[b][e], cu[b][j - e], cm[j]); };
#define il(nr, per) do { for (int i_ = 0; i_ < nr; ++i_) { SG(0x100, per); SG(0x8, 1); } SG(0x8, 36); SB(); } while (0)
#define ilv() do { for (int i_ = 0; i_ < 5; ++i_) { SG(0x100, 1); SG(0x8, 1); SG(0x100, 1); SG(0x8, 1); SG(0x100, 1); SG(0x8, 1); SG(0x100, 1); SG(0x8, 1); SG(0x100, 2); SG(0x8, 2); SG(0x20, 1); SG(0x8, 1); } SG(0x8, 36); SB(); } while (0)
    lds_mn(1, 1); mm_mn(0); il(24, 1); STAMP(15);
    lds_mn(0, 2); mm_mn(1); il(24, 1); STAMP(16); addr_sh(); SB();
    lds_sh(1, 0); mm_mn(0); il(28, 1);
    lds_sh(0, 1); mm_sh(1); il(10, 3);
    lds_mn(1, 3); mm_sh(0); il(12, 2);
    if (wid != 0) *(LAS f32x4*)(sm + CV_PS + (wid - 1) * 1024 + lane * 16) = s0;
    if (wid != 1) *(LAS f32x4*)(sm + CV_PS + (3 + (wid ? wid - 1 : 0)) * 1024 + lane * 16) = s1;
    SB(); STAMP(17); WAITV(0); WAITL(0); BAR(); SB();
    STAMP(10);
    f32x4 ys = wf ? s1 : s0;
    if (wid < 2) {
#pragma unroll
      for (int w = 0; w < 3; ++w) ys += lds<f32x4>(sm + CV_PS + (3 * wid + w) * 1024 + lane * 16);
    }
    SB(); ldw(0, 5); ldw(1, 6); if (WD > 2) ldw(2, 7); mm_mn(1); SG(0x8, 4); for (int i_ = 0; i_ < 15; ++i_) { SG(0x8, 2); SG(0x20, 1); } SG(0x8, 36); SB();
    LAS char* pm = sm + (wt ? CV_PM23 : CV + 2 * CV_CH);
#pragma unroll
    for (int j = 0; j < 4; ++j) { f32x4 lo = cm[j], hi = cm[4 + j]; asm volatile("" : "+v"(lo), "+v"(hi));
      *(LAS f32x4*)(pm + wf * 4096 + j * 1024 + lane * 16) = wf ? lo : hi; cm[j] = wf ? hi : lo; }
    SB(); WAITL(0); BAR(); SB();
    STAMP(11);
    ldh(0, 0); ldh(1, 1); ldh(2, 2);
    auto yout = [&](f32x4 a, int chn, LAS char* ub, int j, float dv) {
      const u32x2 u = lds<u32x2>(ub + CV_U + (2044 - 256 * j - 16 * c - 4 * q) * 2);
      const f32x4 y = a + (f32x4){bhi(u.y), blo(u.y), bhi(u.x), blo(u.x)} * dv;
      st8<SC1>((u32x2){pk(y[0], y[1]), pk(y[2], y[3])}, rws, (16 * c + 4 * q) * 2, W_YT + chn * (S * 2) + j * 512); };
#pragma unroll
    for (int j = 0; j < 4; ++j) yout(cm[j] + lds<f32x4>(pm + (wf ^ 1) * 4096 + j * 1024 + lane * 16), 2 * id.b + wt, cb, 4 * wf + j, dvm);
    if (wid < 2) yout(ys, 512 + (id.b >> 2), cs_, wid ? 7 - jq : jq, dvs);
    SB();
  }
  STAMP(19);
  f32x4 o[4][4], ox[2];
#pragma unroll
  for (int i = 0; i < 4; ++i)
#pragma unroll
    for (int t = 0; t < 4; ++t) o[i][t] = (f32x4){0.f, 0.f, 0.f, 0.f};
  ox[0] = ox[1] = (f32x4){0.f, 0.f, 0.f, 0.f};
  const int dp = 144 * (g >> 1) + 80 * gp, ds = 144 * (g >> 1) + 64, trow = 64 * half + prow(wid, c);
  const unsigned xo = (id.tp * 128 + trow) * (D * 2);
  f32x4 rs[5];
  { LAS char* wl = sm + wid * G3W + lane * 16;
    bf16x8 wq[2][5];
#pragma unroll
    for (int t = 0; t < 5; ++t) wq[0][t] = lds<bf16x8>(wl + t * 1024);
#pragma unroll
    for (int i = 0; i < 18; ++i) {
      if (i < 4) {
#pragma unroll
        for (int t = 0; t < 5; ++t) wq[(i + 1) & 1][t] = lds<bf16x8>(wl + ((i + 1) * 5 + t) * 1024); }
#pragma unroll
      for (int r = 0; r < 4; ++r) { const bf16x8 hf = __builtin_bit_cast(bf16x8, hr[i % 3][r]);
#pragma unroll
        for (int t = 0; t < 5; ++t) if (t < 4 || r < 2) { const int tt = (r & 1) && t < 4 ? 3 - t : t;
          const bf16x8 wf_ = i < 5 ? wq[i & 1][tt] : __builtin_bit_cast(bf16x8, wr[(i - 5) % WD][tt]);
          if (tt < 4) o[r][tt] = MFMA(wf_, hf, o[r][tt]); else ox[r] = MFMA(wf_, hf, ox[r]); } }
      SB();
      if (i == 0 || i == 2 || i == 4 || i == 7 || i == 10 || i == 13 || i == 15) STAMP(20 + i);
      if (i + 3 < 18) ldh(i % 3, i + 3);
      if (i >= 5 && i + WD < 18) ldw((i - 5) % WD, i + WD);
      if (i == 2) { WAITV(12); if (lane == 0) __hip_atomic_fetch_add((LAS unsigned*)(sm + T_CNT), 1u, __ATOMIC_RELAXED, __HIP_MEMORY_SCOPE_WORKGROUP); }
      if (i == 16) {
        const u64 t0 = __builtin_amdgcn_s_memrealtime();
        while (*(volatile LAS unsigned*)(sm + T_YOK) == 0u && !expired(t0)) __builtin_amdgcn_s_sleep(1);
        FENCE();
        const float m = lds<float>(sm + T_M1 + trow * 4), r = lds<float>(sm + T_R1 + trow * 4);
        LAS char* yb = sm + YTILE + (c >> 2) * 128 + (32 * wt + 4 * wf + 8 * (c & 3)) * 2;
        auto res = [&](u32x2 xv, bf16x4 yv) { const u32x2 yu = __builtin_bit_cast(u32x2, yv);
          const f32x4 xf = {blo(xv.x), bhi(xv.x), blo(xv.y), bhi(xv.y)}, yf = {blo(yu.x), bhi(yu.x), blo(yu.y), bhi(yu.y)};
          return xf + (xf - m) * r * yf; };
        const int yp = 16 * gp, ysh = 64 * (gp ^ 1);
        LAS char* xb = sm + XTILE + prow(wid, c) * 160;
        const u32x4 x0 = lds<u32x4>(xb + yp * 2 + q * 16), x1 = lds<u32x4>(xb + yp * 2 + 64 + q * 16); const u32x2 x2 = lds<u32x2>(xb + ysh * 2 + q * 8);
        rs[0] = res(x0.xy, ldtr(yb + (yp + 8 * q) * 128)); rs[1] = res(x0.zw, ldtr(yb + (yp + 8 * q + 4) * 128));
        rs[2] = res(x1.xy, ldtr(yb + (yp + 32 + 8 * q) * 128)); rs[3] = res(x1.zw, ldtr(yb + (yp + 36 + 8 * q) * 128));
        rs[4] = res(x2, ldtr(yb + (ysh + 4 * q) * 128));
        asm volatile("" : "+v"(rs[0]), "+v"(rs[1]), "+v"(rs[2]), "+v"(rs[3]), "+v"(rs[4])); }
      SB();
    }
  }
  STAMP(12);
  const bool own0 = wt == gp;
  { LAS char* pw = sm + wid * G3W + lane * 16;
    auto put = [&](int r) { LAS char* p = pw + ((unsigned)r ^ (wf ^ (2 * gp))) * 5120;
#pragma unroll
      for (int t = 0; t < 4; ++t) *(LAS f32x4*)(p + t * 1024) = o[r][t];
      if (r < 2) *(LAS f32x4*)(p + 4096) = ox[r]; };
    put(1); put(3);
    if (own0) { put(2);
#pragma unroll
      for (int t = 0; t < 4; ++t) rs[t] += o[0][t];
      rs[4] += ox[0]; }
    else { put(0);
#pragma unroll
      for (int t = 0; t < 4; ++t) rs[t] += o[2][t]; }
  }
  SB(); WAITL(0); BAR(); SB();
  STAMP(13);
  { f32x4 pt[3][5];
#pragma unroll
    for (int t = 0; t < 5; ++t)
#pragma unroll
      for (int w = 0; w < 3; ++w) pt[w][t] = lds<f32x4>(sm + (wid ^ (w + 1)) * G3W + wid * 5120 + lane * 16 + t * 1024);
    const rsrc_t ro = mkrs(P.out, S * D * 2);
    auto fin = [&](int t) { const f32x4 v = (rs[t] + pt[0][t]) + (pt[1][t] + pt[2][t]); return (u32x2){pk(v[0], v[1]), pk(v[2], v[3])}; };
#pragma unroll
    for (int pp = 0; pp < 2; ++pp) { const u32x2 lo = fin(2 * pp), hi = fin(2 * pp + 1); st16<0>((u32x4){lo.x, lo.y, hi.x, hi.y}, ro, xo + q * 16, dp * 2 + 64 * pp); }
    const u32x2 ls = fin(4);
    if (own0) st8<0>(ls, ro, xo + q * 8, ds * 2);
  }
  STAMP(14);
}
}

extern "C" __global__ __launch_bounds__(512, 2) void h3_gmlp_fused(const Params P) {
  extern __shared__ __attribute__((aligned(16))) char smem_[];
  LAS char* sm = (LAS char*)smem_;
  const int wid = __builtin_amdgcn_readfirstlane(threadIdx.x >> 6);
  Ids id; id.b = blockIdx.x; id.lane = threadIdx.x & 63;
  const int xcd = id.b & 7, loc = id.b >> 3;
  id.tp = (xcd >> 1) * 4 + (loc >> 3); id.ft = (xcd & 1) * 8 + (loc & 7);
  if (wid < 4) { unsigned sink = 0;
    if (wid == 0) code_touch(id, sink);
    else if (OPT_TWIN) { const rsrc_t r = mkrs(P.win, D * F * 2); const unsigned n = (wid - 1) * 64 + id.lane;
      if (OPT_TWIN > 1) __builtin_amdgcn_s_sleep(OPT_TWIN);
      unsigned o;
      if (TWZ == 0) o = n < 162 ? id.b * 20736 + n * 128 : 0xF0000000u;
      else { const unsigned m = id.b * 81 + n, row = m / 36; o = n < 81 ? row * (F * 2) + FH * 2 + (m - row * 36) * 128 : 0xF0000000u; }
      asm volatile("buffer_load_dword %0, %1, %2, 0 offen" : "+v"(sink) : "v"(o), "s"(r) : "memory"); }
    asm volatile("s_waitcnt vmcnt(0)" : "+v"(sink) :: "memory"); }
  if (__builtin_expect(wid == 4 || wid == 5, 1)) role_L(P, sm, id, wid - 4 STAMP_PASS);
  else if (wid >= 6) role_X(P, sm, id, wid - 6 STAMP_PASS);
  else role_C(P, sm, id, wid STAMP_PASS);
}
extern "C" __global__ void h3_gmlp_end() {}

extern "C" void kernel_launch(void* const* d_in, const int* in_sizes, int n_in, void* d_out, int out_size, void* d_ws, size_t ws_size, hipStream_t stream) {
  Params p;
  p.x = (const u16*)d_in[0]; p.k = (const u16*)d_in[1]; p.v = (const u16*)d_in[2]; p.shk = (const u16*)d_in[3]; p.ssmk = (const u16*)d_in[4]; p.ssmd = (const u16*)d_in[5];
  p.win = (const u16*)d_in[7]; p.wsp = (const u16*)d_in[8]; p.bsp = (const u16*)d_in[9]; p.wout = (const u16*)d_in[10];
  p.out = (u16*)d_out; p.ws = (char*)d_ws;
  (void)hipFuncSetAttribute((const void*)h3_gmlp_fused, hipFuncAttributeMaxDynamicSharedMemorySize, LDS_BYTES);
  h3_gmlp_fused<<<256, 512, LDS_BYTES, stream>>>(p);
}
```

```cpp
#define OPT_TWIN 1
#define TWZ 3
#include <hip/hip_runtime.h>
#include <hip/hip_bf16.h>
#include <type_traits>

namespace {
typedef unsigned short u16;
typedef unsigned long long u64;
typedef short bf16x8 __attribute__((ext_vector_type(8)));
typedef short bf16x4 __attribute__((ext_vector_type(4)));
typedef __bf16 bf2 __attribute__((ext_vector_type(2)));
typedef float f32x2 __attribute__((ext_vector_type(2)));
typedef float f32x4 __attribute__((ext_vector_type(4)));
typedef unsigned u32x2 __attribute__((ext_vector_type(2)));
typedef unsigned u32x4 __attribute__((ext_vector_type(4)));
typedef __amdgpu_buffer_rsrc_t rsrc_t;
#define LAS __attribute__((address_space(3)))
#define CAS __attribute__((address_space(4)))
#define DEV __device__ __forceinline__

constexpr int S = 2048, D = 576, F = 4608, FH = 2304;
constexpr int ASLOT = 16384, BSLOT = 18432, BBASE = 4 * ASLOT;
constexpr int STG = 139264, STG_W = 9216;
constexpr int T_M1 = 157952, T_R1 = T_M1 + 512, T_A0 = T_R1 + 512, T_A1 = T_A0 + 512, T_IR = T_A1 + 512;
constexpr int T_CS2 = T_IR + 512, T_CS1 = T_CS2;
constexpr int T_PART = T_CS1 + 640, T_N2 = T_PART, T_R2 = T_N2 + 512;
constexpr int T_TAG = T_PART + 2048, T_CNT = T_TAG + 16, T_GO = T_CNT + 4, T_YOK = T_CNT + 8, T_CVF = T_CNT + 16;
constexpr int LDS_BYTES = 163840;
static_assert(T_CNT + 32 <= LDS_BYTES, "LDS map");
constexpr int G3W = 25600;
constexpr int B2BASE = 3 * ASLOT;
constexpr int CV = B2BASE + 3 * BSLOT, CV_CH = 13056, CV_O = 4160, CV_U = 8320;
constexpr int CV_PS = CV + 2 * CV_CH + 15104;
constexpr int YTILE = 122880, XTILE = 133120;
constexpr int W_EPOCH = 0, W_F2 = 4096, W_F3 = 8192, W_F4 = 12288, W_F5 = 16384;
constexpr int W_G0X = 0x8000, W_G1F = 0x10000, W_G1S = 0x60000;
constexpr int W_KVT = 1 << 20, W_YT = 4 << 20, W_WOF = 7 << 20, W_ZF = 10 << 20, W_HF = 20 << 20, W_END = 30 << 20;
constexpr int SC1 = 16;
#define OPT_TWSP 1
#define OPT_TG3 0
#define EXP 0
#define RH 0
#define RW 0
#define KB 1
#define PR 0
#define XM 0
#define RT 0
#define WD 3

#define BAR0() do { asm volatile("" ::: "memory"); __builtin_amdgcn_s_barrier(); asm volatile("" ::: "memory"); } while (0)
#define BAR() BAR0()
#define SB() __builtin_amdgcn_sched_barrier(0)
#define SG(m, n) __builtin_amdgcn_sched_group_barrier(m, n, 0)
#define WAITV(n) asm volatile("s_waitcnt vmcnt(%0)" ::"i"(n) : "memory")
#define WAITL(n) asm volatile("s_waitcnt lgkmcnt(%0)" ::"i"(n) : "memory")
#define FENCE() asm volatile("" ::: "memory")
#define MFMA(a, b, c) __builtin_amdgcn_mfma_f32_16x16x32_bf16(a, b, c, 0, 0, 0)
#define STAMP(i) do {} while (0)
#define LBAR() BAR()
#define STAMP_ARG
#define STAMP_PASS

struct Params {
  const u16 *x, *k, *v, *shk, *ssmk, *ssmd, *win, *wsp, *bsp, *wout;
  u16* out; char* ws;
};

DEV rsrc_t mkrs(const void* p, unsigned bytes) { return __builtin_amdgcn_make_buffer_rsrc((void*)p, 0, bytes, 0x00020000); }
template <int AUX> DEV void dma(rsrc_t rs, LAS char* l, unsigned voff, unsigned soff) { __builtin_amdgcn_raw_ptr_buffer_load_lds(rs, (LAS void*)l, 16, voff, soff, 0, AUX); }
DEV void dma_hidden(rsrc_t rs, LAS char* l, unsigned voff) { unsigned keep;
  asm volatile("s_mov_b32 %0, m0\n\ts_mov_b32 m0, %3\n\ts_nop 0\n\tbuffer_load_dwordx4 %1, %2, 0 offen lds\n\ts_mov_b32 m0, %0" : "=&s"(keep) : "v"(voff), "s"(rs), "s"(__builtin_amdgcn_readfirstlane((unsigned)(size_t)l)) : "memory"); }
template <int AUX> DEV u32x4 ld16(rsrc_t rs, unsigned voff, unsigned soff) { return __builtin_amdgcn_raw_buffer_load_b128(rs, voff, soff, AUX); }
template <int AUX> DEV void st16(u32x4 v, rsrc_t rs, unsigned voff, unsigned soff) { __builtin_amdgcn_raw_buffer_store_b128(v, rs, voff, soff, AUX); }
template <int AUX> DEV void st8(u32x2 v, rsrc_t rs, unsigned voff, unsigned soff) { __builtin_amdgcn_raw_buffer_store_b64(v, rs, voff, soff, AUX); }
template <class T> DEV T lds(LAS char* p) { return *(LAS T*)p; }
DEV bf16x4 ldtr(LAS char* p) { return __builtin_amdgcn_ds_read_tr16_b64_v4i16((LAS bf16x4*)p); }
DEV bf16x8 cat(bf16x4 a, bf16x4 b) { return __builtin_shufflevector(a, b, 0, 1, 2, 3, 4, 5, 6, 7); }
DEV float blo(unsigned u) { return __uint_as_float(u << 16); }
DEV float bhi(unsigned u) { return __uint_as_float(u & 0xffff0000u); }
DEV unsigned pk(float a, float b) { return __builtin_bit_cast(unsigned, __builtin_convertvector((f32x2){a, b}, bf2)); }
DEV float dot2(unsigned a, unsigned b, float c) { return __builtin_amdgcn_fdot2_f32_bf16(__builtin_bit_cast(bf2, a), __builtin_bit_cast(bf2, b), c, false); }
template <int CTRL> DEV float dpp(float x) { return __builtin_bit_cast(float, __builtin_amdgcn_mov_dpp(__builtin_bit_cast(int, x), CTRL, 0xf, 0xf, true)); }
constexpr int XOR1 = 0xB1, XOR2 = 0x4E, XOR7 = 0x141, XOR8 = 0x128;
constexpr float GC0 = -2.30220819f, GC1 = -0.10294324f;
template <class T> DEV f32x2 gelu2(f32x2 a, T a0, T a1, T ir) {
  const f32x2 g = a * (a * a * a1 + a0);
  f32x2 e; e.x = __builtin_amdgcn_exp2f(g.x); e.y = __builtin_amdgcn_exp2f(g.y);
  e = e * ir + ir;
  f32x2 r; r.x = __builtin_amdgcn_rcpf(e.x); r.y = __builtin_amdgcn_rcpf(e.y);
  return a * r;
}
DEV f32x4 gelu4(f32x4 a, f32x4 a0, f32x4 a1, f32x4 ir) { const f32x2 p = gelu2(a.xy, a0.xy, a1.xy, ir.xy), q = gelu2(a.zw, a0.zw, a1.zw, ir.zw); return (f32x4){p.x, p.y, q.x, q.y}; }
DEV f32x4 gelu4(f32x4 a, float a0, float a1, float ir) { const f32x2 p = gelu2(a.xy, a0, a1, ir), q = gelu2(a.zw, a0, a1, ir); return (f32x4){p.x, p.y, q.x, q.y}; }
DEV u32x4 seal(float a, float b, u32x2 tag) { const unsigned ua = __float_as_uint(a), ub = __float_as_uint(b); return (u32x4){ua, ub, ua ^ tag.x, ub ^ tag.y}; }
DEV bool sealed(u32x4 g, u32x2 tag) { return (g.x ^ g.z) == tag.x && (g.y ^ g.w) == tag.y; }
DEV bool expired(u64 t0) { return __builtin_amdgcn_s_memrealtime() - t0 > 2000000ull; }
template <int NL, int NAP> DEV void wait_flags(rsrc_t ws, unsigned voff, u32x2 tag) {
  const u64 t0 = __builtin_amdgcn_s_memrealtime();
  for (;;) {
    bool ok = true;
#pragma unroll
    for (int i = 0; i < NL; ++i) { const u32x4 f = ld16<SC1>(ws, voff, i * 1024); ok &= f.x == tag.x && f.y == tag.y && f.z == tag.x && f.w == tag.y; }
    if (__all(ok) || expired(t0)) break;
    __builtin_amdgcn_s_sleep(NAP); FENCE();
  }
  FENCE();
}
DEV bool flags_ok(u32x4 f, u32x2 tag) { return f.x == tag.x && f.y == tag.y && f.z == tag.x && f.w == tag.y; }
DEV int prow(int T, int c) { return 32 * (T >> 1) + 8 * (c >> 2) + 4 * (T & 1) + (c & 3); }
DEV void set_flag(rsrc_t ws, unsigned off, u32x2 tag, int lane) { if (lane == 0) st8<SC1>(tag, ws, off, 0); }

struct Ids { int b, tp, ft, lane; };
DEV void code_touch(const Ids& id, unsigned& sink) {
  unsigned lo, hi, bytes;
  asm volatile("s_getpc_b64 vcc\n1:\n\ts_sub_u32 %0, vcc_lo, 1b-h3_gmlp_fused\n\ts_subb_u32 %1, vcc_hi, 0\n\ts_mov_b32 %2, h3_gmlp_end-h3_gmlp_fused" : "=s"(lo), "=s"(hi), "=s"(bytes) :: "vcc", "scc");
  const rsrc_t rc = mkrs((const void*)(((u64)hi << 32) | lo), bytes);
  asm volatile("buffer_load_dword %0, %1, %2, 0 offen" : "+v"(sink) : "v"((id.lane < 20 ? id.lane * 32 + (id.b >> 3) : 0x100000) * 128), "s"(rc) : "memory");
}
DEV int krot(const Ids& id) { return (RH * (id.ft & 7) + ((RW * (id.tp & 3)) >> 1)) % 18; }
DEV int kwrap(int i, int r) { const int k = i + r; return k >= 18 ? k - 18 : k; }

DEV void role_L(const Params& P, LAS char* sm, const Ids id, const int lw STAMP_ARG) {
  int bcount = 0; (void)bcount;
  const int lane = id.lane, rr = lane >> 3, sl = lane & 7;
  const rsrc_t rx = mkrs(P.x, S * D * 2), rwin = mkrs(P.win, D * F * 2);
  const unsigned swz = (sl ^ (4 * lw + (rr >> 1))) * 16, prr = 16 * lw + 8 * (rr >> 2) + (rr & 3);
  const unsigned vx = prr * (D * 2) + swz;
  unsigned vb[9];
  auto issue1a = [&](int slot, int i, int ks) { dma<0>(rx, sm + slot * ASLOT + lw * 1024 + i * 2048, vx, (id.tp * 128 + 32 * (i >> 1) + 4 * (i & 1)) * (D * 2) + ks * 128); };
  auto issue1b = [&](int slot, int i, int ks, int col) { dma<0>(rwin, sm + BBASE + slot * BSLOT + lw * 1024 + i * 2048, vb[i], ks * 64 * (F * 2) + (col + id.ft * 144) * 2); };
  STAMP(56);
#pragma unroll
  for (int i = 0; i < 9; ++i) {
    if (i < 8) issue1a(0, i, 0);
    if (i == 0) STAMP(57);
    SB();
    const unsigned ci = 64 * (2 * i + lw) + lane, rho = __umul24(ci, 3641) >> 16;
    vb[i] = __umul24(rho, F * 2 - 288) + 16 * ci + __mul24((int)(rho & 4) - (int)((rho >> 1) & 4), F * 2);
    issue1b(0, i, 0, FH);
    SB();
  }
  auto xA = [&](int n) {
#pragma unroll
    for (int i = 0; i < 8; ++i) issue1a(n < 9 ? n & 3 : (9 - n) & 3, i, n < 9 ? n : 17 - n); };
  auto wB = [&](int n) {
#pragma unroll
    for (int i = 0; i < 9; ++i) issue1b(n & 3, i, n < 9 ? n : 17 - n, n < 9 ? FH : 0); };
  STAMP(58);
  xA(1); wB(1); STAMP(59); xA(2);
  STAMP(60);
  const rsrc_t rwsp = mkrs(P.wsp, S * S * 2), rws = mkrs(P.ws, W_END);
  const unsigned vw = prr * (S * 2) + swz, vl = lane * 16 + lw * 1024;
  auto issue2a = [&](int k, int slot) { LAS char* a = sm + slot * ASLOT + lw * 1024; const unsigned sa = id.tp * 128 * (S * 2) + k * 128;
#pragma unroll
    for (int i = 0; i < 8; ++i) dma<0>(rwsp, a + i * 2048, vw, sa + (32 * (i >> 1) + 4 * (i & 1)) * (S * 2)); };
  auto issue2b = [&](int k, int slot) { LAS char* bb = sm + B2BASE + slot * BSLOT + lw * 1024; const unsigned sb = W_ZF + (id.ft * 576 + k * 18) * 1024;
#pragma unroll
    for (int i = 0; i < 9; ++i) dma<SC1>(rws, bb + i * 2048, vl, sb + i * 2048); };
#pragma unroll 1
  for (int n = 0; n < 6; ++n) { WAITV(25); LBAR(); wB(n + 2); xA(n + 3); }
  WAITV(25); LBAR(); wB(8);
  WAITV(17); LBAR(); wB(9);
  WAITV(9); LBAR(); wB(10);
  BAR(); wB(11); wB(12);
  WAITV(27); LBAR();
  WAITV(18); LBAR(); xA(13); wB(13);
  WAITV(26); LBAR(); xA(14); wB(14);
#pragma unroll 1
  for (int n = 12; n < 15; ++n) { WAITV(34); LBAR(); xA(n + 3); wB(n + 3); }
  WAITV(34); LBAR();
  WAITV(17); LBAR(); issue2a(0, 2);
  WAITV(8); LBAR(); issue2a(1, 1);
  BAR();
  { const u32x2 tag = lds<u32x2>(sm + T_TAG); wait_flags<1, 2>(rws, W_F2 + id.ft * 128 + (lane & 7) * 16, tag); }
  STAMP(61);
  issue2b(0, 0); issue2b(1, 1);
  asm volatile("ds_write_b32 %0, %1" :: "v"((unsigned)(size_t)(sm + T_GO)), "v"(1u) : "memory");
  WAITV(9); LBAR(); issue2a(2, 0); issue2b(2, 2);
  { int sb = 0;
#pragma unroll 1
    for (int k = 3; k < 32; ++k) { WAITV(17); LBAR(); issue2a(k, 2 - sb); issue2b(k, sb); sb = sb == 2 ? 0 : sb + 1; } }
  const int g = id.ft & 7;
  const unsigned wb0 = W_WOF + (g >> 1) * 72 * 9 * 1024, wb1 = wb0 + (g & 1) * 5 * 1024, vq = lane * 16;
  const int rot = krot(id);
  auto issue3 = [&](auto lo_, auto hi_) { constexpr int lo = decltype(lo_)::value, hi = decltype(hi_)::value;
#pragma unroll
    for (int p0 = lo; p0 < hi; p0 += 2) {
      auto off = [&](int pc) { return (pc % 5 == 4 ? wb0 : wb1) + (18 * (pc / 25) * 9 + pc % 5) * 1024 + kwrap((pc % 25) / 5, rot) * 9216; };
      dma<SC1>(rws, sm + p0 * 1024 + lw * 1024, vq, lw ? off(p0 + 1) : off(p0)); } };
#define IC(n) std::integral_constant<int, n>{}
  WAITV(17); LBAR(); issue3(IC(0), IC(16)); issue3(IC(84), IC(100));
  WAITV(16); LBAR(); issue3(IC(32), IC(66));
  LBAR(); issue3(IC(16), IC(32)); issue3(IC(66), IC(84));
  unsigned sink = 0;
  auto wtouch = [&]() {
    constexpr int K0 = 5 + WD, NK = 18 - K0;
#pragma unroll
    for (int i = 0; i < (NK * 40 + 127) / 128; ++i) { const unsigned n = (i * 2 + lw) * 64 + lane, blk = n >> 1, kw = __umul24(blk, 13108) >> 16, t = blk - kw * 5;
      const unsigned o = n < NK * 40 ? (t == 4 ? wb0 : wb1) + ((18 * (kw & 3) + K0 + (kw >> 2)) * 9 + t) * 1024 + ((id.tp & 3) * 2 + (n & 1)) * 128 : 0xF0000000u;
      asm volatile("buffer_load_dword %0, %1, %2, 0 offen sc1" : "+v"(sink) : "v"(o), "s"(rws) : "memory"); } };
  if ((OPT_TG3 & 3) == 1) wtouch();
  LBAR();
  if ((OPT_TG3 & 3) == 2) { __builtin_amdgcn_s_sleep(2); wtouch(); }
  WAITV((OPT_TG3 & 3) == 2 ? (( 18 - 5 - WD) * 40 + 127) / 128 : 0); LBAR();
  asm volatile("s_waitcnt vmcnt(0)" : "+v"(sink) :: "memory"); LBAR();
}

DEV void role_X(const Params& P, LAS char* sm, const Ids id, const int xw STAMP_ARG) {
  int bcount = 0; (void)bcount;
  const int lane = id.lane;
  const rsrc_t rws = mkrs(P.ws, W_END);
  unsigned sink = 0;
  const u32x2 ev = __builtin_amdgcn_raw_buffer_load_b64(rws, W_EPOCH, 0, SC1);
  const int srow = id.tp * 128 + id.ft * 8 + 4 * xw + (lane >> 4);
  u32x4 xv[5];
  { const rsrc_t rx = mkrs(P.x, S * D * 2);
#pragma unroll
    for (int i = 0; i < 5; ++i) xv[i] = ld16<0>(rx, i < 4 || (lane & 8) == 0 ? srow * (D * 2) + (lane & 15) * 16 : 0xF0000000u, i * 256); }
  const u64 epoch = ((u64)__builtin_amdgcn_readfirstlane(ev.y) << 32) | __builtin_amdgcn_readfirstlane(ev.x);
  u32x2 tag; { const u64 t = ((epoch + 1) * 0x9E3779B97F4A7C15ull) | 1ull; tag.x = (unsigned)t; tag.y = (unsigned)(t >> 32); }
  { float s = 0.f, q = 0.f; unsigned ones = 0x3f803f80u; asm volatile("" : "+v"(ones));
#pragma unroll
    for (int i = 0; i < 5; ++i)
#pragma unroll
      for (int e = 0; e < 4; ++e) { s = dot2(xv[i][e], ones, s); q = dot2(xv[i][e], xv[i][e], q); }
    s += dpp<XOR1>(s); q += dpp<XOR1>(q); s += dpp<XOR2>(s); q += dpp<XOR2>(q); s += dpp<XOR7>(s); q += dpp<XOR7>(q); s += dpp<XOR8>(s); q += dpp<XOR8>(q);
    const float m = s * (1.0f / D), var = fmaxf(q * (1.0f / D) - m * m, 0.f) + 1e-5f;
    if ((lane & 15) == 0) st16<SC1>(seal(m, var, tag), rws, W_G0X + srow * 16, 0);
    if (xw == 0 && lane == 0) { *(LAS u32x2*)(sm + T_TAG) = tag; *(LAS u32x4*)(sm + T_CNT) = (u32x4){0u, 0u, 0u, 0u}; *(LAS u32x4*)(sm + T_CVF) = (u32x4){0u, 0u, 0u, 0u}; }
  }
  STAMP(0);
  BAR(); BAR();
  if (TWZ == 2) { const rsrc_t r = mkrs(P.win, D * F * 2); const unsigned n = xw * 64 + lane, m = id.b * 81 + n, row = m / 36;
    asm volatile("buffer_load_dword %0, %1, %2, 0 offen" : "+v"(sink) : "v"(n < 81 ? row * (F * 2) + (m - row * 36) * 128 : 0xF0000000u), "s"(r) : "memory"); }
  BAR(); BAR();
  LAS char* stg = sm + STG + xw * STG_W;
  const int unit = xw ? 256 + id.b : id.b, wks = unit >> 2, wu = unit & 3; const bool won = !xw || id.b < 32;
  const int tile = xw ? id.b : 256 + id.b - 32; const bool kon = xw || (id.b >= 32 && id.b < 64);
  const int tb = tile / 9, db = tile - tb * 9, cc = lane & 7, rg = lane >> 3;
  u32x4 kk[11], vv[8], sh[4];
  { const int row = 64 * xw + lane; const u64 t0 = __builtin_amdgcn_s_memrealtime();
    u32x4 gx = ld16<SC1>(rws, W_G0X + (id.tp * 128 + row) * 16, 0);
    BAR(); BAR();
    while (!__all(sealed(gx, tag)) && !expired(t0)) { __builtin_amdgcn_s_sleep(4); FENCE(); gx = ld16<SC1>(rws, W_G0X + (id.tp * 128 + row) * 16, 0); }
    const float var = __uint_as_float(gx.y), r = __builtin_amdgcn_rsqf(var);
    *(LAS unsigned*)(sm + T_M1 + row * 4) = gx.x; *(LAS float*)(sm + T_R1 + row * 4) = r;
    *(LAS float*)(sm + T_A0 + row * 4) = GC0 * r; *(LAS float*)(sm + T_A1 + row * 4) = GC1 * r * r * r; *(LAS float*)(sm + T_IR + row * 4) = var * r;
    if ((RT & 1) && xw == 0) code_touch(id, sink);
    WAITL(0); BAR();
  }
  STAMP(1);
  { const rsrc_t rk = mkrs(P.k, S * D * 2), rv = mkrs(P.v, S * D * 2), rs = mkrs(P.shk, D * 4 * 2), rwo = mkrs(P.wout, FH * D * 2);
    const int t0 = 64 * tb + 8 * rg; const unsigned col = (64 * db + 8 * cc) * 2;
    if (kon) {
#pragma unroll
      for (int i = 0; i < 11; ++i) kk[i] = ld16<0>(rk, t0 + i - 3 >= 0 ? (unsigned)(t0 + i - 3) * (D * 2) + col : 0xF0000000u, 0);
    }
    BAR();
    if (kon) {
#pragma unroll
      for (int i = 0; i < 8; ++i) vv[i] = ld16<0>(rv, t0 * (D * 2) + col, i * (D * 2));
#pragma unroll
      for (int i = 0; i < 4; ++i) sh[i] = ld16<0>(rs, col * 4, i * 16);
    }
    BAR();
    if (won) { const int fb0 = wks < 64 ? 144 * (wks >> 2) + 32 * (wks & 3) : 288 * (wks - 64) + 128, fst = wks < 64 ? 0 : 128;
#pragma unroll
      for (int i = 0; i < 9; ++i) { const int ci = 64 * i + lane, rho = __umul24(ci, 3641) >> 16, ch = ci - rho * 18;
        dma_hidden(rwo, stg + i * 1024, (fb0 + rho + (rho >> 4) * fst) * (D * 2) + wu * 288 + ch * 16); } }
    BAR();
    if (kon) {
#pragma unroll
      for (int i = 0; i < 4; ++i) asm volatile("" : "+v"(sh[i]), "+v"(vv[i]), "+v"(vv[4 + i]), "+v"(kk[i]), "+v"(kk[4 + i]), "+v"(kk[i == 3 ? 0 : 8 + i])); }
    if (OPT_TWSP) { const rsrc_t r = mkrs(P.wsp, S * S * 2);
#pragma unroll
      for (int i = 0; i < 2; ++i) asm volatile("buffer_load_dword %0, %1, %2, 0 offen" : "+v"(sink) : "v"(id.b * 32768 + ((2 * i + xw) * 64 + lane) * 128), "s"(r) : "memory"); }
  }
  BAR();
  u32x4 ko[8];
  auto kvstep = [&](int e) {
    if (kon) {
      float c[4], kf[11], y[8];
#pragma unroll
      for (int j = 0; j < 4; ++j) { const unsigned w = sh[e >> 1][(e & 1) * 2 + (j >> 1)]; c[j] = (j & 1) ? bhi(w) : blo(w); }
#pragma unroll
      for (int i = 0; i < 11; ++i) { const unsigned w = kk[i][e >> 1]; kf[i] = (e & 1) ? bhi(w) : blo(w); }
#pragma unroll
      for (int i = 0; i < 8; ++i) { const unsigned w = vv[i][e >> 1];
        float a = c[3] * kf[i]; asm volatile("" : "+v"(a)); a = fmaf(c[2], kf[i + 1], a); asm volatile("" : "+v"(a)); a = fmaf(c[1], kf[i + 2], a); asm volatile("" : "+v"(a)); a = fmaf(c[0], kf[i + 3], a);
        asm volatile("" : "+v"(a)); y[i] = a * ((e & 1) ? bhi(w) : blo(w)); asm volatile("" : "+v"(y[i])); }
      ko[e] = (u32x4){pk(y[7], y[6]), pk(y[5], y[4]), pk(y[3], y[2]), pk(y[1], y[0])};
      asm volatile("" : "+v"(ko[e]));
    } };
  { const int row = 64 * xw + lane;
    const f32x2 a = lds<f32x2>(sm + T_PART + row * 8) + lds<f32x2>(sm + T_PART + 1024 + row * 8);
    st16<SC1>(seal(a.x, a.y, tag), rws, W_G1S + ((id.tp * 16 + id.ft) * 128 + row) * 16, 0);
    BAR(); kvstep(0); BAR();
    u32x4 gs[16];
    auto gather = [&]() {
#pragma unroll
      for (int i = 0; i < 16; ++i) gs[i] = ld16<SC1>(rws, W_G1S + (id.tp * 16 * 128 + row) * 16, i * 2048); };
    unsigned bad = 0; float s = 0.f, q = 0.f;
    auto sum = [&](int lo, int hi) {
#pragma unroll
      for (int i = lo; i < hi; ++i) { bad |= (gs[i].x ^ gs[i].z ^ tag.x) | (gs[i].y ^ gs[i].w ^ tag.y); s += __uint_as_float(gs[i].x); q += __uint_as_float(gs[i].y); }
      asm volatile("" : "+v"(bad), "+v"(s), "+v"(q)); };
    gather(); BAR(); sum(0, 10); BAR();
    sum(10, 16);
    if (__builtin_expect(!__all(bad == 0), 0)) { const u64 t0 = __builtin_amdgcn_s_memrealtime();
      do { FENCE(); gather(); bad = 0; s = 0.f; q = 0.f; sum(0, 16); } while (!__all(bad == 0) && !expired(t0)); }
    const float m = s * (1.0f / FH), var = fmaxf(q * (1.0f / FH) - m * m, 0.f), r = __builtin_amdgcn_rsqf(var + 1e-5f);
    *(LAS float*)(sm + T_R2 + row * 4) = r; *(LAS float*)(sm + T_N2 + row * 4) = -m * r;
    asm volatile("s_waitcnt vmcnt(0) lgkmcnt(0)" : "+v"(sink) :: "memory"); BAR();
  }
  STAMP(2);
  kvstep(1); kvstep(2); BAR(); kvstep(3); BAR(); kvstep(4); BAR();
  if (xw == 0) set_flag(rws, W_F2 + (id.ft * 16 + id.tp) * 8, tag, lane);
  kvstep(5); kvstep(6); BAR(); kvstep(7);
  const unsigned h0a = ((const CAS unsigned*)P.ssmk)[id.b * S], h0b = ((const CAS unsigned*)P.ssmk)[(xw ? 2 * id.b + 1 : 512 + (id.b >> 2)) * (S / 2)];
  { u32x4 wf[9];
    if (won) {
      const int q = lane >> 4, qq = (lane & 15) >> 2, p = lane & 3;
      LAS char* base = stg + (4 * q + qq) * 288;
#pragma unroll
      for (int T = 0; T < 9; ++T) { const int cb = T < 4 ? 32 * (T >> 1) + 4 * (T & 1) : T == 4 ? 64 : 80 + 32 * ((T - 5) >> 1) + 4 * ((T - 5) & 1);
        LAS char* a = base + cb * 2 + p * (T == 4 ? 8 : 16);
        wf[T] = __builtin_bit_cast(u32x4, cat(ldtr(a), ldtr(a + 16 * 288))); }
      WAITL(0);
    }
    if (kon) {
#pragma unroll
      for (int e = 0; e < 8; ++e) *(LAS u32x4*)(stg + (8 * cc + e) * 128 + (((7 - rg) ^ cc) * 16)) = ko[e];
#pragma unroll
      for (int i = 0; i < 8; ++i) ko[i] = lds<u32x4>(stg + (8 * i + rg) * 128 + ((cc ^ i) * 16));
    }
    { const rsrc_t rh = mkrs(P.ssmk, D * S * 2);
      WAITL(0);
      auto filt = [&](int ci, unsigned h0) { const int chn = ci == 2 ? 512 + (id.b >> 2) : 2 * id.b + ci; LAS char* cb = sm + CV + ci * CV_CH;
        if (lane < 8) { *(LAS unsigned*)(cb + lane * 4) = 0u; *(LAS unsigned*)(cb + CV_O + lane * 4) = lane == 7 ? h0 << 16 : 0u; }
        if (lane < 38) *(LAS u32x4*)(cb + CV_U + 4096 + lane * 16) = (u32x4){0u, 0u, 0u, 0u};
        if (ci == 2) { *(LAS u32x4*)(cb + CV_U + 4704 + lane * 16) = (u32x4){0u, 0u, 0u, 0u}; *(LAS u32x4*)(cb + CV_U + 5728 + lane * 16) = (u32x4){0u, 0u, 0u, 0u}; }
#pragma unroll
        for (int i = 0; i < 4; ++i) { dma_hidden(rh, cb + 32 + i * 1024, chn * (S * 2) + i * 1024 + lane * 16); dma_hidden(rh, cb + CV_O + 32 + i * 1024, chn * (S * 2) + i * 1024 + lane * 16 + 2); } };
      if (xw == 0) filt(0, h0a);
      filt(2 - xw, h0b); }
    const u64 t0 = __builtin_amdgcn_s_memrealtime();
    while (*(volatile LAS unsigned*)(sm + T_GO) == 0u && !expired(t0)) __builtin_amdgcn_s_sleep(2);
    FENCE();
    if (won) {
#pragma unroll
      for (int T = 0; T < 9; ++T) st16<SC1>(wf[T], rws, W_WOF + lane * 16, ((wu * 72 + wks) * 9 + T) * 1024); }
    if (kon) {
#pragma unroll
      for (int i = 0; i < 8; ++i) st16<SC1>(ko[i], rws, W_KVT + (64 * db + rg) * (S * 2) + (S - 64 * (tb + 1)) * 2 + cc * 16, 8 * i * (S * 2)); }
  }
  BAR(); BAR(); BAR();
  BAR(); WAITV(0); BAR();
  if (xw == 0) set_flag(rws, W_F5 + id.b * 8, tag, lane);
  STAMP(3);
#pragma unroll 1
  for (int i = 0; i < 7; ++i) BAR();
  { u32x4 f0 = {tag.x, tag.y, tag.x, tag.y}, f1 = f0;
    if (xw == 0) { f0 = ld16<SC1>(rws, W_F5 + lane * 16, 0); f1 = ld16<SC1>(rws, W_F5 + lane * 16, 1024); }
    BAR(); BAR();
    if (!__all(flags_ok(f0, tag) && flags_ok(f1, tag))) wait_flags<2, 8>(rws, W_F5 + lane * 16, tag); }
  BAR();
  STAMP(4);
  { auto rows = [&](int ci) { const int chn = ci == 2 ? 512 + (id.b >> 2) : 2 * id.b + ci;
#pragma unroll
      for (int i = 0; i < 4; ++i) dma<SC1>(rws, sm + CV + ci * CV_CH + CV_U + i * 1024, lane * 16, W_KVT + chn * (S * 2) + i * 1024); };
    rows(xw); BAR(); if (xw == 0) rows(2); }
#pragma unroll 1
  for (int i = 0; i < 17; ++i) { if (i == 5) { WAITV(0); WAITL(0); } BAR(); }
  BAR();
  if (xw == 0) { set_flag(rws, W_F3 + (id.tp * 16 + id.ft) * 8, tag, lane); wait_flags<1, 1>(rws, W_F3 + id.tp * 128 + (lane & 7) * 16, tag); }
  STAMP(5);
  BAR();
  if (xw == 1 && (OPT_TG3 & 4)) {
#pragma unroll
    for (int i = 0; i < 4; ++i) { const unsigned n = i * 64 + lane, o = n < 240 ? W_HF + ((id.tp * 8 + 4 * (id.ft >> 3) + (n & 3)) * 72 + 18 * ((n >> 2) & 3) + 3 + (n >> 4)) * 1024 + (id.ft & 7) * 128 : 0xF0000000u;
      asm volatile("buffer_load_dword %0, %1, %2, 0 offen sc1" : "+v"(sink) : "v"(o), "s"(rws) : "memory"); } }
  if (xw == 0) {
    const u64 t0 = __builtin_amdgcn_s_memrealtime();
    while (*(volatile LAS unsigned*)(sm + T_CNT) != 4u && !expired(t0)) __builtin_amdgcn_s_sleep(2);
    FENCE();
    set_flag(rws, W_F4 + id.b * 8, tag, lane);
    const int g = id.ft & 7, d0 = 144 * (g >> 1) + 64 * (g & 1);
    { const rsrc_t rx = mkrs(P.x, S * D * 2);
#pragma unroll
      for (int i = 0; i < 10; ++i) { const unsigned ci = 64 * i + lane, r = __umul24(ci, 6554) >> 16; dma<0>(rx, sm + XTILE + i * 1024, (id.tp * 128 + 64 * (id.ft >> 3) + r) * (D * 2) + d0 * 2 + (ci - 10 * r) * 16, 0); } }
    __builtin_amdgcn_s_sleep(24); wait_flags<2, 12>(rws, W_F4 + lane * 16, tag);
#pragma unroll
    for (int i = 0; i < 10; ++i) dma<SC1>(rws, sm + YTILE + i * 1024, W_YT + (d0 + (lane >> 3)) * (S * 2) + (id.tp * 128 + 64 * (id.ft >> 3)) * 2 + (lane & 7) * 16, i * 8 * (S * 2));
    WAITV(0);
    asm volatile("ds_write_b32 %0, %1" :: "v"((unsigned)(size_t)(sm + T_YOK)), "v"(1u) : "memory");
    if (id.b == 0 && lane == 0) st8<SC1>((u32x2){(unsigned)(epoch + 1), (unsigned)((epoch + 1) >> 32)}, rws, W_EPOCH, 0);
  }
  STAMP(6);
  asm volatile("s_waitcnt vmcnt(0)" : "+v"(sink) :: "memory"); BAR();
}

DEV void role_C(const Params& P, LAS char* sm, const Ids id, const int wid STAMP_ARG) {
  int bcount = 0; (void)bcount;
  const int lane = id.lane, wt = wid >> 1, wf = wid & 1, c = lane & 15, q = lane >> 4;
  const rsrc_t rws = mkrs(P.ws, W_END);
  __builtin_amdgcn_s_setprio(1);
  unsigned dm = ((const CAS unsigned*)P.ssmd)[id.b], dsh = ((const CAS unsigned*)P.ssmd)[256 + (id.b >> 3)];
  asm volatile("" : "+s"(dm), "+s"(dsh));
  f32x4 acc[4][4], accx[2], csa[3];
  bf16x8 tf[2][4], ff[2][5], ones;
  LAS char *ta[2][2], *fb[2][2], *fx[2];
#pragma unroll
  for (int s = 0; s < 2; ++s) {
    const int o = (32 * (s ^ wt) + 16 * (q >> 1) + 4 * (q & 1) + (c >> 2)) * 288 + 8 * (c & 3);
    fx[s] = sm + BBASE + o + 256;
#pragma unroll
    for (int h = 0; h < 2; ++h) { ta[s][h] = sm + (64 * wt + 32 * (h ^ wf) + c) * 128 + (((q + 4 * (s ^ wt)) ^ ((c >> 1) & 7)) * 16); fb[s][h] = sm + BBASE + o + wf * 128 + (h ^ wt) * 64; }
  }
#pragma unroll
  for (int e = 0; e < 8; ++e) ones[e] = 0x3f80;
  asm volatile("" : "+v"(ones));
  auto rd = [&](auto mode, int b, int as, int bs, int s) {
    constexpr int MODE = decltype(mode)::value;
#pragma unroll
    for (int j = 0; j < 4; ++j) { LAS char* p = fb[s][j >> 1] + bs * BSLOT;
      ff[b][j] = MODE < 2 ? cat(ldtr(p + (j & 1) * 32), ldtr(p + (j & 1) * 32 + 8 * 288)) : lds<bf16x8>(p + (j & 1) * 1024); }
#pragma unroll
    for (int i = 0; i < 4; ++i) tf[b][i] = lds<bf16x8>(ta[s][i >> 1] + as * ASLOT + (i & 1) * 2048);
    ff[b][4] = MODE < 2 ? cat(ldtr(fx[s] + bs * BSLOT), ldtr(fx[s] + bs * BSLOT + 8 * 288)) : lds<bf16x8>(fx[s] + bs * BSLOT);
  };
  auto mm = [&](auto mode, int b, int lo, int hi) {
    constexpr int MODE = decltype(mode)::value;
#pragma unroll
    for (int n = 0; n < (MODE == 2 ? 18 : 21 - b); ++n) if (n >= lo && n < hi) {
      if (n < 16) { const int i = n >> 2, j = (i & 1) ? 3 - (n & 3) : (n & 3);
        acc[i][j] = MODE == 0 ? MFMA(tf[b][i], ff[b][j], acc[i][j]) : MFMA(ff[b][j], tf[b][i], acc[i][j]); }
      else if (n < 18) accx[n - 16] = MODE == 0 ? MFMA(tf[b][n - 16], ff[b][4], accx[n - 16]) : MFMA(ff[b][4], tf[b][n - 16], accx[n - 16]);
      else { const int j = n == 20 ? 4 : n - 18; csa[n - 18] = MODE == 0 ? MFMA(ones, ff[b][j], csa[n - 18]) : MFMA(ff[b][j], ones, csa[n - 18]); }
    }
  };
  auto stage = [&](auto mode, int as, int bs, bool drain) {
    constexpr int MODE = decltype(mode)::value;
    mm(mode, 1, 0, 4); SB();
    if (drain) WAITV(0);
    WAITL(0); BAR(); SB();
    rd(mode, 0, as, bs, 0); mm(mode, 1, 4, 21);
    if (MODE < 2) { for (int i = 0; i < 14; ++i) { SG(0x100, 1); SG(0x8, 1); } SG(0x8, 2); }
    else { for (int i = 0; i < 4; ++i) { SG(0x100, 1); SG(0x8, 1); SG(0x100, 1); SG(0x8, 2); } SG(0x100, 1); SG(0x8, 2); }
    SB();
    rd(mode, 1, as, bs, 1); mm(mode, 0, 0, 21);
    if (MODE < 2) { for (int i = 0; i < 7; ++i) { SG(0x100, 1); SG(0x8, 1); SG(0x100, 1); SG(0x8, 2); } }
    else { for (int i = 0; i < 9; ++i) { SG(0x100, 1); SG(0x8, 2); } }
    SB();
  };
  auto first = [&](auto mode, int as, int bs) { WAITL(0); BAR(); SB(); rd(mode, 0, as, bs, 0); rd(mode, 1, as, bs, 1); mm(mode, 0, 0, 21); SB(); };
  auto zero = [&]() {
#pragma unroll
    for (int i = 0; i < 4; ++i)
#pragma unroll
      for (int j = 0; j < 4; ++j) acc[i][j] = (f32x4){0.f, 0.f, 0.f, 0.f};
    accx[0] = accx[1] = csa[0] = csa[1] = csa[2] = (f32x4){0.f, 0.f, 0.f, 0.f}; };
  const std::integral_constant<int, 0> M0; const std::integral_constant<int, 1> M1; const std::integral_constant<int, 2> M2;
  const int jg0 = 4 * wf + 2 * wt, jg2 = 4 * wf + 2 - 2 * wt;

  zero();
  first(M0, 0, 0);
  STAMP(0);
#pragma unroll 1
  for (int it = 0; it < 2; ++it) { stage(M0, 1, 1, false); stage(M0, 2, 2, false); stage(M0, 3, 3, false); stage(M0, 0, 0, false); }
  mm(M0, 1, 18, 21); mm(M0, 1, 0, 8); SB();
  if (q == 0) { *(LAS float*)(sm + T_CS2 + (jg0 * 16 + c) * 4) = csa[0][0]; *(LAS float*)(sm + T_CS2 + (jg0 * 16 + 16 + c) * 4) = csa[1][0]; *(LAS float*)(sm + T_CS2 + (128 + 16 * wt + c) * 4) = csa[2][0]; }
  mm(M0, 1, 8, 18); SB();
  WAITL(0); BAR();
  STAMP(1);
  u32x4 z2p[2][4], z2x;
  { float cs[5];
    cs[0] = csa[0][0]; cs[1] = csa[1][0]; cs[2] = lds<float>(sm + T_CS2 + (jg2 * 16 + c) * 4); cs[3] = lds<float>(sm + T_CS2 + (jg2 * 16 + 16 + c) * 4);
    cs[4] = csa[2][0] + lds<float>(sm + T_CS2 + (144 - 16 * wt + c) * 4);
    float red[2][16];
#pragma unroll
    for (int i = 0; i < 4; ++i) {
      const int ro = (64 * wt + 32 * ((i >> 1) ^ wf) + 8 * q + 4 * (i & 1)) * 4;
      const f32x4 m = lds<f32x4>(sm + T_M1 + ro), a0 = lds<f32x4>(sm + T_A0 + ro), a1 = lds<f32x4>(sm + T_A1 + ro), ir = lds<f32x4>(sm + T_IR + ro);
      f32x4 su = {0.f, 0.f, 0.f, 0.f}, sq = {0.f, 0.f, 0.f, 0.f};
#pragma unroll
      for (int j = 0; j < (i < 2 ? 5 : 4); ++j) {
        const f32x4 z = gelu4((j < 4 ? acc[i][j] : accx[i]) - m * cs[j], a0, a1, ir);
        su = j ? su + z : z; sq = j ? sq + z * z : z * z;
        u32x4& dst = j < 4 ? z2p[i >> 1][j] : z2x;
        dst[2 * (i & 1)] = pk(z[0], z[1]); dst[2 * (i & 1) + 1] = pk(z[2], z[3]);
      }
#pragma unroll
      for (int e = 0; e < 4; ++e) { red[0][4 * i + e] = su[e]; red[1][4 * i + e] = sq[e]; }
    }
    float r8[2][8], r4[2][4];
#pragma unroll
    for (int st = 0; st < 2; ++st) {
#pragma unroll
      for (int e = 0; e < 8; e += 4)
        asm volatile("s_nop 1\n\t"
                     "v_add_f32_dpp %0, %4, %4 row_ror:8 row_mask:0xf bank_mask:0x3\n\tv_add_f32_dpp %1, %5, %5 row_ror:8 row_mask:0xf bank_mask:0x3\n\t"
                     "v_add_f32_dpp %2, %6, %6 row_ror:8 row_mask:0xf bank_mask:0x3\n\tv_add_f32_dpp %3, %7, %7 row_ror:8 row_mask:0xf bank_mask:0x3\n\t"
                     "v_add_f32_dpp %0, %8, %8 row_ror:8 row_mask:0xf bank_mask:0xc\n\tv_add_f32_dpp %1, %9, %9 row_ror:8 row_mask:0xf bank_mask:0xc\n\t"
                     "v_add_f32_dpp %2, %10, %10 row_ror:8 row_mask:0xf bank_mask:0xc\n\tv_add_f32_dpp %3, %11, %11 row_ror:8 row_mask:0xf bank_mask:0xc"
                     : "=&v"(r8[st][e]), "=&v"(r8[st][e + 1]), "=&v"(r8[st][e + 2]), "=&v"(r8[st][e + 3])
                     : "v"(red[st][e]), "v"(red[st][e + 1]), "v"(red[st][e + 2]), "v"(red[st][e + 3]), "v"(red[st][8 + e]), "v"(red[st][9 + e]), "v"(red[st][10 + e]), "v"(red[st][11 + e]));
      asm volatile("s_nop 1\n\t"
                   "v_add_f32_dpp %0, %4, %4 row_half_mirror row_mask:0xf bank_mask:0x5\n\tv_add_f32_dpp %1, %5, %5 row_half_mirror row_mask:0xf bank_mask:0x5\n\t"
                   "v_add_f32_dpp %2, %6, %6 row_half_mirror row_mask:0xf bank_mask:0x5\n\tv_add_f32_dpp %3, %7, %7 row_half_mirror row_mask:0xf bank_mask:0x5\n\t"
                   "v_add_f32_dpp %0, %8, %8 row_half_mirror row_mask:0xf bank_mask:0xa\n\tv_add_f32_dpp %1, %9, %9 row_half_mirror row_mask:0xf bank_mask:0xa\n\t"
                   "v_add_f32_dpp %2, %10, %10 row_half_mirror row_mask:0xf bank_mask:0xa\n\tv_add_f32_dpp %3, %11, %11 row_half_mirror row_mask:0xf bank_mask:0xa\n\t"
                   "v_add_f32_dpp %0, %0, %0 quad_perm:[1,0,3,2] row_mask:0xf bank_mask:0xf\n\tv_add_f32_dpp %1, %1, %1 quad_perm:[1,0,3,2] row_mask:0xf bank_mask:0xf\n\t"
                   "v_add_f32_dpp %2, %2, %2 quad_perm:[1,0,3,2] row_mask:0xf bank_mask:0xf\n\tv_add_f32_dpp %3, %3, %3 quad_perm:[1,0,3,2] row_mask:0xf bank_mask:0xf\n\t"
                   "v_add_f32_dpp %0, %0, %0 quad_perm:[2,3,0,1] row_mask:0xf bank_mask:0xf\n\tv_add_f32_dpp %1, %1, %1 quad_perm:[2,3,0,1] row_mask:0xf bank_mask:0xf\n\t"
                   "v_add_f32_dpp %2, %2, %2 quad_perm:[2,3,0,1] row_mask:0xf bank_mask:0xf\n\tv_add_f32_dpp %3, %3, %3 quad_perm:[2,3,0,1] row_mask:0xf bank_mask:0xf"
                   : "=&v"(r4[st][0]), "=&v"(r4[st][1]), "=&v"(r4[st][2]), "=&v"(r4[st][3])
                   : "v"(r8[st][0]), "v"(r8[st][1]), "v"(r8[st][2]), "v"(r8[st][3]), "v"(r8[st][4]), "v"(r8[st][5]), "v"(r8[st][6]), "v"(r8[st][7]));
    }
    if ((c & 3) == 0) { LAS char* pp = sm + T_PART + wf * 1024 + (64 * wt + 32 * ((c >> 3) ^ wf) + 8 * q + 4 * ((c >> 2) & 1)) * 8;
      *(LAS f32x4*)pp = (f32x4){r4[0][0], r4[1][0], r4[0][1], r4[1][1]}; *(LAS f32x4*)(pp + 16) = (f32x4){r4[0][2], r4[1][2], r4[0][3], r4[1][3]}; }
#pragma unroll
    for (int i = 0; i < 2; ++i) asm volatile("" : "+v"(z2p[i][0]), "+v"(z2p[i][1]), "+v"(z2p[i][2]), "+v"(z2p[i][3]));
    asm volatile("" : "+v"(z2x));
  }
  STAMP(2);
  zero();
  first(M1, 0, 1);
  stage(M1, 3, 2, false); stage(M1, 2, 3, false); stage(M1, 1, 0, false);
  stage(M1, 0, 1, false); stage(M1, 3, 2, false);
  STAMP(3);
  {
#pragma unroll
    for (int i = 0; i < 2; ++i) asm volatile("" : "+v"(z2p[i][0]), "+v"(z2p[i][1]), "+v"(z2p[i][2]), "+v"(z2p[i][3]));
    asm volatile("" : "+v"(z2x));
#pragma unroll
    for (int ip = 0; ip < 2; ++ip) {
      const int ro = (64 * wt + 32 * (ip ^ wf) + 8 * q) * 4;
      const f32x4 r0 = lds<f32x4>(sm + T_R2 + ro), r1 = lds<f32x4>(sm + T_R2 + ro + 16), n0 = lds<f32x4>(sm + T_N2 + ro), n1 = lds<f32x4>(sm + T_N2 + ro + 16);
      const unsigned so = W_ZF + ((id.ft * 64 + 4 * id.tp + 2 * wt + (ip ^ wf)) * 9) * 1024;
#pragma unroll
      for (int j = 0; j < (ip == 0 ? 5 : 4); ++j) { const u32x4 z = j < 4 ? z2p[ip][j] : z2x;
        const f32x4 a = (f32x4){blo(z[0]), bhi(z[0]), blo(z[1]), bhi(z[1])} * r0 + n0, b = (f32x4){blo(z[2]), bhi(z[2]), blo(z[3]), bhi(z[3])} * r1 + n1;
        st16<SC1>((u32x4){pk(a[0], a[1]), pk(a[2], a[3]), pk(b[0], b[1]), pk(b[2], b[3])}, rws, lane * 16, so + (j < 2 ? jg0 + j : j < 4 ? jg2 + j - 2 : 8) * 1024); }
    }
    SB();
  }
  unsigned braw[4];
  { const rsrc_t rb = mkrs(P.bsp, S * 2);
#pragma unroll
    for (int i = 0; i < 4; ++i) braw[i] = __builtin_amdgcn_raw_buffer_load_b16(rb, (id.tp * 128 + 64 * wt + prow(i ^ (2 * wf), c)) * 2, 0, 0); }
  STAMP(4);
  stage(M1, 2, 3, false); stage(M1, 1, 0, false); stage(M1, 0, 1, true);
  mm(M1, 1, 18, 21); mm(M1, 1, 0, 8); SB();
  if (c == 0) { *(LAS f32x4*)(sm + T_CS1 + (jg0 * 16 + 4 * q) * 4) = csa[0]; *(LAS f32x4*)(sm + T_CS1 + (jg0 * 16 + 16 + 4 * q) * 4) = csa[1]; *(LAS f32x4*)(sm + T_CS1 + (128 + 16 * wt + 4 * q) * 4) = csa[2]; }
  mm(M1, 1, 8, 18); SB();
  WAITL(0); BAR();
  STAMP(5);
  u32x2 z1p[4][4], z1x[2];
  { f32x4 cs[5];
    cs[0] = csa[0]; cs[1] = csa[1]; cs[2] = lds<f32x4>(sm + T_CS1 + (jg2 * 16 + 4 * q) * 4); cs[3] = lds<f32x4>(sm + T_CS1 + (jg2 * 16 + 16 + 4 * q) * 4);
    cs[4] = csa[2] + lds<f32x4>(sm + T_CS1 + (144 - 16 * wt + 4 * q) * 4);
#pragma unroll
    for (int i = 0; i < 4; ++i) { const int ro = (64 * wt + prow(i ^ (2 * wf), c)) * 4;
      const float m = lds<float>(sm + T_M1 + ro), a0 = lds<float>(sm + T_A0 + ro), a1 = lds<float>(sm + T_A1 + ro), ir = lds<float>(sm + T_IR + ro), bias = blo(braw[i]);
#pragma unroll
      for (int j = 0; j < (i < 2 ? 5 : 4); ++j) {
        f32x4& a = j < 4 ? acc[i][j] : accx[i];
        const f32x4 z = gelu4(a - m * cs[j], a0, a1, ir);
        (j < 4 ? z1p[i][j] : z1x[i]) = (u32x2){pk(z[0], z[1]), pk(z[2], z[3])};
        a = (f32x4){bias, bias, bias, bias};
      } }
#pragma unroll
    for (int s = 0; s < 2; ++s) { fx[s] = sm + B2BASE + (s * 9 + 8) * 1024 + lane * 16;
#pragma unroll
      for (int h = 0; h < 2; ++h) { fb[s][h] = sm + B2BASE + (s * 9 + (h ? jg2 : jg0)) * 1024 + lane * 16; ta[s][h] = sm + (64 * wt + 32 * (h ^ wf) + c) * 128 + (((q + 4 * s) ^ ((c >> 1) & 7)) * 16); } }
#pragma unroll
    for (int i = 0; i < 4; ++i) asm volatile("" : "+v"(z1p[i][0]), "+v"(z1p[i][1]), "+v"(z1p[i][2]), "+v"(z1p[i][3]), "+v"(acc[i][0]), "+v"(acc[i][1]), "+v"(acc[i][2]), "+v"(acc[i][3]));
    asm volatile("" : "+v"(z1x[0]), "+v"(z1x[1]), "+v"(accx[0]), "+v"(accx[1]));
  }
  STAMP(6);
  const int jq = id.b & 3;
  LAS char *cs_ = sm + CV + 2 * CV_CH, *cb = sm + CV + wt * CV_CH;
  LAS char *am[4], *um = cb + CV_U + (2032 - 16 * c + 8 * q) * 2 + 256 * wf - 3584;
#pragma unroll
  for (int i = 0; i < 4; ++i) { am[i] = cb + ((c & 1) ? 0 : CV_O) + (4 * q + ((c + 1) >> 1)) * 4 + 256 * wf + 1024 * i; asm volatile("" : "+v"(am[i])); }
  asm volatile("" : "+v"(um));
  LAS char *as[4], *u1, *u0;
  { const int da = (2 - wt) * CV_CH + 128 * wid - 256 * wf;
#pragma unroll
    for (int i = 0; i < 4; ++i) { as[i] = am[i] + da; asm volatile("" : "+v"(as[i])); }
    u1 = um + da + 512 * jq; u0 = um + da + 512 * (7 - jq); asm volatile("" : "+v"(u1), "+v"(u0)); }
  bf16x8 ca[2][8], cu[2][8];
  auto afrag = [&](LAS char* p) { const unsigned d0 = lds<unsigned>(p), d1 = lds<unsigned>(p + 4), d2 = lds<unsigned>(p + 8), d3 = lds<unsigned>(p + 12);
    return __builtin_bit_cast(bf16x8, (u32x4){d0, d1, d2, d3}); };
  auto lds_mn = [&](int b, int kk) {
#pragma unroll
    for (int e = 0; e < 8; ++e) { cu[b][e] = lds<bf16x8>(um + kk * 64 + 512 * (7 - e)); if (e == 0) ca[b][0] = afrag(am[0] + kk * 64); }
#pragma unroll
    for (int e = 1; e < 8; ++e) ca[b][e] = afrag(am[e >> 1] + kk * 64 + (e & 1) * 512); };
  first(M2, 2, 0);
  STAMP(7);
#pragma unroll 1
  for (int it = 0; it < 10; ++it) { stage(M2, 1, 1, false); stage(M2, 0, 2, false); stage(M2, 2, 0, false); }
  stage(M2, 1, 1, false);
#pragma unroll
  for (int i = 0; i < 4; ++i) asm volatile("" : "+v"(z1p[i][0]), "+v"(z1p[i][1]), "+v"(z1p[i][2]), "+v"(z1p[i][3]));
  asm volatile("" : "+v"(z1x[0]), "+v"(z1x[1]));
  { auto gate = [&](u32x2 z, f32x4 a) { const f32x4 h = (f32x4){blo(z.x), bhi(z.x), blo(z.y), bhi(z.y)} * a; return (u32x2){pk(h[0], h[1]), pk(h[2], h[3])}; };
    auto emit = [&](int i) { const unsigned so = W_HF + (id.tp * 8 + 4 * wt + (i ^ (2 * wf))) * 72 * 1024;
#pragma unroll
      for (int pp = 0; pp < 2; ++pp) { const u32x2 lo = gate(z1p[i][2 * pp], acc[i][2 * pp]), hi = gate(z1p[i][2 * pp + 1], acc[i][2 * pp + 1]);
        st16<SC1>((u32x4){lo.x, lo.y, hi.x, hi.y}, rws, lane * 16, so + (4 * id.ft + 2 * wf + (pp ^ wt)) * 1024); }
      if (i < 2) st8<SC1>(gate(z1x[i], accx[i]), rws, lane * 16 + (id.ft & 1) * 8, so + (64 + (id.ft >> 1)) * 1024);
      SB(); };
    mm(M2, 1, 0, 4); mm(M2, 1, 16, 17); mm(M2, 1, 4, 6); SB();
    STAMP(8);
    mm(M2, 1, 6, 8); mm(M2, 1, 17, 18); mm(M2, 1, 8, 16); emit(0);
    lds_mn(0, 0); emit(1); emit(2); emit(3); }
  const int g = id.ft & 7, gp = g & 1, half = id.ft >> 3, rot = krot(id);
  const unsigned wso = W_WOF + ((((EXP & 4) ? 0 : g >> 1) * 72 + 18 * wid) * 9) * 1024;
  u32x4 wr[WD][5];
  auto ldw = [&](int slot, int i) {
#pragma unroll
    for (int t = 0; t < 5; ++t) wr[slot][t] = ld16<(EXP & 8) ? 0 : SC1>(rws, lane * 16, wso + kwrap(i, rot) * 9216 + (t == 4 ? 4 : t + gp * 5) * 1024); };
  SB();
  SB(); BAR(); SB();
  STAMP(9);
  u32x4 hr[3][4];
  const unsigned hso = W_HF + (((EXP & 2) ? (id.b & 7) * 4 : id.tp * 8 + 4 * half) * 72 + 18 * wid) * 1024;
  auto ldh = [&](int slot, int i) {
#pragma unroll
    for (int t = 0; t < 4; ++t) hr[slot][t] = ld16<(EXP & 8) ? 0 : SC1>(rws, lane * 16, hso + (((unsigned)t ^ (wf ^ (2 * gp))) * 72 + kwrap(i, rot)) * 1024); };
  { const float dvm = wt ? bhi(dm) : blo(dm), dvs = (id.b & 4) ? bhi(dsh) : blo(dsh);
    f32x4 cm[8], s0 = {0.f, 0.f, 0.f, 0.f}, s1 = s0;
#pragma unroll
    for (int j = 0; j < 8; ++j) cm[j] = s0;
    bf16x8 sa[4], su[8];
    auto lds_sh = [&](int p) { const int kk = p >> 1, e0 = 4 * (p & 1);
#pragma unroll
      for (int e = 0; e < 4; ++e) { sa[e] = afrag(as[(e0 + e) >> 1] + kk * 64 + (e & 1) * 512); su[e] = lds<bf16x8>(u1 + kk * 64 + 512 * (e0 + e));
        if (e0 == 0) su[4 + e] = lds<bf16x8>(u0 + kk * 64 + 512 * e); } };
    auto mm_sh = [&](int p) {
#pragma unroll
      for (int e = 0; e < 4; ++e) { s1 = MFMA(sa[e], su[e], s1); if (!(p & 1)) s0 = MFMA(sa[e], su[4 + e], s0); } };
    auto mm_mn = [&](int b) {
#pragma unroll
      for (int e = 0; e < 8; ++e)
#pragma unroll
        for (int j = e; j < 8; ++j) cm[j] = MFMA(ca[b][e], cu[b][j - e], cm[j]); };
#define il(nr) do { for (int i_ = 0; i_ < nr; ++i_) { SG(0x8, 1); SG(0x100, 1); } SG(0x8, 44); SB(); } while (0)
    lds_sh(0); lds_mn(1, 1); mm_mn(0); mm_sh(0); il(40); STAMP(15);
    lds_sh(1); lds_mn(0, 2); mm_mn(1); mm_sh(1); il(36); STAMP(17);
    WAITV(0); BAR(); SB();
    STAMP(10);
    lds_sh(2); lds_mn(1, 3); mm_mn(0); mm_sh(2); il(40); STAMP(16);
    lds_sh(3); ldw(0, 5); ldw(1, 6); if (WD > 2) ldw(2, 7); mm_mn(1); mm_sh(3);
    for (int i_ = 0; i_ < 12; ++i_) { SG(0x8, 1); SG(0x100, 1); } for (int i_ = 0; i_ < 12; ++i_) { SG(0x8, 2); SG(0x20, 1); } SG(0x20, 3); SG(0x8, 8); SB();
    if (wid != 0) *(LAS f32x4*)(sm + CV_PS + (wid - 1) * 1024 + lane * 16) = s0;
    if (wid != 1) *(LAS f32x4*)(sm + CV_PS + (3 + (wid ? wid - 1 : 0)) * 1024 + lane * 16) = s1;
    LAS char* pm = cb;
    *(volatile LAS unsigned*)(sm + T_CVF + wid * 4) = 1u;
    { const u64 t0 = __builtin_amdgcn_s_memrealtime(); while (*(volatile LAS unsigned*)(sm + T_CVF + (wid ^ 1) * 4) == 0u && !expired(t0)) {} }
    FENCE();
#pragma unroll
    for (int j = 0; j < 4; ++j) { f32x4 lo = cm[j], hi = cm[4 + j]; asm volatile("" : "+v"(lo), "+v"(hi));
      *(LAS f32x4*)(pm + wf * 4096 + j * 1024 + lane * 16) = wf ? lo : hi; cm[j] = wf ? hi : lo; }
    SB(); WAITL(0); BAR(); SB();
    STAMP(11);
    ldh(0, 0); ldh(1, 1); ldh(2, 2);
    auto yout = [&](f32x4 a, int chn, LAS char* ub, int j, float dv) {
      const u32x2 u = lds<u32x2>(ub + CV_U + (2044 - 256 * j - 16 * c - 4 * q) * 2);
      const f32x4 y = a + (f32x4){bhi(u.y), blo(u.y), bhi(u.x), blo(u.x)} * dv;
      st8<SC1>((u32x2){pk(y[0], y[1]), pk(y[2], y[3])}, rws, (16 * c + 4 * q) * 2, W_YT + chn * (S * 2) + j * 512); };
#pragma unroll
    for (int j = 0; j < 4; ++j) yout(cm[j] + lds<f32x4>(pm + (wf ^ 1) * 4096 + j * 1024 + lane * 16), 2 * id.b + wt, cb, 4 * wf + j, dvm);
    if (wid < 2) { f32x4 ys = wf ? s1 : s0;
#pragma unroll
      for (int w = 0; w < 3; ++w) ys += lds<f32x4>(sm + CV_PS + (3 * wid + w) * 1024 + lane * 16);
      yout(ys, 512 + (id.b >> 2), cs_, wid ? 7 - jq : jq, dvs); }
    SB();
  }
  STAMP(19);
  f32x4 o[4][4], ox[2];
#pragma unroll
  for (int i = 0; i < 4; ++i)
#pragma unroll
    for (int t = 0; t < 4; ++t) o[i][t] = (f32x4){0.f, 0.f, 0.f, 0.f};
  ox[0] = ox[1] = (f32x4){0.f, 0.f, 0.f, 0.f};
  const int dp = 144 * (g >> 1) + 80 * gp, ds = 144 * (g >> 1) + 64, trow = 64 * half + prow(wid, c);
  const unsigned xo = (id.tp * 128 + trow) * (D * 2);
  f32x4 rs[5];
  { LAS char* wl = sm + wid * G3W + lane * 16;
    bf16x8 wq[2][5];
#pragma unroll
    for (int t = 0; t < 5; ++t) wq[0][t] = lds<bf16x8>(wl + t * 1024);
#pragma unroll
    for (int i = 0; i < 18; ++i) {
      if (i < 4) {
#pragma unroll
        for (int t = 0; t < 5; ++t) wq[(i + 1) & 1][t] = lds<bf16x8>(wl + ((i + 1) * 5 + t) * 1024); }
#pragma unroll
      for (int r = 0; r < 4; ++r) { const bf16x8 hf = __builtin_bit_cast(bf16x8, hr[i % 3][r]);
#pragma unroll
        for (int t = 0; t < 5; ++t) if (t < 4 || r < 2) { const int tt = (r & 1) && t < 4 ? 3 - t : t;
          const bf16x8 wf_ = i < 5 ? wq[i & 1][tt] : __builtin_bit_cast(bf16x8, wr[(i - 5) % WD][tt]);
          if (tt < 4) o[r][tt] = MFMA(wf_, hf, o[r][tt]); else ox[r] = MFMA(wf_, hf, ox[r]); } }
      SB();
      if (i == 0 || i == 2 || i == 4 || i == 7 || i == 10 || i == 13 || i == 15) STAMP(20 + i);
      if (i + 3 < 18) ldh(i % 3, i + 3);
      if (i >= 5 && i + WD < 18) ldw((i - 5) % WD, i + WD);
      if (i == 2) { WAITV(12); if (lane == 0) __hip_atomic_fetch_add((LAS unsigned*)(sm + T_CNT), 1u, __ATOMIC_RELAXED, __HIP_MEMORY_SCOPE_WORKGROUP); }
      if (i == 16) {
        const u64 t0 = __builtin_amdgcn_s_memrealtime();
        while (*(volatile LAS unsigned*)(sm + T_YOK) == 0u && !expired(t0)) __builtin_amdgcn_s_sleep(1);
        FENCE();
        const float m = lds<float>(sm + T_M1 + trow * 4), r = lds<float>(sm + T_R1 + trow * 4);
        LAS char* yb = sm + YTILE + (c >> 2) * 128 + (32 * wt + 4 * wf + 8 * (c & 3)) * 2;
        auto res = [&](u32x2 xv, bf16x4 yv) { const u32x2 yu = __builtin_bit_cast(u32x2, yv);
          const f32x4 xf = {blo(xv.x), bhi(xv.x), blo(xv.y), bhi(xv.y)}, yf = {blo(yu.x), bhi(yu.x), blo(yu.y), bhi(yu.y)};
          return xf + (xf - m) * r * yf; };
        const int yp = 16 * gp, ysh = 64 * (gp ^ 1);
        LAS char* xb = sm + XTILE + prow(wid, c) * 160;
        const u32x4 x0 = lds<u32x4>(xb + yp * 2 + q * 16), x1 = lds<u32x4>(xb + yp * 2 + 64 + q * 16); const u32x2 x2 = lds<u32x2>(xb + ysh * 2 + q * 8);
        rs[0] = res(x0.xy, ldtr(yb + (yp + 8 * q) * 128)); rs[1] = res(x0.zw, ldtr(yb + (yp + 8 * q + 4) * 128));
        rs[2] = res(x1.xy, ldtr(yb + (yp + 32 + 8 * q) * 128)); rs[3] = res(x1.zw, ldtr(yb + (yp + 36 + 8 * q) * 128));
        rs[4] = res(x2, ldtr(yb + (ysh + 4 * q) * 128));
        asm volatile("" : "+v"(rs[0]), "+v"(rs[1]), "+v"(rs[2]), "+v"(rs[3]), "+v"(rs[4])); }
      SB();
    }
  }
  STAMP(12);
  const bool own0 = wt == gp;
  { LAS char* pw = sm + wid * G3W + lane * 16;
    auto put = [&](int r) { LAS char* p = pw + ((unsigned)r ^ (wf ^ (2 * gp))) * 5120;
#pragma unroll
      for (int t = 0; t < 4; ++t) *(LAS f32x4*)(p + t * 1024) = o[r][t];
      if (r < 2) *(LAS f32x4*)(p + 4096) = ox[r]; };
    put(1); put(3);
    if (own0) { put(2);
#pragma unroll
      for (int t = 0; t < 4; ++t) rs[t] += o[0][t];
      rs[4] += ox[0]; }
    else { put(0);
#pragma unroll
      for (int t = 0; t < 4; ++t) rs[t] += o[2][t]; }
  }
  SB(); WAITL(0); BAR(); SB();
  STAMP(13);
  { f32x4 pt[3][5];
#pragma unroll
    for (int t = 0; t < 5; ++t)
#pragma unroll
      for (int w = 0; w < 3; ++w) pt[w][t] = lds<f32x4>(sm + (wid ^ (w + 1)) * G3W + wid * 5120 + lane * 16 + t * 1024);
    const rsrc_t ro = mkrs(P.out, S * D * 2);
    auto fin = [&](int t) { const f32x4 v = (rs[t] + pt[0][t]) + (pt[1][t] + pt[2][t]); return (u32x2){pk(v[0], v[1]), pk(v[2], v[3])}; };
#pragma unroll
    for (int pp = 0; pp < 2; ++pp) { const u32x2 lo = fin(2 * pp), hi = fin(2 * pp + 1); st16<0>((u32x4){lo.x, lo.y, hi.x, hi.y}, ro, xo + q * 16, dp * 2 + 64 * pp); }
    const u32x2 ls = fin(4);
    if (own0) st8<0>(ls, ro, xo + q * 8, ds * 2);
  }
  STAMP(14);
}
}

extern "C" __global__ __launch_bounds__(512, 2) void h3_gmlp_fused(const Params P) {
  extern __shared__ __attribute__((aligned(16))) char smem_[];
  LAS char* sm = (LAS char*)smem_;
  const int wid = __builtin_amdgcn_readfirstlane(threadIdx.x >> 6);
  Ids id; id.b = blockIdx.x; id.lane = threadIdx.x & 63;
  const int xcd = id.b & 7, loc = id.b >> 3;
  id.tp = (xcd >> 1) * 4 + (loc >> 3); id.ft = (xcd & 1) * 8 + (loc & 7);
  if (wid < 4) { unsigned sink = 0;
    if (wid == 0) code_touch(id, sink);
    else if (OPT_TWIN) { const rsrc_t r = mkrs(P.win, D * F * 2); const unsigned n = (wid - 1) * 64 + id.lane;
      if (OPT_TWIN > 1) __builtin_amdgcn_s_sleep(OPT_TWIN);
      unsigned o;
      if (TWZ == 0) o = n < 162 ? id.b * 20736 + n * 128 : 0xF0000000u;
      else if (TWZ == 3) { const unsigned m = id.b * 63 + n, row = m / 36; o = n < 63 ? (row + 128) * (F * 2) + FH * 2 + (m - row * 36) * 128 : 0xF0000000u; }
      else { const unsigned m = id.b * 81 + n, row = m / 36; o = n < 81 ? row * (F * 2) + FH * 2 + (m - row * 36) * 128 : 0xF0000000u; }
      asm volatile("buffer_load_dword %0, %1, %2, 0 offen" : "+v"(sink) : "v"(o), "s"(r) : "memory"); }
    asm volatile("s_waitcnt vmcnt(0)" : "+v"(sink) :: "memory"); }
  if (__builtin_expect(wid == 4 || wid == 5, 1)) role_L(P, sm, id, wid - 4 STAMP_PASS);
  else if (wid >= 6) role_X(P, sm, id, wid - 6 STAMP_PASS);
  else role_C(P, sm, id, wid STAMP_PASS);
}
extern "C" __global__ void h3_gmlp_end() {}

extern "C" void kernel_launch(void* const* d_in, const int* in_sizes, int n_in, void* d_out, int out_size, void* d_ws, size_t ws_size, hipStream_t stream) {
  Params p;
  p.x = (const u16*)d_in[0]; p.k = (const u16*)d_in[1]; p.v = (const u16*)d_in[2]; p.shk = (const u16*)d_in[3]; p.ssmk = (const u16*)d_in[4]; p.ssmd = (const u16*)d_in[5];
  p.win = (const u16*)d_in[7]; p.wsp = (const u16*)d_in[8]; p.bsp = (const u16*)d_in[9]; p.wout = (const u16*)d_in[10];
  p.out = (u16*)d_out; p.ws = (char*)d_ws;
  (void)hipFuncSetAttribute((const void*)h3_gmlp_fused, hipFuncAttributeMaxDynamicSharedMemorySize, LDS_BYTES);
  h3_gmlp_fused<<<256, 512, LDS_BYTES, stream>>>(p);
}
```

```cpp
#define PT 2
#include <hip/hip_runtime.h>
#include <hip/hip_bf16.h>
#include <type_traits>

namespace {
typedef unsigned short u16;
typedef unsigned long long u64;
typedef short bf16x8 __attribute__((ext_vector_type(8)));
typedef short bf16x4 __attribute__((ext_vector_type(4)));
typedef __bf16 bf2 __attribute__((ext_vector_type(2)));
typedef float f32x2 __attribute__((ext_vector_type(2)));
typedef float f32x4 __attribute__((ext_vector_type(4)));
typedef unsigned u32x2 __attribute__((ext_vector_type(2)));
typedef unsigned u32x4 __attribute__((ext_vector_type(4)));
typedef __amdgpu_buffer_rsrc_t rsrc_t;
#define LAS __attribute__((address_space(3)))
#define CAS __attribute__((address_space(4)))
#define DEV __device__ __forceinline__

constexpr int S = 2048, D = 576, F = 4608, FH = 2304;
constexpr int ASLOT = 16384, BSLOT = 18432, BBASE = 4 * ASLOT;
constexpr int STG = 139264, STG_W = 9216;
constexpr int T_M1 = 157952, T_R1 = T_M1 + 512, T_A0 = T_R1 + 512, T_A1 = T_A0 + 512, T_IR = T_A1 + 512;
constexpr int T_CS2 = T_IR + 512, T_CS1 = T_CS2;
constexpr int T_PART = T_CS1 + 640, T_N2 = T_PART, T_R2 = T_N2 + 512;
constexpr int T_TAG = T_PART + 2048, T_CNT = T_TAG + 16, T_GO = T_CNT + 4, T_YOK = T_CNT + 8, T_CVF = T_CNT + 16;
constexpr int LDS_BYTES = 163840;
static_assert(T_CNT + 32 <= LDS_BYTES, "LDS map");
constexpr int G3W = 25600;
constexpr int B2BASE = 3 * ASLOT;
constexpr int CV = B2BASE + 3 * BSLOT, CV_CH = 13056, CV_O = 4160, CV_U = 8320;
constexpr int CV_PS = CV + 2 * CV_CH + 15104;
constexpr int YTILE = 122880, XTILE = 133120;
constexpr int W_EPOCH = 0, W_F2 = 4096, W_F3 = 8192, W_F4 = 12288, W_F5 = 16384;
constexpr int W_G0X = 0x8000, W_G1F = 0x10000, W_G1S = 0x60000;
constexpr int W_KVT = 1 << 20, W_YT = 4 << 20, W_WOF = 7 << 20, W_ZF = 10 << 20, W_HF = 20 << 20, W_END = 30 << 20;
constexpr int SC1 = 16;
#define OPT_TWIN 12
#define OPT_TWSP 1
#define OPT_TG3 0
#define EXP 0
#define RH 0
#define RW 0
#define KB 1
#define PR 0
#define XM 0
#define RT 0
#define TWZ 0
#define WD 3

#define BAR0() do { asm volatile("" ::: "memory"); __builtin_amdgcn_s_barrier(); asm volatile("" ::: "memory"); } while (0)
#define BAR() BAR0()
#define SB() __builtin_amdgcn_sched_barrier(0)
#define SG(m, n) __builtin_amdgcn_sched_group_barrier(m, n, 0)
#define WAITV(n) asm volatile("s_waitcnt vmcnt(%0)" ::"i"(n) : "memory")
#define WAITL(n) asm volatile("s_waitcnt lgkmcnt(%0)" ::"i"(n) : "memory")
#define FENCE() asm volatile("" ::: "memory")
#define MFMA(a, b, c) __builtin_amdgcn_mfma_f32_16x16x32_bf16(a, b, c, 0, 0, 0)
#define STAMP(i) do {} while (0)
#define LBAR() BAR()
#define STAMP_ARG
#define STAMP_PASS

struct Params {
  const u16 *x, *k, *v, *shk, *ssmk, *ssmd, *win, *wsp, *bsp, *wout;
  u16* out; char* ws;
};

DEV rsrc_t mkrs(const void* p, unsigned bytes) { return __builtin_amdgcn_make_buffer_rsrc((void*)p, 0, bytes, 0x00020000); }
template <int AUX> DEV void dma(rsrc_t rs, LAS char* l, unsigned voff, unsigned soff) { __builtin_amdgcn_raw_ptr_buffer_load_lds(rs, (LAS void*)l, 16, voff, soff, 0, AUX); }
DEV void dma_hidden(rsrc_t rs, LAS char* l, unsigned voff) { unsigned keep;
  asm volatile("s_mov_b32 %0, m0\n\ts_mov_b32 m0, %3\n\ts_nop 0\n\tbuffer_load_dwordx4 %1, %2, 0 offen lds\n\ts_mov_b32 m0, %0" : "=&s"(keep) : "v"(voff), "s"(rs), "s"(__builtin_amdgcn_readfirstlane((unsigned)(size_t)l)) : "memory"); }
template <int AUX> DEV u32x4 ld16(rsrc_t rs, unsigned voff, unsigned soff) { return __builtin_amdgcn_raw_buffer_load_b128(rs, voff, soff, AUX); }
template <int AUX> DEV void st16(u32x4 v, rsrc_t rs, unsigned voff, unsigned soff) { __builtin_amdgcn_raw_buffer_store_b128(v, rs, voff, soff, AUX); }
template <int AUX> DEV void st8(u32x2 v, rsrc_t rs, unsigned voff, unsigned soff) { __builtin_amdgcn_raw_buffer_store_b64(v, rs, voff, soff, AUX); }
template <class T> DEV T lds(LAS char* p) { return *(LAS T*)p; }
DEV bf16x4 ldtr(LAS char* p) { return __builtin_amdgcn_ds_read_tr16_b64_v4i16((LAS bf16x4*)p); }
DEV bf16x8 cat(bf16x4 a, bf16x4 b) { return __builtin_shufflevector(a, b, 0, 1, 2, 3, 4, 5, 6, 7); }
DEV float blo(unsigned u) { return __uint_as_float(u << 16); }
DEV float bhi(unsigned u) { return __uint_as_float(u & 0xffff0000u); }
DEV unsigned pk(float a, float b) { return __builtin_bit_cast(unsigned, __builtin_convertvector((f32x2){a, b}, bf2)); }
DEV float dot2(unsigned a, unsigned b, float c) { return __builtin_amdgcn_fdot2_f32_bf16(__builtin_bit_cast(bf2, a), __builtin_bit_cast(bf2, b), c, false); }
template <int CTRL> DEV float dpp(float x) { return __builtin_bit_cast(float, __builtin_amdgcn_mov_dpp(__builtin_bit_cast(int, x), CTRL, 0xf, 0xf, true)); }
constexpr int XOR1 = 0xB1, XOR2 = 0x4E, XOR7 = 0x141, XOR8 = 0x128;
constexpr float GC0 = -2.30220819f, GC1 = -0.10294324f;
template <class T> DEV f32x2 gelu2(f32x2 a, T a0, T a1, T ir) {
  const f32x2 g = a * (a * a * a1 + a0);
  f32x2 e; e.x = __builtin_amdgcn_exp2f(g.x); e.y = __builtin_amdgcn_exp2f(g.y);
  e = e * ir + ir;
  f32x2 r; r.x = __builtin_amdgcn_rcpf(e.x); r.y = __builtin_amdgcn_rcpf(e.y);
  return a * r;
}
DEV f32x4 gelu4(f32x4 a, f32x4 a0, f32x4 a1, f32x4 ir) { const f32x2 p = gelu2(a.xy, a0.xy, a1.xy, ir.xy), q = gelu2(a.zw, a0.zw, a1.zw, ir.zw); return (f32x4){p.x, p.y, q.x, q.y}; }
DEV f32x4 gelu4(f32x4 a, float a0, float a1, float ir) { const f32x2 p = gelu2(a.xy, a0, a1, ir), q = gelu2(a.zw, a0, a1, ir); return (f32x4){p.x, p.y, q.x, q.y}; }
DEV u32x4 seal(float a, float b, u32x2 tag) { const unsigned ua = __float_as_uint(a), ub = __float_as_uint(b); return (u32x4){ua, ub, ua ^ tag.x, ub ^ tag.y}; }
DEV bool sealed(u32x4 g, u32x2 tag) { return (g.x ^ g.z) == tag.x && (g.y ^ g.w) == tag.y; }
DEV bool expired(u64 t0) { return __builtin_amdgcn_s_memrealtime() - t0 > 2000000ull; }
template <int NL, int NAP> DEV void wait_flags(rsrc_t ws, unsigned voff, u32x2 tag) {
  const u64 t0 = __builtin_amdgcn_s_memrealtime();
  for (;;) {
    bool ok = true;
#pragma unroll
    for (int i = 0; i < NL; ++i) { const u32x4 f = ld16<SC1>(ws, voff, i * 1024); ok &= f.x == tag.x && f.y == tag.y && f.z == tag.x && f.w == tag.y; }
    if (__all(ok) || expired(t0)) break;
    __builtin_amdgcn_s_sleep(NAP); FENCE();
  }
  FENCE();
}
DEV bool flags_ok(u32x4 f, u32x2 tag) { return f.x == tag.x && f.y == tag.y && f.z == tag.x && f.w == tag.y; }
DEV int prow(int T, int c) { return 32 * (T >> 1) + 8 * (c >> 2) + 4 * (T & 1) + (c & 3); }
DEV void set_flag(rsrc_t ws, unsigned off, u32x2 tag, int lane) { if (lane == 0) st8<SC1>(tag, ws, off, 0); }

struct Ids { int b, tp, ft, lane; };
DEV void code_touch(const Ids& id, unsigned& sink) {
  unsigned lo, hi, bytes;
  asm volatile("s_getpc_b64 vcc\n1:\n\ts_sub_u32 %0, vcc_lo, 1b-h3_gmlp_fused\n\ts_subb_u32 %1, vcc_hi, 0\n\ts_mov_b32 %2, h3_gmlp_end-h3_gmlp_fused" : "=s"(lo), "=s"(hi), "=s"(bytes) :: "vcc", "scc");
  const rsrc_t rc = mkrs((const void*)(((u64)hi << 32) | lo), bytes);
  asm volatile("buffer_load_dword %0, %1, %2, 0 offen" : "+v"(sink) : "v"((id.lane < 20 ? id.lane * 32 + (id.b >> 3) : 0x100000) * 128), "s"(rc) : "memory");
}
DEV int krot(const Ids& id) { return (RH * (id.ft & 7) + ((RW * (id.tp & 3)) >> 1)) % 18; }
DEV int kwrap(int i, int r) { const int k = i + r; return k >= 18 ? k - 18 : k; }

DEV void role_L(const Params& P, LAS char* sm, const Ids id, const int lw STAMP_ARG) {
  int bcount = 0; (void)bcount;
  const int lane = id.lane, rr = lane >> 3, sl = lane & 7;
  const rsrc_t rx = mkrs(P.x, S * D * 2), rwin = mkrs(P.win, D * F * 2);
  const unsigned swz = (sl ^ (4 * lw + (rr >> 1))) * 16, prr = 16 * lw + 8 * (rr >> 2) + (rr & 3);
  const unsigned vx = prr * (D * 2) + swz;
  unsigned vb[9];
  auto issue1a = [&](int slot, int i, int ks) { dma<0>(rx, sm + slot * ASLOT + lw * 1024 + i * 2048, vx, (id.tp * 128 + 32 * (i >> 1) + 4 * (i & 1)) * (D * 2) + ks * 128); };
  auto issue1b = [&](int slot, int i, int ks, int col) { dma<0>(rwin, sm + BBASE + slot * BSLOT + lw * 1024 + i * 2048, vb[i], ks * 64 * (F * 2) + (col + id.ft * 144) * 2); };
  STAMP(56);
#pragma unroll
  for (int i = 0; i < 9; ++i) {
    if (i < 8) issue1a(0, i, 0);
    if (i == 0) STAMP(57);
    SB();
    const unsigned ci = 64 * (2 * i + lw) + lane, rho = __umul24(ci, 3641) >> 16;
    vb[i] = __umul24(rho, F * 2 - 288) + 16 * ci + __mul24((int)(rho & 4) - (int)((rho >> 1) & 4), F * 2);
    issue1b(0, i, 0, FH);
    SB();
  }
  auto xA = [&](int n) {
#pragma unroll
    for (int i = 0; i < 8; ++i) issue1a(n < 9 ? n & 3 : (9 - n) & 3, i, n < 9 ? n : 17 - n); };
  auto wB = [&](int n) {
#pragma unroll
    for (int i = 0; i < 9; ++i) issue1b(n & 3, i, n < 9 ? n : 17 - n, n < 9 ? FH : 0); };
  STAMP(58);
  xA(1); wB(1); STAMP(59); xA(2);
  STAMP(60);
  const rsrc_t rwsp = mkrs(P.wsp, S * S * 2), rws = mkrs(P.ws, W_END);
  const unsigned vw = prr * (S * 2) + swz, vl = lane * 16 + lw * 1024;
  auto issue2a = [&](int k, int slot) { LAS char* a = sm + slot * ASLOT + lw * 1024; const unsigned sa = id.tp * 128 * (S * 2) + k * 128;
#pragma unroll
    for (int i = 0; i < 8; ++i) dma<0>(rwsp, a + i * 2048, vw, sa + (32 * (i >> 1) + 4 * (i & 1)) * (S * 2)); };
  auto issue2b = [&](int k, int slot) { LAS char* bb = sm + B2BASE + slot * BSLOT + lw * 1024; const unsigned sb = W_ZF + (id.ft * 576 + k * 18) * 1024;
#pragma unroll
    for (int i = 0; i < 9; ++i) dma<SC1>(rws, bb + i * 2048, vl, sb + i * 2048); };
#pragma unroll 1
  for (int n = 0; n < 6; ++n) { WAITV(25); LBAR(); wB(n + 2); xA(n + 3); }
  WAITV(25); LBAR(); wB(8);
  WAITV(17); LBAR(); wB(9);
  WAITV(9); LBAR(); wB(10);
  BAR(); wB(11); wB(12);
  WAITV(27); LBAR();
  WAITV(18); LBAR(); xA(13); wB(13);
  WAITV(26); LBAR(); xA(14); wB(14);
#pragma unroll 1
  for (int n = 12; n < 15; ++n) { WAITV(34); LBAR(); xA(n + 3); wB(n + 3); }
  WAITV(34); LBAR();
  WAITV(17); LBAR(); issue2a(0, 2);
  WAITV(8); LBAR(); issue2a(1, 1);
  BAR();
  { const u32x2 tag = lds<u32x2>(sm + T_TAG); wait_flags<1, 2>(rws, W_F2 + id.ft * 128 + (lane & 7) * 16, tag); }
  STAMP(61);
  issue2b(0, 0); issue2b(1, 1);
  asm volatile("ds_write_b32 %0, %1" :: "v"((unsigned)(size_t)(sm + T_GO)), "v"(1u) : "memory");
  WAITV(9); LBAR(); issue2a(2, 0); issue2b(2, 2);
  { int sb = 0;
#pragma unroll 1
    for (int k = 3; k < 32; ++k) { WAITV(17); LBAR(); issue2a(k, 2 - sb); issue2b(k, sb); sb = sb == 2 ? 0 : sb + 1; } }
  const int g = id.ft & 7;
  const unsigned wb0 = W_WOF + (g >> 1) * 72 * 9 * 1024, wb1 = wb0 + (g & 1) * 5 * 1024, vq = lane * 16;
  const int rot = krot(id);
  auto issue3 = [&](auto lo_, auto hi_) { constexpr int lo = decltype(lo_)::value, hi = decltype(hi_)::value;
#pragma unroll
    for (int p0 = lo; p0 < hi; p0 += 2) {
      auto off = [&](int pc) { return (pc % 5 == 4 ? wb0 : wb1) + (18 * (pc / 25) * 9 + pc % 5) * 1024 + kwrap((pc % 25) / 5, rot) * 9216; };
      dma<SC1>(rws, sm + p0 * 1024 + lw * 1024, vq, lw ? off(p0 + 1) : off(p0)); } };
#define IC(n) std::integral_constant<int, n>{}
  WAITV(17); LBAR(); issue3(IC(0), IC(16)); issue3(IC(84), IC(100));
  WAITV(16); LBAR(); issue3(IC(32), IC(66));
  LBAR(); issue3(IC(16), IC(32)); issue3(IC(66), IC(84));
  unsigned sink = 0;
  auto wtouch = [&]() {
    constexpr int K0 = 5 + WD, NK = 18 - K0;
#pragma unroll
    for (int i = 0; i < (NK * 40 + 127) / 128; ++i) { const unsigned n = (i * 2 + lw) * 64 + lane, blk = n >> 1, kw = __umul24(blk, 13108) >> 16, t = blk - kw * 5;
      const unsigned o = n < NK * 40 ? (t == 4 ? wb0 : wb1) + ((18 * (kw & 3) + K0 + (kw >> 2)) * 9 + t) * 1024 + ((id.tp & 3) * 2 + (n & 1)) * 128 : 0xF0000000u;
      asm volatile("buffer_load_dword %0, %1, %2, 0 offen sc1" : "+v"(sink) : "v"(o), "s"(rws) : "memory"); } };
  if ((OPT_TG3 & 3) == 1) wtouch();
  LBAR();
  if ((OPT_TG3 & 3) == 2) { __builtin_amdgcn_s_sleep(2); wtouch(); }
  WAITV((OPT_TG3 & 3) == 2 ? (( 18 - 5 - WD) * 40 + 127) / 128 : 0); LBAR();
  asm volatile("s_waitcnt vmcnt(0)" : "+v"(sink) :: "memory"); LBAR();
}

DEV void role_X(const Params& P, LAS char* sm, const Ids id, const int xw STAMP_ARG) {
  int bcount = 0; (void)bcount;
  const int lane = id.lane;
  const rsrc_t rws = mkrs(P.ws, W_END);
  unsigned sink = 0;
  const u32x2 ev = __builtin_amdgcn_raw_buffer_load_b64(rws, W_EPOCH, 0, SC1);
  const int srow = id.tp * 128 + id.ft * 8 + 4 * xw + (lane >> 4);
  u32x4 xv[5];
  { const rsrc_t rx = mkrs(P.x, S * D * 2);
#pragma unroll
    for (int i = 0; i < 5; ++i) xv[i] = ld16<0>(rx, i < 4 || (lane & 8) == 0 ? srow * (D * 2) + (lane & 15) * 16 : 0xF0000000u, i * 256); }
  const u64 epoch = ((u64)__builtin_amdgcn_readfirstlane(ev.y) << 32) | __builtin_amdgcn_readfirstlane(ev.x);
  u32x2 tag; { const u64 t = ((epoch + 1) * 0x9E3779B97F4A7C15ull) | 1ull; tag.x = (unsigned)t; tag.y = (unsigned)(t >> 32); }
  { float s = 0.f, q = 0.f; unsigned ones = 0x3f803f80u; asm volatile("" : "+v"(ones));
#pragma unroll
    for (int i = 0; i < 5; ++i)
#pragma unroll
      for (int e = 0; e < 4; ++e) { s = dot2(xv[i][e], ones, s); q = dot2(xv[i][e], xv[i][e], q); }
    s += dpp<XOR1>(s); q += dpp<XOR1>(q); s += dpp<XOR2>(s); q += dpp<XOR2>(q); s += dpp<XOR7>(s); q += dpp<XOR7>(q); s += dpp<XOR8>(s); q += dpp<XOR8>(q);
    const float m = s * (1.0f / D), var = fmaxf(q * (1.0f / D) - m * m, 0.f) + 1e-5f;
    if ((lane & 15) == 0) st16<SC1>(seal(m, var, tag), rws, W_G0X + srow * 16, 0);
    if (xw == 0 && lane == 0) { *(LAS u32x2*)(sm + T_TAG) = tag; *(LAS u32x4*)(sm + T_CNT) = (u32x4){0u, 0u, 0u, 0u}; *(LAS u32x4*)(sm + T_CVF) = (u32x4){0u, 0u, 0u, 0u}; }
  }
  STAMP(0);
  BAR(); BAR();
  if (TWZ == 2) { const rsrc_t r = mkrs(P.win, D * F * 2); const unsigned n = xw * 64 + lane, m = id.b * 81 + n, row = m / 36;
    asm volatile("buffer_load_dword %0, %1, %2, 0 offen" : "+v"(sink) : "v"(n < 81 ? row * (F * 2) + (m - row * 36) * 128 : 0xF0000000u), "s"(r) : "memory"); }
  BAR(); BAR();
  LAS char* stg = sm + STG + xw * STG_W;
  const int unit = xw ? 256 + id.b : id.b, wks = unit >> 2, wu = unit & 3; const bool won = !xw || id.b < 32;
  const int tile = xw ? id.b : 256 + id.b - 32; const bool kon = xw || (id.b >= 32 && id.b < 64);
  const int tb = tile / 9, db = tile - tb * 9, cc = lane & 7, rg = lane >> 3;
  u32x4 kk[11], vv[8], sh[4];
  { const int row = 64 * xw + lane; const u64 t0 = __builtin_amdgcn_s_memrealtime();
    u32x4 gx = ld16<SC1>(rws, W_G0X + (id.tp * 128 + row) * 16, 0);
    BAR(); BAR();
    while (!__all(sealed(gx, tag)) && !expired(t0)) { __builtin_amdgcn_s_sleep(4); FENCE(); gx = ld16<SC1>(rws, W_G0X + (id.tp * 128 + row) * 16, 0); }
    const float var = __uint_as_float(gx.y), r = __builtin_amdgcn_rsqf(var);
    *(LAS unsigned*)(sm + T_M1 + row * 4) = gx.x; *(LAS float*)(sm + T_R1 + row * 4) = r;
    *(LAS float*)(sm + T_A0 + row * 4) = GC0 * r; *(LAS float*)(sm + T_A1 + row * 4) = GC1 * r * r * r; *(LAS float*)(sm + T_IR + row * 4) = var * r;
    if ((RT & 1) && xw == 0) code_touch(id, sink);
    WAITL(0); BAR();
  }
  STAMP(1);
  { const rsrc_t rk = mkrs(P.k, S * D * 2), rv = mkrs(P.v, S * D * 2), rs = mkrs(P.shk, D * 4 * 2), rwo = mkrs(P.wout, FH * D * 2);
    const int t0 = 64 * tb + 8 * rg; const unsigned col = (64 * db + 8 * cc) * 2;
    if (kon) {
#pragma unroll
      for (int i = 0; i < 11; ++i) kk[i] = ld16<0>(rk, t0 + i - 3 >= 0 ? (unsigned)(t0 + i - 3) * (D * 2) + col : 0xF0000000u, 0);
    }
    BAR();
    if (kon) {
#pragma unroll
      for (int i = 0; i < 8; ++i) vv[i] = ld16<0>(rv, t0 * (D * 2) + col, i * (D * 2));
#pragma unroll
      for (int i = 0; i < 4; ++i) sh[i] = ld16<0>(rs, col * 4, i * 16);
    }
    BAR();
    if (won) { const int fb0 = wks < 64 ? 144 * (wks >> 2) + 32 * (wks & 3) : 288 * (wks - 64) + 128, fst = wks < 64 ? 0 : 128;
#pragma unroll
      for (int i = 0; i < 9; ++i) { const int ci = 64 * i + lane, rho = __umul24(ci, 3641) >> 16, ch = ci - rho * 18;
        dma_hidden(rwo, stg + i * 1024, (fb0 + rho + (rho >> 4) * fst) * (D * 2) + wu * 288 + ch * 16); } }
    BAR();
    if (kon) {
#pragma unroll
      for (int i = 0; i < 4; ++i) asm volatile("" : "+v"(sh[i]), "+v"(vv[i]), "+v"(vv[4 + i]), "+v"(kk[i]), "+v"(kk[4 + i]), "+v"(kk[i == 3 ? 0 : 8 + i])); }
    if (OPT_TWSP) { const rsrc_t r = mkrs(P.wsp, S * S * 2);
#pragma unroll
      for (int i = 0; i < 2; ++i) asm volatile("buffer_load_dword %0, %1, %2, 0 offen" : "+v"(sink) : "v"(id.b * 32768 + ((2 * i + xw) * 64 + lane) * 128), "s"(r) : "memory"); }
  }
  BAR();
  u32x4 ko[8];
  auto kvstep = [&](int e) {
    if (kon) {
      float c[4], kf[11], y[8];
#pragma unroll
      for (int j = 0; j < 4; ++j) { const unsigned w = sh[e >> 1][(e & 1) * 2 + (j >> 1)]; c[j] = (j & 1) ? bhi(w) : blo(w); }
#pragma unroll
      for (int i = 0; i < 11; ++i) { const unsigned w = kk[i][e >> 1]; kf[i] = (e & 1) ? bhi(w) : blo(w); }
#pragma unroll
      for (int i = 0; i < 8; ++i) { const unsigned w = vv[i][e >> 1];
        float a = c[3] * kf[i]; asm volatile("" : "+v"(a)); a = fmaf(c[2], kf[i + 1], a); asm volatile("" : "+v"(a)); a = fmaf(c[1], kf[i + 2], a); asm volatile("" : "+v"(a)); a = fmaf(c[0], kf[i + 3], a);
        asm volatile("" : "+v"(a)); y[i] = a * ((e & 1) ? bhi(w) : blo(w)); asm volatile("" : "+v"(y[i])); }
      ko[e] = (u32x4){pk(y[7], y[6]), pk(y[5], y[4]), pk(y[3], y[2]), pk(y[1], y[0])};
      asm volatile("" : "+v"(ko[e]));
    } };
  { const int row = 64 * xw + lane;
    const f32x2 a = lds<f32x2>(sm + T_PART + row * 8) + lds<f32x2>(sm + T_PART + 1024 + row * 8);
    st16<SC1>(seal(a.x, a.y, tag), rws, W_G1S + ((id.tp * 16 + id.ft) * 128 + row) * 16, 0);
    BAR(); kvstep(0); BAR();
    u32x4 gs[16];
    auto gather = [&]() {
#pragma unroll
      for (int i = 0; i < 16; ++i) gs[i] = ld16<SC1>(rws, W_G1S + (id.tp * 16 * 128 + row) * 16, i * 2048); };
    unsigned bad = 0; float s = 0.f, q = 0.f;
    auto sum = [&](int lo, int hi) {
#pragma unroll
      for (int i = lo; i < hi; ++i) { bad |= (gs[i].x ^ gs[i].z ^ tag.x) | (gs[i].y ^ gs[i].w ^ tag.y); s += __uint_as_float(gs[i].x); q += __uint_as_float(gs[i].y); }
      asm volatile("" : "+v"(bad), "+v"(s), "+v"(q)); };
    gather(); BAR(); sum(0, 10); BAR();
    sum(10, 16);
    if (__builtin_expect(!__all(bad == 0), 0)) { const u64 t0 = __builtin_amdgcn_s_memrealtime();
      do { FENCE(); gather(); bad = 0; s = 0.f; q = 0.f; sum(0, 16); } while (!__all(bad == 0) && !expired(t0)); }
    const float m = s * (1.0f / FH), var = fmaxf(q * (1.0f / FH) - m * m, 0.f), r = __builtin_amdgcn_rsqf(var + 1e-5f);
    *(LAS float*)(sm + T_R2 + row * 4) = r; *(LAS float*)(sm + T_N2 + row * 4) = -m * r;
    asm volatile("s_waitcnt vmcnt(0) lgkmcnt(0)" : "+v"(sink) :: "memory"); BAR();
  }
  STAMP(2);
  kvstep(1); kvstep(2); BAR(); kvstep(3); BAR(); kvstep(4); BAR();
  if (xw == 0) set_flag(rws, W_F2 + (id.ft * 16 + id.tp) * 8, tag, lane);
  kvstep(5); kvstep(6); BAR(); kvstep(7);
  const unsigned h0a = ((const CAS unsigned*)P.ssmk)[id.b * S], h0b = ((const CAS unsigned*)P.ssmk)[(xw ? 2 * id.b + 1 : 512 + (id.b >> 2)) * (S / 2)];
  { u32x4 wf[9];
    if (won) {
      const int q = lane >> 4, qq = (lane & 15) >> 2, p = lane & 3;
      LAS char* base = stg + (4 * q + qq) * 288;
#pragma unroll
      for (int T = 0; T < 9; ++T) { const int cb = T < 4 ? 32 * (T >> 1) + 4 * (T & 1) : T == 4 ? 64 : 80 + 32 * ((T - 5) >> 1) + 4 * ((T - 5) & 1);
        LAS char* a = base + cb * 2 + p * (T == 4 ? 8 : 16);
        wf[T] = __builtin_bit_cast(u32x4, cat(ldtr(a), ldtr(a + 16 * 288))); }
      WAITL(0);
    }
    if (kon) {
#pragma unroll
      for (int e = 0; e < 8; ++e) *(LAS u32x4*)(stg + (8 * cc + e) * 128 + (((7 - rg) ^ cc) * 16)) = ko[e];
#pragma unroll
      for (int i = 0; i < 8; ++i) ko[i] = lds<u32x4>(stg + (8 * i + rg) * 128 + ((cc ^ i) * 16));
    }
    { const rsrc_t rh = mkrs(P.ssmk, D * S * 2);
      WAITL(0);
      auto filt = [&](int ci, unsigned h0) { const int chn = ci == 2 ? 512 + (id.b >> 2) : 2 * id.b + ci; LAS char* cb = sm + CV + ci * CV_CH;
        if (lane < 8) { *(LAS unsigned*)(cb + lane * 4) = 0u; *(LAS unsigned*)(cb + CV_O + lane * 4) = lane == 7 ? h0 << 16 : 0u; }
        if (lane < 38) *(LAS u32x4*)(cb + CV_U + 4096 + lane * 16) = (u32x4){0u, 0u, 0u, 0u};
        if (ci == 2) { *(LAS u32x4*)(cb + CV_U + 4704 + lane * 16) = (u32x4){0u, 0u, 0u, 0u}; *(LAS u32x4*)(cb + CV_U + 5728 + lane * 16) = (u32x4){0u, 0u, 0u, 0u}; }
#pragma unroll
        for (int i = 0; i < 4; ++i) { dma_hidden(rh, cb + 32 + i * 1024, chn * (S * 2) + i * 1024 + lane * 16); dma_hidden(rh, cb + CV_O + 32 + i * 1024, chn * (S * 2) + i * 1024 + lane * 16 + 2); } };
      if (xw == 0) filt(0, h0a);
      filt(2 - xw, h0b); }
    const u64 t0 = __builtin_amdgcn_s_memrealtime();
    while (*(volatile LAS unsigned*)(sm + T_GO) == 0u && !expired(t0)) __builtin_amdgcn_s_sleep(2);
    FENCE();
    if (won) {
#pragma unroll
      for (int T = 0; T < 9; ++T) st16<SC1>(wf[T], rws, W_WOF + lane * 16, ((wu * 72 + wks) * 9 + T) * 1024); }
    if (kon) {
#pragma unroll
      for (int i = 0; i < 8; ++i) st16<SC1>(ko[i], rws, W_KVT + (64 * db + rg) * (S * 2) + (S - 64 * (tb + 1)) * 2 + cc * 16, 8 * i * (S * 2)); }
  }
  BAR(); BAR(); BAR();
  BAR(); WAITV(0); BAR();
  if (xw == 0) set_flag(rws, W_F5 + id.b * 8, tag, lane);
  STAMP(3);
#pragma unroll 1
  for (int i = 0; i < 7; ++i) BAR();
  { u32x4 f0 = {tag.x, tag.y, tag.x, tag.y}, f1 = f0;
    if (xw == 0) { f0 = ld16<SC1>(rws, W_F5 + lane * 16, 0); f1 = ld16<SC1>(rws, W_F5 + lane * 16, 1024); }
    BAR(); BAR();
    if (!__all(flags_ok(f0, tag) && flags_ok(f1, tag))) wait_flags<2, 8>(rws, W_F5 + lane * 16, tag); }
  BAR();
  STAMP(4);
  { auto rows = [&](int ci) { const int chn = ci == 2 ? 512 + (id.b >> 2) : 2 * id.b + ci;
#pragma unroll
      for (int i = 0; i < 4; ++i) dma<SC1>(rws, sm + CV + ci * CV_CH + CV_U + i * 1024, lane * 16, W_KVT + chn * (S * 2) + i * 1024); };
    rows(xw); BAR(); if (xw == 0) rows(2); }
#pragma unroll 1
  for (int i = 0; i < 17; ++i) { if (i == 5) { WAITV(0); WAITL(0); } BAR(); }
  BAR();
  if (xw == 0) { set_flag(rws, W_F3 + (id.tp * 16 + id.ft) * 8, tag, lane); wait_flags<1, 1>(rws, W_F3 + id.tp * 128 + (lane & 7) * 16, tag); }
  STAMP(5);
  BAR();
  if (xw == 1 && (OPT_TG3 & 4)) {
#pragma unroll
    for (int i = 0; i < 4; ++i) { const unsigned n = i * 64 + lane, o = n < 240 ? W_HF + ((id.tp * 8 + 4 * (id.ft >> 3) + (n & 3)) * 72 + 18 * ((n >> 2) & 3) + 3 + (n >> 4)) * 1024 + (id.ft & 7) * 128 : 0xF0000000u;
      asm volatile("buffer_load_dword %0, %1, %2, 0 offen sc1" : "+v"(sink) : "v"(o), "s"(rws) : "memory"); } }
  if (xw == 0) {
    const u64 t0 = __builtin_amdgcn_s_memrealtime();
    while (*(volatile LAS unsigned*)(sm + T_CNT) != 4u && !expired(t0)) __builtin_amdgcn_s_sleep(2);
    FENCE();
    set_flag(rws, W_F4 + id.b * 8, tag, lane);
    const int g = id.ft & 7, d0 = 144 * (g >> 1) + 64 * (g & 1);
    { const rsrc_t rx = mkrs(P.x, S * D * 2);
#pragma unroll
      for (int i = 0; i < 10; ++i) { const unsigned ci = 64 * i + lane, r = __umul24(ci, 6554) >> 16; dma<0>(rx, sm + XTILE + i * 1024, (id.tp * 128 + 64 * (id.ft >> 3) + r) * (D * 2) + d0 * 2 + (ci - 10 * r) * 16, 0); } }
    __builtin_amdgcn_s_sleep(24); wait_flags<2, 12>(rws, W_F4 + lane * 16, tag);
#pragma unroll
    for (int i = 0; i < 10; ++i) dma<SC1>(rws, sm + YTILE + i * 1024, W_YT + (d0 + (lane >> 3)) * (S * 2) + (id.tp * 128 + 64 * (id.ft >> 3)) * 2 + (lane & 7) * 16, i * 8 * (S * 2));
    WAITV(0);
    asm volatile("ds_write_b32 %0, %1" :: "v"((unsigned)(size_t)(sm + T_YOK)), "v"(1u) : "memory");
    if (id.b == 0 && lane == 0) st8<SC1>((u32x2){(unsigned)(epoch + 1), (unsigned)((epoch + 1) >> 32)}, rws, W_EPOCH, 0);
  }
  STAMP(6);
  asm volatile("s_waitcnt vmcnt(0)" : "+v"(sink) :: "memory"); BAR();
}

DEV void role_C(const Params& P, LAS char* sm, const Ids id, const int wid STAMP_ARG) {
  int bcount = 0; (void)bcount;
  const int lane = id.lane, wt = wid >> 1, wf = wid & 1, c = lane & 15, q = lane >> 4;
  const rsrc_t rws = mkrs(P.ws, W_END);
  __builtin_amdgcn_s_setprio(1);
  unsigned dm = ((const CAS unsigned*)P.ssmd)[id.b], dsh = ((const CAS unsigned*)P.ssmd)[256 + (id.b >> 3)];
  asm volatile("" : "+s"(dm), "+s"(dsh));
  f32x4 acc[4][4], accx[2], csa[3];
  bf16x8 tf[2][4], ff[2][5], ones;
  LAS char *ta[2][2], *fb[2][2], *fx[2];
#pragma unroll
  for (int s = 0; s < 2; ++s) {
    const int o = (32 * (s ^ wt) + 16 * (q >> 1) + 4 * (q & 1) + (c >> 2)) * 288 + 8 * (c & 3);
    fx[s] = sm + BBASE + o + 256;
#pragma unroll
    for (int h = 0; h < 2; ++h) { ta[s][h] = sm + (64 * wt + 32 * (h ^ wf) + c) * 128 + (((q + 4 * (s ^ wt)) ^ ((c >> 1) & 7)) * 16); fb[s][h] = sm + BBASE + o + wf * 128 + (h ^ wt) * 64; }
  }
#pragma unroll
  for (int e = 0; e < 8; ++e) ones[e] = 0x3f80;
  asm volatile("" : "+v"(ones));
  auto rd = [&](auto mode, int b, int as, int bs, int s) {
    constexpr int MODE = decltype(mode)::value;
#pragma unroll
    for (int j = 0; j < 4; ++j) { LAS char* p = fb[s][j >> 1] + bs * BSLOT;
      ff[b][j] = MODE < 2 ? cat(ldtr(p + (j & 1) * 32), ldtr(p + (j & 1) * 32 + 8 * 288)) : lds<bf16x8>(p + (j & 1) * 1024); }
#pragma unroll
    for (int i = 0; i < 4; ++i) tf[b][i] = lds<bf16x8>(ta[s][i >> 1] + as * ASLOT + (i & 1) * 2048);
    ff[b][4] = MODE < 2 ? cat(ldtr(fx[s] + bs * BSLOT), ldtr(fx[s] + bs * BSLOT + 8 * 288)) : lds<bf16x8>(fx[s] + bs * BSLOT);
  };
  auto mm = [&](auto mode, int b, int lo, int hi) {
    constexpr int MODE = decltype(mode)::value;
#pragma unroll
    for (int n = 0; n < (MODE == 2 ? 18 : 21 - b); ++n) if (n >= lo && n < hi) {
      if (n < 16) { const int i = n >> 2, j = (i & 1) ? 3 - (n & 3) : (n & 3);
        acc[i][j] = MODE == 0 ? MFMA(tf[b][i], ff[b][j], acc[i][j]) : MFMA(ff[b][j], tf[b][i], acc[i][j]); }
      else if (n < 18) accx[n - 16] = MODE == 0 ? MFMA(tf[b][n - 16], ff[b][4], accx[n - 16]) : MFMA(ff[b][4], tf[b][n - 16], accx[n - 16]);
      else { const int j = n == 20 ? 4 : n - 18; csa[n - 18] = MODE == 0 ? MFMA(ones, ff[b][j], csa[n - 18]) : MFMA(ff[b][j], ones, csa[n - 18]); }
    }
  };
  auto stage = [&](auto mode, int as, int bs, bool drain) {
    constexpr int MODE = decltype(mode)::value;
    mm(mode, 1, 0, 4); SB();
    if (drain) WAITV(0);
    WAITL(0); BAR(); SB();
    rd(mode, 0, as, bs, 0); mm(mode, 1, 4, 21);
    if (MODE < 2) { for (int i = 0; i < 14; ++i) { SG(0x100, 1); SG(0x8, 1); } SG(0x8, 2); }
    else { for (int i = 0; i < 4; ++i) { SG(0x100, 1); SG(0x8, 1); SG(0x100, 1); SG(0x8, 2); } SG(0x100, 1); SG(0x8, 2); }
    SB();
    rd(mode, 1, as, bs, 1); mm(mode, 0, 0, 21);
    if (MODE < 2) { for (int i = 0; i < 7; ++i) { SG(0x100, 1); SG(0x8, 1); SG(0x100, 1); SG(0x8, 2); } }
    else { for (int i = 0; i < 9; ++i) { SG(0x100, 1); SG(0x8, 2); } }
    SB();
  };
  auto first = [&](auto mode, int as, int bs) { WAITL(0); BAR(); SB(); rd(mode, 0, as, bs, 0); rd(mode, 1, as, bs, 1); mm(mode, 0, 0, 21); SB(); };
  auto zero = [&]() {
#pragma unroll
    for (int i = 0; i < 4; ++i)
#pragma unroll
      for (int j = 0; j < 4; ++j) acc[i][j] = (f32x4){0.f, 0.f, 0.f, 0.f};
    accx[0] = accx[1] = csa[0] = csa[1] = csa[2] = (f32x4){0.f, 0.f, 0.f, 0.f}; };
  const std::integral_constant<int, 0> M0; const std::integral_constant<int, 1> M1; const std::integral_constant<int, 2> M2;
  const int jg0 = 4 * wf + 2 * wt, jg2 = 4 * wf + 2 - 2 * wt;

  zero();
  first(M0, 0, 0);
  STAMP(0);
#pragma unroll 1
  for (int it = 0; it < 2; ++it) { stage(M0, 1, 1, false); stage(M0, 2, 2, false); stage(M0, 3, 3, false); stage(M0, 0, 0, false); }
  mm(M0, 1, 18, 21); mm(M0, 1, 0, 8); SB();
  if (q == 0) { *(LAS float*)(sm + T_CS2 + (jg0 * 16 + c) * 4) = csa[0][0]; *(LAS float*)(sm + T_CS2 + (jg0 * 16 + 16 + c) * 4) = csa[1][0]; *(LAS float*)(sm + T_CS2 + (128 + 16 * wt + c) * 4) = csa[2][0]; }
  mm(M0, 1, 8, 18); SB();
  WAITL(0); BAR();
  STAMP(1);
  u32x4 z2p[2][4], z2x;
  { float cs[5];
    cs[0] = csa[0][0]; cs[1] = csa[1][0]; cs[2] = lds<float>(sm + T_CS2 + (jg2 * 16 + c) * 4); cs[3] = lds<float>(sm + T_CS2 + (jg2 * 16 + 16 + c) * 4);
    cs[4] = csa[2][0] + lds<float>(sm + T_CS2 + (144 - 16 * wt + c) * 4);
    float red[2][16];
#pragma unroll
    for (int i = 0; i < 4; ++i) {
      const int ro = (64 * wt + 32 * ((i >> 1) ^ wf) + 8 * q + 4 * (i & 1)) * 4;
      const f32x4 m = lds<f32x4>(sm + T_M1 + ro), a0 = lds<f32x4>(sm + T_A0 + ro), a1 = lds<f32x4>(sm + T_A1 + ro), ir = lds<f32x4>(sm + T_IR + ro);
      f32x4 su = {0.f, 0.f, 0.f, 0.f}, sq = {0.f, 0.f, 0.f, 0.f};
#pragma unroll
      for (int j = 0; j < (i < 2 ? 5 : 4); ++j) {
        const f32x4 z = gelu4((j < 4 ? acc[i][j] : accx[i]) - m * cs[j], a0, a1, ir);
        su = j ? su + z : z; sq = j ? sq + z * z : z * z;
        u32x4& dst = j < 4 ? z2p[i >> 1][j] : z2x;
        dst[2 * (i & 1)] = pk(z[0], z[1]); dst[2 * (i & 1) + 1] = pk(z[2], z[3]);
      }
#pragma unroll
      for (int e = 0; e < 4; ++e) { red[0][4 * i + e] = su[e]; red[1][4 * i + e] = sq[e]; }
    }
    float r8[2][8], r4[2][4];
#pragma unroll
    for (int st = 0; st < 2; ++st) {
#pragma unroll
      for (int e = 0; e < 8; e += 4)
        asm volatile("s_nop 1\n\t"
                     "v_add_f32_dpp %0, %4, %4 row_ror:8 row_mask:0xf bank_mask:0x3\n\tv_add_f32_dpp %1, %5, %5 row_ror:8 row_mask:0xf bank_mask:0x3\n\t"
                     "v_add_f32_dpp %2, %6, %6 row_ror:8 row_mask:0xf bank_mask:0x3\n\tv_add_f32_dpp %3, %7, %7 row_ror:8 row_mask:0xf bank_mask:0x3\n\t"
                     "v_add_f32_dpp %0, %8, %8 row_ror:8 row_mask:0xf bank_mask:0xc\n\tv_add_f32_dpp %1, %9, %9 row_ror:8 row_mask:0xf bank_mask:0xc\n\t"
                     "v_add_f32_dpp %2, %10, %10 row_ror:8 row_mask:0xf bank_mask:0xc\n\tv_add_f32_dpp %3, %11, %11 row_ror:8 row_mask:0xf bank_mask:0xc"
                     : "=&v"(r8[st][e]), "=&v"(r8[st][e + 1]), "=&v"(r8[st][e + 2]), "=&v"(r8[st][e + 3])
                     : "v"(red[st][e]), "v"(red[st][e + 1]), "v"(red[st][e + 2]), "v"(red[st][e + 3]), "v"(red[st][8 + e]), "v"(red[st][9 + e]), "v"(red[st][10 + e]), "v"(red[st][11 + e]));
      asm volatile("s_nop 1\n\t"
                   "v_add_f32_dpp %0, %4, %4 row_half_mirror row_mask:0xf bank_mask:0x5\n\tv_add_f32_dpp %1, %5, %5 row_half_mirror row_mask:0xf bank_mask:0x5\n\t"
                   "v_add_f32_dpp %2, %6, %6 row_half_mirror row_mask:0xf bank_mask:0x5\n\tv_add_f32_dpp %3, %7, %7 row_half_mirror row_mask:0xf bank_mask:0x5\n\t"
                   "v_add_f32_dpp %0, %8, %8 row_half_mirror row_mask:0xf bank_mask:0xa\n\tv_add_f32_dpp %1, %9, %9 row_half_mirror row_mask:0xf bank_mask:0xa\n\t"
                   "v_add_f32_dpp %2, %10, %10 row_half_mirror row_mask:0xf bank_mask:0xa\n\tv_add_f32_dpp %3, %11, %11 row_half_mirror row_mask:0xf bank_mask:0xa\n\t"
                   "v_add_f32_dpp %0, %0, %0 quad_perm:[1,0,3,2] row_mask:0xf bank_mask:0xf\n\tv_add_f32_dpp %1, %1, %1 quad_perm:[1,0,3,2] row_mask:0xf bank_mask:0xf\n\t"
                   "v_add_f32_dpp %2, %2, %2 quad_perm:[1,0,3,2] row_mask:0xf bank_mask:0xf\n\tv_add_f32_dpp %3, %3, %3 quad_perm:[1,0,3,2] row_mask:0xf bank_mask:0xf\n\t"
                   "v_add_f32_dpp %0, %0, %0 quad_perm:[2,3,0,1] row_mask:0xf bank_mask:0xf\n\tv_add_f32_dpp %1, %1, %1 quad_perm:[2,3,0,1] row_mask:0xf bank_mask:0xf\n\t"
                   "v_add_f32_dpp %2, %2, %2 quad_perm:[2,3,0,1] row_mask:0xf bank_mask:0xf\n\tv_add_f32_dpp %3, %3, %3 quad_perm:[2,3,0,1] row_mask:0xf bank_mask:0xf"
                   : "=&v"(r4[st][0]), "=&v"(r4[st][1]), "=&v"(r4[st][2]), "=&v"(r4[st][3])
                   : "v"(r8[st][0]), "v"(r8[st][1]), "v"(r8[st][2]), "v"(r8[st][3]), "v"(r8[st][4]), "v"(r8[st][5]), "v"(r8[st][6]), "v"(r8[st][7]));
    }
    if ((c & 3) == 0) { LAS char* pp = sm + T_PART + wf * 1024 + (64 * wt + 32 * ((c >> 3) ^ wf) + 8 * q + 4 * ((c >> 2) & 1)) * 8;
      *(LAS f32x4*)pp = (f32x4){r4[0][0], r4[1][0], r4[0][1], r4[1][1]}; *(LAS f32x4*)(pp + 16) = (f32x4){r4[0][2], r4[1][2], r4[0][3], r4[1][3]}; }
#pragma unroll
    for (int i = 0; i < 2; ++i) asm volatile("" : "+v"(z2p[i][0]), "+v"(z2p[i][1]), "+v"(z2p[i][2]), "+v"(z2p[i][3]));
    asm volatile("" : "+v"(z2x));
  }
  STAMP(2);
  zero();
  first(M1, 0, 1);
  stage(M1, 3, 2, false); stage(M1, 2, 3, false); stage(M1, 1, 0, false);
  stage(M1, 0, 1, false); stage(M1, 3, 2, false);
  STAMP(3);
  {
#pragma unroll
    for (int i = 0; i < 2; ++i) asm volatile("" : "+v"(z2p[i][0]), "+v"(z2p[i][1]), "+v"(z2p[i][2]), "+v"(z2p[i][3]));
    asm volatile("" : "+v"(z2x));
#pragma unroll
    for (int ip = 0; ip < 2; ++ip) {
      const int ro = (64 * wt + 32 * (ip ^ wf) + 8 * q) * 4;
      const f32x4 r0 = lds<f32x4>(sm + T_R2 + ro), r1 = lds<f32x4>(sm + T_R2 + ro + 16), n0 = lds<f32x4>(sm + T_N2 + ro), n1 = lds<f32x4>(sm + T_N2 + ro + 16);
      const unsigned so = W_ZF + ((id.ft * 64 + 4 * id.tp + 2 * wt + (ip ^ wf)) * 9) * 1024;
#pragma unroll
      for (int j = 0; j < (ip == 0 ? 5 : 4); ++j) { const u32x4 z = j < 4 ? z2p[ip][j] : z2x;
        const f32x4 a = (f32x4){blo(z[0]), bhi(z[0]), blo(z[1]), bhi(z[1])} * r0 + n0, b = (f32x4){blo(z[2]), bhi(z[2]), blo(z[3]), bhi(z[3])} * r1 + n1;
        st16<SC1>((u32x4){pk(a[0], a[1]), pk(a[2], a[3]), pk(b[0], b[1]), pk(b[2], b[3])}, rws, lane * 16, so + (j < 2 ? jg0 + j : j < 4 ? jg2 + j - 2 : 8) * 1024); }
    }
    SB();
  }
  unsigned braw[4];
  { const rsrc_t rb = mkrs(P.bsp, S * 2);
#pragma unroll
    for (int i = 0; i < 4; ++i) braw[i] = __builtin_amdgcn_raw_buffer_load_b16(rb, (id.tp * 128 + 64 * wt + prow(i ^ (2 * wf), c)) * 2, 0, 0); }
  STAMP(4);
  stage(M1, 2, 3, false); stage(M1, 1, 0, false); stage(M1, 0, 1, true);
  mm(M1, 1, 18, 21); mm(M1, 1, 0, 8); SB();
  if (c == 0) { *(LAS f32x4*)(sm + T_CS1 + (jg0 * 16 + 4 * q) * 4) = csa[0]; *(LAS f32x4*)(sm + T_CS1 + (jg0 * 16 + 16 + 4 * q) * 4) = csa[1]; *(LAS f32x4*)(sm + T_CS1 + (128 + 16 * wt + 4 * q) * 4) = csa[2]; }
  mm(M1, 1, 8, 18); SB();
  WAITL(0); BAR();
  STAMP(5);
  u32x2 z1p[4][4], z1x[2];
  { f32x4 cs[5];
    cs[0] = csa[0]; cs[1] = csa[1]; cs[2] = lds<f32x4>(sm + T_CS1 + (jg2 * 16 + 4 * q) * 4); cs[3] = lds<f32x4>(sm + T_CS1 + (jg2 * 16 + 16 + 4 * q) * 4);
    cs[4] = csa[2] + lds<f32x4>(sm + T_CS1 + (144 - 16 * wt + 4 * q) * 4);
#pragma unroll
    for (int i = 0; i < 4; ++i) { const int ro = (64 * wt + prow(i ^ (2 * wf), c)) * 4;
      const float m = lds<float>(sm + T_M1 + ro), a0 = lds<float>(sm + T_A0 + ro), a1 = lds<float>(sm + T_A1 + ro), ir = lds<float>(sm + T_IR + ro), bias = blo(braw[i]);
#pragma unroll
      for (int j = 0; j < (i < 2 ? 5 : 4); ++j) {
        f32x4& a = j < 4 ? acc[i][j] : accx[i];
        const f32x4 z = gelu4(a - m * cs[j], a0, a1, ir);
        (j < 4 ? z1p[i][j] : z1x[i]) = (u32x2){pk(z[0], z[1]), pk(z[2], z[3])};
        a = (f32x4){bias, bias, bias, bias};
      } }
#pragma unroll
    for (int s = 0; s < 2; ++s) { fx[s] = sm + B2BASE + (s * 9 + 8) * 1024 + lane * 16;
#pragma unroll
      for (int h = 0; h < 2; ++h) { fb[s][h] = sm + B2BASE + (s * 9 + (h ? jg2 : jg0)) * 1024 + lane * 16; ta[s][h] = sm + (64 * wt + 32 * (h ^ wf) + c) * 128 + (((q + 4 * s) ^ ((c >> 1) & 7)) * 16); } }
#pragma unroll
    for (int i = 0; i < 4; ++i) asm volatile("" : "+v"(z1p[i][0]), "+v"(z1p[i][1]), "+v"(z1p[i][2]), "+v"(z1p[i][3]), "+v"(acc[i][0]), "+v"(acc[i][1]), "+v"(acc[i][2]), "+v"(acc[i][3]));
    asm volatile("" : "+v"(z1x[0]), "+v"(z1x[1]), "+v"(accx[0]), "+v"(accx[1]));
  }
  STAMP(6);
  const int jq = id.b & 3;
  LAS char *cs_ = sm + CV + 2 * CV_CH, *cb = sm + CV + wt * CV_CH;
  LAS char *am[4], *um = cb + CV_U + (2032 - 16 * c + 8 * q) * 2 + 256 * wf - 3584;
#pragma unroll
  for (int i = 0; i < 4; ++i) { am[i] = cb + ((c & 1) ? 0 : CV_O) + (4 * q + ((c + 1) >> 1)) * 4 + 256 * wf + 1024 * i; asm volatile("" : "+v"(am[i])); }
  asm volatile("" : "+v"(um));
  LAS char *as[4], *u1, *u0;
  { const int da = (2 - wt) * CV_CH + 128 * wid - 256 * wf;
#pragma unroll
    for (int i = 0; i < 4; ++i) { as[i] = am[i] + da; asm volatile("" : "+v"(as[i])); }
    u1 = um + da + 512 * jq; u0 = um + da + 512 * (7 - jq); asm volatile("" : "+v"(u1), "+v"(u0)); }
  bf16x8 ca[2][8], cu[2][8];
  auto afrag = [&](LAS char* p) { const unsigned d0 = lds<unsigned>(p), d1 = lds<unsigned>(p + 4), d2 = lds<unsigned>(p + 8), d3 = lds<unsigned>(p + 12);
    return __builtin_bit_cast(bf16x8, (u32x4){d0, d1, d2, d3}); };
  auto lds_mn = [&](int b, int kk) {
#pragma unroll
    for (int e = 0; e < 8; ++e) { cu[b][e] = lds<bf16x8>(um + kk * 64 + 512 * (7 - e)); if (e == 0) ca[b][0] = afrag(am[0] + kk * 64); }
#pragma unroll
    for (int e = 1; e < 8; ++e) ca[b][e] = afrag(am[e >> 1] + kk * 64 + (e & 1) * 512); };
  first(M2, 2, 0);
  STAMP(7);
#pragma unroll 1
  for (int it = 0; it < 10; ++it) { stage(M2, 1, 1, false); stage(M2, 0, 2, false); stage(M2, 2, 0, false); }
  stage(M2, 1, 1, false);
#pragma unroll
  for (int i = 0; i < 4; ++i) asm volatile("" : "+v"(z1p[i][0]), "+v"(z1p[i][1]), "+v"(z1p[i][2]), "+v"(z1p[i][3]));
  asm volatile("" : "+v"(z1x[0]), "+v"(z1x[1]));
  { auto gate = [&](u32x2 z, f32x4 a) { const f32x4 h = (f32x4){blo(z.x), bhi(z.x), blo(z.y), bhi(z.y)} * a; return (u32x2){pk(h[0], h[1]), pk(h[2], h[3])}; };
    auto emit = [&](int i) { const unsigned so = W_HF + (id.tp * 8 + 4 * wt + (i ^ (2 * wf))) * 72 * 1024;
#pragma unroll
      for (int pp = 0; pp < 2; ++pp) { const u32x2 lo = gate(z1p[i][2 * pp], acc[i][2 * pp]), hi = gate(z1p[i][2 * pp + 1], acc[i][2 * pp + 1]);
        st16<SC1>((u32x4){lo.x, lo.y, hi.x, hi.y}, rws, lane * 16, so + (4 * id.ft + 2 * wf + (pp ^ wt)) * 1024); }
      if (i < 2) st8<SC1>(gate(z1x[i], accx[i]), rws, lane * 16 + (id.ft & 1) * 8, so + (64 + (id.ft >> 1)) * 1024);
      SB(); };
    mm(M2, 1, 0, 4); mm(M2, 1, 16, 17); mm(M2, 1, 4, 6); SB();
    STAMP(8);
    mm(M2, 1, 6, 8); mm(M2, 1, 17, 18); mm(M2, 1, 8, 16); emit(0);
    lds_mn(0, 0); emit(1); emit(2); emit(3); }
  const int g = id.ft & 7, gp = g & 1, half = id.ft >> 3, rot = krot(id);
  const unsigned wso = W_WOF + ((((EXP & 4) ? 0 : g >> 1) * 72 + 18 * wid) * 9) * 1024;
  u32x4 wr[WD][5];
  auto ldw = [&](int slot, int i) {
#pragma unroll
    for (int t = 0; t < 5; ++t) wr[slot][t] = ld16<(EXP & 8) ? 0 : SC1>(rws, lane * 16, wso + kwrap(i, rot) * 9216 + (t == 4 ? 4 : t + gp * 5) * 1024); };
  SB();
  SB(); BAR(); SB();
  STAMP(9);
  u32x4 hr[3][4];
  const unsigned hso = W_HF + (((EXP & 2) ? (id.b & 7) * 4 : id.tp * 8 + 4 * half) * 72 + 18 * wid) * 1024;
  auto ldh = [&](int slot, int i) {
#pragma unroll
    for (int t = 0; t < 4; ++t) hr[slot][t] = ld16<(EXP & 8) ? 0 : SC1>(rws, lane * 16, hso + (((unsigned)t ^ (wf ^ (2 * gp))) * 72 + kwrap(i, rot)) * 1024); };
  { const float dvm = wt ? bhi(dm) : blo(dm), dvs = (id.b & 4) ? bhi(dsh) : blo(dsh);
    f32x4 cm[8], s0 = {0.f, 0.f, 0.f, 0.f}, s1 = s0;
#pragma unroll
    for (int j = 0; j < 8; ++j) cm[j] = s0;
    bf16x8 sa[4], su[8];
    auto lds_sh = [&](int p) { const int kk = p >> 1, e0 = 4 * (p & 1);
#pragma unroll
      for (int e = 0; e < 4; ++e) { sa[e] = afrag(as[(e0 + e) >> 1] + kk * 64 + (e & 1) * 512); su[e] = lds<bf16x8>(u1 + kk * 64 + 512 * (e0 + e));
        if (e0 == 0) su[4 + e] = lds<bf16x8>(u0 + kk * 64 + 512 * e); } };
    auto mm_sh = [&](int p) {
#pragma unroll
      for (int e = 0; e < 4; ++e) { s1 = MFMA(sa[e], su[e], s1); if (!(p & 1)) s0 = MFMA(sa[e], su[4 + e], s0); } };
    auto mm_mn = [&](int b) {
#pragma unroll
      for (int e = 0; e < 8; ++e)
#pragma unroll
        for (int j = e; j < 8; ++j) cm[j] = MFMA(ca[b][e], cu[b][j - e], cm[j]); };
#define il(nr) do { for (int i_ = 0; i_ < nr; ++i_) { SG(0x8, 1); SG(0x100, 1); } SG(0x8, 44); SB(); } while (0)
    lds_sh(0); lds_mn(1, 1); mm_mn(0); mm_sh(0); il(40); STAMP(15);
    lds_sh(1); lds_mn(0, 2); mm_mn(1); mm_sh(1); il(36); STAMP(17);
    WAITV(0); BAR(); SB();
    STAMP(10);
    lds_sh(2); lds_mn(1, 3); mm_mn(0); mm_sh(2); il(40); STAMP(16);
    lds_sh(3); ldw(0, 5); ldw(1, 6); if (WD > 2) ldw(2, 7); mm_mn(1); mm_sh(3);
    for (int i_ = 0; i_ < 12; ++i_) { SG(0x8, 1); SG(0x100, 1); } for (int i_ = 0; i_ < 12; ++i_) { SG(0x8, 2); SG(0x20, 1); } SG(0x20, 3); SG(0x8, 8); SB();
    if (wid != 0) *(LAS f32x4*)(sm + CV_PS + (wid - 1) * 1024 + lane * 16) = s0;
    if (wid != 1) *(LAS f32x4*)(sm + CV_PS + (3 + (wid ? wid - 1 : 0)) * 1024 + lane * 16) = s1;
    LAS char* pm = cb;
    *(volatile LAS unsigned*)(sm + T_CVF + wid * 4) = 1u;
    { const u64 t0 = __builtin_amdgcn_s_memrealtime(); while (*(volatile LAS unsigned*)(sm + T_CVF + (wid ^ 1) * 4) == 0u && !expired(t0)) {} }
    FENCE();
#pragma unroll
    for (int j = 0; j < 4; ++j) { f32x4 lo = cm[j], hi = cm[4 + j]; asm volatile("" : "+v"(lo), "+v"(hi));
      *(LAS f32x4*)(pm + wf * 4096 + j * 1024 + lane * 16) = wf ? lo : hi; cm[j] = wf ? hi : lo; }
    SB(); WAITL(0); BAR(); SB();
    STAMP(11);
    ldh(0, 0); ldh(1, 1); ldh(2, 2);
    auto yout = [&](f32x4 a, int chn, LAS char* ub, int j, float dv) {
      const u32x2 u = lds<u32x2>(ub + CV_U + (2044 - 256 * j - 16 * c - 4 * q) * 2);
      const f32x4 y = a + (f32x4){bhi(u.y), blo(u.y), bhi(u.x), blo(u.x)} * dv;
      st8<SC1>((u32x2){pk(y[0], y[1]), pk(y[2], y[3])}, rws, (16 * c + 4 * q) * 2, W_YT + chn * (S * 2) + j * 512); };
#pragma unroll
    for (int j = 0; j < 4; ++j) yout(cm[j] + lds<f32x4>(pm + (wf ^ 1) * 4096 + j * 1024 + lane * 16), 2 * id.b + wt, cb, 4 * wf + j, dvm);
    if (wid < 2) { f32x4 ys = wf ? s1 : s0;
#pragma unroll
      for (int w = 0; w < 3; ++w) ys += lds<f32x4>(sm + CV_PS + (3 * wid + w) * 1024 + lane * 16);
      yout(ys, 512 + (id.b >> 2), cs_, wid ? 7 - jq : jq, dvs); }
    SB();
  }
  STAMP(19);
  f32x4 o[4][4], ox[2];
#pragma unroll
  for (int i = 0; i < 4; ++i)
#pragma unroll
    for (int t = 0; t < 4; ++t) o[i][t] = (f32x4){0.f, 0.f, 0.f, 0.f};
  ox[0] = ox[1] = (f32x4){0.f, 0.f, 0.f, 0.f};
  const int dp = 144 * (g >> 1) + 80 * gp, ds = 144 * (g >> 1) + 64, trow = 64 * half + prow(wid, c);
  const unsigned xo = (id.tp * 128 + trow) * (D * 2);
  f32x4 rs[5];
  { LAS char* wl = sm + wid * G3W + lane * 16;
    bf16x8 wq[2][5];
#pragma unroll
    for (int t = 0; t < 5; ++t) wq[0][t] = lds<bf16x8>(wl + t * 1024);
#pragma unroll
    for (int i = 0; i < 18; ++i) {
      if (i < 4) {
#pragma unroll
        for (int t = 0; t < 5; ++t) wq[(i + 1) & 1][t] = lds<bf16x8>(wl + ((i + 1) * 5 + t) * 1024); }
#pragma unroll
      for (int r = 0; r < 4; ++r) { const bf16x8 hf = __builtin_bit_cast(bf16x8, hr[i % 3][r]);
#pragma unroll
        for (int t = 0; t < 5; ++t) if (t < 4 || r < 2) { const int tt = (r & 1) && t < 4 ? 3 - t : t;
          const bf16x8 wf_ = i < 5 ? wq[i & 1][tt] : __builtin_bit_cast(bf16x8, wr[(i - 5) % WD][tt]);
          if (tt < 4) o[r][tt] = MFMA(wf_, hf, o[r][tt]); else ox[r] = MFMA(wf_, hf, ox[r]); } }
      SB();
      if (i == 0 || i == 2 || i == 4 || i == 7 || i == 10 || i == 13 || i == 15) STAMP(20 + i);
      if (i + 3 < 18) ldh(i % 3, i + 3);
      if (i >= 5 && i + WD < 18) ldw((i - 5) % WD, i + WD);
      if (i == 2) { WAITV(12); if (lane == 0) __hip_atomic_fetch_add((LAS unsigned*)(sm + T_CNT), 1u, __ATOMIC_RELAXED, __HIP_MEMORY_SCOPE_WORKGROUP); }
      if (i == 16) {
        const u64 t0 = __builtin_amdgcn_s_memrealtime();
        while (*(volatile LAS unsigned*)(sm + T_YOK) == 0u && !expired(t0)) __builtin_amdgcn_s_sleep(1);
        FENCE();
        const float m = lds<float>(sm + T_M1 + trow * 4), r = lds<float>(sm + T_R1 + trow * 4);
        LAS char* yb = sm + YTILE + (c >> 2) * 128 + (32 * wt + 4 * wf + 8 * (c & 3)) * 2;
        auto res = [&](u32x2 xv, bf16x4 yv) { const u32x2 yu = __builtin_bit_cast(u32x2, yv);
          const f32x4 xf = {blo(xv.x), bhi(xv.x), blo(xv.y), bhi(xv.y)}, yf = {blo(yu.x), bhi(yu.x), blo(yu.y), bhi(yu.y)};
          return xf + (xf - m) * r * yf; };
        const int yp = 16 * gp, ysh = 64 * (gp ^ 1);
        LAS char* xb = sm + XTILE + prow(wid, c) * 160;
        const u32x4 x0 = lds<u32x4>(xb + yp * 2 + q * 16), x1 = lds<u32x4>(xb + yp * 2 + 64 + q * 16); const u32x2 x2 = lds<u32x2>(xb + ysh * 2 + q * 8);
        rs[0] = res(x0.xy, ldtr(yb + (yp + 8 * q) * 128)); rs[1] = res(x0.zw, ldtr(yb + (yp + 8 * q + 4) * 128));
        rs[2] = res(x1.xy, ldtr(yb + (yp + 32 + 8 * q) * 128)); rs[3] = res(x1.zw, ldtr(yb + (yp + 36 + 8 * q) * 128));
        rs[4] = res(x2, ldtr(yb + (ysh + 4 * q) * 128));
        asm volatile("" : "+v"(rs[0]), "+v"(rs[1]), "+v"(rs[2]), "+v"(rs[3]), "+v"(rs[4])); }
      SB();
    }
  }
  STAMP(12);
  const bool own0 = wt == gp;
  { LAS char* pw = sm + wid * G3W + lane * 16;
    auto put = [&](int r) { LAS char* p = pw + ((unsigned)r ^ (wf ^ (2 * gp))) * 5120;
#pragma unroll
      for (int t = 0; t < 4; ++t) *(LAS f32x4*)(p + t * 1024) = o[r][t];
      if (r < 2) *(LAS f32x4*)(p + 4096) = ox[r]; };
    put(1); put(3);
    if (own0) { put(2);
#pragma unroll
      for (int t = 0; t < 4; ++t) rs[t] += o[0][t];
      rs[4] += ox[0]; }
    else { put(0);
#pragma unroll
      for (int t = 0; t < 4; ++t) rs[t] += o[2][t]; }
  }
  SB(); WAITL(0); BAR(); SB();
  STAMP(13);
  { f32x4 pt[3][5];
#pragma unroll
    for (int t = 0; t < 5; ++t)
#pragma unroll
      for (int w = 0; w < 3; ++w) pt[w][t] = lds<f32x4>(sm + (wid ^ (w + 1)) * G3W + wid * 5120 + lane * 16 + t * 1024);
    const rsrc_t ro = mkrs(P.out, S * D * 2);
    auto fin = [&](int t) { const f32x4 v = (rs[t] + pt[0][t]) + (pt[1][t] + pt[2][t]); return (u32x2){pk(v[0], v[1]), pk(v[2], v[3])}; };
#pragma unroll
    for (int pp = 0; pp < 2; ++pp) { const u32x2 lo = fin(2 * pp), hi = fin(2 * pp + 1); st16<0>((u32x4){lo.x, lo.y, hi.x, hi.y}, ro, xo + q * 16, dp * 2 + 64 * pp); }
    const u32x2 ls = fin(4);
    if (own0) st8<0>(ls, ro, xo + q * 8, ds * 2);
  }
  STAMP(14);
}
}

extern "C" __global__ __launch_bounds__(512, 2) void h3_gmlp_fused(const Params P) {
  extern __shared__ __attribute__((aligned(16))) char smem_[];
  LAS char* sm = (LAS char*)smem_;
  const int wid = __builtin_amdgcn_readfirstlane(threadIdx.x >> 6);
  Ids id; id.b = blockIdx.x; id.lane = threadIdx.x & 63;
  const int xcd = id.b & 7, loc = id.b >> 3;
  id.tp = (xcd >> 1) * 4 + (loc >> 3); id.ft = (xcd & 1) * 8 + (loc & 7);
  if (wid < 4) { unsigned sink = 0;
    if (wid == 0) { code_touch(id, sink);
      if (PT) {
        const int l = id.lane; const char* p = (const char*)P.wsp + (size_t)(l & 7) * (1 << 20);
        if (l >= 8) p = (const char*)(l < 11 ? P.k : l < 14 ? P.v : l < 17 ? P.ssmk : l < 20 ? P.wout : P.out) + (size_t)((l - 8) % 3) * (1 << 20);
        if (l >= 23) p = (const char*)(l == 23 ? P.shk : l == 24 ? P.ssmd : P.bsp);
        if (l < 26 && (PT == 1 || (id.b >> 3) == 0)) asm volatile("global_load_dword %0, %1, off" : "+v"(sink) : "v"(p) : "memory"); } }
    else if (OPT_TWIN) { const rsrc_t r = mkrs(P.win, D * F * 2); const unsigned n = (wid - 1) * 64 + id.lane;
      if (OPT_TWIN > 1) __builtin_amdgcn_s_sleep(OPT_TWIN);
      unsigned o;
      if (TWZ == 0) o = n < 162 ? id.b * 20736 + n * 128 : 0xF0000000u;
      else if (TWZ == 3) { const unsigned m = id.b * 63 + n, row = m / 36; o = n < 63 ? (row + 128) * (F * 2) + FH * 2 + (m - row * 36) * 128 : 0xF0000000u; }
      else { const unsigned m = id.b * 81 + n, row = m / 36; o = n < 81 ? row * (F * 2) + FH * 2 + (m - row * 36) * 128 : 0xF0000000u; }
      asm volatile("buffer_load_dword %0, %1, %2, 0 offen" : "+v"(sink) : "v"(o), "s"(r) : "memory"); }
    asm volatile("s_waitcnt vmcnt(0)" : "+v"(sink) :: "memory"); }
  if (__builtin_expect(wid == 4 || wid == 5, 1)) role_L(P, sm, id, wid - 4 STAMP_PASS);
  else if (wid >= 6) role_X(P, sm, id, wid - 6 STAMP_PASS);
  else role_C(P, sm, id, wid STAMP_PASS);
}
extern "C" __global__ void h3_gmlp_end() {}

extern "C" void kernel_launch(void* const* d_in, const int* in_sizes, int n_in, void* d_out, int out_size, void* d_ws, size_t ws_size, hipStream_t stream) {
  Params p;
  p.x = (const u16*)d_in[0]; p.k = (const u16*)d_in[1]; p.v = (const u16*)d_in[2]; p.shk = (const u16*)d_in[3]; p.ssmk = (const u16*)d_in[4]; p.ssmd = (const u16*)d_in[5];
  p.win = (const u16*)d_in[7]; p.wsp = (const u16*)d_in[8]; p.bsp = (const u16*)d_in[9]; p.wout = (const u16*)d_in[10];
  p.out = (u16*)d_out; p.ws = (char*)d_ws;
  (void)hipFuncSetAttribute((const void*)h3_gmlp_fused, hipFuncAttributeMaxDynamicSharedMemorySize, LDS_BYTES);
  h3_gmlp_fused<<<256, 512, LDS_BYTES, stream>>>(p);
}
```

```cpp
#define OAUX 0
#include <hip/hip_runtime.h>
#include <hip/hip_bf16.h>
#include <type_traits>

namespace {
typedef unsigned short u16;
typedef unsigned long long u64;
typedef short bf16x8 __attribute__((ext_vector_type(8)));
typedef short bf16x4 __attribute__((ext_vector_type(4)));
typedef __bf16 bf2 __attribute__((ext_vector_type(2)));
typedef float f32x2 __attribute__((ext_vector_type(2)));
typedef float f32x4 __attribute__((ext_vector_type(4)));
typedef unsigned u32x2 __attribute__((ext_vector_type(2)));
typedef unsigned u32x4 __attribute__((ext_vector_type(4)));
typedef __amdgpu_buffer_rsrc_t rsrc_t;
#define LAS __attribute__((address_space(3)))
#define CAS __attribute__((address_space(4)))
#define DEV __device__ __forceinline__

constexpr int S = 2048, D = 576, F = 4608, FH = 2304;
constexpr int ASLOT = 16384, BSLOT = 18432, BBASE = 4 * ASLOT;
constexpr int STG = 139264, STG_W = 9216;
constexpr int T_M1 = 157952, T_R1 = T_M1 + 512, T_A0 = T_R1 + 512, T_A1 = T_A0 + 512, T_IR = T_A1 + 512;
constexpr int T_CS2 = T_IR + 512, T_CS1 = T_CS2;
constexpr int T_PART = T_CS1 + 640, T_N2 = T_PART, T_R2 = T_N2 + 512;
constexpr int T_TAG = T_PART + 2048, T_CNT = T_TAG + 16, T_GO = T_CNT + 4, T_YOK = T_CNT + 8, T_CVF = T_CNT + 16;
constexpr int LDS_BYTES = 163840;
static_assert(T_CNT + 32 <= LDS_BYTES, "LDS map");
constexpr int G3W = 25600;
constexpr int B2BASE = 3 * ASLOT;
constexpr int CV = B2BASE + 3 * BSLOT, CV_CH = 13056, CV_O = 4160, CV_U = 8320;
constexpr int CV_PS = CV + 2 * CV_CH + 15104;
constexpr int YTILE = 122880, XTILE = 133120;
constexpr int W_EPOCH = 0, W_F2 = 4096, W_F3 = 8192, W_F4 = 12288, W_F5 = 16384;
constexpr int W_G0X = 0x8000, W_G1F = 0x10000, W_G1S = 0x60000;
constexpr int W_KVT = 1 << 20, W_YT = 4 << 20, W_WOF = 7 << 20, W_ZF = 10 << 20, W_HF = 20 << 20, W_END = 30 << 20;
constexpr int SC1 = 16;
#define OPT_TWIN 12
#define OPT_TWSP 1
#define OPT_TG3 0
#define EXP 0
#define RH 0
#define RW 0
#define KB 1
#define PR 0
#define XM 0
#define RT 0
#define TWZ 0
#define WD 3

#define BAR0() do { asm volatile("" ::: "memory"); __builtin_amdgcn_s_barrier(); asm volatile("" ::: "memory"); } while (0)
#define BAR() BAR0()
#define SB() __builtin_amdgcn_sched_barrier(0)
#define SG(m, n) __builtin_amdgcn_sched_group_barrier(m, n, 0)
#define WAITV(n) asm volatile("s_waitcnt vmcnt(%0)" ::"i"(n) : "memory")
#define WAITL(n) asm volatile("s_waitcnt lgkmcnt(%0)" ::"i"(n) : "memory")
#define FENCE() asm volatile("" ::: "memory")
#define MFMA(a, b, c) __builtin_amdgcn_mfma_f32_16x16x32_bf16(a, b, c, 0, 0, 0)
#define STAMP(i) do {} while (0)
#define LBAR() BAR()
#define STAMP_ARG
#define STAMP_PASS

struct Params {
  const u16 *x, *k, *v, *shk, *ssmk, *ssmd, *win, *wsp, *bsp, *wout;
  u16* out; char* ws;
};

DEV rsrc_t mkrs(const void* p, unsigned bytes) { return __builtin_amdgcn_make_buffer_rsrc((void*)p, 0, bytes, 0x00020000); }
template <int AUX> DEV void dma(rsrc_t rs, LAS char* l, unsigned voff, unsigned soff) { __builtin_amdgcn_raw_ptr_buffer_load_lds(rs, (LAS void*)l, 16, voff, soff, 0, AUX); }
DEV void dma_hidden(rsrc_t rs, LAS char* l, unsigned voff) { unsigned keep;
  asm volatile("s_mov_b32 %0, m0\n\ts_mov_b32 m0, %3\n\ts_nop 0\n\tbuffer_load_dwordx4 %1, %2, 0 offen lds\n\ts_mov_b32 m0, %0" : "=&s"(keep) : "v"(voff), "s"(rs), "s"(__builtin_amdgcn_readfirstlane((unsigned)(size_t)l)) : "memory"); }
template <int AUX> DEV u32x4 ld16(rsrc_t rs, unsigned voff, unsigned soff) { return __builtin_amdgcn_raw_buffer_load_b128(rs, voff, soff, AUX); }
template <int AUX> DEV void st16(u32x4 v, rsrc_t rs, unsigned voff, unsigned soff) { __builtin_amdgcn_raw_buffer_store_b128(v, rs, voff, soff, AUX); }
template <int AUX> DEV void st8(u32x2 v, rsrc_t rs, unsigned voff, unsigned soff) { __builtin_amdgcn_raw_buffer_store_b64(v, rs, voff, soff, AUX); }
template <class T> DEV T lds(LAS char* p) { return *(LAS T*)p; }
DEV bf16x4 ldtr(LAS char* p) { return __builtin_amdgcn_ds_read_tr16_b64_v4i16((LAS bf16x4*)p); }
DEV bf16x8 cat(bf16x4 a, bf16x4 b) { return __builtin_shufflevector(a, b, 0, 1, 2, 3, 4, 5, 6, 7); }
DEV float blo(unsigned u) { return __uint_as_float(u << 16); }
DEV float bhi(unsigned u) { return __uint_as_float(u & 0xffff0000u); }
DEV unsigned pk(float a, float b) { return __builtin_bit_cast(unsigned, __builtin_convertvector((f32x2){a, b}, bf2)); }
DEV float dot2(unsigned a, unsigned b, float c) { return __builtin_amdgcn_fdot2_f32_bf16(__builtin_bit_cast(bf2, a), __builtin_bit_cast(bf2, b), c, false); }
template <int CTRL> DEV float dpp(float x) { return __builtin_bit_cast(float, __builtin_amdgcn_mov_dpp(__builtin_bit_cast(int, x), CTRL, 0xf, 0xf, true)); }
constexpr int XOR1 = 0xB1, XOR2 = 0x4E, XOR7 = 0x141, XOR8 = 0x128;
constexpr float GC0 = -2.30220819f, GC1 = -0.10294324f;
template <class T> DEV f32x2 gelu2(f32x2 a, T a0, T a1, T ir) {
  const f32x2 g = a * (a * a * a1 + a0);
  f32x2 e; e.x = __builtin_amdgcn_exp2f(g.x); e.y = __builtin_amdgcn_exp2f(g.y);
  e = e * ir + ir;
  f32x2 r; r.x = __builtin_amdgcn_rcpf(e.x); r.y = __builtin_amdgcn_rcpf(e.y);
  return a * r;
}
DEV f32x4 gelu4(f32x4 a, f32x4 a0, f32x4 a1, f32x4 ir) { const f32x2 p = gelu2(a.xy, a0.xy, a1.xy, ir.xy), q = gelu2(a.zw, a0.zw, a1.zw, ir.zw); return (f32x4){p.x, p.y, q.x, q.y}; }
DEV f32x4 gelu4(f32x4 a, float a0, float a1, float ir) { const f32x2 p = gelu2(a.xy, a0, a1, ir), q = gelu2(a.zw, a0, a1, ir); return (f32x4){p.x, p.y, q.x, q.y}; }
DEV u32x4 seal(float a, float b, u32x2 tag) { const unsigned ua = __float_as_uint(a), ub = __float_as_uint(b); return (u32x4){ua, ub, ua ^ tag.x, ub ^ tag.y}; }
DEV bool sealed(u32x4 g, u32x2 tag) { return (g.x ^ g.z) == tag.x && (g.y ^ g.w) == tag.y; }
DEV bool expired(u64 t0) { return __builtin_amdgcn_s_memrealtime() - t0 > 2000000ull; }
template <int NL, int NAP> DEV void wait_flags(rsrc_t ws, unsigned voff, u32x2 tag) {
  const u64 t0 = __builtin_amdgcn_s_memrealtime();
  for (;;) {
    bool ok = true;
#pragma unroll
    for (int i = 0; i < NL; ++i) { const u32x4 f = ld16<SC1>(ws, voff, i * 1024); ok &= f.x == tag.x && f.y == tag.y && f.z == tag.x && f.w == tag.y; }
    if (__all(ok) || expired(t0)) break;
    __builtin_amdgcn_s_sleep(NAP); FENCE();
  }
  FENCE();
}
DEV bool flags_ok(u32x4 f, u32x2 tag) { return f.x == tag.x && f.y == tag.y && f.z == tag.x && f.w == tag.y; }
DEV int prow(int T, int c) { return 32 * (T >> 1) + 8 * (c >> 2) + 4 * (T & 1) + (c & 3); }
DEV void set_flag(rsrc_t ws, unsigned off, u32x2 tag, int lane) { if (lane == 0) st8<SC1>(tag, ws, off, 0); }

struct Ids { int b, tp, ft, lane; };
DEV void code_touch(const Ids& id, unsigned& sink) {
  unsigned lo, hi, bytes;
  asm volatile("s_getpc_b64 vcc\n1:\n\ts_sub_u32 %0, vcc_lo, 1b-h3_gmlp_fused\n\ts_subb_u32 %1, vcc_hi, 0\n\ts_mov_b32 %2, h3_gmlp_end-h3_gmlp_fused" : "=s"(lo), "=s"(hi), "=s"(bytes) :: "vcc", "scc");
  const rsrc_t rc = mkrs((const void*)(((u64)hi << 32) | lo), bytes);
  asm volatile("buffer_load_dword %0, %1, %2, 0 offen" : "+v"(sink) : "v"((id.lane < 20 ? id.lane * 32 + (id.b >> 3) : 0x100000) * 128), "s"(rc) : "memory");
}
DEV int krot(const Ids& id) { return (RH * (id.ft & 7) + ((RW * (id.tp & 3)) >> 1)) % 18; }
DEV int kwrap(int i, int r) { const int k = i + r; return k >= 18 ? k - 18 : k; }

DEV void role_L(const Params& P, LAS char* sm, const Ids id, const int lw STAMP_ARG) {
  int bcount = 0; (void)bcount;
  const int lane = id.lane, rr = lane >> 3, sl = lane & 7;
  const rsrc_t rx = mkrs(P.x, S * D * 2), rwin = mkrs(P.win, D * F * 2);
  const unsigned swz = (sl ^ (4 * lw + (rr >> 1))) * 16, prr = 16 * lw + 8 * (rr >> 2) + (rr & 3);
  const unsigned vx = prr * (D * 2) + swz;
  unsigned vb[9];
  auto issue1a = [&](int slot, int i, int ks) { dma<0>(rx, sm + slot * ASLOT + lw * 1024 + i * 2048, vx, (id.tp * 128 + 32 * (i >> 1) + 4 * (i & 1)) * (D * 2) + ks * 128); };
  auto issue1b = [&](int slot, int i, int ks, int col) { dma<0>(rwin, sm + BBASE + slot * BSLOT + lw * 1024 + i * 2048, vb[i], ks * 64 * (F * 2) + (col + id.ft * 144) * 2); };
  STAMP(56);
#pragma unroll
  for (int i = 0; i < 9; ++i) {
    if (i < 8) issue1a(0, i, 0);
    if (i == 0) STAMP(57);
    SB();
    const unsigned ci = 64 * (2 * i + lw) + lane, rho = __umul24(ci, 3641) >> 16;
    vb[i] = __umul24(rho, F * 2 - 288) + 16 * ci + __mul24((int)(rho & 4) - (int)((rho >> 1) & 4), F * 2);
    issue1b(0, i, 0, FH);
    SB();
  }
  auto xA = [&](int n) {
#pragma unroll
    for (int i = 0; i < 8; ++i) issue1a(n < 9 ? n & 3 : (9 - n) & 3, i, n < 9 ? n : 17 - n); };
  auto wB = [&](int n) {
#pragma unroll
    for (int i = 0; i < 9; ++i) issue1b(n & 3, i, n < 9 ? n : 17 - n, n < 9 ? FH : 0); };
  STAMP(58);
  xA(1); wB(1); STAMP(59); xA(2);
  STAMP(60);
  const rsrc_t rwsp = mkrs(P.wsp, S * S * 2), rws = mkrs(P.ws, W_END);
  const unsigned vw = prr * (S * 2) + swz, vl = lane * 16 + lw * 1024;
  auto issue2a = [&](int k, int slot) { LAS char* a = sm + slot * ASLOT + lw * 1024; const unsigned sa = id.tp * 128 * (S * 2) + k * 128;
#pragma unroll
    for (int i = 0; i < 8; ++i) dma<0>(rwsp, a + i * 2048, vw, sa + (32 * (i >> 1) + 4 * (i & 1)) * (S * 2)); };
  auto issue2b = [&](int k, int slot) { LAS char* bb = sm + B2BASE + slot * BSLOT + lw * 1024; const unsigned sb = W_ZF + (id.ft * 576 + k * 18) * 1024;
#pragma unroll
    for (int i = 0; i < 9; ++i) dma<SC1>(rws, bb + i * 2048, vl, sb + i * 2048); };
#pragma unroll 1
  for (int n = 0; n < 6; ++n) { WAITV(25); LBAR(); wB(n + 2); xA(n + 3); }
  WAITV(25); LBAR(); wB(8);
  WAITV(17); LBAR(); wB(9);
  WAITV(9); LBAR(); wB(10);
  BAR(); wB(11); wB(12);
  WAITV(27); LBAR();
  WAITV(18); LBAR(); xA(13); wB(13);
  WAITV(26); LBAR(); xA(14); wB(14);
#pragma unroll 1
  for (int n = 12; n < 15; ++n) { WAITV(34); LBAR(); xA(n + 3); wB(n + 3); }
  WAITV(34); LBAR();
  WAITV(17); LBAR(); issue2a(0, 2);
  WAITV(8); LBAR(); issue2a(1, 1);
  BAR();
  { const u32x2 tag = lds<u32x2>(sm + T_TAG); wait_flags<1, 2>(rws, W_F2 + id.ft * 128 + (lane & 7) * 16, tag); }
  STAMP(61);
  issue2b(0, 0); issue2b(1, 1);
  asm volatile("ds_write_b32 %0, %1" :: "v"((unsigned)(size_t)(sm + T_GO)), "v"(1u) : "memory");
  WAITV(9); LBAR(); issue2a(2, 0); issue2b(2, 2);
  { int sb = 0;
#pragma unroll 1
    for (int k = 3; k < 32; ++k) { WAITV(17); LBAR(); issue2a(k, 2 - sb); issue2b(k, sb); sb = sb == 2 ? 0 : sb + 1; } }
  const int g = id.ft & 7;
  const unsigned wb0 = W_WOF + (g >> 1) * 72 * 9 * 1024, wb1 = wb0 + (g & 1) * 5 * 1024, vq = lane * 16;
  const int rot = krot(id);
  auto issue3 = [&](auto lo_, auto hi_) { constexpr int lo = decltype(lo_)::value, hi = decltype(hi_)::value;
#pragma unroll
    for (int p0 = lo; p0 < hi; p0 += 2) {
      auto off = [&](int pc) { return (pc % 5 == 4 ? wb0 : wb1) + (18 * (pc / 25) * 9 + pc % 5) * 1024 + kwrap((pc % 25) / 5, rot) * 9216; };
      dma<SC1>(rws, sm + p0 * 1024 + lw * 1024, vq, lw ? off(p0 + 1) : off(p0)); } };
#define IC(n) std::integral_constant<int, n>{}
  WAITV(17); LBAR(); issue3(IC(0), IC(16)); issue3(IC(84), IC(100));
  WAITV(16); LBAR(); issue3(IC(32), IC(66));
  LBAR(); issue3(IC(16), IC(32)); issue3(IC(66), IC(84));
  unsigned sink = 0;
  auto wtouch = [&]() {
    constexpr int K0 = 5 + WD, NK = 18 - K0;
#pragma unroll
    for (int i = 0; i < (NK * 40 + 127) / 128; ++i) { const unsigned n = (i * 2 + lw) * 64 + lane, blk = n >> 1, kw = __umul24(blk, 13108) >> 16, t = blk - kw * 5;
      const unsigned o = n < NK * 40 ? (t == 4 ? wb0 : wb1) + ((18 * (kw & 3) + K0 + (kw >> 2)) * 9 + t) * 1024 + ((id.tp & 3) * 2 + (n & 1)) * 128 : 0xF0000000u;
      asm volatile("buffer_load_dword %0, %1, %2, 0 offen sc1" : "+v"(sink) : "v"(o), "s"(rws) : "memory"); } };
  if ((OPT_TG3 & 3) == 1) wtouch();
  LBAR();
  if ((OPT_TG3 & 3) == 2) { __builtin_amdgcn_s_sleep(2); wtouch(); }
  WAITV((OPT_TG3 & 3) == 2 ? (( 18 - 5 - WD) * 40 + 127) / 128 : 0); LBAR();
  asm volatile("s_waitcnt vmcnt(0)" : "+v"(sink) :: "memory"); LBAR();
}

DEV void role_X(const Params& P, LAS char* sm, const Ids id, const int xw STAMP_ARG) {
  int bcount = 0; (void)bcount;
  const int lane = id.lane;
  const rsrc_t rws = mkrs(P.ws, W_END);
  unsigned sink = 0;
  const u32x2 ev = __builtin_amdgcn_raw_buffer_load_b64(rws, W_EPOCH, 0, SC1);
  const int srow = id.tp * 128 + id.ft * 8 + 4 * xw + (lane >> 4);
  u32x4 xv[5];
  { const rsrc_t rx = mkrs(P.x, S * D * 2);
#pragma unroll
    for (int i = 0; i < 5; ++i) xv[i] = ld16<0>(rx, i < 4 || (lane & 8) == 0 ? srow * (D * 2) + (lane & 15) * 16 : 0xF0000000u, i * 256); }
  const u64 epoch = ((u64)__builtin_amdgcn_readfirstlane(ev.y) << 32) | __builtin_amdgcn_readfirstlane(ev.x);
  u32x2 tag; { const u64 t = ((epoch + 1) * 0x9E3779B97F4A7C15ull) | 1ull; tag.x = (unsigned)t; tag.y = (unsigned)(t >> 32); }
  { float s = 0.f, q = 0.f; unsigned ones = 0x3f803f80u; asm volatile("" : "+v"(ones));
#pragma unroll
    for (int i = 0; i < 5; ++i)
#pragma unroll
      for (int e = 0; e < 4; ++e) { s = dot2(xv[i][e], ones, s); q = dot2(xv[i][e], xv[i][e], q); }
    s += dpp<XOR1>(s); q += dpp<XOR1>(q); s += dpp<XOR2>(s); q += dpp<XOR2>(q); s += dpp<XOR7>(s); q += dpp<XOR7>(q); s += dpp<XOR8>(s); q += dpp<XOR8>(q);
    const float m = s * (1.0f / D), var = fmaxf(q * (1.0f / D) - m * m, 0.f) + 1e-5f;
    if ((lane & 15) == 0) st16<SC1>(seal(m, var, tag), rws, W_G0X + srow * 16, 0);
    if (xw == 0 && lane == 0) { *(LAS u32x2*)(sm + T_TAG) = tag; *(LAS u32x4*)(sm + T_CNT) = (u32x4){0u, 0u, 0u, 0u}; *(LAS u32x4*)(sm + T_CVF) = (u32x4){0u, 0u, 0u, 0u}; }
  }
  STAMP(0);
  BAR(); BAR();
  if (TWZ == 2) { const rsrc_t r = mkrs(P.win, D * F * 2); const unsigned n = xw * 64 + lane, m = id.b * 81 + n, row = m / 36;
    asm volatile("buffer_load_dword %0, %1, %2, 0 offen" : "+v"(sink) : "v"(n < 81 ? row * (F * 2) + (m - row * 36) * 128 : 0xF0000000u), "s"(r) : "memory"); }
  BAR(); BAR();
  LAS char* stg = sm + STG + xw * STG_W;
  const int unit = xw ? 256 + id.b : id.b, wks = unit >> 2, wu = unit & 3; const bool won = !xw || id.b < 32;
  const int tile = xw ? id.b : 256 + id.b - 32; const bool kon = xw || (id.b >= 32 && id.b < 64);
  const int tb = tile / 9, db = tile - tb * 9, cc = lane & 7, rg = lane >> 3;
  u32x4 kk[11], vv[8], sh[4];
  { const int row = 64 * xw + lane; const u64 t0 = __builtin_amdgcn_s_memrealtime();
    u32x4 gx = ld16<SC1>(rws, W_G0X + (id.tp * 128 + row) * 16, 0);
    BAR(); BAR();
    while (!__all(sealed(gx, tag)) && !expired(t0)) { __builtin_amdgcn_s_sleep(4); FENCE(); gx = ld16<SC1>(rws, W_G0X + (id.tp * 128 + row) * 16, 0); }
    const float var = __uint_as_float(gx.y), r = __builtin_amdgcn_rsqf(var);
    *(LAS unsigned*)(sm + T_M1 + row * 4) = gx.x; *(LAS float*)(sm + T_R1 + row * 4) = r;
    *(LAS float*)(sm + T_A0 + row * 4) = GC0 * r; *(LAS float*)(sm + T_A1 + row * 4) = GC1 * r * r * r; *(LAS float*)(sm + T_IR + row * 4) = var * r;
    if ((RT & 1) && xw == 0) code_touch(id, sink);
    WAITL(0); BAR();
  }
  STAMP(1);
  { const rsrc_t rk = mkrs(P.k, S * D * 2), rv = mkrs(P.v, S * D * 2), rs = mkrs(P.shk, D * 4 * 2), rwo = mkrs(P.wout, FH * D * 2);
    const int t0 = 64 * tb + 8 * rg; const unsigned col = (64 * db + 8 * cc) * 2;
    if (kon) {
#pragma unroll
      for (int i = 0; i < 11; ++i) kk[i] = ld16<0>(rk, t0 + i - 3 >= 0 ? (unsigned)(t0 + i - 3) * (D * 2) + col : 0xF0000000u, 0);
    }
    BAR();
    if (kon) {
#pragma unroll
      for (int i = 0; i < 8; ++i) vv[i] = ld16<0>(rv, t0 * (D * 2) + col, i * (D * 2));
#pragma unroll
      for (int i = 0; i < 4; ++i) sh[i] = ld16<0>(rs, col * 4, i * 16);
    }
    BAR();
    if (won) { const int fb0 = wks < 64 ? 144 * (wks >> 2) + 32 * (wks & 3) : 288 * (wks - 64) + 128, fst = wks < 64 ? 0 : 128;
#pragma unroll
      for (int i = 0; i < 9; ++i) { const int ci = 64 * i + lane, rho = __umul24(ci, 3641) >> 16, ch = ci - rho * 18;
        dma_hidden(rwo, stg + i * 1024, (fb0 + rho + (rho >> 4) * fst) * (D * 2) + wu * 288 + ch * 16); } }
    BAR();
    if (kon) {
#pragma unroll
      for (int i = 0; i < 4; ++i) asm volatile("" : "+v"(sh[i]), "+v"(vv[i]), "+v"(vv[4 + i]), "+v"(kk[i]), "+v"(kk[4 + i]), "+v"(kk[i == 3 ? 0 : 8 + i])); }
    if (OPT_TWSP) { const rsrc_t r = mkrs(P.wsp, S * S * 2);
#pragma unroll
      for (int i = 0; i < 2; ++i) asm volatile("buffer_load_dword %0, %1, %2, 0 offen" : "+v"(sink) : "v"(id.b * 32768 + ((2 * i + xw) * 64 + lane) * 128), "s"(r) : "memory"); }
  }
  BAR();
  u32x4 ko[8];
  auto kvstep = [&](int e) {
    if (kon) {
      float c[4], kf[11], y[8];
#pragma unroll
      for (int j = 0; j < 4; ++j) { const unsigned w = sh[e >> 1][(e & 1) * 2 + (j >> 1)]; c[j] = (j & 1) ? bhi(w) : blo(w); }
#pragma unroll
      for (int i = 0; i < 11; ++i) { const unsigned w = kk[i][e >> 1]; kf[i] = (e & 1) ? bhi(w) : blo(w); }
#pragma unroll
      for (int i = 0; i < 8; ++i) { const unsigned w = vv[i][e >> 1];
        float a = c[3] * kf[i]; asm volatile("" : "+v"(a)); a = fmaf(c[2], kf[i + 1], a); asm volatile("" : "+v"(a)); a = fmaf(c[1], kf[i + 2], a); asm volatile("" : "+v"(a)); a = fmaf(c[0], kf[i + 3], a);
        asm volatile("" : "+v"(a)); y[i] = a * ((e & 1) ? bhi(w) : blo(w)); asm volatile("" : "+v"(y[i])); }
      ko[e] = (u32x4){pk(y[7], y[6]), pk(y[5], y[4]), pk(y[3], y[2]), pk(y[1], y[0])};
      asm volatile("" : "+v"(ko[e]));
    } };
  { const int row = 64 * xw + lane;
    const f32x2 a = lds<f32x2>(sm + T_PART + row * 8) + lds<f32x2>(sm + T_PART + 1024 + row * 8);
    st16<SC1>(seal(a.x, a.y, tag), rws, W_G1S + ((id.tp * 16 + id.ft) * 128 + row) * 16, 0);
    BAR(); kvstep(0); BAR();
    u32x4 gs[16];
    auto gather = [&]() {
#pragma unroll
      for (int i = 0; i < 16; ++i) gs[i] = ld16<SC1>(rws, W_G1S + (id.tp * 16 * 128 + row) * 16, i * 2048); };
    unsigned bad = 0; float s = 0.f, q = 0.f;
    auto sum = [&](int lo, int hi) {
#pragma unroll
      for (int i = lo; i < hi; ++i) { bad |= (gs[i].x ^ gs[i].z ^ tag.x) | (gs[i].y ^ gs[i].w ^ tag.y); s += __uint_as_float(gs[i].x); q += __uint_as_float(gs[i].y); }
      asm volatile("" : "+v"(bad), "+v"(s), "+v"(q)); };
    gather(); BAR(); sum(0, 10); BAR();
    sum(10, 16);
    if (__builtin_expect(!__all(bad == 0), 0)) { const u64 t0 = __builtin_amdgcn_s_memrealtime();
      do { FENCE(); gather(); bad = 0; s = 0.f; q = 0.f; sum(0, 16); } while (!__all(bad == 0) && !expired(t0)); }
    const float m = s * (1.0f / FH), var = fmaxf(q * (1.0f / FH) - m * m, 0.f), r = __builtin_amdgcn_rsqf(var + 1e-5f);
    *(LAS float*)(sm + T_R2 + row * 4) = r; *(LAS float*)(sm + T_N2 + row * 4) = -m * r;
    asm volatile("s_waitcnt vmcnt(0) lgkmcnt(0)" : "+v"(sink) :: "memory"); BAR();
  }
  STAMP(2);
  kvstep(1); kvstep(2); BAR(); kvstep(3); BAR(); kvstep(4); BAR();
  if (xw == 0) set_flag(rws, W_F2 + (id.ft * 16 + id.tp) * 8, tag, lane);
  kvstep(5); kvstep(6); BAR(); kvstep(7);
  const unsigned h0a = ((const CAS unsigned*)P.ssmk)[id.b * S], h0b = ((const CAS unsigned*)P.ssmk)[(xw ? 2 * id.b + 1 : 512 + (id.b >> 2)) * (S / 2)];
  { u32x4 wf[9];
    if (won) {
      const int q = lane >> 4, qq = (lane & 15) >> 2, p = lane & 3;
      LAS char* base = stg + (4 * q + qq) * 288;
#pragma unroll
      for (int T = 0; T < 9; ++T) { const int cb = T < 4 ? 32 * (T >> 1) + 4 * (T & 1) : T == 4 ? 64 : 80 + 32 * ((T - 5) >> 1) + 4 * ((T - 5) & 1);
        LAS char* a = base + cb * 2 + p * (T == 4 ? 8 : 16);
        wf[T] = __builtin_bit_cast(u32x4, cat(ldtr(a), ldtr(a + 16 * 288))); }
      WAITL(0);
    }
    if (kon) {
#pragma unroll
      for (int e = 0; e < 8; ++e) *(LAS u32x4*)(stg + (8 * cc + e) * 128 + (((7 - rg) ^ cc) * 16)) = ko[e];
#pragma unroll
      for (int i = 0; i < 8; ++i) ko[i] = lds<u32x4>(stg + (8 * i + rg) * 128 + ((cc ^ i) * 16));
    }
    { const rsrc_t rh = mkrs(P.ssmk, D * S * 2);
      WAITL(0);
      auto filt = [&](int ci, unsigned h0) { const int chn = ci == 2 ? 512 + (id.b >> 2) : 2 * id.b + ci; LAS char* cb = sm + CV + ci * CV_CH;
        if (lane < 8) { *(LAS unsigned*)(cb + lane * 4) = 0u; *(LAS unsigned*)(cb + CV_O + lane * 4) = lane == 7 ? h0 << 16 : 0u; }
        if (lane < 38) *(LAS u32x4*)(cb + CV_U + 4096 + lane * 16) = (u32x4){0u, 0u, 0u, 0u};
        if (ci == 2) { *(LAS u32x4*)(cb + CV_U + 4704 + lane * 16) = (u32x4){0u, 0u, 0u, 0u}; *(LAS u32x4*)(cb + CV_U + 5728 + lane * 16) = (u32x4){0u, 0u, 0u, 0u}; }
#pragma unroll
        for (int i = 0; i < 4; ++i) { dma_hidden(rh, cb + 32 + i * 1024, chn * (S * 2) + i * 1024 + lane * 16); dma_hidden(rh, cb + CV_O + 32 + i * 1024, chn * (S * 2) + i * 1024 + lane * 16 + 2); } };
      if (xw == 0) filt(0, h0a);
      filt(2 - xw, h0b); }
    const u64 t0 = __builtin_amdgcn_s_memrealtime();
    while (*(volatile LAS unsigned*)(sm + T_GO) == 0u && !expired(t0)) __builtin_amdgcn_s_sleep(2);
    FENCE();
    if (won) {
#pragma unroll
      for (int T = 0; T < 9; ++T) st16<SC1>(wf[T], rws, W_WOF + lane * 16, ((wu * 72 + wks) * 9 + T) * 1024); }
    if (kon) {
#pragma unroll
      for (int i = 0; i < 8; ++i) st16<SC1>(ko[i], rws, W_KVT + (64 * db + rg) * (S * 2) + (S - 64 * (tb + 1)) * 2 + cc * 16, 8 * i * (S * 2)); }
  }
  BAR(); BAR(); BAR();
  BAR(); WAITV(0); BAR();
  if (xw == 0) set_flag(rws, W_F5 + id.b * 8, tag, lane);
  STAMP(3);
#pragma unroll 1
  for (int i = 0; i < 7; ++i) BAR();
  { u32x4 f0 = {tag.x, tag.y, tag.x, tag.y}, f1 = f0;
    if (xw == 0) { f0 = ld16<SC1>(rws, W_F5 + lane * 16, 0); f1 = ld16<SC1>(rws, W_F5 + lane * 16, 1024); }
    BAR(); BAR();
    if (!__all(flags_ok(f0, tag) && flags_ok(f1, tag))) wait_flags<2, 8>(rws, W_F5 + lane * 16, tag); }
  BAR();
  STAMP(4);
  { auto rows = [&](int ci) { const int chn = ci == 2 ? 512 + (id.b >> 2) : 2 * id.b + ci;
#pragma unroll
      for (int i = 0; i < 4; ++i) dma<SC1>(rws, sm + CV + ci * CV_CH + CV_U + i * 1024, lane * 16, W_KVT + chn * (S * 2) + i * 1024); };
    rows(xw); BAR(); if (xw == 0) rows(2); }
#pragma unroll 1
  for (int i = 0; i < 17; ++i) { if (i == 5) { WAITV(0); WAITL(0); } BAR(); }
  BAR();
  if (xw == 0) { set_flag(rws, W_F3 + (id.tp * 16 + id.ft) * 8, tag, lane); wait_flags<1, 1>(rws, W_F3 + id.tp * 128 + (lane & 7) * 16, tag); }
  STAMP(5);
  BAR();
  if (xw == 1 && (OPT_TG3 & 4)) {
#pragma unroll
    for (int i = 0; i < 4; ++i) { const unsigned n = i * 64 + lane, o = n < 240 ? W_HF + ((id.tp * 8 + 4 * (id.ft >> 3) + (n & 3)) * 72 + 18 * ((n >> 2) & 3) + 3 + (n >> 4)) * 1024 + (id.ft & 7) * 128 : 0xF0000000u;
      asm volatile("buffer_load_dword %0, %1, %2, 0 offen sc1" : "+v"(sink) : "v"(o), "s"(rws) : "memory"); } }
  if (xw == 0) {
    const u64 t0 = __builtin_amdgcn_s_memrealtime();
    while (*(volatile LAS unsigned*)(sm + T_CNT) != 4u && !expired(t0)) __builtin_amdgcn_s_sleep(2);
    FENCE();
    set_flag(rws, W_F4 + id.b * 8, tag, lane);
    const int g = id.ft & 7, d0 = 144 * (g >> 1) + 64 * (g & 1);
    { const rsrc_t rx = mkrs(P.x, S * D * 2);
#pragma unroll
      for (int i = 0; i < 10; ++i) { const unsigned ci = 64 * i + lane, r = __umul24(ci, 6554) >> 16; dma<0>(rx, sm + XTILE + i * 1024, (id.tp * 128 + 64 * (id.ft >> 3) + r) * (D * 2) + d0 * 2 + (ci - 10 * r) * 16, 0); } }
    __builtin_amdgcn_s_sleep(24); wait_flags<2, 12>(rws, W_F4 + lane * 16, tag);
#pragma unroll
    for (int i = 0; i < 10; ++i) dma<SC1>(rws, sm + YTILE + i * 1024, W_YT + (d0 + (lane >> 3)) * (S * 2) + (id.tp * 128 + 64 * (id.ft >> 3)) * 2 + (lane & 7) * 16, i * 8 * (S * 2));
    WAITV(0);
    asm volatile("ds_write_b32 %0, %1" :: "v"((unsigned)(size_t)(sm + T_YOK)), "v"(1u) : "memory");
    if (id.b == 0 && lane == 0) st8<SC1>((u32x2){(unsigned)(epoch + 1), (unsigned)((epoch + 1) >> 32)}, rws, W_EPOCH, 0);
  }
  STAMP(6);
  asm volatile("s_waitcnt vmcnt(0)" : "+v"(sink) :: "memory"); BAR();
}

DEV void role_C(const Params& P, LAS char* sm, const Ids id, const int wid STAMP_ARG) {
  int bcount = 0; (void)bcount;
  const int lane = id.lane, wt = wid >> 1, wf = wid & 1, c = lane & 15, q = lane >> 4;
  const rsrc_t rws = mkrs(P.ws, W_END);
  __builtin_amdgcn_s_setprio(1);
  unsigned dm = ((const CAS unsigned*)P.ssmd)[id.b], dsh = ((const CAS unsigned*)P.ssmd)[256 + (id.b >> 3)];
  asm volatile("" : "+s"(dm), "+s"(dsh));
  f32x4 acc[4][4], accx[2], csa[3];
  bf16x8 tf[2][4], ff[2][5], ones;
  LAS char *ta[2][2], *fb[2][2], *fx[2];
#pragma unroll
  for (int s = 0; s < 2; ++s) {
    const int o = (32 * (s ^ wt) + 16 * (q >> 1) + 4 * (q & 1) + (c >> 2)) * 288 + 8 * (c & 3);
    fx[s] = sm + BBASE + o + 256;
#pragma unroll
    for (int h = 0; h < 2; ++h) { ta[s][h] = sm + (64 * wt + 32 * (h ^ wf) + c) * 128 + (((q + 4 * (s ^ wt)) ^ ((c >> 1) & 7)) * 16); fb[s][h] = sm + BBASE + o + wf * 128 + (h ^ wt) * 64; }
  }
#pragma unroll
  for (int e = 0; e < 8; ++e) ones[e] = 0x3f80;
  asm volatile("" : "+v"(ones));
  auto rd = [&](auto mode, int b, int as, int bs, int s) {
    constexpr int MODE = decltype(mode)::value;
#pragma unroll
    for (int j = 0; j < 4; ++j) { LAS char* p = fb[s][j >> 1] + bs * BSLOT;
      ff[b][j] = MODE < 2 ? cat(ldtr(p + (j & 1) * 32), ldtr(p + (j & 1) * 32 + 8 * 288)) : lds<bf16x8>(p + (j & 1) * 1024); }
#pragma unroll
    for (int i = 0; i < 4; ++i) tf[b][i] = lds<bf16x8>(ta[s][i >> 1] + as * ASLOT + (i & 1) * 2048);
    ff[b][4] = MODE < 2 ? cat(ldtr(fx[s] + bs * BSLOT), ldtr(fx[s] + bs * BSLOT + 8 * 288)) : lds<bf16x8>(fx[s] + bs * BSLOT);
  };
  auto mm = [&](auto mode, int b, int lo, int hi) {
    constexpr int MODE = decltype(mode)::value;
#pragma unroll
    for (int n = 0; n < (MODE == 2 ? 18 : 21 - b); ++n) if (n >= lo && n < hi) {
      if (n < 16) { const int i = n >> 2, j = (i & 1) ? 3 - (n & 3) : (n & 3);
        acc[i][j] = MODE == 0 ? MFMA(tf[b][i], ff[b][j], acc[i][j]) : MFMA(ff[b][j], tf[b][i], acc[i][j]); }
      else if (n < 18) accx[n - 16] = MODE == 0 ? MFMA(tf[b][n - 16], ff[b][4], accx[n - 16]) : MFMA(ff[b][4], tf[b][n - 16], accx[n - 16]);
      else { const int j = n == 20 ? 4 : n - 18; csa[n - 18] = MODE == 0 ? MFMA(ones, ff[b][j], csa[n - 18]) : MFMA(ff[b][j], ones, csa[n - 18]); }
    }
  };
  auto stage = [&](auto mode, int as, int bs, bool drain) {
    constexpr int MODE = decltype(mode)::value;
    mm(mode, 1, 0, 4); SB();
    if (drain) WAITV(0);
    WAITL(0); BAR(); SB();
    rd(mode, 0, as, bs, 0); mm(mode, 1, 4, 21);
    if (MODE < 2) { for (int i = 0; i < 14; ++i) { SG(0x100, 1); SG(0x8, 1); } SG(0x8, 2); }
    else { for (int i = 0; i < 4; ++i) { SG(0x100, 1); SG(0x8, 1); SG(0x100, 1); SG(0x8, 2); } SG(0x100, 1); SG(0x8, 2); }
    SB();
    rd(mode, 1, as, bs, 1); mm(mode, 0, 0, 21);
    if (MODE < 2) { for (int i = 0; i < 7; ++i) { SG(0x100, 1); SG(0x8, 1); SG(0x100, 1); SG(0x8, 2); } }
    else { for (int i = 0; i < 9; ++i) { SG(0x100, 1); SG(0x8, 2); } }
    SB();
  };
  auto first = [&](auto mode, int as, int bs) { WAITL(0); BAR(); SB(); rd(mode, 0, as, bs, 0); rd(mode, 1, as, bs, 1); mm(mode, 0, 0, 21); SB(); };
  auto zero = [&]() {
#pragma unroll
    for (int i = 0; i < 4; ++i)
#pragma unroll
      for (int j = 0; j < 4; ++j) acc[i][j] = (f32x4){0.f, 0.f, 0.f, 0.f};
    accx[0] = accx[1] = csa[0] = csa[1] = csa[2] = (f32x4){0.f, 0.f, 0.f, 0.f}; };
  const std::integral_constant<int, 0> M0; const std::integral_constant<int, 1> M1; const std::integral_constant<int, 2> M2;
  const int jg0 = 4 * wf + 2 * wt, jg2 = 4 * wf + 2 - 2 * wt;

  zero();
  first(M0, 0, 0);
  STAMP(0);
#pragma unroll 1
  for (int it = 0; it < 2; ++it) { stage(M0, 1, 1, false); stage(M0, 2, 2, false); stage(M0, 3, 3, false); stage(M0, 0, 0, false); }
  mm(M0, 1, 18, 21); mm(M0, 1, 0, 8); SB();
  if (q == 0) { *(LAS float*)(sm + T_CS2 + (jg0 * 16 + c) * 4) = csa[0][0]; *(LAS float*)(sm + T_CS2 + (jg0 * 16 + 16 + c) * 4) = csa[1][0]; *(LAS float*)(sm + T_CS2 + (128 + 16 * wt + c) * 4) = csa[2][0]; }
  mm(M0, 1, 8, 18); SB();
  WAITL(0); BAR();
  STAMP(1);
  u32x4 z2p[2][4], z2x;
  { float cs[5];
    cs[0] = csa[0][0]; cs[1] = csa[1][0]; cs[2] = lds<float>(sm + T_CS2 + (jg2 * 16 + c) * 4); cs[3] = lds<float>(sm + T_CS2 + (jg2 * 16 + 16 + c) * 4);
    cs[4] = csa[2][0] + lds<float>(sm + T_CS2 + (144 - 16 * wt + c) * 4);
    float red[2][16];
#pragma unroll
    for (int i = 0; i < 4; ++i) {
      const int ro = (64 * wt + 32 * ((i >> 1) ^ wf) + 8 * q + 4 * (i & 1)) * 4;
      const f32x4 m = lds<f32x4>(sm + T_M1 + ro), a0 = lds<f32x4>(sm + T_A0 + ro), a1 = lds<f32x4>(sm + T_A1 + ro), ir = lds<f32x4>(sm + T_IR + ro);
      f32x4 su = {0.f, 0.f, 0.f, 0.f}, sq = {0.f, 0.f, 0.f, 0.f};
#pragma unroll
      for (int j = 0; j < (i < 2 ? 5 : 4); ++j) {
        const f32x4 z = gelu4((j < 4 ? acc[i][j] : accx[i]) - m * cs[j], a0, a1, ir);
        su = j ? su + z : z; sq = j ? sq + z * z : z * z;
        u32x4& dst = j < 4 ? z2p[i >> 1][j] : z2x;
        dst[2 * (i & 1)] = pk(z[0], z[1]); dst[2 * (i & 1) + 1] = pk(z[2], z[3]);
      }
#pragma unroll
      for (int e = 0; e < 4; ++e) { red[0][4 * i + e] = su[e]; red[1][4 * i + e] = sq[e]; }
    }
    float r8[2][8], r4[2][4];
#pragma unroll
    for (int st = 0; st < 2; ++st) {
#pragma unroll
      for (int e = 0; e < 8; e += 4)
        asm volatile("s_nop 1\n\t"
                     "v_add_f32_dpp %0, %4, %4 row_ror:8 row_mask:0xf bank_mask:0x3\n\tv_add_f32_dpp %1, %5, %5 row_ror:8 row_mask:0xf bank_mask:0x3\n\t"
                     "v_add_f32_dpp %2, %6, %6 row_ror:8 row_mask:0xf bank_mask:0x3\n\tv_add_f32_dpp %3, %7, %7 row_ror:8 row_mask:0xf bank_mask:0x3\n\t"
                     "v_add_f32_dpp %0, %8, %8 row_ror:8 row_mask:0xf bank_mask:0xc\n\tv_add_f32_dpp %1, %9, %9 row_ror:8 row_mask:0xf bank_mask:0xc\n\t"
                     "v_add_f32_dpp %2, %10, %10 row_ror:8 row_mask:0xf bank_mask:0xc\n\tv_add_f32_dpp %3, %11, %11 row_ror:8 row_mask:0xf bank_mask:0xc"
                     : "=&v"(r8[st][e]), "=&v"(r8[st][e + 1]), "=&v"(r8[st][e + 2]), "=&v"(r8[st][e + 3])
                     : "v"(red[st][e]), "v"(red[st][e + 1]), "v"(red[st][e + 2]), "v"(red[st][e + 3]), "v"(red[st][8 + e]), "v"(red[st][9 + e]), "v"(red[st][10 + e]), "v"(red[st][11 + e]));
      asm volatile("s_nop 1\n\t"
                   "v_add_f32_dpp %0, %4, %4 row_half_mirror row_mask:0xf bank_mask:0x5\n\tv_add_f32_dpp %1, %5, %5 row_half_mirror row_mask:0xf bank_mask:0x5\n\t"
                   "v_add_f32_dpp %2, %6, %6 row_half_mirror row_mask:0xf bank_mask:0x5\n\tv_add_f32_dpp %3, %7, %7 row_half_mirror row_mask:0xf bank_mask:0x5\n\t"
                   "v_add_f32_dpp %0, %8, %8 row_half_mirror row_mask:0xf bank_mask:0xa\n\tv_add_f32_dpp %1, %9, %9 row_half_mirror row_mask:0xf bank_mask:0xa\n\t"
                   "v_add_f32_dpp %2, %10, %10 row_half_mirror row_mask:0xf bank_mask:0xa\n\tv_add_f32_dpp %3, %11, %11 row_half_mirror row_mask:0xf bank_mask:0xa\n\t"
                   "v_add_f32_dpp %0, %0, %0 quad_perm:[1,0,3,2] row_mask:0xf bank_mask:0xf\n\tv_add_f32_dpp %1, %1, %1 quad_perm:[1,0,3,2] row_mask:0xf bank_mask:0xf\n\t"
                   "v_add_f32_dpp %2, %2, %2 quad_perm:[1,0,3,2] row_mask:0xf bank_mask:0xf\n\tv_add_f32_dpp %3, %3, %3 quad_perm:[1,0,3,2] row_mask:0xf bank_mask:0xf\n\t"
                   "v_add_f32_dpp %0, %0, %0 quad_perm:[2,3,0,1] row_mask:0xf bank_mask:0xf\n\tv_add_f32_dpp %1, %1, %1 quad_perm:[2,3,0,1] row_mask:0xf bank_mask:0xf\n\t"
                   "v_add_f32_dpp %2, %2, %2 quad_perm:[2,3,0,1] row_mask:0xf bank_mask:0xf\n\tv_add_f32_dpp %3, %3, %3 quad_perm:[2,3,0,1] row_mask:0xf bank_mask:0xf"
                   : "=&v"(r4[st][0]), "=&v"(r4[st][1]), "=&v"(r4[st][2]), "=&v"(r4[st][3])
                   : "v"(r8[st][0]), "v"(r8[st][1]), "v"(r8[st][2]), "v"(r8[st][3]), "v"(r8[st][4]), "v"(r8[st][5]), "v"(r8[st][6]), "v"(r8[st][7]));
    }
    if ((c & 3) == 0) { LAS char* pp = sm + T_PART + wf * 1024 + (64 * wt + 32 * ((c >> 3) ^ wf) + 8 * q + 4 * ((c >> 2) & 1)) * 8;
      *(LAS f32x4*)pp = (f32x4){r4[0][0], r4[1][0], r4[0][1], r4[1][1]}; *(LAS f32x4*)(pp + 16) = (f32x4){r4[0][2], r4[1][2], r4[0][3], r4[1][3]}; }
#pragma unroll
    for (int i = 0; i < 2; ++i) asm volatile("" : "+v"(z2p[i][0]), "+v"(z2p[i][1]), "+v"(z2p[i][2]), "+v"(z2p[i][3]));
    asm volatile("" : "+v"(z2x));
  }
  STAMP(2);
  zero();
  first(M1, 0, 1);
  stage(M1, 3, 2, false); stage(M1, 2, 3, false); stage(M1, 1, 0, false);
  stage(M1, 0, 1, false); stage(M1, 3, 2, false);
  STAMP(3);
  {
#pragma unroll
    for (int i = 0; i < 2; ++i) asm volatile("" : "+v"(z2p[i][0]), "+v"(z2p[i][1]), "+v"(z2p[i][2]), "+v"(z2p[i][3]));
    asm volatile("" : "+v"(z2x));
#pragma unroll
    for (int ip = 0; ip < 2; ++ip) {
      const int ro = (64 * wt + 32 * (ip ^ wf) + 8 * q) * 4;
      const f32x4 r0 = lds<f32x4>(sm + T_R2 + ro), r1 = lds<f32x4>(sm + T_R2 + ro + 16), n0 = lds<f32x4>(sm + T_N2 + ro), n1 = lds<f32x4>(sm + T_N2 + ro + 16);
      const unsigned so = W_ZF + ((id.ft * 64 + 4 * id.tp + 2 * wt + (ip ^ wf)) * 9) * 1024;
#pragma unroll
      for (int j = 0; j < (ip == 0 ? 5 : 4); ++j) { const u32x4 z = j < 4 ? z2p[ip][j] : z2x;
        const f32x4 a = (f32x4){blo(z[0]), bhi(z[0]), blo(z[1]), bhi(z[1])} * r0 + n0, b = (f32x4){blo(z[2]), bhi(z[2]), blo(z[3]), bhi(z[3])} * r1 + n1;
        st16<SC1>((u32x4){pk(a[0], a[1]), pk(a[2], a[3]), pk(b[0], b[1]), pk(b[2], b[3])}, rws, lane * 16, so + (j < 2 ? jg0 + j : j < 4 ? jg2 + j - 2 : 8) * 1024); }
    }
    SB();
  }
  unsigned braw[4];
  { const rsrc_t rb = mkrs(P.bsp, S * 2);
#pragma unroll
    for (int i = 0; i < 4; ++i) braw[i] = __builtin_amdgcn_raw_buffer_load_b16(rb, (id.tp * 128 + 64 * wt + prow(i ^ (2 * wf), c)) * 2, 0, 0); }
  STAMP(4);
  stage(M1, 2, 3, false); stage(M1, 1, 0, false); stage(M1, 0, 1, true);
  mm(M1, 1, 18, 21); mm(M1, 1, 0, 8); SB();
  if (c == 0) { *(LAS f32x4*)(sm + T_CS1 + (jg0 * 16 + 4 * q) * 4) = csa[0]; *(LAS f32x4*)(sm + T_CS1 + (jg0 * 16 + 16 + 4 * q) * 4) = csa[1]; *(LAS f32x4*)(sm + T_CS1 + (128 + 16 * wt + 4 * q) * 4) = csa[2]; }
  mm(M1, 1, 8, 18); SB();
  WAITL(0); BAR();
  STAMP(5);
  u32x2 z1p[4][4], z1x[2];
  { f32x4 cs[5];
    cs[0] = csa[0]; cs[1] = csa[1]; cs[2] = lds<f32x4>(sm + T_CS1 + (jg2 * 16 + 4 * q) * 4); cs[3] = lds<f32x4>(sm + T_CS1 + (jg2 * 16 + 16 + 4 * q) * 4);
    cs[4] = csa[2] + lds<f32x4>(sm + T_CS1 + (144 - 16 * wt + 4 * q) * 4);
#pragma unroll
    for (int i = 0; i < 4; ++i) { const int ro = (64 * wt + prow(i ^ (2 * wf), c)) * 4;
      const float m = lds<float>(sm + T_M1 + ro), a0 = lds<float>(sm + T_A0 + ro), a1 = lds<float>(sm + T_A1 + ro), ir = lds<float>(sm + T_IR + ro), bias = blo(braw[i]);
#pragma unroll
      for (int j = 0; j < (i < 2 ? 5 : 4); ++j) {
        f32x4& a = j < 4 ? acc[i][j] : accx[i];
        const f32x4 z = gelu4(a - m * cs[j], a0, a1, ir);
        (j < 4 ? z1p[i][j] : z1x[i]) = (u32x2){pk(z[0], z[1]), pk(z[2], z[3])};
        a = (f32x4){bias, bias, bias, bias};
      } }
#pragma unroll
    for (int s = 0; s < 2; ++s) { fx[s] = sm + B2BASE + (s * 9 + 8) * 1024 + lane * 16;
#pragma unroll
      for (int h = 0; h < 2; ++h) { fb[s][h] = sm + B2BASE + (s * 9 + (h ? jg2 : jg0)) * 1024 + lane * 16; ta[s][h] = sm + (64 * wt + 32 * (h ^ wf) + c) * 128 + (((q + 4 * s) ^ ((c >> 1) & 7)) * 16); } }
#pragma unroll
    for (int i = 0; i < 4; ++i) asm volatile("" : "+v"(z1p[i][0]), "+v"(z1p[i][1]), "+v"(z1p[i][2]), "+v"(z1p[i][3]), "+v"(acc[i][0]), "+v"(acc[i][1]), "+v"(acc[i][2]), "+v"(acc[i][3]));
    asm volatile("" : "+v"(z1x[0]), "+v"(z1x[1]), "+v"(accx[0]), "+v"(accx[1]));
  }
  STAMP(6);
  const int jq = id.b & 3;
  LAS char *cs_ = sm + CV + 2 * CV_CH, *cb = sm + CV + wt * CV_CH;
  LAS char *am[4], *um = cb + CV_U + (2032 - 16 * c + 8 * q) * 2 + 256 * wf - 3584;
#pragma unroll
  for (int i = 0; i < 4; ++i) { am[i] = cb + ((c & 1) ? 0 : CV_O) + (4 * q + ((c + 1) >> 1)) * 4 + 256 * wf + 1024 * i; asm volatile("" : "+v"(am[i])); }
  asm volatile("" : "+v"(um));
  LAS char *as[4], *u1, *u0;
  { const int da = (2 - wt) * CV_CH + 128 * wid - 256 * wf;
#pragma unroll
    for (int i = 0; i < 4; ++i) { as[i] = am[i] + da; asm volatile("" : "+v"(as[i])); }
    u1 = um + da + 512 * jq; u0 = um + da + 512 * (7 - jq); asm volatile("" : "+v"(u1), "+v"(u0)); }
  bf16x8 ca[2][8], cu[2][8];
  auto afrag = [&](LAS char* p) { const unsigned d0 = lds<unsigned>(p), d1 = lds<unsigned>(p + 4), d2 = lds<unsigned>(p + 8), d3 = lds<unsigned>(p + 12);
    return __builtin_bit_cast(bf16x8, (u32x4){d0, d1, d2, d3}); };
  auto lds_mn = [&](int b, int kk) {
#pragma unroll
    for (int e = 0; e < 8; ++e) { cu[b][e] = lds<bf16x8>(um + kk * 64 + 512 * (7 - e)); if (e == 0) ca[b][0] = afrag(am[0] + kk * 64); }
#pragma unroll
    for (int e = 1; e < 8; ++e) ca[b][e] = afrag(am[e >> 1] + kk * 64 + (e & 1) * 512); };
  first(M2, 2, 0);
  STAMP(7);
#pragma unroll 1
  for (int it = 0; it < 10; ++it) { stage(M2, 1, 1, false); stage(M2, 0, 2, false); stage(M2, 2, 0, false); }
  stage(M2, 1, 1, false);
#pragma unroll
  for (int i = 0; i < 4; ++i) asm volatile("" : "+v"(z1p[i][0]), "+v"(z1p[i][1]), "+v"(z1p[i][2]), "+v"(z1p[i][3]));
  asm volatile("" : "+v"(z1x[0]), "+v"(z1x[1]));
  { auto gate = [&](u32x2 z, f32x4 a) { const f32x4 h = (f32x4){blo(z.x), bhi(z.x), blo(z.y), bhi(z.y)} * a; return (u32x2){pk(h[0], h[1]), pk(h[2], h[3])}; };
    auto emit = [&](int i) { const unsigned so = W_HF + (id.tp * 8 + 4 * wt + (i ^ (2 * wf))) * 72 * 1024;
#pragma unroll
      for (int pp = 0; pp < 2; ++pp) { const u32x2 lo = gate(z1p[i][2 * pp], acc[i][2 * pp]), hi = gate(z1p[i][2 * pp + 1], acc[i][2 * pp + 1]);
        st16<SC1>((u32x4){lo.x, lo.y, hi.x, hi.y}, rws, lane * 16, so + (4 * id.ft + 2 * wf + (pp ^ wt)) * 1024); }
      if (i < 2) st8<SC1>(gate(z1x[i], accx[i]), rws, lane * 16 + (id.ft & 1) * 8, so + (64 + (id.ft >> 1)) * 1024);
      SB(); };
    mm(M2, 1, 0, 4); mm(M2, 1, 16, 17); mm(M2, 1, 4, 6); SB();
    STAMP(8);
    mm(M2, 1, 6, 8); mm(M2, 1, 17, 18); mm(M2, 1, 8, 16); emit(0);
    lds_mn(0, 0); emit(1); emit(2); emit(3); }
  const int g = id.ft & 7, gp = g & 1, half = id.ft >> 3, rot = krot(id);
  const unsigned wso = W_WOF + ((((EXP & 4) ? 0 : g >> 1) * 72 + 18 * wid) * 9) * 1024;
  u32x4 wr[WD][5];
  auto ldw = [&](int slot, int i) {
#pragma unroll
    for (int t = 0; t < 5; ++t) wr[slot][t] = ld16<(EXP & 8) ? 0 : SC1>(rws, lane * 16, wso + kwrap(i, rot) * 9216 + (t == 4 ? 4 : t + gp * 5) * 1024); };
  SB();
  SB(); BAR(); SB();
  STAMP(9);
  u32x4 hr[3][4];
  const unsigned hso = W_HF + (((EXP & 2) ? (id.b & 7) * 4 : id.tp * 8 + 4 * half) * 72 + 18 * wid) * 1024;
  auto ldh = [&](int slot, int i) {
#pragma unroll
    for (int t = 0; t < 4; ++t) hr[slot][t] = ld16<(EXP & 8) ? 0 : SC1>(rws, lane * 16, hso + (((unsigned)t ^ (wf ^ (2 * gp))) * 72 + kwrap(i, rot)) * 1024); };
  { const float dvm = wt ? bhi(dm) : blo(dm), dvs = (id.b & 4) ? bhi(dsh) : blo(dsh);
    f32x4 cm[8], s0 = {0.f, 0.f, 0.f, 0.f}, s1 = s0;
#pragma unroll
    for (int j = 0; j < 8; ++j) cm[j] = s0;
    bf16x8 sa[4], su[8];
    auto lds_sh = [&](int p) { const int kk = p >> 1, e0 = 4 * (p & 1);
#pragma unroll
      for (int e = 0; e < 4; ++e) { sa[e] = afrag(as[(e0 + e) >> 1] + kk * 64 + (e & 1) * 512); su[e] = lds<bf16x8>(u1 + kk * 64 + 512 * (e0 + e));
        if (e0 == 0) su[4 + e] = lds<bf16x8>(u0 + kk * 64 + 512 * e); } };
    auto mm_sh = [&](int p) {
#pragma unroll
      for (int e = 0; e < 4; ++e) { s1 = MFMA(sa[e], su[e], s1); if (!(p & 1)) s0 = MFMA(sa[e], su[4 + e], s0); } };
    auto mm_mn = [&](int b) {
#pragma unroll
      for (int e = 0; e < 8; ++e)
#pragma unroll
        for (int j = e; j < 8; ++j) cm[j] = MFMA(ca[b][e], cu[b][j - e], cm[j]); };
#define il(nr) do { for (int i_ = 0; i_ < nr; ++i_) { SG(0x8, 1); SG(0x100, 1); } SG(0x8, 44); SB(); } while (0)
    lds_sh(0); lds_mn(1, 1); mm_mn(0); mm_sh(0); il(40); STAMP(15);
    lds_sh(1); lds_mn(0, 2); mm_mn(1); mm_sh(1); il(36); STAMP(17);
    WAITV(0); BAR(); SB();
    STAMP(10);
    lds_sh(2); lds_mn(1, 3); mm_mn(0); mm_sh(2); il(40); STAMP(16);
    lds_sh(3); ldw(0, 5); ldw(1, 6); if (WD > 2) ldw(2, 7); mm_mn(1); mm_sh(3);
    for (int i_ = 0; i_ < 12; ++i_) { SG(0x8, 1); SG(0x100, 1); } for (int i_ = 0; i_ < 12; ++i_) { SG(0x8, 2); SG(0x20, 1); } SG(0x20, 3); SG(0x8, 8); SB();
    if (wid != 0) *(LAS f32x4*)(sm + CV_PS + (wid - 1) * 1024 + lane * 16) = s0;
    if (wid != 1) *(LAS f32x4*)(sm + CV_PS + (3 + (wid ? wid - 1 : 0)) * 1024 + lane * 16) = s1;
    LAS char* pm = cb;
    *(volatile LAS unsigned*)(sm + T_CVF + wid * 4) = 1u;
    { const u64 t0 = __builtin_amdgcn_s_memrealtime(); while (*(volatile LAS unsigned*)(sm + T_CVF + (wid ^ 1) * 4) == 0u && !expired(t0)) {} }
    FENCE();
#pragma unroll
    for (int j = 0; j < 4; ++j) { f32x4 lo = cm[j], hi = cm[4 + j]; asm volatile("" : "+v"(lo), "+v"(hi));
      *(LAS f32x4*)(pm + wf * 4096 + j * 1024 + lane * 16) = wf ? lo : hi; cm[j] = wf ? hi : lo; }
    SB(); WAITL(0); BAR(); SB();
    STAMP(11);
    ldh(0, 0); ldh(1, 1); ldh(2, 2);
    auto yout = [&](f32x4 a, int chn, LAS char* ub, int j, float dv) {
      const u32x2 u = lds<u32x2>(ub + CV_U + (2044 - 256 * j - 16 * c - 4 * q) * 2);
      const f32x4 y = a + (f32x4){bhi(u.y), blo(u.y), bhi(u.x), blo(u.x)} * dv;
      st8<SC1>((u32x2){pk(y[0], y[1]), pk(y[2], y[3])}, rws, (16 * c + 4 * q) * 2, W_YT + chn * (S * 2) + j * 512); };
#pragma unroll
    for (int j = 0; j < 4; ++j) yout(cm[j] + lds<f32x4>(pm + (wf ^ 1) * 4096 + j * 1024 + lane * 16), 2 * id.b + wt, cb, 4 * wf + j, dvm);
    if (wid < 2) { f32x4 ys = wf ? s1 : s0;
#pragma unroll
      for (int w = 0; w < 3; ++w) ys += lds<f32x4>(sm + CV_PS + (3 * wid + w) * 1024 + lane * 16);
      yout(ys, 512 + (id.b >> 2), cs_, wid ? 7 - jq : jq, dvs); }
    SB();
  }
  STAMP(19);
  f32x4 o[4][4], ox[2];
#pragma unroll
  for (int i = 0; i < 4; ++i)
#pragma unroll
    for (int t = 0; t < 4; ++t) o[i][t] = (f32x4){0.f, 0.f, 0.f, 0.f};
  ox[0] = ox[1] = (f32x4){0.f, 0.f, 0.f, 0.f};
  const int dp = 144 * (g >> 1) + 80 * gp, ds = 144 * (g >> 1) + 64, trow = 64 * half + prow(wid, c);
  const unsigned xo = (id.tp * 128 + trow) * (D * 2);
  f32x4 rs[5];
  { LAS char* wl = sm + wid * G3W + lane * 16;
    bf16x8 wq[2][5];
#pragma unroll
    for (int t = 0; t < 5; ++t) wq[0][t] = lds<bf16x8>(wl + t * 1024);
#pragma unroll
    for (int i = 0; i < 18; ++i) {
      if (i < 4) {
#pragma unroll
        for (int t = 0; t < 5; ++t) wq[(i + 1) & 1][t] = lds<bf16x8>(wl + ((i + 1) * 5 + t) * 1024); }
#pragma unroll
      for (int r = 0; r < 4; ++r) { const bf16x8 hf = __builtin_bit_cast(bf16x8, hr[i % 3][r]);
#pragma unroll
        for (int t = 0; t < 5; ++t) if (t < 4 || r < 2) { const int tt = (r & 1) && t < 4 ? 3 - t : t;
          const bf16x8 wf_ = i < 5 ? wq[i & 1][tt] : __builtin_bit_cast(bf16x8, wr[(i - 5) % WD][tt]);
          if (tt < 4) o[r][tt] = MFMA(wf_, hf, o[r][tt]); else ox[r] = MFMA(wf_, hf, ox[r]); } }
      SB();
      if (i == 0 || i == 2 || i == 4 || i == 7 || i == 10 || i == 13 || i == 15) STAMP(20 + i);
      if (i + 3 < 18) ldh(i % 3, i + 3);
      if (i >= 5 && i + WD < 18) ldw((i - 5) % WD, i + WD);
      if (i == 2) { WAITV(12); if (lane == 0) __hip_atomic_fetch_add((LAS unsigned*)(sm + T_CNT), 1u, __ATOMIC_RELAXED, __HIP_MEMORY_SCOPE_WORKGROUP); }
      if (i == 16) {
        const u64 t0 = __builtin_amdgcn_s_memrealtime();
        while (*(volatile LAS unsigned*)(sm + T_YOK) == 0u && !expired(t0)) __builtin_amdgcn_s_sleep(1);
        FENCE();
        const float m = lds<float>(sm + T_M1 + trow * 4), r = lds<float>(sm + T_R1 + trow * 4);
        LAS char* yb = sm + YTILE + (c >> 2) * 128 + (32 * wt + 4 * wf + 8 * (c & 3)) * 2;
        auto res = [&](u32x2 xv, bf16x4 yv) { const u32x2 yu = __builtin_bit_cast(u32x2, yv);
          const f32x4 xf = {blo(xv.x), bhi(xv.x), blo(xv.y), bhi(xv.y)}, yf = {blo(yu.x), bhi(yu.x), blo(yu.y), bhi(yu.y)};
          return xf + (xf - m) * r * yf; };
        const int yp = 16 * gp, ysh = 64 * (gp ^ 1);
        LAS char* xb = sm + XTILE + prow(wid, c) * 160;
        const u32x4 x0 = lds<u32x4>(xb + yp * 2 + q * 16), x1 = lds<u32x4>(xb + yp * 2 + 64 + q * 16); const u32x2 x2 = lds<u32x2>(xb + ysh * 2 + q * 8);
        rs[0] = res(x0.xy, ldtr(yb + (yp + 8 * q) * 128)); rs[1] = res(x0.zw, ldtr(yb + (yp + 8 * q + 4) * 128));
        rs[2] = res(x1.xy, ldtr(yb + (yp + 32 + 8 * q) * 128)); rs[3] = res(x1.zw, ldtr(yb + (yp + 36 + 8 * q) * 128));
        rs[4] = res(x2, ldtr(yb + (ysh + 4 * q) * 128));
        asm volatile("" : "+v"(rs[0]), "+v"(rs[1]), "+v"(rs[2]), "+v"(rs[3]), "+v"(rs[4])); }
      SB();
    }
  }
  STAMP(12);
  const bool own0 = wt == gp;
  { LAS char* pw = sm + wid * G3W + lane * 16;
    auto put = [&](int r) { LAS char* p = pw + ((unsigned)r ^ (wf ^ (2 * gp))) * 5120;
#pragma unroll
      for (int t = 0; t < 4; ++t) *(LAS f32x4*)(p + t * 1024) = o[r][t];
      if (r < 2) *(LAS f32x4*)(p + 4096) = ox[r]; };
    put(1); put(3);
    if (own0) { put(2);
#pragma unroll
      for (int t = 0; t < 4; ++t) rs[t] += o[0][t];
      rs[4] += ox[0]; }
    else { put(0);
#pragma unroll
      for (int t = 0; t < 4; ++t) rs[t] += o[2][t]; }
  }
  SB(); WAITL(0); BAR(); SB();
  STAMP(13);
  { f32x4 pt[3][5];
#pragma unroll
    for (int t = 0; t < 5; ++t)
#pragma unroll
      for (int w = 0; w < 3; ++w) pt[w][t] = lds<f32x4>(sm + (wid ^ (w + 1)) * G3W + wid * 5120 + lane * 16 + t * 1024);
    const rsrc_t ro = mkrs(P.out, S * D * 2);
    auto fin = [&](int t) { const f32x4 v = (rs[t] + pt[0][t]) + (pt[1][t] + pt[2][t]); return (u32x2){pk(v[0], v[1]), pk(v[2], v[3])}; };
#pragma unroll
    for (int pp = 0; pp < 2; ++pp) { const u32x2 lo = fin(2 * pp), hi = fin(2 * pp + 1); st16<OAUX>((u32x4){lo.x, lo.y, hi.x, hi.y}, ro, xo + q * 16, dp * 2 + 64 * pp); }
    const u32x2 ls = fin(4);
    if (own0) st8<OAUX>(ls, ro, xo + q * 8, ds * 2);
  }
  STAMP(14);
}
}

extern "C" __global__ __launch_bounds__(512, 2) void h3_gmlp_fused(const Params P) {
  extern __shared__ __attribute__((aligned(16))) char smem_[];
  LAS char* sm = (LAS char*)smem_;
  const int wid = __builtin_amdgcn_readfirstlane(threadIdx.x >> 6);
  Ids id; id.b = blockIdx.x; id.lane = threadIdx.x & 63;
  const int xcd = id.b & 7, loc = id.b >> 3;
  id.tp = (xcd >> 1) * 4 + (loc >> 3); id.ft = (xcd & 1) * 8 + (loc & 7);
  if (wid < 4) { unsigned sink = 0;
    if (wid == 0) code_touch(id, sink);
    else if (OPT_TWIN) { const rsrc_t r = mkrs(P.win, D * F * 2); const unsigned n = (wid - 1) * 64 + id.lane;
      if (OPT_TWIN > 1) __builtin_amdgcn_s_sleep(OPT_TWIN);
      unsigned o;
      if (TWZ == 0) o = n < 162 ? id.b * 20736 + n * 128 : 0xF0000000u;
      else if (TWZ == 3) { const unsigned m = id.b * 63 + n, row = m / 36; o = n < 63 ? (row + 128) * (F * 2) + FH * 2 + (m - row * 36) * 128 : 0xF0000000u; }
      else { const unsigned m = id.b * 81 + n, row = m / 36; o = n < 81 ? row * (F * 2) + FH * 2 + (m - row * 36) * 128 : 0xF0000000u; }
      asm volatile("buffer_load_dword %0, %1, %2, 0 offen" : "+v"(sink) : "v"(o), "s"(r) : "memory"); }
    asm volatile("s_waitcnt vmcnt(0)" : "+v"(sink) :: "memory"); }
  if (__builtin_expect(wid == 4 || wid == 5, 1)) role_L(P, sm, id, wid - 4 STAMP_PASS);
  else if (wid >= 6) role_X(P, sm, id, wid - 6 STAMP_PASS);
  else role_C(P, sm, id, wid STAMP_PASS);
}
extern "C" __global__ void h3_gmlp_end() {}

extern "C" void kernel_launch(void* const* d_in, const int* in_sizes, int n_in, void* d_out, int out_size, void* d_ws, size_t ws_size, hipStream_t stream) {
  Params p;
  p.x = (const u16*)d_in[0]; p.k = (const u16*)d_in[1]; p.v = (const u16*)d_in[2]; p.shk = (const u16*)d_in[3]; p.ssmk = (const u16*)d_in[4]; p.ssmd = (const u16*)d_in[5];
  p.win = (const u16*)d_in[7]; p.wsp = (const u16*)d_in[8]; p.bsp = (const u16*)d_in[9]; p.wout = (const u16*)d_in[10];
  p.out = (u16*)d_out; p.ws = (char*)d_ws;
  (void)hipFuncSetAttribute((const void*)h3_gmlp_fused, hipFuncAttributeMaxDynamicSharedMemorySize, LDS_BYTES);
  h3_gmlp_fused<<<256, 512, LDS_BYTES, stream>>>(p);
}
```

```cpp
#include <hip/hip_runtime.h>
#include <hip/hip_bf16.h>
#include <type_traits>

namespace {
typedef unsigned short u16;
typedef unsigned long long u64;
typedef short bf16x8 __attribute__((ext_vector_type(8)));
typedef short bf16x4 __attribute__((ext_vector_type(4)));
typedef __bf16 bf2 __attribute__((ext_vector_type(2)));
typedef float f32x2 __attribute__((ext_vector_type(2)));
typedef float f32x4 __attribute__((ext_vector_type(4)));
typedef unsigned u32x2 __attribute__((ext_vector_type(2)));
typedef unsigned u32x4 __attribute__((ext_vector_type(4)));
typedef __amdgpu_buffer_rsrc_t rsrc_t;
#define LAS __attribute__((address_space(3)))
#define CAS __attribute__((address_space(4)))
#define DEV __device__ __forceinline__

constexpr int S = 2048, D = 576, F = 4608, FH = 2304;
constexpr int ASLOT = 16384, BSLOT = 18432, BBASE = 4 * ASLOT;
constexpr int STG = 139264, STG_W = 9216;
constexpr int T_M1 = 157952, T_R1 = T_M1 + 512, T_A0 = T_R1 + 512, T_A1 = T_A0 + 512, T_IR = T_A1 + 512;
constexpr int T_CS2 = T_IR + 512, T_CS1 = T_CS2;
constexpr int T_PART = T_CS1 + 640, T_N2 = T_PART, T_R2 = T_N2 + 512;
constexpr int T_TAG = T_PART + 2048, T_CNT = T_TAG + 16, T_GO = T_CNT + 4, T_YOK = T_CNT + 8, T_CVF = T_CNT + 16;
constexpr int LDS_BYTES = 163840;
static_assert(T_CNT + 32 <= LDS_BYTES, "LDS map");
constexpr int G3W = 25600;
constexpr int B2BASE = 3 * ASLOT;
constexpr int CV = B2BASE + 3 * BSLOT, CV_CH = 13056, CV_O = 4160, CV_U = 8320;
constexpr int CV_PS = CV + 2 * CV_CH + 15104;
constexpr int YTILE = 122880, XTILE = 133120;
constexpr int W_EPOCH = 0, W_F2 = 4096, W_F3 = 8192, W_F4 = 12288, W_F5 = 16384;
constexpr int W_G0X = 0x8000, W_G1F = 0x10000, W_G1S = 0x60000;
constexpr int W_KVT = 1 << 20, W_YT = 4 << 20, W_WOF = 7 << 20, W_ZF = 10 << 20, W_HF = 20 << 20, W_END = 30 << 20;
constexpr int SC1 = 16;
#define OPT_TWIN 12
#define OPT_TWSP 1
#define OPT_TG3 0
#define EXP 0
#define RH 0
#define RW 0
#define KB 1
#define PR 0
#define XM 0
#define RT 0
#define TWZ 0
#define WD 3

#define BAR0() do { asm volatile("" ::: "memory"); __builtin_amdgcn_s_barrier(); asm volatile("" ::: "memory"); } while (0)
#define BAR() BAR0()
#define SB() __builtin_amdgcn_sched_barrier(0)
#define SG(m, n) __builtin_amdgcn_sched_group_barrier(m, n, 0)
#define WAITV(n) asm volatile("s_waitcnt vmcnt(%0)" ::"i"(n) : "memory")
#define WAITL(n) asm volatile("s_waitcnt lgkmcnt(%0)" ::"i"(n) : "memory")
#define FENCE() asm volatile("" ::: "memory")
#define MFMA(a, b, c) __builtin_amdgcn_mfma_f32_16x16x32_bf16(a, b, c, 0, 0, 0)
#define STAMP(i) do {} while (0)
#define LBAR() BAR()
#define STAMP_ARG
#define STAMP_PASS

struct Params {
  const u16 *x, *k, *v, *shk, *ssmk, *ssmd, *win, *wsp, *bsp, *wout;
  u16* out; char* ws;
};

DEV rsrc_t mkrs(const void* p, unsigned bytes) { return __builtin_amdgcn_make_buffer_rsrc((void*)p, 0, bytes, 0x00020000); }
template <int AUX> DEV void dma(rsrc_t rs, LAS char* l, unsigned voff, unsigned soff) { __builtin_amdgcn_raw_ptr_buffer_load_lds(rs, (LAS void*)l, 16, voff, soff, 0, AUX); }
DEV void dma_hidden(rsrc_t rs, LAS char* l, unsigned voff) { unsigned keep;
  asm volatile("s_mov_b32 %0, m0\n\ts_mov_b32 m0, %3\n\ts_nop 0\n\tbuffer_load_dwordx4 %1, %2, 0 offen lds\n\ts_mov_b32 m0, %0" : "=&s"(keep) : "v"(voff), "s"(rs), "s"(__builtin_amdgcn_readfirstlane((unsigned)(size_t)l)) : "memory"); }
template <int AUX> DEV u32x4 ld16(rsrc_t rs, unsigned voff, unsigned soff) { return __builtin_amdgcn_raw_buffer_load_b128(rs, voff, soff, AUX); }
template <int AUX> DEV void st16(u32x4 v, rsrc_t rs, unsigned voff, unsigned soff) { __builtin_amdgcn_raw_buffer_store_b128(v, rs, voff, soff, AUX); }
template <int AUX> DEV void st8(u32x2 v, rsrc_t rs, unsigned voff, unsigned soff) { __builtin_amdgcn_raw_buffer_store_b64(v, rs, voff, soff, AUX); }
template <class T> DEV T lds(LAS char* p) { return *(LAS T*)p; }
DEV bf16x4 ldtr(LAS char* p) { return __builtin_amdgcn_ds_read_tr16_b64_v4i16((LAS bf16x4*)p); }
DEV bf16x8 cat(bf16x4 a, bf16x4 b) { return __builtin_shufflevector(a, b, 0, 1, 2, 3, 4, 5, 6, 7); }
DEV float blo(unsigned u) { return __uint_as_float(u << 16); }
DEV float bhi(unsigned u) { return __uint_as_float(u & 0xffff0000u); }
DEV unsigned pk(float a, float b) { return __builtin_bit_cast(unsigned, __builtin_convertvector((f32x2){a, b}, bf2)); }
DEV float dot2(unsigned a, unsigned b, float c) { return __builtin_amdgcn_fdot2_f32_bf16(__builtin_bit_cast(bf2, a), __builtin_bit_cast(bf2, b), c, false); }
template <int CTRL> DEV float dpp(float x) { return __builtin_bit_cast(float, __builtin_amdgcn_mov_dpp(__builtin_bit_cast(int, x), CTRL, 0xf, 0xf, true)); }
constexpr int XOR1 = 0xB1, XOR2 = 0x4E, XOR7 = 0x141, XOR8 = 0x128;
constexpr float GC0 = -2.30220819f, GC1 = -0.10294324f;
template <class T> DEV f32x2 gelu2(f32x2 a, T a0, T a1, T ir) {
  const f32x2 g = a * (a * a * a1 + a0);
  f32x2 e; e.x = __builtin_amdgcn_exp2f(g.x); e.y = __builtin_amdgcn_exp2f(g.y);
  e = e * ir + ir;
  f32x2 r; r.x = __builtin_amdgcn_rcpf(e.x); r.y = __builtin_amdgcn_rcpf(e.y);
  return a * r;
}
DEV f32x4 gelu4(f32x4 a, f32x4 a0, f32x4 a1, f32x4 ir) { const f32x2 p = gelu2(a.xy, a0.xy, a1.xy, ir.xy), q = gelu2(a.zw, a0.zw, a1.zw, ir.zw); return (f32x4){p.x, p.y, q.x, q.y}; }
DEV f32x4 gelu4(f32x4 a, float a0, float a1, float ir) { const f32x2 p = gelu2(a.xy, a0, a1, ir), q = gelu2(a.zw, a0, a1, ir); return (f32x4){p.x, p.y, q.x, q.y}; }
DEV u32x4 seal(float a, float b, u32x2 tag) { const unsigned ua = __float_as_uint(a), ub = __float_as_uint(b); return (u32x4){ua, ub, ua ^ tag.x, ub ^ tag.y}; }
DEV bool sealed(u32x4 g, u32x2 tag) { return (g.x ^ g.z) == tag.x && (g.y ^ g.w) == tag.y; }
DEV bool expired(u64 t0) { return __builtin_amdgcn_s_memrealtime() - t0 > 2000000ull; }
template <int NL, int NAP> DEV void wait_flags(rsrc_t ws, unsigned voff, u32x2 tag) {
  const u64 t0 = __builtin_amdgcn_s_memrealtime();
  for (;;) {
    bool ok = true;
#pragma unroll
    for (int i = 0; i < NL; ++i) { const u32x4 f = ld16<SC1>(ws, voff, i * 1024); ok &= f.x == tag.x && f.y == tag.y && f.z == tag.x && f.w == tag.y; }
    if (__all(ok) || expired(t0)) break;
    __builtin_amdgcn_s_sleep(NAP); FENCE();
  }
  FENCE();
}
DEV bool flags_ok(u32x4 f, u32x2 tag) { return f.x == tag.x && f.y == tag.y && f.z == tag.x && f.w == tag.y; }
DEV int prow(int T, int c) { return 32 * (T >> 1) + 8 * (c >> 2) + 4 * (T & 1) + (c & 3); }
DEV void set_flag(rsrc_t ws, unsigned off, u32x2 tag, int lane) { if (lane == 0) st8<SC1>(tag, ws, off, 0); }

struct Ids { int b, tp, ft, lane; };
DEV void code_touch(const Ids& id, unsigned& sink) {
  unsigned lo, hi, bytes;
  asm volatile("s_getpc_b64 vcc\n1:\n\ts_sub_u32 %0, vcc_lo, 1b-h3_gmlp_fused\n\ts_subb_u32 %1, vcc_hi, 0\n\ts_mov_b32 %2, h3_gmlp_end-h3_gmlp_fused" : "=s"(lo), "=s"(hi), "=s"(bytes) :: "vcc", "scc");
  const rsrc_t rc = mkrs((const void*)(((u64)hi << 32) | lo), bytes);
  asm volatile("buffer_load_dword %0, %1, %2, 0 offen" : "+v"(sink) : "v"((id.lane < 20 ? id.lane * 32 + (id.b >> 3) : 0x100000) * 128), "s"(rc) : "memory");
}
DEV int krot(const Ids& id) { return (RH * (id.ft & 7) + ((RW * (id.tp & 3)) >> 1)) % 18; }
DEV int kwrap(int i, int r) { const int k = i + r; return k >= 18 ? k - 18 : k; }

DEV void role_L(const Params& P, LAS char* sm, const Ids id, const int lw STAMP_ARG) {
  int bcount = 0; (void)bcount;
  const int lane = id.lane, rr = lane >> 3, sl = lane & 7;
  const rsrc_t rx = mkrs(P.x, S * D * 2), rwin = mkrs(P.win, D * F * 2);
  const unsigned swz = (sl ^ (4 * lw + (rr >> 1))) * 16, prr = 16 * lw + 8 * (rr >> 2) + (rr & 3);
  const unsigned vx = prr * (D * 2) + swz;
  unsigned vb[9];
  auto issue1a = [&](int slot, int i, int ks) { dma<0>(rx, sm + slot * ASLOT + lw * 1024 + i * 2048, vx, (id.tp * 128 + 32 * (i >> 1) + 4 * (i & 1)) * (D * 2) + ks * 128); };
  auto issue1b = [&](int slot, int i, int ks, int col) { dma<0>(rwin, sm + BBASE + slot * BSLOT + lw * 1024 + i * 2048, vb[i], ks * 64 * (F * 2) + (col + id.ft * 144) * 2); };
  STAMP(56);
#pragma unroll
  for (int i = 0; i < 9; ++i) {
    if (i < 8) issue1a(0, i, 0);
    if (i == 0) STAMP(57);
    SB();
    const unsigned ci = 64 * (2 * i + lw) + lane, rho = __umul24(ci, 3641) >> 16;
    vb[i] = __umul24(rho, F * 2 - 288) + 16 * ci + __mul24((int)(rho & 4) - (int)((rho >> 1) & 4), F * 2);
    issue1b(0, i, 0, FH);
    SB();
  }
  auto xA = [&](int n) {
#pragma unroll
    for (int i = 0; i < 8; ++i) issue1a(n < 9 ? n & 3 : (9 - n) & 3, i, n < 9 ? n : 17 - n); };
  auto wB = [&](int n) {
#pragma unroll
    for (int i = 0; i < 9; ++i) issue1b(n & 3, i, n < 9 ? n : 17 - n, n < 9 ? FH : 0); };
  STAMP(58);
  xA(1); wB(1); STAMP(59); xA(2);
  STAMP(60);
  const rsrc_t rwsp = mkrs(P.wsp, S * S * 2), rws = mkrs(P.ws, W_END);
  const unsigned vw = prr * (S * 2) + swz, vl = lane * 16 + lw * 1024;
  auto issue2a = [&](int k, int slot) { LAS char* a = sm + slot * ASLOT + lw * 1024; const unsigned sa = id.tp * 128 * (S * 2) + k * 128;
#pragma unroll
    for (int i = 0; i < 8; ++i) dma<0>(rwsp, a + i * 2048, vw, sa + (32 * (i >> 1) + 4 * (i & 1)) * (S * 2)); };
  auto issue2b = [&](int k, int slot) { LAS char* bb = sm + B2BASE + slot * BSLOT + lw * 1024; const unsigned sb = W_ZF + (id.ft * 576 + k * 18) * 1024;
#pragma unroll
    for (int i = 0; i < 9; ++i) dma<SC1>(rws, bb + i * 2048, vl, sb + i * 2048); };
#pragma unroll 1
  for (int n = 0; n < 6; ++n) { WAITV(25); LBAR(); wB(n + 2); xA(n + 3); }
  WAITV(25); LBAR(); wB(8);
  WAITV(17); LBAR(); wB(9);
  WAITV(9); LBAR(); wB(10);
  BAR(); wB(11); wB(12);
  WAITV(27); LBAR();
  WAITV(18); LBAR(); xA(13); wB(13);
  WAITV(26); LBAR(); xA(14); wB(14);
#pragma unroll 1
  for (int n = 12; n < 15; ++n) { WAITV(34); LBAR(); xA(n + 3); wB(n + 3); }
  WAITV(34); LBAR();
  WAITV(17); LBAR(); issue2a(0, 2);
  WAITV(8); LBAR(); issue2a(1, 1);
  BAR();
  { const u32x2 tag = lds<u32x2>(sm + T_TAG); wait_flags<1, 2>(rws, W_F2 + id.ft * 128 + (lane & 7) * 16, tag); }
  STAMP(61);
  issue2b(0, 0); issue2b(1, 1);
  asm volatile("ds_write_b32 %0, %1" :: "v"((unsigned)(size_t)(sm + T_GO)), "v"(1u) : "memory");
  WAITV(9); LBAR(); issue2a(2, 0); issue2b(2, 2);
  { int sb = 0;
#pragma unroll 1
    for (int k = 3; k < 32; ++k) { WAITV(17); LBAR(); issue2a(k, 2 - sb); issue2b(k, sb); sb = sb == 2 ? 0 : sb + 1; } }
  const int g = id.ft & 7;
  const unsigned wb0 = W_WOF + (g >> 1) * 72 * 9 * 1024, wb1 = wb0 + (g & 1) * 5 * 1024, vq = lane * 16;
  const int rot = krot(id);
  auto issue3 = [&](auto lo_, auto hi_) { constexpr int lo = decltype(lo_)::value, hi = decltype(hi_)::value;
#pragma unroll
    for (int p0 = lo; p0 < hi; p0 += 2) {
      auto off = [&](int pc) { return (pc % 5 == 4 ? wb0 : wb1) + (18 * (pc / 25) * 9 + pc % 5) * 1024 + kwrap((pc % 25) / 5, rot) * 9216; };
      dma<SC1>(rws, sm + p0 * 1024 + lw * 1024, vq, lw ? off(p0 + 1) : off(p0)); } };
#define IC(n) std::integral_constant<int, n>{}
  WAITV(17); LBAR(); issue3(IC(0), IC(16)); issue3(IC(84), IC(100));
  WAITV(16); LBAR(); issue3(IC(32), IC(66));
  LBAR(); issue3(IC(16), IC(32)); issue3(IC(66), IC(84));
  unsigned sink = 0;
  auto wtouch = [&]() {
    constexpr int K0 = 5 + WD, NK = 18 - K0;
#pragma unroll
    for (int i = 0; i < (NK * 40 + 127) / 128; ++i) { const unsigned n = (i * 2 + lw) * 64 + lane, blk = n >> 1, kw = __umul24(blk, 13108) >> 16, t = blk - kw * 5;
      const unsigned o = n < NK * 40 ? (t == 4 ? wb0 : wb1) + ((18 * (kw & 3) + K0 + (kw >> 2)) * 9 + t) * 1024 + ((id.tp & 3) * 2 + (n & 1)) * 128 : 0xF0000000u;
      asm volatile("buffer_load_dword %0, %1, %2, 0 offen sc1" : "+v"(sink) : "v"(o), "s"(rws) : "memory"); } };
  if ((OPT_TG3 & 3) == 1) wtouch();
  LBAR();
  if ((OPT_TG3 & 3) == 2) { __builtin_amdgcn_s_sleep(2); wtouch(); }
  WAITV((OPT_TG3 & 3) == 2 ? (( 18 - 5 - WD) * 40 + 127) / 128 : 0); LBAR();
  asm volatile("s_waitcnt vmcnt(0)" : "+v"(sink) :: "memory"); LBAR();
}

DEV void role_X(const Params& P, LAS char* sm, const Ids id, const int xw STAMP_ARG) {
  int bcount = 0; (void)bcount;
  const int lane = id.lane;
  const rsrc_t rws = mkrs(P.ws, W_END);
  unsigned sink = 0;
  const u32x2 ev = __builtin_amdgcn_raw_buffer_load_b64(rws, W_EPOCH, 0, SC1);
  const int srow = id.tp * 128 + id.ft * 8 + 4 * xw + (lane >> 4);
  u32x4 xv[5];
  { const rsrc_t rx = mkrs(P.x, S * D * 2);
#pragma unroll
    for (int i = 0; i < 5; ++i) xv[i] = ld16<0>(rx, i < 4 || (lane & 8) == 0 ? srow * (D * 2) + (lane & 15) * 16 : 0xF0000000u, i * 256); }
  const u64 epoch = ((u64)__builtin_amdgcn_readfirstlane(ev.y) << 32) | __builtin_amdgcn_readfirstlane(ev.x);
  u32x2 tag; { const u64 t = ((epoch + 1) * 0x9E3779B97F4A7C15ull) | 1ull; tag.x = (unsigned)t; tag.y = (unsigned)(t >> 32); }
  { float s = 0.f, q = 0.f; unsigned ones = 0x3f803f80u; asm volatile("" : "+v"(ones));
#pragma unroll
    for (int i = 0; i < 5; ++i)
#pragma unroll
      for (int e = 0; e < 4; ++e) { s = dot2(xv[i][e], ones, s); q = dot2(xv[i][e], xv[i][e], q); }
    s += dpp<XOR1>(s); q += dpp<XOR1>(q); s += dpp<XOR2>(s); q += dpp<XOR2>(q); s += dpp<XOR7>(s); q += dpp<XOR7>(q); s += dpp<XOR8>(s); q += dpp<XOR8>(q);
    const float m = s * (1.0f / D), var = fmaxf(q * (1.0f / D) - m * m, 0.f) + 1e-5f;
    if ((lane & 15) == 0) st16<SC1>(seal(m, var, tag), rws, W_G0X + srow * 16, 0);
    if (xw == 0 && lane == 0) { *(LAS u32x2*)(sm + T_TAG) = tag; *(LAS u32x4*)(sm + T_CNT) = (u32x4){0u, 0u, 0u, 0u}; *(LAS u32x4*)(sm + T_CVF) = (u32x4){0u, 0u, 0u, 0u}; }
  }
  STAMP(0);
  BAR(); BAR();
  if (TWZ == 2) { const rsrc_t r = mkrs(P.win, D * F * 2); const unsigned n = xw * 64 + lane, m = id.b * 81 + n, row = m / 36;
    asm volatile("buffer_load_dword %0, %1, %2, 0 offen" : "+v"(sink) : "v"(n < 81 ? row * (F * 2) + (m - row * 36) * 128 : 0xF0000000u), "s"(r) : "memory"); }
  BAR(); BAR();
  LAS char* stg = sm + STG + xw * STG_W;
  const int unit = xw ? 256 + id.b : id.b, wks = unit >> 2, wu = unit & 3; const bool won = !xw || id.b < 32;
  const int tile = xw ? id.b : 256 + id.b - 32; const bool kon = xw || (id.b >= 32 && id.b < 64);
  const int tb = tile / 9, db = tile - tb * 9, cc = lane & 7, rg = lane >> 3;
  u32x4 kk[11], vv[8], sh[4];
  { const int row = 64 * xw + lane; const u64 t0 = __builtin_amdgcn_s_memrealtime();
    u32x4 gx = ld16<SC1>(rws, W_G0X + (id.tp * 128 + row) * 16, 0);
    BAR(); BAR();
    while (!__all(sealed(gx, tag)) && !expired(t0)) { __builtin_amdgcn_s_sleep(4); FENCE(); gx = ld16<SC1>(rws, W_G0X + (id.tp * 128 + row) * 16, 0); }
    const float var = __uint_as_float(gx.y), r = __builtin_amdgcn_rsqf(var);
    *(LAS unsigned*)(sm + T_M1 + row * 4) = gx.x; *(LAS float*)(sm + T_R1 + row * 4) = r;
    *(LAS float*)(sm + T_A0 + row * 4) = GC0 * r; *(LAS float*)(sm + T_A1 + row * 4) = GC1 * r * r * r; *(LAS float*)(sm + T_IR + row * 4) = var * r;
    if ((RT & 1) && xw == 0) code_touch(id, sink);
    WAITL(0); BAR();
  }
  STAMP(1);
  { const rsrc_t rk = mkrs(P.k, S * D * 2), rv = mkrs(P.v, S * D * 2), rs = mkrs(P.shk, D * 4 * 2), rwo = mkrs(P.wout, FH * D * 2);
    const int t0 = 64 * tb + 8 * rg; const unsigned col = (64 * db + 8 * cc) * 2;
    if (kon) {
#pragma unroll
      for (int i = 0; i < 11; ++i) kk[i] = ld16<0>(rk, t0 + i - 3 >= 0 ? (unsigned)(t0 + i - 3) * (D * 2) + col : 0xF0000000u, 0);
    }
    BAR();
    if (kon) {
#pragma unroll
      for (int i = 0; i < 8; ++i) vv[i] = ld16<0>(rv, t0 * (D * 2) + col, i * (D * 2));
#pragma unroll
      for (int i = 0; i < 4; ++i) sh[i] = ld16<0>(rs, col * 4, i * 16);
    }
    BAR();
    if (won) { const int fb0 = wks < 64 ? 144 * (wks >> 2) + 32 * (wks & 3) : 288 * (wks - 64) + 128, fst = wks < 64 ? 0 : 128;
#pragma unroll
      for (int i = 0; i < 9; ++i) { const int ci = 64 * i + lane, rho = __umul24(ci, 3641) >> 16, ch = ci - rho * 18;
        dma_hidden(rwo, stg + i * 1024, (fb0 + rho + (rho >> 4) * fst) * (D * 2) + wu * 288 + ch * 16); } }
    BAR();
    if (kon) {
#pragma unroll
      for (int i = 0; i < 4; ++i) asm volatile("" : "+v"(sh[i]), "+v"(vv[i]), "+v"(vv[4 + i]), "+v"(kk[i]), "+v"(kk[4 + i]), "+v"(kk[i == 3 ? 0 : 8 + i])); }
    if (OPT_TWSP) { const rsrc_t r = mkrs(P.wsp, S * S * 2);
#pragma unroll
      for (int i = 0; i < 2; ++i) asm volatile("buffer_load_dword %0, %1, %2, 0 offen" : "+v"(sink) : "v"(id.b * 32768 + ((2 * i + xw) * 64 + lane) * 128), "s"(r) : "memory"); }
  }
  BAR();
  u32x4 ko[8];
  auto kvstep = [&](int e) {
    if (kon) {
      float c[4], kf[11], y[8];
#pragma unroll
      for (int j = 0; j < 4; ++j) { const unsigned w = sh[e >> 1][(e & 1) * 2 + (j >> 1)]; c[j] = (j & 1) ? bhi(w) : blo(w); }
#pragma unroll
      for (int i = 0; i < 11; ++i) { const unsigned w = kk[i][e >> 1]; kf[i] = (e & 1) ? bhi(w) : blo(w); }
#pragma unroll
      for (int i = 0; i < 8; ++i) { const unsigned w = vv[i][e >> 1];
        float a = c[3] * kf[i]; asm volatile("" : "+v"(a)); a = fmaf(c[2], kf[i + 1], a); asm volatile("" : "+v"(a)); a = fmaf(c[1], kf[i + 2], a); asm volatile("" : "+v"(a)); a = fmaf(c[0], kf[i + 3], a);
        asm volatile("" : "+v"(a)); y[i] = a * ((e & 1) ? bhi(w) : blo(w)); asm volatile("" : "+v"(y[i])); }
      ko[e] = (u32x4){pk(y[7], y[6]), pk(y[5], y[4]), pk(y[3], y[2]), pk(y[1], y[0])};
      asm volatile("" : "+v"(ko[e]));
    } };
  { const int row = 64 * xw + lane;
    const f32x2 a = lds<f32x2>(sm + T_PART + row * 8) + lds<f32x2>(sm + T_PART + 1024 + row * 8);
    st16<SC1>(seal(a.x, a.y, tag), rws, W_G1S + ((id.tp * 16 + id.ft) * 128 + row) * 16, 0);
    kvstep(0); BAR(); kvstep(1); BAR();
    u32x4 gs[16];
    auto gather = [&]() {
#pragma unroll
      for (int i = 0; i < 16; ++i) gs[i] = ld16<SC1>(rws, W_G1S + (id.tp * 16 * 128 + row) * 16, i * 2048); };
    unsigned bad = 0; float s = 0.f, q = 0.f;
    auto sum = [&](int lo, int hi) {
#pragma unroll
      for (int i = lo; i < hi; ++i) { bad |= (gs[i].x ^ gs[i].z ^ tag.x) | (gs[i].y ^ gs[i].w ^ tag.y); s += __uint_as_float(gs[i].x); q += __uint_as_float(gs[i].y); }
      asm volatile("" : "+v"(bad), "+v"(s), "+v"(q)); };
    gather(); kvstep(2); BAR(); sum(0, 10); BAR();
    sum(10, 16);
    if (__builtin_expect(!__all(bad == 0), 0)) { const u64 t0 = __builtin_amdgcn_s_memrealtime();
      do { FENCE(); gather(); bad = 0; s = 0.f; q = 0.f; sum(0, 16); } while (!__all(bad == 0) && !expired(t0)); }
    const float m = s * (1.0f / FH), var = fmaxf(q * (1.0f / FH) - m * m, 0.f), r = __builtin_amdgcn_rsqf(var + 1e-5f);
    *(LAS float*)(sm + T_R2 + row * 4) = r; *(LAS float*)(sm + T_N2 + row * 4) = -m * r;
    asm volatile("s_waitcnt vmcnt(0) lgkmcnt(0)" : "+v"(sink) :: "memory"); BAR();
  }
  STAMP(2);
  kvstep(3); kvstep(4); BAR(); kvstep(5); BAR(); kvstep(6); BAR();
  if (xw == 0) set_flag(rws, W_F2 + (id.ft * 16 + id.tp) * 8, tag, lane);
  kvstep(7); BAR();
  const unsigned h0a = ((const CAS unsigned*)P.ssmk)[id.b * S], h0b = ((const CAS unsigned*)P.ssmk)[(xw ? 2 * id.b + 1 : 512 + (id.b >> 2)) * (S / 2)];
  { u32x4 wf[9];
    if (won) {
      const int q = lane >> 4, qq = (lane & 15) >> 2, p = lane & 3;
      LAS char* base = stg + (4 * q + qq) * 288;
#pragma unroll
      for (int T = 0; T < 9; ++T) { const int cb = T < 4 ? 32 * (T >> 1) + 4 * (T & 1) : T == 4 ? 64 : 80 + 32 * ((T - 5) >> 1) + 4 * ((T - 5) & 1);
        LAS char* a = base + cb * 2 + p * (T == 4 ? 8 : 16);
        wf[T] = __builtin_bit_cast(u32x4, cat(ldtr(a), ldtr(a + 16 * 288))); }
      WAITL(0);
    }
    if (kon) {
#pragma unroll
      for (int e = 0; e < 8; ++e) *(LAS u32x4*)(stg + (8 * cc + e) * 128 + (((7 - rg) ^ cc) * 16)) = ko[e];
#pragma unroll
      for (int i = 0; i < 8; ++i) ko[i] = lds<u32x4>(stg + (8 * i + rg) * 128 + ((cc ^ i) * 16));
    }
    { const rsrc_t rh = mkrs(P.ssmk, D * S * 2);
      WAITL(0);
      auto filt = [&](int ci, unsigned h0) { const int chn = ci == 2 ? 512 + (id.b >> 2) : 2 * id.b + ci; LAS char* cb = sm + CV + ci * CV_CH;
        if (lane < 8) { *(LAS unsigned*)(cb + lane * 4) = 0u; *(LAS unsigned*)(cb + CV_O + lane * 4) = lane == 7 ? h0 << 16 : 0u; }
        if (lane < 38) *(LAS u32x4*)(cb + CV_U + 4096 + lane * 16) = (u32x4){0u, 0u, 0u, 0u};
        if (ci == 2) { *(LAS u32x4*)(cb + CV_U + 4704 + lane * 16) = (u32x4){0u, 0u, 0u, 0u}; *(LAS u32x4*)(cb + CV_U + 5728 + lane * 16) = (u32x4){0u, 0u, 0u, 0u}; }
#pragma unroll
        for (int i = 0; i < 4; ++i) { dma_hidden(rh, cb + 32 + i * 1024, chn * (S * 2) + i * 1024 + lane * 16); dma_hidden(rh, cb + CV_O + 32 + i * 1024, chn * (S * 2) + i * 1024 + lane * 16 + 2); } };
      if (xw == 0) filt(0, h0a);
      filt(2 - xw, h0b); }
    const u64 t0 = __builtin_amdgcn_s_memrealtime();
    while (*(volatile LAS unsigned*)(sm + T_GO) == 0u && !expired(t0)) __builtin_amdgcn_s_sleep(2);
    FENCE();
    if (won) {
#pragma unroll
      for (int T = 0; T < 9; ++T) st16<SC1>(wf[T], rws, W_WOF + lane * 16, ((wu * 72 + wks) * 9 + T) * 1024); }
    if (kon) {
#pragma unroll
      for (int i = 0; i < 8; ++i) st16<SC1>(ko[i], rws, W_KVT + (64 * db + rg) * (S * 2) + (S - 64 * (tb + 1)) * 2 + cc * 16, 8 * i * (S * 2)); }
  }
  BAR(); BAR(); BAR();
  BAR(); WAITV(0); BAR();
  if (xw == 0) set_flag(rws, W_F5 + id.b * 8, tag, lane);
  STAMP(3);
#pragma unroll 1
  for (int i = 0; i < 7; ++i) BAR();
  { u32x4 f0 = {tag.x, tag.y, tag.x, tag.y}, f1 = f0;
    if (xw == 0) { f0 = ld16<SC1>(rws, W_F5 + lane * 16, 0); f1 = ld16<SC1>(rws, W_F5 + lane * 16, 1024); }
    BAR(); BAR();
    if (!__all(flags_ok(f0, tag) && flags_ok(f1, tag))) wait_flags<2, 8>(rws, W_F5 + lane * 16, tag); }
  BAR();
  STAMP(4);
  { auto rows = [&](int ci) { const int chn = ci == 2 ? 512 + (id.b >> 2) : 2 * id.b + ci;
#pragma unroll
      for (int i = 0; i < 4; ++i) dma<SC1>(rws, sm + CV + ci * CV_CH + CV_U + i * 1024, lane * 16, W_KVT + chn * (S * 2) + i * 1024); };
    rows(xw); BAR(); if (xw == 0) rows(2); }
#pragma unroll 1
  for (int i = 0; i < 17; ++i) { if (i == 5) { WAITV(0); WAITL(0); } BAR(); }
  BAR();
  if (xw == 0) { set_flag(rws, W_F3 + (id.tp * 16 + id.ft) * 8, tag, lane); wait_flags<1, 1>(rws, W_F3 + id.tp * 128 + (lane & 7) * 16, tag); }
  STAMP(5);
  BAR();
  if (xw == 1 && (OPT_TG3 & 4)) {
#pragma unroll
    for (int i = 0; i < 4; ++i) { const unsigned n = i * 64 + lane, o = n < 240 ? W_HF + ((id.tp * 8 + 4 * (id.ft >> 3) + (n & 3)) * 72 + 18 * ((n >> 2) & 3) + 3 + (n >> 4)) * 1024 + (id.ft & 7) * 128 : 0xF0000000u;
      asm volatile("buffer_load_dword %0, %1, %2, 0 offen sc1" : "+v"(sink) : "v"(o), "s"(rws) : "memory"); } }
  if (xw == 0) {
    const u64 t0 = __builtin_amdgcn_s_memrealtime();
    while (*(volatile LAS unsigned*)(sm + T_CNT) != 4u && !expired(t0)) __builtin_amdgcn_s_sleep(2);
    FENCE();
    set_flag(rws, W_F4 + id.b * 8, tag, lane);
    const int g = id.ft & 7, d0 = 144 * (g >> 1) + 64 * (g & 1);
    { const rsrc_t rx = mkrs(P.x, S * D * 2);
#pragma unroll
      for (int i = 0; i < 10; ++i) { const unsigned ci = 64 * i + lane, r = __umul24(ci, 6554) >> 16; dma<0>(rx, sm + XTILE + i * 1024, (id.tp * 128 + 64 * (id.ft >> 3) + r) * (D * 2) + d0 * 2 + (ci - 10 * r) * 16, 0); } }
    __builtin_amdgcn_s_sleep(24); wait_flags<2, 12>(rws, W_F4 + lane * 16, tag);
#pragma unroll
    for (int i = 0; i < 10; ++i) dma<SC1>(rws, sm + YTILE + i * 1024, W_YT + (d0 + (lane >> 3)) * (S * 2) + (id.tp * 128 + 64 * (id.ft >> 3)) * 2 + (lane & 7) * 16, i * 8 * (S * 2));
    WAITV(0);
    asm volatile("ds_write_b32 %0, %1" :: "v"((unsigned)(size_t)(sm + T_YOK)), "v"(1u) : "memory");
    if (id.b == 0 && lane == 0) st8<SC1>((u32x2){(unsigned)(epoch + 1), (unsigned)((epoch + 1) >> 32)}, rws, W_EPOCH, 0);
  }
  STAMP(6);
  asm volatile("s_waitcnt vmcnt(0)" : "+v"(sink) :: "memory"); BAR();
}

DEV void role_C(const Params& P, LAS char* sm, const Ids id, const int wid STAMP_ARG) {
  int bcount = 0; (void)bcount;
  const int lane = id.lane, wt = wid >> 1, wf = wid & 1, c = lane & 15, q = lane >> 4;
  const rsrc_t rws = mkrs(P.ws, W_END);
  __builtin_amdgcn_s_setprio(1);
  unsigned dm = ((const CAS unsigned*)P.ssmd)[id.b], dsh = ((const CAS unsigned*)P.ssmd)[256 + (id.b >> 3)];
  asm volatile("" : "+s"(dm), "+s"(dsh));
  f32x4 acc[4][4], accx[2], csa[3];
  bf16x8 tf[2][4], ff[2][5], ones;
  LAS char *ta[2][2], *fb[2][2], *fx[2];
#pragma unroll
  for (int s = 0; s < 2; ++s) {
    const int o = (32 * (s ^ wt) + 16 * (q >> 1) + 4 * (q & 1) + (c >> 2)) * 288 + 8 * (c & 3);
    fx[s] = sm + BBASE + o + 256;
#pragma unroll
    for (int h = 0; h < 2; ++h) { ta[s][h] = sm + (64 * wt + 32 * (h ^ wf) + c) * 128 + (((q + 4 * (s ^ wt)) ^ ((c >> 1) & 7)) * 16); fb[s][h] = sm + BBASE + o + wf * 128 + (h ^ wt) * 64; }
  }
#pragma unroll
  for (int e = 0; e < 8; ++e) ones[e] = 0x3f80;
  asm volatile("" : "+v"(ones));
  auto rd = [&](auto mode, int b, int as, int bs, int s) {
    constexpr int MODE = decltype(mode)::value;
#pragma unroll
    for (int j = 0; j < 4; ++j) { LAS char* p = fb[s][j >> 1] + bs * BSLOT;
      ff[b][j] = MODE < 2 ? cat(ldtr(p + (j & 1) * 32), ldtr(p + (j & 1) * 32 + 8 * 288)) : lds<bf16x8>(p + (j & 1) * 1024); }
#pragma unroll
    for (int i = 0; i < 4; ++i) tf[b][i] = lds<bf16x8>(ta[s][i >> 1] + as * ASLOT + (i & 1) * 2048);
    ff[b][4] = MODE < 2 ? cat(ldtr(fx[s] + bs * BSLOT), ldtr(fx[s] + bs * BSLOT + 8 * 288)) : lds<bf16x8>(fx[s] + bs * BSLOT);
  };
  auto mm = [&](auto mode, int b, int lo, int hi) {
    constexpr int MODE = decltype(mode)::value;
#pragma unroll
    for (int n = 0; n < (MODE == 2 ? 18 : 21 - b); ++n) if (n >= lo && n < hi) {
      if (n < 16) { const int i = n >> 2, j = (i & 1) ? 3 - (n & 3) : (n & 3);
        acc[i][j] = MODE == 0 ? MFMA(tf[b][i], ff[b][j], acc[i][j]) : MFMA(ff[b][j], tf[b][i], acc[i][j]); }
      else if (n < 18) accx[n - 16] = MODE == 0 ? MFMA(tf[b][n - 16], ff[b][4], accx[n - 16]) : MFMA(ff[b][4], tf[b][n - 16], accx[n - 16]);
      else { const int j = n == 20 ? 4 : n - 18; csa[n - 18] = MODE == 0 ? MFMA(ones, ff[b][j], csa[n - 18]) : MFMA(ff[b][j], ones, csa[n - 18]); }
    }
  };
  auto stage = [&](auto mode, int as, int bs, bool drain) {
    constexpr int MODE = decltype(mode)::value;
    mm(mode, 1, 0, 4); SB();
    if (drain) WAITV(0);
    WAITL(0); BAR(); SB();
    rd(mode, 0, as, bs, 0); mm(mode, 1, 4, 21);
    if (MODE < 2) { for (int i = 0; i < 14; ++i) { SG(0x100, 1); SG(0x8, 1); } SG(0x8, 2); }
    else { for (int i = 0; i < 4; ++i) { SG(0x100, 1); SG(0x8, 1); SG(0x100, 1); SG(0x8, 2); } SG(0x100, 1); SG(0x8, 2); }
    SB();
    rd(mode, 1, as, bs, 1); mm(mode, 0, 0, 21);
    if (MODE < 2) { for (int i = 0; i < 7; ++i) { SG(0x100, 1); SG(0x8, 1); SG(0x100, 1); SG(0x8, 2); } }
    else { for (int i = 0; i < 9; ++i) { SG(0x100, 1); SG(0x8, 2); } }
    SB();
  };
  auto first = [&](auto mode, int as, int bs) { WAITL(0); BAR(); SB(); rd(mode, 0, as, bs, 0); rd(mode, 1, as, bs, 1); mm(mode, 0, 0, 21); SB(); };
  auto zero = [&]() {
#pragma unroll
    for (int i = 0; i < 4; ++i)
#pragma unroll
      for (int j = 0; j < 4; ++j) acc[i][j] = (f32x4){0.f, 0.f, 0.f, 0.f};
    accx[0] = accx[1] = csa[0] = csa[1] = csa[2] = (f32x4){0.f, 0.f, 0.f, 0.f}; };
  const std::integral_constant<int, 0> M0; const std::integral_constant<int, 1> M1; const std::integral_constant<int, 2> M2;
  const int jg0 = 4 * wf + 2 * wt, jg2 = 4 * wf + 2 - 2 * wt;

  zero();
  first(M0, 0, 0);
  STAMP(0);
#pragma unroll 1
  for (int it = 0; it < 2; ++it) { stage(M0, 1, 1, false); stage(M0, 2, 2, false); stage(M0, 3, 3, false); stage(M0, 0, 0, false); }
  mm(M0, 1, 18, 21); mm(M0, 1, 0, 8); SB();
  if (q == 0) { *(LAS float*)(sm + T_CS2 + (jg0 * 16 + c) * 4) = csa[0][0]; *(LAS float*)(sm + T_CS2 + (jg0 * 16 + 16 + c) * 4) = csa[1][0]; *(LAS float*)(sm + T_CS2 + (128 + 16 * wt + c) * 4) = csa[2][0]; }
  mm(M0, 1, 8, 18); SB();
  WAITL(0); BAR();
  STAMP(1);
  u32x4 z2p[2][4], z2x;
  { float cs[5];
    cs[0] = csa[0][0]; cs[1] = csa[1][0]; cs[2] = lds<float>(sm + T_CS2 + (jg2 * 16 + c) * 4); cs[3] = lds<float>(sm + T_CS2 + (jg2 * 16 + 16 + c) * 4);
    cs[4] = csa[2][0] + lds<float>(sm + T_CS2 + (144 - 16 * wt + c) * 4);
    float red[2][16];
#pragma unroll
    for (int i = 0; i < 4; ++i) {
      const int ro = (64 * wt + 32 * ((i >> 1) ^ wf) + 8 * q + 4 * (i & 1)) * 4;
      const f32x4 m = lds<f32x4>(sm + T_M1 + ro), a0 = lds<f32x4>(sm + T_A0 + ro), a1 = lds<f32x4>(sm + T_A1 + ro), ir = lds<f32x4>(sm + T_IR + ro);
      f32x4 su = {0.f, 0.f, 0.f, 0.f}, sq = {0.f, 0.f, 0.f, 0.f};
#pragma unroll
      for (int j = 0; j < (i < 2 ? 5 : 4); ++j) {
        const f32x4 z = gelu4((j < 4 ? acc[i][j] : accx[i]) - m * cs[j], a0, a1, ir);
        su = j ? su + z : z; sq = j ? sq + z * z : z * z;
        u32x4& dst = j < 4 ? z2p[i >> 1][j] : z2x;
        dst[2 * (i & 1)] = pk(z[0], z[1]); dst[2 * (i & 1) + 1] = pk(z[2], z[3]);
      }
#pragma unroll
      for (int e = 0; e < 4; ++e) { red[0][4 * i + e] = su[e]; red[1][4 * i + e] = sq[e]; }
    }
    float r8[2][8], r4[2][4];
#pragma unroll
    for (int st = 0; st < 2; ++st) {
#pragma unroll
      for (int e = 0; e < 8; e += 4)
        asm volatile("s_nop 1\n\t"
                     "v_add_f32_dpp %0, %4, %4 row_ror:8 row_mask:0xf bank_mask:0x3\n\tv_add_f32_dpp %1, %5, %5 row_ror:8 row_mask:0xf bank_mask:0x3\n\t"
                     "v_add_f32_dpp %2, %6, %6 row_ror:8 row_mask:0xf bank_mask:0x3\n\tv_add_f32_dpp %3, %7, %7 row_ror:8 row_mask:0xf bank_mask:0x3\n\t"
                     "v_add_f32_dpp %0, %8, %8 row_ror:8 row_mask:0xf bank_mask:0xc\n\tv_add_f32_dpp %1, %9, %9 row_ror:8 row_mask:0xf bank_mask:0xc\n\t"
                     "v_add_f32_dpp %2, %10, %10 row_ror:8 row_mask:0xf bank_mask:0xc\n\tv_add_f32_dpp %3, %11, %11 row_ror:8 row_mask:0xf bank_mask:0xc"
                     : "=&v"(r8[st][e]), "=&v"(r8[st][e + 1]), "=&v"(r8[st][e + 2]), "=&v"(r8[st][e + 3])
                     : "v"(red[st][e]), "v"(red[st][e + 1]), "v"(red[st][e + 2]), "v"(red[st][e + 3]), "v"(red[st][8 + e]), "v"(red[st][9 + e]), "v"(red[st][10 + e]), "v"(red[st][11 + e]));
      asm volatile("s_nop 1\n\t"
                   "v_add_f32_dpp %0, %4, %4 row_half_mirror row_mask:0xf bank_mask:0x5\n\tv_add_f32_dpp %1, %5, %5 row_half_mirror row_mask:0xf bank_mask:0x5\n\t"
                   "v_add_f32_dpp %2, %6, %6 row_half_mirror row_mask:0xf bank_mask:0x5\n\tv_add_f32_dpp %3, %7, %7 row_half_mirror row_mask:0xf bank_mask:0x5\n\t"
                   "v_add_f32_dpp %0, %8, %8 row_half_mirror row_mask:0xf bank_mask:0xa\n\tv_add_f32_dpp %1, %9, %9 row_half_mirror row_mask:0xf bank_mask:0xa\n\t"
                   "v_add_f32_dpp %2, %10, %10 row_half_mirror row_mask:0xf bank_mask:0xa\n\tv_add_f32_dpp %3, %11, %11 row_half_mirror row_mask:0xf bank_mask:0xa\n\t"
                   "v_add_f32_dpp %0, %0, %0 quad_perm:[1,0,3,2] row_mask:0xf bank_mask:0xf\n\tv_add_f32_dpp %1, %1, %1 quad_perm:[1,0,3,2] row_mask:0xf bank_mask:0xf\n\t"
                   "v_add_f32_dpp %2, %2, %2 quad_perm:[1,0,3,2] row_mask:0xf bank_mask:0xf\n\tv_add_f32_dpp %3, %3, %3 quad_perm:[1,0,3,2] row_mask:0xf bank_mask:0xf\n\t"
                   "v_add_f32_dpp %0, %0, %0 quad_perm:[2,3,0,1] row_mask:0xf bank_mask:0xf\n\tv_add_f32_dpp %1, %1, %1 quad_perm:[2,3,0,1] row_mask:0xf bank_mask:0xf\n\t"
                   "v_add_f32_dpp %2, %2, %2 quad_perm:[2,3,0,1] row_mask:0xf bank_mask:0xf\n\tv_add_f32_dpp %3, %3, %3 quad_perm:[2,3,0,1] row_mask:0xf bank_mask:0xf"
                   : "=&v"(r4[st][0]), "=&v"(r4[st][1]), "=&v"(r4[st][2]), "=&v"(r4[st][3])
                   : "v"(r8[st][0]), "v"(r8[st][1]), "v"(r8[st][2]), "v"(r8[st][3]), "v"(r8[st][4]), "v"(r8[st][5]), "v"(r8[st][6]), "v"(r8[st][7]));
    }
    if ((c & 3) == 0) { LAS char* pp = sm + T_PART + wf * 1024 + (64 * wt + 32 * ((c >> 3) ^ wf) + 8 * q + 4 * ((c >> 2) & 1)) * 8;
      *(LAS f32x4*)pp = (f32x4){r4[0][0], r4[1][0], r4[0][1], r4[1][1]}; *(LAS f32x4*)(pp + 16) = (f32x4){r4[0][2], r4[1][2], r4[0][3], r4[1][3]}; }
#pragma unroll
    for (int i = 0; i < 2; ++i) asm volatile("" : "+v"(z2p[i][0]), "+v"(z2p[i][1]), "+v"(z2p[i][2]), "+v"(z2p[i][3]));
    asm volatile("" : "+v"(z2x));
  }
  STAMP(2);
  zero();
  first(M1, 0, 1);
  stage(M1, 3, 2, false); stage(M1, 2, 3, false); stage(M1, 1, 0, false);
  stage(M1, 0, 1, false); stage(M1, 3, 2, false);
  STAMP(3);
  {
#pragma unroll
    for (int i = 0; i < 2; ++i) asm volatile("" : "+v"(z2p[i][0]), "+v"(z2p[i][1]), "+v"(z2p[i][2]), "+v"(z2p[i][3]));
    asm volatile("" : "+v"(z2x));
#pragma unroll
    for (int ip = 0; ip < 2; ++ip) {
      const int ro = (64 * wt + 32 * (ip ^ wf) + 8 * q) * 4;
      const f32x4 r0 = lds<f32x4>(sm + T_R2 + ro), r1 = lds<f32x4>(sm + T_R2 + ro + 16), n0 = lds<f32x4>(sm + T_N2 + ro), n1 = lds<f32x4>(sm + T_N2 + ro + 16);
      const unsigned so = W_ZF + ((id.ft * 64 + 4 * id.tp + 2 * wt + (ip ^ wf)) * 9) * 1024;
#pragma unroll
      for (int j = 0; j < (ip == 0 ? 5 : 4); ++j) { const u32x4 z = j < 4 ? z2p[ip][j] : z2x;
        const f32x4 a = (f32x4){blo(z[0]), bhi(z[0]), blo(z[1]), bhi(z[1])} * r0 + n0, b = (f32x4){blo(z[2]), bhi(z[2]), blo(z[3]), bhi(z[3])} * r1 + n1;
        st16<SC1>((u32x4){pk(a[0], a[1]), pk(a[2], a[3]), pk(b[0], b[1]), pk(b[2], b[3])}, rws, lane * 16, so + (j < 2 ? jg0 + j : j < 4 ? jg2 + j - 2 : 8) * 1024); }
    }
    SB();
  }
  unsigned braw[4];
  { const rsrc_t rb = mkrs(P.bsp, S * 2);
#pragma unroll
    for (int i = 0; i < 4; ++i) braw[i] = __builtin_amdgcn_raw_buffer_load_b16(rb, (id.tp * 128 + 64 * wt + prow(i ^ (2 * wf), c)) * 2, 0, 0); }
  STAMP(4);
  stage(M1, 2, 3, false); stage(M1, 1, 0, false); stage(M1, 0, 1, true);
  mm(M1, 1, 18, 21); mm(M1, 1, 0, 8); SB();
  if (c == 0) { *(LAS f32x4*)(sm + T_CS1 + (jg0 * 16 + 4 * q) * 4) = csa[0]; *(LAS f32x4*)(sm + T_CS1 + (jg0 * 16 + 16 + 4 * q) * 4) = csa[1]; *(LAS f32x4*)(sm + T_CS1 + (128 + 16 * wt + 4 * q) * 4) = csa[2]; }
  mm(M1, 1, 8, 18); SB();
  WAITL(0); BAR();
  STAMP(5);
  u32x2 z1p[4][4], z1x[2];
  { f32x4 cs[5];
    cs[0] = csa[0]; cs[1] = csa[1]; cs[2] = lds<f32x4>(sm + T_CS1 + (jg2 * 16 + 4 * q) * 4); cs[3] = lds<f32x4>(sm + T_CS1 + (jg2 * 16 + 16 + 4 * q) * 4);
    cs[4] = csa[2] + lds<f32x4>(sm + T_CS1 + (144 - 16 * wt + 4 * q) * 4);
#pragma unroll
    for (int i = 0; i < 4; ++i) { const int ro = (64 * wt + prow(i ^ (2 * wf), c)) * 4;
      const float m = lds<float>(sm + T_M1 + ro), a0 = lds<float>(sm + T_A0 + ro), a1 = lds<float>(sm + T_A1 + ro), ir = lds<float>(sm + T_IR + ro), bias = blo(braw[i]);
#pragma unroll
      for (int j = 0; j < (i < 2 ? 5 : 4); ++j) {
        f32x4& a = j < 4 ? acc[i][j] : accx[i];
        const f32x4 z = gelu4(a - m * cs[j], a0, a1, ir);
        (j < 4 ? z1p[i][j] : z1x[i]) = (u32x2){pk(z[0], z[1]), pk(z[2], z[3])};
        a = (f32x4){bias, bias, bias, bias};
      } }
#pragma unroll
    for (int s = 0; s < 2; ++s) { fx[s] = sm + B2BASE + (s * 9 + 8) * 1024 + lane * 16;
#pragma unroll
      for (int h = 0; h < 2; ++h) { fb[s][h] = sm + B2BASE + (s * 9 + (h ? jg2 : jg0)) * 1024 + lane * 16; ta[s][h] = sm + (64 * wt + 32 * (h ^ wf) + c) * 128 + (((q + 4 * s) ^ ((c >> 1) & 7)) * 16); } }
#pragma unroll
    for (int i = 0; i < 4; ++i) asm volatile("" : "+v"(z1p[i][0]), "+v"(z1p[i][1]), "+v"(z1p[i][2]), "+v"(z1p[i][3]), "+v"(acc[i][0]), "+v"(acc[i][1]), "+v"(acc[i][2]), "+v"(acc[i][3]));
    asm volatile("" : "+v"(z1x[0]), "+v"(z1x[1]), "+v"(accx[0]), "+v"(accx[1]));
  }
  STAMP(6);
  const int jq = id.b & 3;
  LAS char *cs_ = sm + CV + 2 * CV_CH, *cb = sm + CV + wt * CV_CH;
  LAS char *am[4], *um = cb + CV_U + (2032 - 16 * c + 8 * q) * 2 + 256 * wf - 3584;
#pragma unroll
  for (int i = 0; i < 4; ++i) { am[i] = cb + ((c & 1) ? 0 : CV_O) + (4 * q + ((c + 1) >> 1)) * 4 + 256 * wf + 1024 * i; asm volatile("" : "+v"(am[i])); }
  asm volatile("" : "+v"(um));
  LAS char *as[4], *u1, *u0;
  { const int da = (2 - wt) * CV_CH + 128 * wid - 256 * wf;
#pragma unroll
    for (int i = 0; i < 4; ++i) { as[i] = am[i] + da; asm volatile("" : "+v"(as[i])); }
    u1 = um + da + 512 * jq; u0 = um + da + 512 * (7 - jq); asm volatile("" : "+v"(u1), "+v"(u0)); }
  bf16x8 ca[2][8], cu[2][8];
  auto afrag = [&](LAS char* p) { const unsigned d0 = lds<unsigned>(p), d1 = lds<unsigned>(p + 4), d2 = lds<unsigned>(p + 8), d3 = lds<unsigned>(p + 12);
    return __builtin_bit_cast(bf16x8, (u32x4){d0, d1, d2, d3}); };
  auto lds_mn = [&](int b, int kk) {
#pragma unroll
    for (int e = 0; e < 8; ++e) { cu[b][e] = lds<bf16x8>(um + kk * 64 + 512 * (7 - e)); if (e == 0) ca[b][0] = afrag(am[0] + kk * 64); }
#pragma unroll
    for (int e = 1; e < 8; ++e) ca[b][e] = afrag(am[e >> 1] + kk * 64 + (e & 1) * 512); };
  first(M2, 2, 0);
  STAMP(7);
#pragma unroll 1
  for (int it = 0; it < 10; ++it) { stage(M2, 1, 1, false); stage(M2, 0, 2, false); stage(M2, 2, 0, false); }
  stage(M2, 1, 1, false);
  mm(M2, 1, 0, 18); SB();
  lds_mn(0, 0); SB();
  STAMP(8);
#pragma unroll
  for (int i = 0; i < 4; ++i) asm volatile("" : "+v"(z1p[i][0]), "+v"(z1p[i][1]), "+v"(z1p[i][2]), "+v"(z1p[i][3]));
  asm volatile("" : "+v"(z1x[0]), "+v"(z1x[1]));
#pragma unroll
  for (int i = 0; i < 4; ++i) {
    const unsigned so = W_HF + (id.tp * 8 + 4 * wt + (i ^ (2 * wf))) * 72 * 1024;
    auto gate = [&](u32x2 z, f32x4 a) { const f32x4 h = (f32x4){blo(z.x), bhi(z.x), blo(z.y), bhi(z.y)} * a; return (u32x2){pk(h[0], h[1]), pk(h[2], h[3])}; };
#pragma unroll
    for (int pp = 0; pp < 2; ++pp) { const u32x2 lo = gate(z1p[i][2 * pp], acc[i][2 * pp]), hi = gate(z1p[i][2 * pp + 1], acc[i][2 * pp + 1]);
      st16<SC1>((u32x4){lo.x, lo.y, hi.x, hi.y}, rws, lane * 16, so + (4 * id.ft + 2 * wf + (pp ^ wt)) * 1024); }
    if (i < 2) st8<SC1>(gate(z1x[i], accx[i]), rws, lane * 16 + (id.ft & 1) * 8, so + (64 + (id.ft >> 1)) * 1024);
    SB();
  }
  const int g = id.ft & 7, gp = g & 1, half = id.ft >> 3, rot = krot(id);
  const unsigned wso = W_WOF + ((((EXP & 4) ? 0 : g >> 1) * 72 + 18 * wid) * 9) * 1024;
  u32x4 wr[WD][5];
  auto ldw = [&](int slot, int i) {
#pragma unroll
    for (int t = 0; t < 5; ++t) wr[slot][t] = ld16<(EXP & 8) ? 0 : SC1>(rws, lane * 16, wso + kwrap(i, rot) * 9216 + (t == 4 ? 4 : t + gp * 5) * 1024); };
  SB();
  SB(); BAR(); SB();
  STAMP(9);
  u32x4 hr[3][4];
  const unsigned hso = W_HF + (((EXP & 2) ? (id.b & 7) * 4 : id.tp * 8 + 4 * half) * 72 + 18 * wid) * 1024;
  auto ldh = [&](int slot, int i) {
#pragma unroll
    for (int t = 0; t < 4; ++t) hr[slot][t] = ld16<(EXP & 8) ? 0 : SC1>(rws, lane * 16, hso + (((unsigned)t ^ (wf ^ (2 * gp))) * 72 + kwrap(i, rot)) * 1024); };
  { const float dvm = wt ? bhi(dm) : blo(dm), dvs = (id.b & 4) ? bhi(dsh) : blo(dsh);
    f32x4 cm[8], s0 = {0.f, 0.f, 0.f, 0.f}, s1 = s0;
#pragma unroll
    for (int j = 0; j < 8; ++j) cm[j] = s0;
    bf16x8 sa[4], su[8];
    auto lds_sh = [&](int p) { const int kk = p >> 1, e0 = 4 * (p & 1);
#pragma unroll
      for (int e = 0; e < 4; ++e) { sa[e] = afrag(as[(e0 + e) >> 1] + kk * 64 + (e & 1) * 512); su[e] = lds<bf16x8>(u1 + kk * 64 + 512 * (e0 + e));
        if (e0 == 0) su[4 + e] = lds<bf16x8>(u0 + kk * 64 + 512 * e); } };
    auto mm_sh = [&](int p) {
#pragma unroll
      for (int e = 0; e < 4; ++e) { s1 = MFMA(sa[e], su[e], s1); if (!(p & 1)) s0 = MFMA(sa[e], su[4 + e], s0); } };
    auto mm_mn = [&](int b) {
#pragma unroll
      for (int e = 0; e < 8; ++e)
#pragma unroll
        for (int j = e; j < 8; ++j) cm[j] = MFMA(ca[b][e], cu[b][j - e], cm[j]); };
#define il(nr) do { for (int i_ = 0; i_ < nr; ++i_) { SG(0x8, 1); SG(0x100, 1); } SG(0x8, 44); SB(); } while (0)
    lds_sh(0); lds_mn(1, 1); mm_mn(0); mm_sh(0); il(40); STAMP(15);
    lds_sh(1); lds_mn(0, 2); mm_mn(1); mm_sh(1); il(36); STAMP(17);
    WAITV(0); BAR(); SB();
    STAMP(10);
    lds_sh(2); lds_mn(1, 3); mm_mn(0); mm_sh(2); il(40); STAMP(16);
    lds_sh(3); ldw(0, 5); ldw(1, 6); if (WD > 2) ldw(2, 7); mm_mn(1); mm_sh(3);
    for (int i_ = 0; i_ < 12; ++i_) { SG(0x8, 1); SG(0x100, 1); } for (int i_ = 0; i_ < 12; ++i_) { SG(0x8, 2); SG(0x20, 1); } SG(0x20, 3); SG(0x8, 8); SB();
    if (wid != 0) *(LAS f32x4*)(sm + CV_PS + (wid - 1) * 1024 + lane * 16) = s0;
    if (wid != 1) *(LAS f32x4*)(sm + CV_PS + (3 + (wid ? wid - 1 : 0)) * 1024 + lane * 16) = s1;
    LAS char* pm = cb;
    *(volatile LAS unsigned*)(sm + T_CVF + wid * 4) = 1u;
    { const u64 t0 = __builtin_amdgcn_s_memrealtime(); while (*(volatile LAS unsigned*)(sm + T_CVF + (wid ^ 1) * 4) == 0u && !expired(t0)) {} }
    FENCE();
#pragma unroll
    for (int j = 0; j < 4; ++j) { f32x4 lo = cm[j], hi = cm[4 + j]; asm volatile("" : "+v"(lo), "+v"(hi));
      *(LAS f32x4*)(pm + wf * 4096 + j * 1024 + lane * 16) = wf ? lo : hi; cm[j] = wf ? hi : lo; }
    SB(); WAITL(0); BAR(); SB();
    STAMP(11);
    ldh(0, 0); ldh(1, 1); ldh(2, 2);
    auto yout = [&](f32x4 a, int chn, LAS char* ub, int j, float dv) {
      const u32x2 u = lds<u32x2>(ub + CV_U + (2044 - 256 * j - 16 * c - 4 * q) * 2);
      const f32x4 y = a + (f32x4){bhi(u.y), blo(u.y), bhi(u.x), blo(u.x)} * dv;
      st8<SC1>((u32x2){pk(y[0], y[1]), pk(y[2], y[3])}, rws, (16 * c + 4 * q) * 2, W_YT + chn * (S * 2) + j * 512); };
#pragma unroll
    for (int j = 0; j < 4; ++j) yout(cm[j] + lds<f32x4>(pm + (wf ^ 1) * 4096 + j * 1024 + lane * 16), 2 * id.b + wt, cb, 4 * wf + j, dvm);
    if (wid < 2) { f32x4 ys = wf ? s1 : s0;
#pragma unroll
      for (int w = 0; w < 3; ++w) ys += lds<f32x4>(sm + CV_PS + (3 * wid + w) * 1024 + lane * 16);
      yout(ys, 512 + (id.b >> 2), cs_, wid ? 7 - jq : jq, dvs); }
    SB();
  }
  STAMP(19);
  f32x4 o[4][4], ox[2];
#pragma unroll
  for (int i = 0; i < 4; ++i)
#pragma unroll
    for (int t = 0; t < 4; ++t) o[i][t] = (f32x4){0.f, 0.f, 0.f, 0.f};
  ox[0] = ox[1] = (f32x4){0.f, 0.f, 0.f, 0.f};
  const int dp = 144 * (g >> 1) + 80 * gp, ds = 144 * (g >> 1) + 64, trow = 64 * half + prow(wid, c);
  const unsigned xo = (id.tp * 128 + trow) * (D * 2);
  f32x4 rs[5];
  { LAS char* wl = sm + wid * G3W + lane * 16;
    bf16x8 wq[2][5];
#pragma unroll
    for (int t = 0; t < 5; ++t) wq[0][t] = lds<bf16x8>(wl + t * 1024);
#pragma unroll
    for (int i = 0; i < 18; ++i) {
      if (i < 4) {
#pragma unroll
        for (int t = 0; t < 5; ++t) wq[(i + 1) & 1][t] = lds<bf16x8>(wl + ((i + 1) * 5 + t) * 1024); }
#pragma unroll
      for (int r = 0; r < 4; ++r) { const bf16x8 hf = __builtin_bit_cast(bf16x8, hr[i % 3][r]);
#pragma unroll
        for (int t = 0; t < 5; ++t) if (t < 4 || r < 2) { const int tt = (r & 1) && t < 4 ? 3 - t : t;
          const bf16x8 wf_ = i < 5 ? wq[i & 1][tt] : __builtin_bit_cast(bf16x8, wr[(i - 5) % WD][tt]);
          if (tt < 4) o[r][tt] = MFMA(wf_, hf, o[r][tt]); else ox[r] = MFMA(wf_, hf, ox[r]); } }
      SB();
      if (i == 0 || i == 2 || i == 4 || i == 7 || i == 10 || i == 13 || i == 15) STAMP(20 + i);
      if (i + 3 < 18) ldh(i % 3, i + 3);
      if (i >= 5 && i + WD < 18) ldw((i - 5) % WD, i + WD);
      if (i == 2) { WAITV(12); if (lane == 0) __hip_atomic_fetch_add((LAS unsigned*)(sm + T_CNT), 1u, __ATOMIC_RELAXED, __HIP_MEMORY_SCOPE_WORKGROUP); }
      if (i == 16) {
        const u64 t0 = __builtin_amdgcn_s_memrealtime();
        while (*(volatile LAS unsigned*)(sm + T_YOK) == 0u && !expired(t0)) __builtin_amdgcn_s_sleep(1);
        FENCE();
        const float m = lds<float>(sm + T_M1 + trow * 4), r = lds<float>(sm + T_R1 + trow * 4);
        LAS char* yb = sm + YTILE + (c >> 2) * 128 + (32 * wt + 4 * wf + 8 * (c & 3)) * 2;
        auto res = [&](u32x2 xv, bf16x4 yv) { const u32x2 yu = __builtin_bit_cast(u32x2, yv);
          const f32x4 xf = {blo(xv.x), bhi(xv.x), blo(xv.y), bhi(xv.y)}, yf = {blo(yu.x), bhi(yu.x), blo(yu.y), bhi(yu.y)};
          return xf + (xf - m) * r * yf; };
        const int yp = 16 * gp, ysh = 64 * (gp ^ 1);
        LAS char* xb = sm + XTILE + prow(wid, c) * 160;
        const u32x4 x0 = lds<u32x4>(xb + yp * 2 + q * 16), x1 = lds<u32x4>(xb + yp * 2 + 64 + q * 16); const u32x2 x2 = lds<u32x2>(xb + ysh * 2 + q * 8);
        rs[0] = res(x0.xy, ldtr(yb + (yp + 8 * q) * 128)); rs[1] = res(x0.zw, ldtr(yb + (yp + 8 * q + 4) * 128));
        rs[2] = res(x1.xy, ldtr(yb + (yp + 32 + 8 * q) * 128)); rs[3] = res(x1.zw, ldtr(yb + (yp + 36 + 8 * q) * 128));
        rs[4] = res(x2, ldtr(yb + (ysh + 4 * q) * 128));
        asm volatile("" : "+v"(rs[0]), "+v"(rs[1]), "+v"(rs[2]), "+v"(rs[3]), "+v"(rs[4])); }
      SB();
    }
  }
  STAMP(12);
  const bool own0 = wt == gp;
  { LAS char* pw = sm + wid * G3W + lane * 16;
    auto put = [&](int r) { LAS char* p = pw + ((unsigned)r ^ (wf ^ (2 * gp))) * 5120;
#pragma unroll
      for (int t = 0; t < 4; ++t) *(LAS f32x4*)(p + t * 1024) = o[r][t];
      if (r < 2) *(LAS f32x4*)(p + 4096) = ox[r]; };
    put(1); put(3);
    if (own0) { put(2);
#pragma unroll
      for (int t = 0; t < 4; ++t) rs[t] += o[0][t];
      rs[4] += ox[0]; }
    else { put(0);
#pragma unroll
      for (int t = 0; t < 4; ++t) rs[t] += o[2][t]; }
  }
  SB(); WAITL(0); BAR(); SB();
  STAMP(13);
  { f32x4 pt[3][5];
#pragma unroll
    for (int t = 0; t < 5; ++t)
#pragma unroll
      for (int w = 0; w < 3; ++w) pt[w][t] = lds<f32x4>(sm + (wid ^ (w + 1)) * G3W + wid * 5120 + lane * 16 + t * 1024);
    const rsrc_t ro = mkrs(P.out, S * D * 2);
    auto fin = [&](int t) { const f32x4 v = (rs[t] + pt[0][t]) + (pt[1][t] + pt[2][t]); return (u32x2){pk(v[0], v[1]), pk(v[2], v[3])}; };
#pragma unroll
    for (int pp = 0; pp < 2; ++pp) { const u32x2 lo = fin(2 * pp), hi = fin(2 * pp + 1); st16<0>((u32x4){lo.x, lo.y, hi.x, hi.y}, ro, xo + q * 16, dp * 2 + 64 * pp); }
    const u32x2 ls = fin(4);
    if (own0) st8<0>(ls, ro, xo + q * 8, ds * 2);
  }
  STAMP(14);
}
}

extern "C" __global__ __launch_bounds__(512, 2) void h3_gmlp_fused(const Params P) {
  extern __shared__ __attribute__((aligned(16))) char smem_[];
  LAS char* sm = (LAS char*)smem_;
  const int wid = __builtin_amdgcn_readfirstlane(threadIdx.x >> 6);
  Ids id; id.b = blockIdx.x; id.lane = threadIdx.x & 63;
  const int xcd = id.b & 7, loc = id.b >> 3;
  id.tp = (xcd >> 1) * 4 + (loc >> 3); id.ft = (xcd & 1) * 8 + (loc & 7);
  if (wid < 4) { unsigned sink = 0;
    if (wid == 0) code_touch(id, sink);
    else if (OPT_TWIN) { const rsrc_t r = mkrs(P.win, D * F * 2); const unsigned n = (wid - 1) * 64 + id.lane;
      if (OPT_TWIN > 1) __builtin_amdgcn_s_sleep(OPT_TWIN);
      unsigned o;
      if (TWZ == 0) o = n < 162 ? id.b * 20736 + n * 128 : 0xF0000000u;
      else { const unsigned m = id.b * 81 + n, row = m / 36; o = n < 81 ? row * (F * 2) + FH * 2 + (m - row * 36) * 128 : 0xF0000000u; }
      asm volatile("buffer_load_dword %0, %1, %2, 0 offen" : "+v"(sink) : "v"(o), "s"(r) : "memory"); }
    asm volatile("s_waitcnt vmcnt(0)" : "+v"(sink) :: "memory"); }
  if (__builtin_expect(wid == 4 || wid == 5, 1)) role_L(P, sm, id, wid - 4 STAMP_PASS);
  else if (wid >= 6) role_X(P, sm, id, wid - 6 STAMP_PASS);
  else role_C(P, sm, id, wid STAMP_PASS);
}
extern "C" __global__ void h3_gmlp_end() {}

extern "C" void kernel_launch(void* const* d_in, const int* in_sizes, int n_in, void* d_out, int out_size, void* d_ws, size_t ws_size, hipStream_t stream) {
  Params p;
  p.x = (const u16*)d_in[0]; p.k = (const u16*)d_in[1]; p.v = (const u16*)d_in[2]; p.shk = (const u16*)d_in[3]; p.ssmk = (const u16*)d_in[4]; p.ssmd = (const u16*)d_in[5];
  p.win = (const u16*)d_in[7]; p.wsp = (const u16*)d_in[8]; p.bsp = (const u16*)d_in[9]; p.wout = (const u16*)d_in[10];
  p.out = (u16*)d_out; p.ws = (char*)d_ws;
  (void)hipFuncSetAttribute((const void*)h3_gmlp_fused, hipFuncAttributeMaxDynamicSharedMemorySize, LDS_BYTES);
  h3_gmlp_fused<<<256, 512, LDS_BYTES, stream>>>(p);
}
```
